```python
import torch

def _softmax(x, axis=-1):
    return torch.softmax(x, dim=axis)
def _gelu(x):
    return torch.nn.functional.gelu(x, approximate="tanh")
def _silu(x):
    return torch.nn.functional.silu(x)
def _sigmoid(x):
    return torch.sigmoid(x)
def _layernorm(x, axis=-1, eps=1e-5):
    m = x.mean(dim=axis, keepdim=True)
    v = x.var(dim=axis, keepdim=True, unbiased=False)
    return (x - m) / torch.sqrt(v + eps)
def _scatter_add(tgt, idx, src, axis=0):
    return tgt.clone().index_add_(axis, idx, src)
def _segment_sum(src, seg, n):
    out = torch.zeros((n,) + src.shape[1:], dtype=src.dtype)
    return out.index_add_(0, seg, src)
def _pad(x, pw):
    flat = tuple(v for pair in reversed(pw) for v in pair)
    return torch.nn.functional.pad(x, flat)

kernel_name = 'parallel_sum__sliding_block__mixer_channel_mlp__prompt'

def setup_inputs():

    _w = (lambda a: (lambda t: t.to(torch.bfloat16) if t.is_floating_point() else t)(torch.as_tensor(a)))
    def _A_setup():
        B, S, D = 1, 2048, 1024
        return {
            "x": _w((lambda *s: torch.randn(*s, dtype=torch.bfloat16) * 0.05)(B, S, D)),
            "wqkv": _w((lambda *s: torch.randn(*s, dtype=torch.bfloat16) * 0.05)(D, 3 * D)),
            "wo": _w((lambda *s: torch.randn(*s, dtype=torch.bfloat16) * 0.05)(D, D)),
        }
    _A_in = _A_setup()

    def _B_setup():
        B, S, D, F = 1, 2048, 1024, 3584
        return {
            "x": _w((lambda *s: torch.randn(*s, dtype=torch.bfloat16) * 0.05)(B, S, D)),
            "w1": _w((lambda *s: torch.randn(*s, dtype=torch.bfloat16) * 3.2)(D, F) * (D ** -0.5)),
            "b1": _w((lambda *s: torch.randn(*s, dtype=torch.bfloat16) * 1.6)(F)),
            "w2": _w((lambda *s: torch.randn(*s, dtype=torch.bfloat16) * 0.4)(F, D) * (F ** -0.5)),
            "b2": _w((lambda *s: torch.randn(*s, dtype=torch.bfloat16) * 0.8)(D)),
        }
    _B_in = _B_setup()

    return {
        "A_x": _A_in["x"],
        "A_wqkv": _A_in["wqkv"],
        "A_wo": _A_in["wo"],
        "B_x": _B_in["x"],
        "B_w1": _B_in["w1"],
        "B_b1": _B_in["b1"],
        "B_w2": _B_in["w2"],
        "B_b2": _B_in["b2"],
    }

def reference(A_x, A_wqkv, A_wo, B_x, B_w1, B_b1, B_w2, B_b2):
    def _A_ref(x, wqkv, wo):
        B, S, D = x.shape
        H = 32; Dh = D // H; W = 256
        qkv = (lambda x, *d: x.permute(*d))((x @ wqkv).reshape(B, S, 3, H, Dh), 2, 0, 3, 1, 4)
        q, k, v = qkv[0], qkv[1], qkv[2]
        scores = torch.einsum("bhqd,bhkd->bhqk", q, k) * (Dh ** -0.5)
        idx = torch.arange(S)
        mask = (idx[:, None] - idx[None, :] >= 0) & (idx[:, None] - idx[None, :] < W)
        scores = torch.where(mask[None, None], scores, -1e9)
        p = _softmax(scores, axis=-1)
        o = torch.einsum("bhqk,bhkd->bhqd", p, v)
        return (lambda x, *d: x.permute(*d))(o, 0, 2, 1, 3).reshape(B, S, D) @ wo

    def _B_ref(x, w1, b1, w2, b2):
        y = _layernorm(x, axis=-1)
        h = _gelu(torch.einsum("bsd,df->bsf", y, w1) + b1)
        return torch.einsum("bsf,fd->bsd", h, w2) + b2

    x = A_x
    xn = _layernorm(x, axis=-1)
    return x + _A_ref(xn, A_wqkv, A_wo) + _B_ref(xn, B_w1, B_b1, B_w2, B_b2)
```

```cpp
#include <hip/hip_runtime.h>
#include <hip/hip_bf16.h>
#include <stdint.h>
typedef unsigned short u16;
typedef short bf16x8 __attribute__((ext_vector_type(8)));
typedef float f32x4 __attribute__((ext_vector_type(4)));
constexpr int S=2048, D=1024, F=3584, H=32, DH=32, W=256;
__device__ __forceinline__ float bf2f(u16 v){ return __uint_as_float(((unsigned)v)<<16); }
__device__ __forceinline__ u16 f2bf(float f){ __bf16 b=(__bf16)f; return __builtin_bit_cast(u16,b); }

__global__ __launch_bounds__(256) void k_ln(const u16* x, u16* xn, u16* y){
  int row = blockIdx.x*4 + (threadIdx.x>>6), lane=threadIdx.x&63;
  float v[16];
  const u16* p = x + (size_t)row*D;
  for(int i=0;i<16;i++) v[i]=bf2f(p[lane+64*i]);
  float s=0; for(int i=0;i<16;i++) s+=v[i];
  for(int o=32;o;o>>=1) s+=__shfl_xor(s,o);
  float m=s/D; float q=0; for(int i=0;i<16;i++){float d=v[i]-m; q+=d*d;}
  for(int o=32;o;o>>=1) q+=__shfl_xor(q,o);
  float r=1.f/sqrtf(q/D+1e-5f);
  for(int i=0;i<16;i++){ u16 b=f2bf((v[i]-m)*r); xn[(size_t)row*D+lane+64*i]=b; v[i]=bf2f(b);}
  s=0; for(int i=0;i<16;i++) s+=v[i];
  for(int o=32;o;o>>=1) s+=__shfl_xor(s,o);
  m=s/D; q=0; for(int i=0;i<16;i++){float d=v[i]-m; q+=d*d;}
  for(int o=32;o;o>>=1) q+=__shfl_xor(q,o);
  r=1.f/sqrtf(q/D+1e-5f);
  for(int i=0;i<16;i++){ y[(size_t)row*D+lane+64*i]=f2bf((v[i]-m)*r);}
}
template<int MODE>
__global__ __launch_bounds__(256) void k_gemm(const u16* A,int lda,const u16* B,int N,int K,u16* C,int ldc,const u16* bias,float* P,const u16* x){
  __shared__ u16 As[64][40]; __shared__ u16 Bs[64][40];
  int tid=threadIdx.x, lane=tid&63, w=tid>>6, wr=w>>1, wc=w&1;
  int m0=blockIdx.y*64, n0=blockIdx.x*64;
  f32x4 acc[2][2]={};
  for(int k0=0;k0<K;k0+=32){
    { int r=tid>>2, c=(tid&3)*8; *(bf16x8*)&As[r][c] = *(const bf16x8*)&A[(size_t)(m0+r)*lda+k0+c]; }
    { int kk=tid>>3, c=(tid&7)*8; bf16x8 v=*(const bf16x8*)&B[(size_t)(k0+kk)*N+n0+c]; for(int j=0;j<8;j++) Bs[c+j][kk]=(u16)v[j]; }
    __syncthreads();
    bf16x8 a[2],b[2];
    for(int i=0;i<2;i++){ a[i]=*(bf16x8*)&As[wr*32+i*16+(lane&15)][(lane>>4)*8]; b[i]=*(bf16x8*)&Bs[wc*32+i*16+(lane&15)][(lane>>4)*8]; }
    for(int i=0;i<2;i++)for(int j=0;j<2;j++) acc[i][j]=__builtin_amdgcn_mfma_f32_16x16x32_bf16(a[i],b[j],acc[i][j],0,0,0);
    __syncthreads();
  }
  for(int i=0;i<2;i++)for(int j=0;j<2;j++)for(int r=0;r<4;r++){
    int m=m0+wr*32+i*16+(lane>>4)*4+r, n=n0+wc*32+j*16+(lane&15);
    float v=acc[i][j][r];
    if(MODE==0) C[(size_t)m*ldc+n]=f2bf(v);
    if(MODE==1){ v+=bf2f(bias[n]); float u=0.7978845608f*(v+0.044715f*v*v*v); float t=tanhf(u); C[(size_t)m*ldc+n]=f2bf(0.5f*v*(1.f+t)); }
    if(MODE==2) P[(size_t)m*ldc+n]=v;
    if(MODE==3) C[(size_t)m*ldc+n]=f2bf(v+P[(size_t)m*ldc+n]+bf2f(bias[n])+bf2f(x[(size_t)m*ldc+n]));
  }
}
__global__ __launch_bounds__(64) void k_attn(const u16* qkv, u16* o){
  int h=blockIdx.y, qi=blockIdx.x*64+threadIdx.x;
  float q[32]; for(int d=0;d<32;d++) q[d]=bf2f(qkv[(size_t)qi*3*D+h*32+d])*0.17677669529663687f;
  float m=-3e38f,l=0,acc[32]; for(int d=0;d<32;d++) acc[d]=0;
  int k0=qi-(W-1); if(k0<0)k0=0;
  for(int k=k0;k<=qi;k++){
    const u16* kp=qkv+(size_t)k*3*D+D+h*32; const u16* vp=kp+D;
    float s=0; for(int d=0;d<32;d++) s+=q[d]*bf2f(kp[d]);
    float mn=fmaxf(m,s), a=__expf(m-mn), p=__expf(s-mn); l=l*a+p; m=mn;
    for(int d=0;d<32;d++) acc[d]=acc[d]*a+p*bf2f(vp[d]);
  }
  for(int d=0;d<32;d++) o[(size_t)qi*D+h*32+d]=f2bf(acc[d]/l);
}
extern "C" void kernel_launch(void* const* d_in, const int* in_sizes, int n_in, void* d_out, int out_size, void* d_ws, size_t ws_size, hipStream_t stream){
  const u16* x=(const u16*)d_in[0]; const u16* wqkv=(const u16*)d_in[1]; const u16* wo=(const u16*)d_in[2];
  const u16* w1=(const u16*)d_in[4]; const u16* b1=(const u16*)d_in[5]; const u16* w2=(const u16*)d_in[6]; const u16* b2=(const u16*)d_in[7];
  char* ws=(char*)d_ws;
  u16* xn=(u16*)ws; u16* y=(u16*)(ws+(4<<20)); u16* qkv=(u16*)(ws+(8<<20)); u16* hh=(u16*)(ws+(20<<20)); u16* o=(u16*)(ws+(36<<20)); float* P=(float*)(ws+(40<<20));
  k_ln<<<S/4,256,0,stream>>>(x,xn,y);
  k_gemm<0><<<dim3(3*D/64,S/64),256,0,stream>>>(xn,D,wqkv,3*D,D,qkv,3*D,nullptr,nullptr,nullptr);
  k_gemm<1><<<dim3(F/64,S/64),256,0,stream>>>(y,D,w1,F,D,hh,F,b1,nullptr,nullptr);
  k_attn<<<dim3(S/64,H),64,0,stream>>>(qkv,o);
  k_gemm<2><<<dim3(D/64,S/64),256,0,stream>>>(o,D,wo,D,D,nullptr,D,nullptr,P,nullptr);
  k_gemm<3><<<dim3(D/64,S/64),256,0,stream>>>(hh,F,w2,D,F,(u16*)d_out,D,b2,P,x);
}
```

```cpp
#include <hip/hip_runtime.h>
struct Buf { const void* p; unsigned n; unsigned f; };
struct Args { Buf x, wqkv, w1, ws, w2, wo, b1, b2, out; };
__global__ __attribute__((aligned(4096))) __launch_bounds__(256,1) void fused_block(Args){
  asm volatile(
"s_load_dwordx16 s[4:19], s[0:1], 0\n"
"s_load_dwordx16 s[20:35], s[0:1], 64\n"
"s_load_dwordx4 s[36:39], s[0:1], 128\n"
"v_readfirstlane_b32 s40, v0\n"
"v_and_b32_e64 v0, 63, v0\n"
"s_xor_b32 s96, s2, 0\n"
"s_getpc_b64 s[86:87]\n"
".Lpc:\n"
"s_lshr_b32 s97, s96, 3\n"
"s_lshr_b32 s40, s40, 6\n"
"s_lshl2_add_u32 s97, s97, s40\n"
"s_mul_i32 s97, s97, 640\n"
"s_and_b32 s85, s87, 0xffff\n"
"s_mov_b32 s84, s86\n"
"s_mov_b32 s86, .Lend-.Lpc\n"
"s_mov_b32 s87, 0x20000\n"
"v_lshlrev_b32_e64 v40, 7, v0\n"
"s_mov_b64 exec, 31\n"
"buffer_load_dword a252, v40, s[84:87], s97 offen\n"
"s_mov_b64 exec, -1\n"
"s_mov_b64 s[76:77], s[84:85]\n"
"s_and_b32 s41, s96, 7\n"
"s_bfe_u32 s42, s96, 0x20003\n"
"s_lshr_b32 s43, s96, 5\n"
"s_lshl2_add_u32 s44, s41, s42\n"
"s_and_b32 s47, s42, 1\n"
"v_and_b32 v40, 7, v0\n"
"v_lshrrev_b32 v41, 3, v0\n"
"v_lshrrev_b32 v42, 4, v0\n"
"v_lshlrev_b32 v43, 4, v40\n"
"v_xor_b32 v45, v40, v42\n"
"v_lshlrev_b32 v45, 4, v45\n"
"v_lshl_add_u32 v8, v41, 11, v45\n"
"v_xor_b32_e64 v9, 64, v8\n"
"v_add_u32 v9, 15360, v9\n"
"s_lshl2_add_u32 s48, s43, s40\n"
"s_lshl_b32 s48, s48, 17\n"
"s_mov_b32 s51, s48\n"
"s_lshl_b32 s91, s40, 13\n"
"s_add_u32 s66, s42, 0\n"
"s_and_b32 s66, s66, 3\n"
"s_lshl_b32 s68, s66, 15\n"
"s_lshl_b32 s66, s66, 11\n"
"s_add_u32 s92, s66, s91\n"
"v_add_u32 v2, s68, v8\n"
"v_add_u32 v3, s68, v9\n"
"s_lshl_b32 s64, s42, 3\n"
"s_lshr_b32 s64, 1227306048, s64\n"
"s_bfe_u32 s69, s64, 0x40004\n"
"s_and_b32 s64, s64, 15\n"
"s_add_u32 s64, s64, s40\n"
"s_lshr_b32 s65, s64, 1\n"
"s_lshl_b32 s65, s65, 11\n"
"s_and_b32 s66, s64, 1\n"
"s_lshl_b32 s66, s66, 7\n"
"s_add_u32 s65, s65, s66\n"
"s_lshl_b32 s66, s41, 8\n"
"s_add_u32 s65, s65, s66\n"
"s_mul_i32 s66, s41, 896\n"
"s_lshl_b32 s67, s64, 7\n"
"s_add_u32 s66, s66, s67\n"
"s_sub_u32 s66, s66, 768\n"
"s_cmp_lt_u32 s64, 6\n"
"s_cselect_b32 s65, s65, s66\n"
"s_movk_i32 s68, 7168\n"
"s_cselect_b32 s67, 6144, s68\n"
"s_lshl_b32 s59, s67, 6\n"
"s_mul_i32 s68, s43, s67\n"
"s_lshl3_add_u32 s58, s68, s65\n"
"v_mad_u32_u24 v25, v41, s67, v43\n"
"s_lshl_b32 s65, s43, 7\n"
"s_lshl2_add_u32 s65, s44, s65\n"
"s_add_u32 s65, s65, s40\n"
"s_lshl_b32 s73, s65, 12\n"
"v_and_b32 v44, 1, v41\n"
"v_lshl_add_u32 v45, v42, 7, v43\n"
"v_lshl_add_u32 v26, v44, 11, v45\n"
"v_lshrrev_b32 v45, 5, v0\n"
"v_and_b32 v46, 3, v0\n"
"v_lshlrev_b32 v45, 1, v45\n"
"v_xor_b32 v45, v45, v46\n"
"v_lshlrev_b32 v45, 4, v45\n"
"v_lshrrev_b32 v46, 2, v0\n"
"v_mul_u32_u24 v10, 0x1800, v46\n"
"v_mul_u32_u24 v13, 0x1c00, v46\n"
"v_add_u32 v10, v10, v45\n"
"v_add_u32 v13, v13, v45\n"
"v_add_u32 v11, 0x400, v10\n"
"v_add_u32 v12, 0x800, v10\n"
"v_add_u32 v14, 3136, v13\n"
"s_lshl_b32 s66, s47, 7\n"
"s_sub_u32 s68, 2176, s66\n"
"v_add_u32 v15, s68, v13\n"
"s_add_u32 s68, s66, 4096\n"
"v_add_u32_e64 v13, s68, v13\n"
"s_mul_i32 s49, s40, 98304\n"
"s_lshl_b32 s66, s44, 6\n"
"s_add_u32 s49, s49, s66\n"
"s_mul_i32 s50, s40, 114688\n"
"s_mul_i32 s66, s44, 224\n"
"s_add_u32 s50, s50, s66\n"
"s_lshl_b32 s66, s47, 5\n"
"s_add_u32 s50, s50, s66\n"
"s_mul_i32 s52, s40, 6656\n"
"s_add_u32 s52, s52, 65536\n"
"v_bfe_u32 v45, v0, 1, 2\n"
"v_lshl_add_u32 v45, v44, 3, v45\n"
"v_and_b32 v46, 1, v42\n"
"v_lshl_add_u32 v45, v46, 2, v45\n"
"v_mul_u32_u24 v16, 0x1c00, v45\n"
"v_and_b32_e64 v45, 1, v0\n"
"v_lshl_add_u32 v16, v45, 4, v16\n"
"s_mul_i32 s66, s47, 224\n"
"s_sub_u32 s66, 1216, s66\n"
"v_add_u32 v16, s66, v16\n"
"s_add_u32 s66, s42, 1\n"
"s_and_b32 s66, s66, 3\n"
"s_lshl_b32 s68, s66, 15\n"
"s_lshl_b32 s66, s66, 11\n"
"s_add_u32 s93, s66, s91\n"
"v_add_u32 v4, s68, v8\n"
"v_add_u32 v5, s68, v9\n"
"s_add_u32 s66, s42, 2\n"
"s_and_b32 s66, s66, 3\n"
"s_lshl_b32 s68, s66, 15\n"
"s_lshl_b32 s66, s66, 11\n"
"s_add_u32 s94, s66, s91\n"
"v_add_u32 v6, s68, v8\n"
"v_add_u32 v7, s68, v9\n"
"s_add_u32 s66, s42, 3\n"
"s_and_b32 s66, s66, 3\n"
"s_lshl_b32 s68, s66, 15\n"
"s_lshl_b32 s66, s66, 11\n"
"s_add_u32 s95, s66, s91\n"
"v_add_u32 v8, s68, v8\n"
"v_add_u32 v9, s68, v9\n"
"s_cmp_lt_u32 s64, 6\n"
"s_waitcnt lgkmcnt(0)\n"
"s_cselect_b64 s[60:61], s[8:9], s[12:13]\n"
"s_cselect_b64 s[62:63], s[10:11], s[14:15]\n"
"s_cmp_lt_u32 s40, s69\n"
"s_cselect_b32 s62, s62, 0\n"
"buffer_load_dwordx4 a[168:171], v25, s[60:63], s58 offen\n"
"s_add_u32 s58, s58, s59\n"
"s_add_u32 m0, s92, 0\n"
"s_nop 0\n"
"s_nop 0\n"
"buffer_load_dwordx4 v2, s[4:7], s51 offen offset:0 lds\n"
"buffer_load_dwordx4 v3, s[4:7], s51 offen offset:1024 lds\n"
"s_add_u32 s51, s51, 128\n"
"buffer_load_dwordx4 v[30:33], v26, s[4:7], s73 offen offset:0\n"
"s_sub_u32 s12, s12, 4096\n"
"s_subb_u32 s13, s13, 0\n"
"s_add_u32 s14, s14, 4096\n"
"s_add_u32 m0, s93, 0\n"
"s_nop 0\n"
"buffer_load_dwordx4 v4, s[4:7], s48 offen offset:0 lds\n"
"buffer_load_dwordx4 v5, s[4:7], s48 offen offset:1024 lds\n"
"s_add_u32 m0, s94, 0\n"
"s_nop 0\n"
"buffer_load_dwordx4 v6, s[4:7], s48 offen offset:0 lds\n"
"buffer_load_dwordx4 v7, s[4:7], s48 offen offset:1024 lds\n"
"s_add_u32 m0, s95, 0\n"
"s_nop 0\n"
"buffer_load_dwordx4 v8, s[4:7], s48 offen offset:0 lds\n"
"buffer_load_dwordx4 v9, s[4:7], s48 offen offset:1024 lds\n"
"s_add_u32 s48, s48, 128\n"
"s_mov_b64 s[0:1], s[76:77]\n"
"s_mov_b32 s2, .Lend-.Lpc\n"
"s_mov_b32 s3, 0x20000\n"
"s_lshr_b32 s45, s41, 1\n"
"s_and_b32 s46, s44, 7\n"
"v_lshlrev_b32 v1, 4, v0\n"
"s_lshl3_add_u32 s71, s45, s43\n"
"s_mul_i32 s70, s71, 589824\n"
"s_lshl_b32 s66, s40, 12\n"
"s_add_u32 s70, s70, s66\n"
"s_add_u32 s70, s70, 9437184\n"
"s_cmp_eq_u32 s40, 0\n"
"s_cselect_b64 s[56:57], 3, 0\n"
"v_and_b32 v40, 15, v0\n"
"v_lshrrev_b32 v45, 1, v40\n"
"v_xor_b32 v45, v45, v42\n"
"v_lshlrev_b32 v45, 4, v45\n"
"v_lshl_add_u32 v45, v40, 7, v45\n"
"v_add_u32 v17, s91, v45\n"
"v_xor_b32 v18, 64, v17\n"
"v_lshrrev_b32_e64 v45, 5, v0\n"
"v_mul_u32_u24 v45, 0x1a00, v45\n"
"v_add_u32 v45, 65536, v45\n"
"v_and_b32_e64 v43, 3, v0\n"
"v_lshl_add_u32 v45, v43, 3, v45\n"
"v_bfe_u32 v43, v0, 2, 2\n"
"v_lshl_add_u32 v21, v43, 5, v45\n"
"v_lshl_add_u32 v21, v46, 7, v21\n"
"v_lshl_add_u32 v19, v43, 6, v45\n"
"v_mul_u32_u24 v43, 0x220, v46\n"
"v_add_u32 v19, v19, v43\n"
"v_xor_b32 v20, 32, v19\n"
"v_add_u32 v22, 53248, v19\n"
"v_add_u32 v23, 53248, v20\n"
"v_add_u32 v24, 53248, v21\n"
"v_mov_b32_e64 v49, -1\n"
"v_accvgpr_write_b32 a236, v49\n"
"v_accvgpr_write_b32 a237, v49\n"
"v_accvgpr_write_b32 a238, v49\n"
"v_accvgpr_write_b32 a239, v49\n"
"v_mov_b32 v48, 2130706432\n"
"v_and_b32 v50, 23, v0\n"
"v_cmp_eq_u32 vcc, 0, v50\n"
"v_lshrrev_b32 v49, 5, v0\n"
"v_lshlrev_b32 v49, 2, v49\n"
"v_lshl_add_u32 v49, v44, 3, v49\n"
"s_lshl_b32 s65, s65, 4\n"
"v_add_u32_e64 v49, s65, v49\n"
"v_cndmask_b32 v49, v48, v49, vcc\n"
"v_accvgpr_write_b32 a240, v49\n"
"v_and_b32_e64 v51, 40, v0\n"
"v_cmp_eq_u32 vcc, 0, v51\n"
"s_lshl3_add_u32 s65, s41, s43\n"
"s_mul_i32 s65, s65, 3328\n"
"s_lshl_b32 s66, s64, 8\n"
"s_add_u32 s65, s65, s66\n"
"s_add_u32 s65, s65, 16384\n"
"s_cmp_lt_u32 s40, s69\n"
"s_cselect_b32 s65, s65, 2130706432\n"
"v_lshlrev_b32 v50, 5, v40\n"
"v_lshl_add_u32 v50, v42, 4, v50\n"
"v_add_u32 v50, s65, v50\n"
"v_cndmask_b32 v50, v48, v50, vcc\n"
"v_accvgpr_write_b32 a241, v50\n"
"s_lshl2_add_u32 s65, s96, s40\n"
"s_lshl_b32 s65, s65, 2\n"
"s_add_u32 s76, s65, 229376\n"
"s_lshl2_add_u32 s65, s71, s40\n"
"s_lshl3_add_u32 s65, s65, s46\n"
"s_lshl_b32 s65, s65, 2\n"
"s_add_u32 s77, s65, 233472\n"
"s_add_u32 s78, s65, 237568\n"
"s_xor_b32 s65, s96, 2\n"
"s_lshl2_add_u32 s79, s65, s40\n"
"s_lshl_b32 s65, s79, 4\n"
"s_add_u32 s65, s65, 241664\n"
"v_writelane_b32 v49, s65, 4\n"
"s_xor_b32 s65, s96, 4\n"
"s_lshl2_add_u32 s80, s65, s40\n"
"s_lshl_b32 s65, s80, 4\n"
"s_add_u32 s65, s65, 241668\n"
"v_writelane_b32 v49, s65, 5\n"
"s_xor_b32 s65, s96, 6\n"
"s_lshl2_add_u32 s81, s65, s40\n"
"s_lshl_b32 s65, s81, 4\n"
"s_add_u32 s65, s65, 241672\n"
"v_writelane_b32 v49, s65, 6\n"
"v_writelane_b32 v49, s76, 1\n"
"v_writelane_b32 v49, s77, 2\n"
"v_writelane_b32 v49, s78, 3\n"
"s_mov_b64 exec, 0\n"
"s_cmp_eq_u32 s40, 0\n"
"s_cbranch_scc1 .Lpad1\n"
"s_cmp_eq_u32 s40, 1\n"
"s_cbranch_scc1 .Lpad2\n"
"s_cmp_eq_u32 s40, 2\n"
"s_cbranch_scc1 .Lpad3\n"
"s_branch .Lpad4\n"
".Lwr0:\n"
"s_mov_b64 exec, -1\n"
"s_add_u32 m0, s52, 3072\n"
"s_nop 0\n"
"buffer_load_dwordx4 v13, s[12:15], s50 offen offset:0 lds\n"
"buffer_load_dwordx4 v14, s[12:15], s50 offen offset:1024 lds\n"
"buffer_load_dwordx4 v15, s[12:15], s50 offen offset:2048 lds\n"
"s_mov_b32 exec_hi, 0\n"
"buffer_load_dwordx4 v16, s[12:15], s50 offen offset:3072 lds\n"
"s_mov_b32 exec_hi, -1\n"
"s_add_u32 s50, s50, 458752\n"
"s_add_u32 m0, s52, 0\n"
"s_nop 0\n"
"buffer_load_dwordx4 v10, s[8:11], s49 offen offset:0 lds\n"
"buffer_load_dwordx4 v11, s[8:11], s49 offen offset:1024 lds\n"
"buffer_load_dwordx4 v12, s[8:11], s49 offen offset:2048 lds\n"
"s_add_u32 s49, s49, 393216\n"
"s_barrier\n"
"buffer_store_dword a236, v49, s[16:19], 0 offen sc1\n"
"buffer_store_dwordx4 a[236:239], v50, s[16:19], 0 offen sc1\n"
"buffer_load_dwordx4 a[172:175], v25, s[60:63], s58 offen\n"
"s_add_u32 s58, s58, s59\n"
"s_add_u32 m0, s92, 32768\n"
"s_nop 0\n"
"buffer_load_dwordx4 v2, s[4:7], s51 offen offset:0 lds\n"
"buffer_load_dwordx4 v3, s[4:7], s51 offen offset:1024 lds\n"
"s_add_u32 s51, s51, 128\n"
"buffer_load_dwordx4 a[176:179], v25, s[60:63], s58 offen\n"
"s_add_u32 s58, s58, s59\n"
"s_waitcnt vmcnt(6)\n"
"s_nop 0\n"
"ds_read_b128 a[0:3], v17 offset:0\n"
"ds_read_b128 a[16:19], v18 offset:0\n"
"ds_read_b128 a[4:7], v17 offset:2048\n"
"ds_read_b128 a[20:23], v18 offset:2048\n"
"ds_read_b128 a[8:11], v17 offset:4096\n"
"ds_read_b128 a[24:27], v18 offset:4096\n"
"ds_read_b128 a[12:15], v17 offset:6144\n"
"ds_read_b128 a[28:31], v18 offset:6144\n"
"s_barrier\n"
"s_nop 0\n"
"ds_read_b64_tr_b16 a[64:65], v19 offset:1024\n"
"ds_read_b64_tr_b16 a[66:67], v19 offset:1280\n"
"ds_read_b64_tr_b16 a[68:69], v19 offset:14336\n"
"ds_read_b64_tr_b16 a[70:71], v19 offset:14592\n"
"ds_read_b64_tr_b16 a[72:73], v20 offset:1024\n"
"ds_read_b64_tr_b16 a[74:75], v20 offset:1280\n"
"ds_read_b64_tr_b16 a[76:77], v20 offset:14336\n"
"ds_read_b64_tr_b16 a[78:79], v20 offset:14592\n"
"ds_read_b64_tr_b16 a[80:81], v19 offset:2048\n"
"ds_read_b64_tr_b16 a[82:83], v19 offset:2304\n"
"ds_read_b64_tr_b16 a[84:85], v19 offset:15360\n"
"ds_read_b64_tr_b16 a[86:87], v19 offset:15616\n"
"s_waitcnt lgkmcnt(8)\n"
"v_mfma_f32_16x16x32_bf16 v[80:83], a[64:67], a[0:3], 0\n"
"ds_read_b64_tr_b16 a[88:89], v20 offset:2048\n"
"s_add_u32 m0, s93, 32768\n"
"v_mfma_f32_16x16x32_bf16 v[80:83], a[68:71], a[16:19], v[80:83]\n"
"v_and_b32 v43, 1, v40\n"
"v_lshlrev_b32 v43, 4, v43\n"
"v_mfma_f32_16x16x32_bf16 v[84:87], a[64:67], a[4:7], 0\n"
"ds_read_b64_tr_b16 a[90:91], v20 offset:2304\n"
"v_mov_b32 v45, 0x3f80\n"
"v_mfma_f32_16x16x32_bf16 v[84:87], a[68:71], a[20:23], v[84:87]\n"
"buffer_load_dwordx4 v4, s[4:7], s48 offen offset:0 lds\n"
"v_lshlrev_b32 v43, v43, v45\n"
"v_mfma_f32_16x16x32_bf16 v[88:91], a[64:67], a[8:11], 0\n"
"ds_read_b64_tr_b16 a[92:93], v20 offset:15360\n"
"v_lshrrev_b32 v45, 1, v40\n"
"v_mfma_f32_16x16x32_bf16 v[88:91], a[68:71], a[24:27], v[88:91]\n"
"buffer_load_dwordx4 v5, s[4:7], s48 offen offset:1024 lds\n"
"v_cmp_eq_u32 vcc, 0, v45\n"
"v_mfma_f32_16x16x32_bf16 v[92:95], a[64:67], a[12:15], 0\n"
"ds_read_b64_tr_b16 a[94:95], v20 offset:15616\n"
"v_mfma_f32_16x16x32_bf16 v[92:95], a[68:71], a[28:31], v[92:95]\n"
"v_cndmask_b32 v27, 0, v43, vcc\n"
"s_waitcnt lgkmcnt(8)\n"
"v_mfma_f32_16x16x32_bf16 v[96:99], a[72:75], a[0:3], 0\n"
"ds_read_b64_tr_b16 a[96:97], v19 offset:0\n"
"v_accvgpr_write_b32 a232, v27\n"
"v_mfma_f32_16x16x32_bf16 v[96:99], a[76:79], a[16:19], v[96:99]\n"
"s_add_u32 m0, s94, 32768\n"
"v_cmp_eq_u32 vcc, 1, v45\n"
"v_mfma_f32_16x16x32_bf16 v[100:103], a[72:75], a[4:7], 0\n"
"ds_read_b64_tr_b16 a[98:99], v19 offset:256\n"
"v_mfma_f32_16x16x32_bf16 v[100:103], a[76:79], a[20:23], v[100:103]\n"
"buffer_load_dwordx4 v6, s[4:7], s48 offen offset:0 lds\n"
"v_cndmask_b32 v27, 0, v43, vcc\n"
"v_mfma_f32_16x16x32_bf16 v[104:107], a[72:75], a[8:11], 0\n"
"ds_read_b64_tr_b16 a[100:101], v19 offset:13312\n"
"v_accvgpr_write_b32 a233, v27\n"
"v_mfma_f32_16x16x32_bf16 v[104:107], a[76:79], a[24:27], v[104:107]\n"
"v_cmp_eq_u32 vcc, 2, v45\n"
"v_mfma_f32_16x16x32_bf16 v[108:111], a[72:75], a[12:15], 0\n"
"ds_read_b64_tr_b16 a[102:103], v19 offset:13568\n"
"v_mfma_f32_16x16x32_bf16 v[108:111], a[76:79], a[28:31], v[108:111]\n"
"v_cndmask_b32 v27, 0, v43, vcc\n"
"s_waitcnt lgkmcnt(8)\n"
"v_mfma_f32_16x16x32_bf16 v[112:115], a[0:3], a[80:83], 0\n"
"ds_read_b64_tr_b16 a[104:105], v20 offset:0\n"
"v_accvgpr_write_b32 a234, v27\n"
"v_mfma_f32_16x16x32_bf16 v[112:115], a[16:19], a[84:87], v[112:115]\n"
"buffer_load_dwordx4 v7, s[4:7], s48 offen offset:1024 lds\n"
"v_cmp_eq_u32 vcc, 3, v45\n"
"v_mfma_f32_16x16x32_bf16 v[116:119], a[4:7], a[80:83], 0\n"
"ds_read_b64_tr_b16 a[106:107], v20 offset:256\n"
"s_add_u32 m0, s95, 32768\n"
"v_mfma_f32_16x16x32_bf16 v[116:119], a[20:23], a[84:87], v[116:119]\n"
"v_cndmask_b32 v27, 0, v43, vcc\n"
"v_accvgpr_write_b32 a235, v27\n"
"v_mfma_f32_16x16x32_bf16 v[120:123], a[8:11], a[80:83], 0\n"
"ds_read_b64_tr_b16 a[108:109], v20 offset:13312\n"
"v_mov_b32 v27, 2130706432\n"
"v_mfma_f32_16x16x32_bf16 v[120:123], a[24:27], a[84:87], v[120:123]\n"
"buffer_load_dwordx4 v8, s[4:7], s48 offen offset:0 lds\n"
"v_mfma_f32_16x16x32_bf16 v[124:127], a[12:15], a[80:83], 0\n"
"ds_read_b64_tr_b16 a[110:111], v20 offset:13568\n"
"s_lshl_b32 s65, s43, 8\n"
"v_mfma_f32_16x16x32_bf16 v[124:127], a[28:31], a[84:87], v[124:127]\n"
"s_add_u32 s65, s65, 258048\n"
"s_waitcnt lgkmcnt(8)\n"
"v_mfma_f32_16x16x32_bf16 v[128:131], a[0:3], a[88:91], 0\n"
"ds_read_b64_tr_b16 a[112:113], v19 offset:3072\n"
"v_mov_b32 v29, s65\n"
"v_mfma_f32_16x16x32_bf16 v[128:131], a[16:19], a[92:95], v[128:131]\n"
"buffer_load_dwordx4 v9, s[4:7], s48 offen offset:1024 lds\n"
"v_mfma_f32_16x16x32_bf16 v[132:135], a[4:7], a[88:91], 0\n"
"ds_read_b64_tr_b16 a[114:115], v19 offset:3328\n"
"s_add_u32 s48, s48, 128\n"
"v_mfma_f32_16x16x32_bf16 v[132:135], a[20:23], a[92:95], v[132:135]\n"
"s_add_u32 m0, s52, 26624\n"
"s_lshl_b32 s65, s45, 8\n"
"s_add_u32 s65, s65, 260096\n"
"v_mfma_f32_16x16x32_bf16 v[136:139], a[8:11], a[88:91], 0\n"
"ds_read_b64_tr_b16 a[116:117], v19 offset:16384\n"
"v_writelane_b32 v29, s65, 1\n"
"v_mfma_f32_16x16x32_bf16 v[136:139], a[24:27], a[92:95], v[136:139]\n"
"buffer_load_dwordx4 v10, s[8:11], s49 offen offset:0 lds\n"
"v_mfma_f32_16x16x32_bf16 v[140:143], a[12:15], a[88:91], 0\n"
"ds_read_b64_tr_b16 a[118:119], v19 offset:16640\n"
"s_lshl_b32 s65, s40, 6\n"
"v_mfma_f32_16x16x32_bf16 v[140:143], a[28:31], a[92:95], v[140:143]\n"
"v_add_u32 v28, s65, v0\n"
"s_waitcnt lgkmcnt(8)\n"
"v_mfma_f32_16x16x32_bf16 v[48:51], a[96:99], a[0:3], 0\n"
"ds_read_b64_tr_b16 a[120:121], v20 offset:3072\n"
"s_lshl_b32 s65, s43, 11\n"
"v_mfma_f32_16x16x32_bf16 v[48:51], a[100:103], a[16:19], v[48:51]\n"
"buffer_load_dwordx4 v11, s[8:11], s49 offen offset:1024 lds\n"
"v_lshl_add_u32 v41, v28, 3, s65\n"
"v_mfma_f32_16x16x32_bf16 v[52:55], a[96:99], a[4:7], 0\n"
"ds_read_b64_tr_b16 a[122:123], v20 offset:3328\n"
"v_accvgpr_write_b32 a242, v41\n"
"v_mfma_f32_16x16x32_bf16 v[52:55], a[100:103], a[20:23], v[52:55]\n"
"v_cmp_gt_u32 vcc, 0xc0, v28\n"
"s_lshl_b32 s65, s47, 4\n"
"v_mfma_f32_16x16x32_bf16 v[56:59], a[96:99], a[8:11], 0\n"
"ds_read_b64_tr_b16 a[124:125], v20 offset:16384\n"
"s_sub_u32 s65, s65, 96\n"
"v_mfma_f32_16x16x32_bf16 v[56:59], a[100:103], a[24:27], v[56:59]\n"
"buffer_load_dwordx4 v12, s[8:11], s49 offen offset:2048 lds\n"
"v_mfma_f32_16x16x32_bf16 v[60:63], a[96:99], a[12:15], 0\n"
"ds_read_b64_tr_b16 a[126:127], v20 offset:16640\n"
"s_add_u32 s49, s49, 393216\n"
"v_mfma_f32_16x16x32_bf16 v[60:63], a[100:103], a[28:31], v[60:63]\n"
"s_mul_i32 s66, s47, 96\n"
"s_sub_u32 s66, 4294967200, s66\n"
"s_waitcnt lgkmcnt(8)\n"
"v_mfma_f32_16x16x32_bf16 v[64:67], a[104:107], a[0:3], 0\n"
"ds_read_b64_tr_b16 a[128:129], v19 offset:4096\n"
"s_add_u32 m0, s52, 29696\n"
"v_mfma_f32_16x16x32_bf16 v[64:67], a[108:111], a[16:19], v[64:67]\n"
"v_add_u32 v41, s65, v28\n"
"v_lshrrev_b32 v43, 5, v28\n"
"v_mfma_f32_16x16x32_bf16 v[68:71], a[104:107], a[4:7], 0\n"
"ds_read_b64_tr_b16 a[130:131], v19 offset:4352\n"
"v_sub_u32 v43, 4, v43\n"
"v_mfma_f32_16x16x32_bf16 v[68:71], a[108:111], a[20:23], v[68:71]\n"
"buffer_load_dwordx4 v13, s[12:15], s50 offen offset:0 lds\n"
"v_mfma_f32_16x16x32_bf16 v[72:75], a[104:107], a[8:11], 0\n"
"ds_read_b64_tr_b16 a[132:133], v19 offset:17408\n"
"s_lshl_b32 s65, s47, 6\n"
"v_mfma_f32_16x16x32_bf16 v[72:75], a[108:111], a[24:27], v[72:75]\n"
"buffer_load_dwordx4 v14, s[12:15], s50 offen offset:1024 lds\n"
"v_mad_i32_i24 v41, v43, s65, v41\n"
"v_mfma_f32_16x16x32_bf16 v[76:79], a[104:107], a[12:15], 0\n"
"ds_read_b64_tr_b16 a[134:135], v19 offset:17664\n"
"v_add_u32 v43, s66, v28\n"
"v_mfma_f32_16x16x32_bf16 v[76:79], a[108:111], a[28:31], v[76:79]\n"
"v_cndmask_b32 v41, v43, v41, vcc\n"
"s_waitcnt lgkmcnt(8)\n"
"v_mfma_f32_16x16x32_bf16 v[144:147], a[112:115], a[0:3], 0\n"
"ds_read_b64_tr_b16 a[136:137], v20 offset:4096\n"
"v_cmp_gt_u32 vcc, 0x60, v28\n"
"v_mfma_f32_16x16x32_bf16 v[144:147], a[116:119], a[16:19], v[144:147]\n"
"buffer_load_dwordx4 v15, s[12:15], s50 offen offset:2048 lds\n"
"v_mfma_f32_16x16x32_bf16 v[148:151], a[112:115], a[4:7], 0\n"
"ds_read_b64_tr_b16 a[138:139], v20 offset:4352\n"
"s_mul_i32 s65, s44, 112\n"
"v_mfma_f32_16x16x32_bf16 v[148:151], a[116:119], a[20:23], v[148:151]\n"
"v_add_lshl_u32 v43, v41, s65, 1\n"
"s_mul_i32 s65, s42, 112\n"
"v_mfma_f32_16x16x32_bf16 v[152:155], a[112:115], a[8:11], 0\n"
"ds_read_b64_tr_b16 a[140:141], v20 offset:17408\n"
"s_add_u32 s65, s65, 384\n"
"v_mfma_f32_16x16x32_bf16 v[152:155], a[116:119], a[24:27], v[152:155]\n"
"s_mov_b32 exec_hi, 0\n"
"buffer_load_dwordx4 v16, s[12:15], s50 offen offset:3072 lds\n"
"s_mov_b32 exec_hi, -1\n"
"v_mfma_f32_16x16x32_bf16 v[156:159], a[112:115], a[12:15], 0\n"
"ds_read_b64_tr_b16 a[142:143], v20 offset:17664\n"
"s_add_u32 s50, s50, 458752\n"
"v_mfma_f32_16x16x32_bf16 v[156:159], a[116:119], a[28:31], v[156:159]\n"
"v_add_u32 v41, s65, v41\n"
"s_waitcnt lgkmcnt(8)\n"
"v_mfma_f32_16x16x32_bf16 v[160:163], a[120:123], a[0:3], 0\n"
"ds_read_b64_tr_b16 a[144:145], v19 offset:5120\n"
"s_add_u32 m0, s92, 0\n"
"v_mfma_f32_16x16x32_bf16 v[160:163], a[124:127], a[16:19], v[160:163]\n"
"v_lshrrev_b32 v45, 5, v28\n"
"v_and_b32 v46, 31, v28\n"
"v_mfma_f32_16x16x32_bf16 v[164:167], a[120:123], a[4:7], 0\n"
"ds_read_b64_tr_b16 a[146:147], v19 offset:5376\n"
"v_lshl_add_u32 v45, v45, 7, v46\n"
"v_mfma_f32_16x16x32_bf16 v[164:167], a[124:127], a[20:23], v[164:167]\n"
"buffer_load_dwordx4 v2, s[4:7], s51 offen offset:0 lds\n"
"v_mfma_f32_16x16x32_bf16 v[168:171], a[120:123], a[8:11], 0\n"
"ds_read_b64_tr_b16 a[148:149], v19 offset:18432\n"
"s_lshl_b32 s65, s42, 5\n"
"v_mfma_f32_16x16x32_bf16 v[168:171], a[124:127], a[24:27], v[168:171]\n"
"buffer_load_dwordx4 v3, s[4:7], s51 offen offset:1024 lds\n"
"v_add_u32 v45, s65, v45\n"
"v_mfma_f32_16x16x32_bf16 v[172:175], a[120:123], a[12:15], 0\n"
"ds_read_b64_tr_b16 a[150:151], v19 offset:18688\n"
"s_add_u32 s51, s51, 128\n"
"v_mfma_f32_16x16x32_bf16 v[172:175], a[124:127], a[28:31], v[172:175]\n"
"v_cndmask_b32 v41, v41, v45, vcc\n"
"s_waitcnt lgkmcnt(8)\n"
"v_mfma_f32_16x16x32_bf16 v[176:179], a[128:131], a[0:3], 0\n"
"ds_read_b64_tr_b16 a[152:153], v20 offset:5120\n"
"v_cndmask_b32 v43, v43, v27, vcc\n"
"v_mfma_f32_16x16x32_bf16 v[176:179], a[132:135], a[16:19], v[176:179]\n"
"buffer_load_dwordx4 a[180:183], v25, s[60:63], s58 offen\n"
"v_cmp_gt_u32 vcc, 0xd0, v28\n"
"v_mfma_f32_16x16x32_bf16 v[180:183], a[128:131], a[4:7], 0\n"
"ds_read_b64_tr_b16 a[154:155], v20 offset:5376\n"
"s_add_u32 s58, s58, s59\n"
"v_mfma_f32_16x16x32_bf16 v[180:183], a[132:135], a[20:23], v[180:183]\n"
"buffer_load_dwordx4 a[184:187], v25, s[60:63], s58 offen\n"
"v_mfma_f32_16x16x32_bf16 v[184:187], a[128:131], a[8:11], 0\n"
"ds_read_b64_tr_b16 a[156:157], v20 offset:18432\n"
"s_add_u32 s58, s58, s59\n"
"v_mfma_f32_16x16x32_bf16 v[184:187], a[132:135], a[24:27], v[184:187]\n"
"buffer_load_dwordx4 a[188:191], v25, s[60:63], s58 offen\n"
"v_mfma_f32_16x16x32_bf16 v[188:191], a[128:131], a[12:15], 0\n"
"ds_read_b64_tr_b16 a[158:159], v20 offset:18688\n"
"s_add_u32 s58, s58, s59\n"
"v_mfma_f32_16x16x32_bf16 v[188:191], a[132:135], a[28:31], v[188:191]\n"
"s_mul_i32 s65, s41, 26624\n"
"s_add_u32 s65, s65, 16384\n"
"s_waitcnt lgkmcnt(8)\n"
"v_mfma_f32_16x16x32_bf16 v[192:195], a[136:139], a[0:3], 0\n"
"ds_read_b64_tr_b16 a[160:161], v21 offset:6144\n"
"s_waitcnt vmcnt(12)\n"
"v_mfma_f32_16x16x32_bf16 v[192:195], a[140:143], a[16:19], v[192:195]\n"
"buffer_load_dwordx4 a[192:195], v25, s[60:63], s58 offen\n"
"v_lshl_add_u32 v41, v41, 2, s65\n"
"v_mfma_f32_16x16x32_bf16 v[196:199], a[136:139], a[4:7], 0\n"
"ds_read_b64_tr_b16 a[162:163], v21 offset:6400\n"
"s_add_u32 s58, s58, s59\n"
"v_mfma_f32_16x16x32_bf16 v[196:199], a[140:143], a[20:23], v[196:199]\n"
"buffer_load_dwordx4 v[34:37], v26, s[4:7], s73 offen offset:512\n"
"v_cndmask_b32 v41, v27, v41, vcc\n"
"v_mfma_f32_16x16x32_bf16 v[200:203], a[136:139], a[8:11], 0\n"
"ds_read_b64_tr_b16 a[164:165], v21 offset:19456\n"
"s_add_u32 m0, s52, 53248\n"
"v_mfma_f32_16x16x32_bf16 v[200:203], a[140:143], a[24:27], v[200:203]\n"
"ds_read_b128 a[32:35], v17 offset:32768\n"
"v_cndmask_b32 v43, v27, v43, vcc\n"
"v_mfma_f32_16x16x32_bf16 v[204:207], a[136:139], a[12:15], 0\n"
"ds_read_b64_tr_b16 a[166:167], v21 offset:19712\n"
"v_accvgpr_write_b32 a243, v41\n"
"v_mfma_f32_16x16x32_bf16 v[204:207], a[140:143], a[28:31], v[204:207]\n"
"ds_read_b128 a[48:51], v18 offset:32768\n"
"s_waitcnt lgkmcnt(10)\n"
"v_mfma_f32_16x16x32_bf16 v[208:211], a[144:147], a[0:3], 0\n"
"buffer_load_dwordx4 v10, s[8:11], s49 offen offset:0 lds\n"
"v_accvgpr_write_b32 a244, v43\n"
"v_mfma_f32_16x16x32_bf16 v[208:211], a[148:151], a[16:19], v[208:211]\n"
"buffer_load_dwordx4 v11, s[8:11], s49 offen offset:1024 lds\n"
"v_mfma_f32_16x16x32_bf16 v[212:215], a[144:147], a[4:7], 0\n"
"ds_read_b128 a[36:39], v17 offset:34816\n"
"v_mfma_f32_16x16x32_bf16 v[212:215], a[148:151], a[20:23], v[212:215]\n"
"ds_read_b128 a[52:55], v18 offset:34816\n"
"v_mfma_f32_16x16x32_bf16 v[216:219], a[144:147], a[8:11], 0\n"
"ds_read_b128 a[40:43], v17 offset:36864\n"
"v_mfma_f32_16x16x32_bf16 v[216:219], a[148:151], a[24:27], v[216:219]\n"
"buffer_load_dwordx4 v12, s[8:11], s49 offen offset:2048 lds\n"
"ds_read_b128 a[56:59], v18 offset:36864\n"
"ds_read_b128 a[44:47], v17 offset:38912\n"
"ds_read_b128 a[60:63], v18 offset:38912\n"
"v_mfma_f32_16x16x32_bf16 v[220:223], a[144:147], a[12:15], 0\n"
"s_add_u32 s49, s49, 393216\n"
"v_mfma_f32_16x16x32_bf16 v[220:223], a[148:151], a[28:31], v[220:223]\n"
"s_mov_b64 exec, 0\n"
"s_cmp_eq_u32 s40, 0\n"
"s_cbranch_scc1 .Lpad9\n"
"s_cmp_eq_u32 s40, 1\n"
"s_cbranch_scc1 .Lpad10\n"
"s_cmp_eq_u32 s40, 2\n"
"s_cbranch_scc1 .Lpad11\n"
"s_branch .Lpad12\n"
".Lwr1:\n"
"s_mov_b64 exec, -1\n"
"s_waitcnt vmcnt(10) lgkmcnt(7)\n"
"s_barrier\n"
"v_mfma_f32_16x16x32_bf16 v[224:227], a[152:155], a[0:3], 0\n"
"ds_read_b64_tr_b16 a[64:65], v19 offset:27648\n"
"v_mfma_f32_16x16x32_bf16 v[224:227], a[156:159], a[16:19], v[224:227]\n"
"ds_read_b64_tr_b16 a[66:67], v19 offset:27904\n"
"v_mfma_f32_16x16x32_bf16 v[228:231], a[152:155], a[4:7], 0\n"
"ds_read_b64_tr_b16 a[68:69], v19 offset:40960\n"
"s_add_u32 m0, s93, 0\n"
"v_mfma_f32_16x16x32_bf16 v[228:231], a[156:159], a[20:23], v[228:231]\n"
"ds_read_b64_tr_b16 a[70:71], v19 offset:41216\n"
"v_mfma_f32_16x16x32_bf16 v[232:235], a[152:155], a[8:11], 0\n"
"ds_read_b64_tr_b16 a[72:73], v20 offset:27648\n"
"v_mfma_f32_16x16x32_bf16 v[232:235], a[156:159], a[24:27], v[232:235]\n"
"ds_read_b64_tr_b16 a[74:75], v20 offset:27904\n"
"v_mfma_f32_16x16x32_bf16 v[236:239], a[152:155], a[12:15], 0\n"
"ds_read_b64_tr_b16 a[76:77], v20 offset:40960\n"
"v_mfma_f32_16x16x32_bf16 v[236:239], a[156:159], a[28:31], v[236:239]\n"
"ds_read_b64_tr_b16 a[78:79], v20 offset:41216\n"
"v_mfma_f32_16x16x32_bf16 v[240:243], a[160:163], a[0:3], 0\n"
"ds_read_b64_tr_b16 a[80:81], v19 offset:28672\n"
"v_mov_b32 v27, 1\n"
"v_mfma_f32_16x16x32_bf16 v[240:243], a[164:167], a[16:19], v[240:243]\n"
"buffer_load_dwordx4 v4, s[4:7], s48 offen offset:0 lds\n"
"v_mfma_f32_16x16x32_bf16 v[244:247], a[160:163], a[4:7], 0\n"
"ds_read_b64_tr_b16 a[82:83], v19 offset:28928\n"
"v_mfma_f32_16x16x32_bf16 v[244:247], a[164:167], a[20:23], v[244:247]\n"
"buffer_load_dwordx4 v5, s[4:7], s48 offen offset:1024 lds\n"
"v_lshlrev_b32 v44, 16, v30\n"
"v_mfma_f32_16x16x32_bf16 v[248:251], a[160:163], a[8:11], 0\n"
"ds_read_b64_tr_b16 a[84:85], v19 offset:41984\n"
"v_and_b32 v30, 0xffff0000, v30\n"
"v_mfma_f32_16x16x32_bf16 v[248:251], a[164:167], a[24:27], v[248:251]\n"
"v_add_f32 v38, v44, v30\n"
"v_mul_f32 v39, v44, v44\n"
"v_mfma_f32_16x16x32_bf16 v[252:255], a[160:163], a[12:15], 0\n"
"ds_read_b64_tr_b16 a[86:87], v19 offset:42240\n"
"v_fmac_f32 v39, v30, v30\n"
"v_mfma_f32_16x16x32_bf16 v[252:255], a[164:167], a[28:31], v[252:255]\n"
"s_add_u32 m0, s94, 0\n"
"s_waitcnt lgkmcnt(8)\n"
"v_mfma_f32_16x16x32_bf16 v[80:83], a[64:67], a[32:35], v[80:83]\n"
"ds_read_b64_tr_b16 a[88:89], v20 offset:28672\n"
"v_lshlrev_b32 v44, 16, v31\n"
"v_mfma_f32_16x16x32_bf16 v[80:83], a[68:71], a[48:51], v[80:83]\n"
"buffer_load_dwordx4 v6, s[4:7], s48 offen offset:0 lds\n"
"v_and_b32 v31, 0xffff0000, v31\n"
"v_mfma_f32_16x16x32_bf16 v[84:87], a[64:67], a[36:39], v[84:87]\n"
"ds_read_b64_tr_b16 a[90:91], v20 offset:28928\n"
"v_add_f32 v38, v38, v44\n"
"v_mfma_f32_16x16x32_bf16 v[84:87], a[68:71], a[52:55], v[84:87]\n"
"v_fmac_f32 v39, v44, v44\n"
"v_add_f32 v38, v38, v31\n"
"v_mfma_f32_16x16x32_bf16 v[88:91], a[64:67], a[40:43], v[88:91]\n"
"ds_read_b64_tr_b16 a[92:93], v20 offset:41984\n"
"v_fmac_f32 v39, v31, v31\n"
"v_mfma_f32_16x16x32_bf16 v[88:91], a[68:71], a[56:59], v[88:91]\n"
"v_lshlrev_b32 v44, 16, v32\n"
"v_and_b32 v32, 0xffff0000, v32\n"
"v_mfma_f32_16x16x32_bf16 v[92:95], a[64:67], a[44:47], v[92:95]\n"
"ds_read_b64_tr_b16 a[94:95], v20 offset:42240\n"
"v_add_f32 v38, v38, v44\n"
"v_mfma_f32_16x16x32_bf16 v[92:95], a[68:71], a[60:63], v[92:95]\n"
"v_fmac_f32 v39, v44, v44\n"
".Lpad1:\n"
"s_cbranch_execz .Lpad5\n"
"s_waitcnt lgkmcnt(8)\n"
"v_mfma_f32_16x16x32_bf16 v[96:99], a[72:75], a[32:35], v[96:99]\n"
"ds_read_b64_tr_b16 a[96:97], v19 offset:26624\n"
"v_add_f32 v38, v38, v32\n"
"v_mfma_f32_16x16x32_bf16 v[96:99], a[76:79], a[48:51], v[96:99]\n"
"buffer_load_dwordx4 v7, s[4:7], s48 offen offset:1024 lds\n"
"v_fmac_f32 v39, v32, v32\n"
"v_mfma_f32_16x16x32_bf16 v[100:103], a[72:75], a[36:39], v[100:103]\n"
"ds_read_b64_tr_b16 a[98:99], v19 offset:26880\n"
"v_lshlrev_b32 v44, 16, v33\n"
"v_mfma_f32_16x16x32_bf16 v[100:103], a[76:79], a[52:55], v[100:103]\n"
"v_and_b32 v33, 0xffff0000, v33\n"
"v_add_f32 v38, v38, v44\n"
"v_mfma_f32_16x16x32_bf16 v[104:107], a[72:75], a[40:43], v[104:107]\n"
"ds_read_b64_tr_b16 a[100:101], v19 offset:39936\n"
"s_add_u32 m0, s95, 0\n"
"v_mfma_f32_16x16x32_bf16 v[104:107], a[76:79], a[56:59], v[104:107]\n"
"v_fmac_f32 v39, v44, v44\n"
"v_add_f32 v38, v38, v33\n"
"v_mfma_f32_16x16x32_bf16 v[108:111], a[72:75], a[44:47], v[108:111]\n"
"ds_read_b64_tr_b16 a[102:103], v19 offset:40192\n"
"v_fmac_f32 v39, v33, v33\n"
"v_mfma_f32_16x16x32_bf16 v[108:111], a[76:79], a[60:63], v[108:111]\n"
"s_waitcnt lgkmcnt(8)\n"
"v_mfma_f32_16x16x32_bf16 v[112:115], a[32:35], a[80:83], v[112:115]\n"
"ds_read_b64_tr_b16 a[104:105], v20 offset:26624\n"
"v_mfma_f32_16x16x32_bf16 v[112:115], a[48:51], a[84:87], v[112:115]\n"
"buffer_load_dwordx4 v8, s[4:7], s48 offen offset:0 lds\n"
"v_mfma_f32_16x16x32_bf16 v[116:119], a[36:39], a[80:83], v[116:119]\n"
"ds_read_b64_tr_b16 a[106:107], v20 offset:26880\n"
"v_mfma_f32_16x16x32_bf16 v[116:119], a[52:55], a[84:87], v[116:119]\n"
"buffer_load_dwordx4 v9, s[4:7], s48 offen offset:1024 lds\n"
"v_mfma_f32_16x16x32_bf16 v[120:123], a[40:43], a[80:83], v[120:123]\n"
"ds_read_b64_tr_b16 a[108:109], v20 offset:39936\n"
"s_add_u32 s48, s48, 128\n"
"v_mfma_f32_16x16x32_bf16 v[120:123], a[56:59], a[84:87], v[120:123]\n"
"v_mfma_f32_16x16x32_bf16 v[124:127], a[44:47], a[80:83], v[124:127]\n"
"ds_read_b64_tr_b16 a[110:111], v20 offset:40192\n"
"v_mfma_f32_16x16x32_bf16 v[124:127], a[60:63], a[84:87], v[124:127]\n"
"s_waitcnt lgkmcnt(8)\n"
"v_mfma_f32_16x16x32_bf16 v[128:131], a[32:35], a[88:91], v[128:131]\n"
"ds_read_b64_tr_b16 a[112:113], v19 offset:29696\n"
"s_add_u32 m0, s52, 56320\n"
"v_mfma_f32_16x16x32_bf16 v[128:131], a[48:51], a[92:95], v[128:131]\n"
"v_mfma_f32_16x16x32_bf16 v[132:135], a[36:39], a[88:91], v[132:135]\n"
"ds_read_b64_tr_b16 a[114:115], v19 offset:29952\n"
"v_mfma_f32_16x16x32_bf16 v[132:135], a[52:55], a[92:95], v[132:135]\n"
"buffer_load_dwordx4 v13, s[12:15], s50 offen offset:0 lds\n"
"v_mfma_f32_16x16x32_bf16 v[136:139], a[40:43], a[88:91], v[136:139]\n"
"ds_read_b64_tr_b16 a[116:117], v19 offset:43008\n"
"v_mfma_f32_16x16x32_bf16 v[136:139], a[56:59], a[92:95], v[136:139]\n"
"v_mfma_f32_16x16x32_bf16 v[140:143], a[44:47], a[88:91], v[140:143]\n"
"ds_read_b64_tr_b16 a[118:119], v19 offset:43264\n"
"v_mfma_f32_16x16x32_bf16 v[140:143], a[60:63], a[92:95], v[140:143]\n"
"s_waitcnt lgkmcnt(8)\n"
"v_mfma_f32_16x16x32_bf16 v[48:51], a[96:99], a[32:35], v[48:51]\n"
"ds_read_b64_tr_b16 a[120:121], v20 offset:29696\n"
"v_mfma_f32_16x16x32_bf16 v[48:51], a[100:103], a[48:51], v[48:51]\n"
"buffer_load_dwordx4 v14, s[12:15], s50 offen offset:1024 lds\n"
"v_mfma_f32_16x16x32_bf16 v[52:55], a[96:99], a[36:39], v[52:55]\n"
"ds_read_b64_tr_b16 a[122:123], v20 offset:29952\n"
"v_mfma_f32_16x16x32_bf16 v[52:55], a[100:103], a[52:55], v[52:55]\n"
"v_mfma_f32_16x16x32_bf16 v[56:59], a[96:99], a[40:43], v[56:59]\n"
"ds_read_b64_tr_b16 a[124:125], v20 offset:43008\n"
"v_mfma_f32_16x16x32_bf16 v[56:59], a[100:103], a[56:59], v[56:59]\n"
"buffer_load_dwordx4 v15, s[12:15], s50 offen offset:2048 lds\n"
"v_mfma_f32_16x16x32_bf16 v[60:63], a[96:99], a[44:47], v[60:63]\n"
"ds_read_b64_tr_b16 a[126:127], v20 offset:43264\n"
"v_mfma_f32_16x16x32_bf16 v[60:63], a[100:103], a[60:63], v[60:63]\n"
"s_waitcnt lgkmcnt(8)\n"
"v_mfma_f32_16x16x32_bf16 v[64:67], a[104:107], a[32:35], v[64:67]\n"
"ds_read_b64_tr_b16 a[128:129], v19 offset:30720\n"
"v_mfma_f32_16x16x32_bf16 v[64:67], a[108:111], a[48:51], v[64:67]\n"
"v_mfma_f32_16x16x32_bf16 v[68:71], a[104:107], a[36:39], v[68:71]\n"
"ds_read_b64_tr_b16 a[130:131], v19 offset:30976\n"
"v_mfma_f32_16x16x32_bf16 v[68:71], a[108:111], a[52:55], v[68:71]\n"
"s_mov_b32 exec_hi, 0\n"
"buffer_load_dwordx4 v16, s[12:15], s50 offen offset:3072 lds\n"
"s_mov_b32 exec_hi, -1\n"
"v_mfma_f32_16x16x32_bf16 v[72:75], a[104:107], a[40:43], v[72:75]\n"
"ds_read_b64_tr_b16 a[132:133], v19 offset:44032\n"
"s_add_u32 s50, s50, 458752\n"
"v_mfma_f32_16x16x32_bf16 v[72:75], a[108:111], a[56:59], v[72:75]\n"
"v_mfma_f32_16x16x32_bf16 v[76:79], a[104:107], a[44:47], v[76:79]\n"
"ds_read_b64_tr_b16 a[134:135], v19 offset:44288\n"
"v_mfma_f32_16x16x32_bf16 v[76:79], a[108:111], a[60:63], v[76:79]\n"
"s_waitcnt lgkmcnt(8)\n"
"v_mfma_f32_16x16x32_bf16 v[144:147], a[112:115], a[32:35], v[144:147]\n"
"ds_read_b64_tr_b16 a[136:137], v20 offset:30720\n"
"s_add_u32 m0, s92, 32768\n"
"v_mfma_f32_16x16x32_bf16 v[144:147], a[116:119], a[48:51], v[144:147]\n"
"s_mov_b64 exec, s[56:57]\n"
"buffer_atomic_add v27, v29, s[16:19], 0 offen sc0 sc1\n"
"s_mov_b64 exec, -1\n"
"v_mfma_f32_16x16x32_bf16 v[148:151], a[112:115], a[36:39], v[148:151]\n"
"ds_read_b64_tr_b16 a[138:139], v20 offset:30976\n"
"v_mfma_f32_16x16x32_bf16 v[148:151], a[116:119], a[52:55], v[148:151]\n"
"buffer_load_dwordx4 v2, s[4:7], s51 offen offset:0 lds\n"
"v_mfma_f32_16x16x32_bf16 v[152:155], a[112:115], a[40:43], v[152:155]\n"
"ds_read_b64_tr_b16 a[140:141], v20 offset:44032\n"
"s_waitcnt vmcnt(14)\n"
"v_mfma_f32_16x16x32_bf16 v[152:155], a[116:119], a[56:59], v[152:155]\n"
"buffer_load_dwordx4 v3, s[4:7], s51 offen offset:1024 lds\n"
"v_lshlrev_b32 v44, 16, v34\n"
"v_mfma_f32_16x16x32_bf16 v[156:159], a[112:115], a[44:47], v[156:159]\n"
"ds_read_b64_tr_b16 a[142:143], v20 offset:44288\n"
"s_add_u32 s51, s51, 128\n"
"v_mfma_f32_16x16x32_bf16 v[156:159], a[116:119], a[60:63], v[156:159]\n"
"v_and_b32 v34, 0xffff0000, v34\n"
"s_waitcnt lgkmcnt(8)\n"
"v_mfma_f32_16x16x32_bf16 v[160:163], a[120:123], a[32:35], v[160:163]\n"
"ds_read_b64_tr_b16 a[144:145], v19 offset:31744\n"
"v_add_f32 v38, v38, v44\n"
"v_mfma_f32_16x16x32_bf16 v[160:163], a[124:127], a[48:51], v[160:163]\n"
"v_fmac_f32 v39, v44, v44\n"
"v_add_f32 v38, v38, v34\n"
"v_mfma_f32_16x16x32_bf16 v[164:167], a[120:123], a[36:39], v[164:167]\n"
"ds_read_b64_tr_b16 a[146:147], v19 offset:32000\n"
"v_fmac_f32 v39, v34, v34\n"
"v_mfma_f32_16x16x32_bf16 v[164:167], a[124:127], a[52:55], v[164:167]\n"
"v_lshlrev_b32 v44, 16, v35\n"
"v_and_b32 v35, 0xffff0000, v35\n"
"v_mfma_f32_16x16x32_bf16 v[168:171], a[120:123], a[40:43], v[168:171]\n"
"ds_read_b64_tr_b16 a[148:149], v19 offset:45056\n"
"v_add_f32 v38, v38, v44\n"
"v_mfma_f32_16x16x32_bf16 v[168:171], a[124:127], a[56:59], v[168:171]\n"
"v_fmac_f32 v39, v44, v44\n"
"v_add_f32 v38, v38, v35\n"
"v_mfma_f32_16x16x32_bf16 v[172:175], a[120:123], a[44:47], v[172:175]\n"
"ds_read_b64_tr_b16 a[150:151], v19 offset:45312\n"
"v_fmac_f32 v39, v35, v35\n"
"v_mfma_f32_16x16x32_bf16 v[172:175], a[124:127], a[60:63], v[172:175]\n"
"v_lshlrev_b32 v44, 16, v36\n"
"s_waitcnt lgkmcnt(8)\n"
"v_mfma_f32_16x16x32_bf16 v[176:179], a[128:131], a[32:35], v[176:179]\n"
"ds_read_b64_tr_b16 a[152:153], v20 offset:31744\n"
"v_and_b32 v36, 0xffff0000, v36\n"
"v_mfma_f32_16x16x32_bf16 v[176:179], a[132:135], a[48:51], v[176:179]\n"
"v_add_f32 v38, v38, v44\n"
"v_fmac_f32 v39, v44, v44\n"
"v_mfma_f32_16x16x32_bf16 v[180:183], a[128:131], a[36:39], v[180:183]\n"
"ds_read_b64_tr_b16 a[154:155], v20 offset:32000\n"
"v_add_f32 v38, v38, v36\n"
"v_mfma_f32_16x16x32_bf16 v[180:183], a[132:135], a[52:55], v[180:183]\n"
"s_waitcnt vmcnt(6)\n"
"v_fmac_f32 v39, v36, v36\n"
"v_mfma_f32_16x16x32_bf16 v[184:187], a[128:131], a[40:43], v[184:187]\n"
"ds_read_b64_tr_b16 a[156:157], v20 offset:45056\n"
"s_add_u32 m0, s52, 0\n"
"v_mfma_f32_16x16x32_bf16 v[184:187], a[132:135], a[56:59], v[184:187]\n"
"ds_read_b128 a[0:3], v17 offset:0\n"
"v_lshlrev_b32 v44, 16, v37\n"
"v_mfma_f32_16x16x32_bf16 v[188:191], a[128:131], a[44:47], v[188:191]\n"
"ds_read_b64_tr_b16 a[158:159], v20 offset:45312\n"
"v_and_b32 v37, 0xffff0000, v37\n"
"v_mfma_f32_16x16x32_bf16 v[188:191], a[132:135], a[60:63], v[188:191]\n"
"ds_read_b128 a[16:19], v18 offset:0\n"
"s_waitcnt lgkmcnt(10)\n"
"v_mfma_f32_16x16x32_bf16 v[192:195], a[136:139], a[32:35], v[192:195]\n"
"ds_read_b64_tr_b16 a[160:161], v21 offset:32768\n"
"v_add_f32 v38, v38, v44\n"
"v_mfma_f32_16x16x32_bf16 v[192:195], a[140:143], a[48:51], v[192:195]\n"
"buffer_load_dwordx4 v10, s[8:11], s49 offen offset:0 lds\n"
"v_fmac_f32 v39, v44, v44\n"
"v_mfma_f32_16x16x32_bf16 v[196:199], a[136:139], a[36:39], v[196:199]\n"
"ds_read_b64_tr_b16 a[162:163], v21 offset:33024\n"
"v_add_f32 v38, v38, v37\n"
"v_mfma_f32_16x16x32_bf16 v[196:199], a[140:143], a[52:55], v[196:199]\n"
"ds_read_b128 a[4:7], v17 offset:2048\n"
"v_fmac_f32 v39, v37, v37\n"
"v_mfma_f32_16x16x32_bf16 v[200:203], a[136:139], a[40:43], v[200:203]\n"
"ds_read_b64_tr_b16 a[164:165], v21 offset:46080\n"
"v_mfma_f32_16x16x32_bf16 v[200:203], a[140:143], a[56:59], v[200:203]\n"
"buffer_load_dwordx4 v11, s[8:11], s49 offen offset:1024 lds\n"
"v_mfma_f32_16x16x32_bf16 v[204:207], a[136:139], a[44:47], v[204:207]\n"
"ds_read_b64_tr_b16 a[166:167], v21 offset:46336\n"
"v_mfma_f32_16x16x32_bf16 v[204:207], a[140:143], a[60:63], v[204:207]\n"
"ds_read_b128 a[20:23], v18 offset:2048\n"
"s_waitcnt lgkmcnt(12)\n"
"v_mfma_f32_16x16x32_bf16 v[208:211], a[144:147], a[32:35], v[208:211]\n"
"ds_read_b128 a[8:11], v17 offset:4096\n"
"v_mfma_f32_16x16x32_bf16 v[208:211], a[148:151], a[48:51], v[208:211]\n"
"ds_read_b128 a[24:27], v18 offset:4096\n"
"v_mfma_f32_16x16x32_bf16 v[212:215], a[144:147], a[36:39], v[212:215]\n"
"ds_read_b128 a[12:15], v17 offset:6144\n"
"v_mfma_f32_16x16x32_bf16 v[212:215], a[148:151], a[52:55], v[212:215]\n"
"buffer_load_dwordx4 v12, s[8:11], s49 offen offset:2048 lds\n"
"v_mfma_f32_16x16x32_bf16 v[216:219], a[144:147], a[40:43], v[216:219]\n"
"s_add_u32 s49, s49, 393216\n"
"ds_read_b128 a[28:31], v18 offset:6144\n"
"v_mfma_f32_16x16x32_bf16 v[216:219], a[148:151], a[56:59], v[216:219]\n"
"v_mfma_f32_16x16x32_bf16 v[220:223], a[144:147], a[44:47], v[220:223]\n"
"v_mfma_f32_16x16x32_bf16 v[220:223], a[148:151], a[60:63], v[220:223]\n"
"s_waitcnt vmcnt(5) lgkmcnt(5)\n"
"s_barrier\n"
"v_mfma_f32_16x16x32_bf16 v[224:227], a[152:155], a[32:35], v[224:227]\n"
"ds_read_b64_tr_b16 a[64:65], v22 offset:1024\n"
"v_mfma_f32_16x16x32_bf16 v[224:227], a[156:159], a[48:51], v[224:227]\n"
"ds_read_b64_tr_b16 a[66:67], v22 offset:1280\n"
"v_mfma_f32_16x16x32_bf16 v[228:231], a[152:155], a[36:39], v[228:231]\n"
"ds_read_b64_tr_b16 a[68:69], v22 offset:14336\n"
"s_add_u32 m0, s93, 32768\n"
"v_mfma_f32_16x16x32_bf16 v[228:231], a[156:159], a[52:55], v[228:231]\n"
"ds_read_b64_tr_b16 a[70:71], v22 offset:14592\n"
"v_mfma_f32_16x16x32_bf16 v[232:235], a[152:155], a[40:43], v[232:235]\n"
"ds_read_b64_tr_b16 a[72:73], v23 offset:1024\n"
"v_mfma_f32_16x16x32_bf16 v[232:235], a[156:159], a[56:59], v[232:235]\n"
"ds_read_b64_tr_b16 a[74:75], v23 offset:1280\n"
"v_mfma_f32_16x16x32_bf16 v[236:239], a[152:155], a[44:47], v[236:239]\n"
"ds_read_b64_tr_b16 a[76:77], v23 offset:14336\n"
"v_mfma_f32_16x16x32_bf16 v[236:239], a[156:159], a[60:63], v[236:239]\n"
"ds_read_b64_tr_b16 a[78:79], v23 offset:14592\n"
"v_mfma_f32_16x16x32_bf16 v[240:243], a[160:163], a[32:35], v[240:243]\n"
"ds_read_b64_tr_b16 a[80:81], v22 offset:2048\n"
"v_mfma_f32_16x16x32_bf16 v[240:243], a[164:167], a[48:51], v[240:243]\n"
"buffer_load_dwordx4 v4, s[4:7], s48 offen offset:0 lds\n"
"v_mfma_f32_16x16x32_bf16 v[244:247], a[160:163], a[36:39], v[244:247]\n"
"ds_read_b64_tr_b16 a[82:83], v22 offset:2304\n"
"v_mfma_f32_16x16x32_bf16 v[244:247], a[164:167], a[52:55], v[244:247]\n"
"buffer_load_dwordx4 v5, s[4:7], s48 offen offset:1024 lds\n"
"v_mfma_f32_16x16x32_bf16 v[248:251], a[160:163], a[40:43], v[248:251]\n"
"ds_read_b64_tr_b16 a[84:85], v22 offset:15360\n"
"v_mfma_f32_16x16x32_bf16 v[248:251], a[164:167], a[56:59], v[248:251]\n"
"v_mfma_f32_16x16x32_bf16 v[252:255], a[160:163], a[44:47], v[252:255]\n"
"ds_read_b64_tr_b16 a[86:87], v22 offset:15616\n"
"v_mfma_f32_16x16x32_bf16 v[252:255], a[164:167], a[60:63], v[252:255]\n"
"s_add_u32 m0, s94, 32768\n"
"s_waitcnt lgkmcnt(8)\n"
"v_mfma_f32_16x16x32_bf16 v[80:83], a[64:67], a[0:3], v[80:83]\n"
"ds_read_b64_tr_b16 a[88:89], v23 offset:2048\n"
"v_mfma_f32_16x16x32_bf16 v[80:83], a[68:71], a[16:19], v[80:83]\n"
"buffer_load_dwordx4 v6, s[4:7], s48 offen offset:0 lds\n"
"v_mfma_f32_16x16x32_bf16 v[84:87], a[64:67], a[4:7], v[84:87]\n"
"ds_read_b64_tr_b16 a[90:91], v23 offset:2304\n"
"v_mfma_f32_16x16x32_bf16 v[84:87], a[68:71], a[20:23], v[84:87]\n"
"v_mfma_f32_16x16x32_bf16 v[88:91], a[64:67], a[8:11], v[88:91]\n"
"ds_read_b64_tr_b16 a[92:93], v23 offset:15360\n"
"v_mfma_f32_16x16x32_bf16 v[88:91], a[68:71], a[24:27], v[88:91]\n"
"v_mfma_f32_16x16x32_bf16 v[92:95], a[64:67], a[12:15], v[92:95]\n"
"ds_read_b64_tr_b16 a[94:95], v23 offset:15616\n"
"v_mfma_f32_16x16x32_bf16 v[92:95], a[68:71], a[28:31], v[92:95]\n"
"s_waitcnt lgkmcnt(8)\n"
"v_mfma_f32_16x16x32_bf16 v[96:99], a[72:75], a[0:3], v[96:99]\n"
"ds_read_b64_tr_b16 a[96:97], v22 offset:0\n"
"v_mfma_f32_16x16x32_bf16 v[96:99], a[76:79], a[16:19], v[96:99]\n"
"buffer_load_dwordx4 v7, s[4:7], s48 offen offset:1024 lds\n"
"v_mfma_f32_16x16x32_bf16 v[100:103], a[72:75], a[4:7], v[100:103]\n"
"ds_read_b64_tr_b16 a[98:99], v22 offset:256\n"
"v_mfma_f32_16x16x32_bf16 v[100:103], a[76:79], a[20:23], v[100:103]\n"
"v_mfma_f32_16x16x32_bf16 v[104:107], a[72:75], a[8:11], v[104:107]\n"
"ds_read_b64_tr_b16 a[100:101], v22 offset:13312\n"
"s_add_u32 m0, s95, 32768\n"
"v_mfma_f32_16x16x32_bf16 v[104:107], a[76:79], a[24:27], v[104:107]\n"
"v_mfma_f32_16x16x32_bf16 v[108:111], a[72:75], a[12:15], v[108:111]\n"
"ds_read_b64_tr_b16 a[102:103], v22 offset:13568\n"
"v_mfma_f32_16x16x32_bf16 v[108:111], a[76:79], a[28:31], v[108:111]\n"
"s_waitcnt lgkmcnt(8)\n"
"v_mfma_f32_16x16x32_bf16 v[112:115], a[0:3], a[80:83], v[112:115]\n"
"ds_read_b64_tr_b16 a[104:105], v23 offset:0\n"
"v_mfma_f32_16x16x32_bf16 v[112:115], a[16:19], a[84:87], v[112:115]\n"
"buffer_load_dwordx4 v8, s[4:7], s48 offen offset:0 lds\n"
"v_mfma_f32_16x16x32_bf16 v[116:119], a[4:7], a[80:83], v[116:119]\n"
"ds_read_b64_tr_b16 a[106:107], v23 offset:256\n"
"v_mfma_f32_16x16x32_bf16 v[116:119], a[20:23], a[84:87], v[116:119]\n"
"buffer_load_dwordx4 v9, s[4:7], s48 offen offset:1024 lds\n"
"v_mfma_f32_16x16x32_bf16 v[120:123], a[8:11], a[80:83], v[120:123]\n"
"ds_read_b64_tr_b16 a[108:109], v23 offset:13312\n"
"s_add_u32 s48, s48, 128\n"
"v_mfma_f32_16x16x32_bf16 v[120:123], a[24:27], a[84:87], v[120:123]\n"
"v_mfma_f32_16x16x32_bf16 v[124:127], a[12:15], a[80:83], v[124:127]\n"
"ds_read_b64_tr_b16 a[110:111], v23 offset:13568\n"
"v_mfma_f32_16x16x32_bf16 v[124:127], a[28:31], a[84:87], v[124:127]\n"
"s_waitcnt lgkmcnt(8)\n"
"v_mfma_f32_16x16x32_bf16 v[128:131], a[0:3], a[88:91], v[128:131]\n"
"ds_read_b64_tr_b16 a[112:113], v22 offset:3072\n"
"s_add_u32 m0, s52, 3072\n"
"v_mfma_f32_16x16x32_bf16 v[128:131], a[16:19], a[92:95], v[128:131]\n"
"v_mfma_f32_16x16x32_bf16 v[132:135], a[4:7], a[88:91], v[132:135]\n"
"ds_read_b64_tr_b16 a[114:115], v22 offset:3328\n"
"v_mfma_f32_16x16x32_bf16 v[132:135], a[20:23], a[92:95], v[132:135]\n"
"buffer_load_dwordx4 v13, s[12:15], s50 offen offset:0 lds\n"
"v_mfma_f32_16x16x32_bf16 v[136:139], a[8:11], a[88:91], v[136:139]\n"
"ds_read_b64_tr_b16 a[116:117], v22 offset:16384\n"
"v_mfma_f32_16x16x32_bf16 v[136:139], a[24:27], a[92:95], v[136:139]\n"
"v_mfma_f32_16x16x32_bf16 v[140:143], a[12:15], a[88:91], v[140:143]\n"
"ds_read_b64_tr_b16 a[118:119], v22 offset:16640\n"
"v_mfma_f32_16x16x32_bf16 v[140:143], a[28:31], a[92:95], v[140:143]\n"
"s_waitcnt lgkmcnt(8)\n"
"v_mfma_f32_16x16x32_bf16 v[48:51], a[96:99], a[0:3], v[48:51]\n"
"ds_read_b64_tr_b16 a[120:121], v23 offset:3072\n"
"v_mfma_f32_16x16x32_bf16 v[48:51], a[100:103], a[16:19], v[48:51]\n"
"buffer_load_dwordx4 v14, s[12:15], s50 offen offset:1024 lds\n"
"v_mfma_f32_16x16x32_bf16 v[52:55], a[96:99], a[4:7], v[52:55]\n"
"ds_read_b64_tr_b16 a[122:123], v23 offset:3328\n"
"v_mfma_f32_16x16x32_bf16 v[52:55], a[100:103], a[20:23], v[52:55]\n"
"v_mfma_f32_16x16x32_bf16 v[56:59], a[96:99], a[8:11], v[56:59]\n"
"ds_read_b64_tr_b16 a[124:125], v23 offset:16384\n"
"v_mfma_f32_16x16x32_bf16 v[56:59], a[100:103], a[24:27], v[56:59]\n"
"buffer_load_dwordx4 v15, s[12:15], s50 offen offset:2048 lds\n"
"v_mfma_f32_16x16x32_bf16 v[60:63], a[96:99], a[12:15], v[60:63]\n"
"ds_read_b64_tr_b16 a[126:127], v23 offset:16640\n"
"v_mfma_f32_16x16x32_bf16 v[60:63], a[100:103], a[28:31], v[60:63]\n"
"s_waitcnt lgkmcnt(8)\n"
"v_mfma_f32_16x16x32_bf16 v[64:67], a[104:107], a[0:3], v[64:67]\n"
"ds_read_b64_tr_b16 a[128:129], v22 offset:4096\n"
"v_mfma_f32_16x16x32_bf16 v[64:67], a[108:111], a[16:19], v[64:67]\n"
"v_mfma_f32_16x16x32_bf16 v[68:71], a[104:107], a[4:7], v[68:71]\n"
"ds_read_b64_tr_b16 a[130:131], v22 offset:4352\n"
"v_mfma_f32_16x16x32_bf16 v[68:71], a[108:111], a[20:23], v[68:71]\n"
"s_mov_b32 exec_hi, 0\n"
"buffer_load_dwordx4 v16, s[12:15], s50 offen offset:3072 lds\n"
"s_mov_b32 exec_hi, -1\n"
"v_mfma_f32_16x16x32_bf16 v[72:75], a[104:107], a[8:11], v[72:75]\n"
"ds_read_b64_tr_b16 a[132:133], v22 offset:17408\n"
"s_add_u32 s50, s50, 458752\n"
"v_mfma_f32_16x16x32_bf16 v[72:75], a[108:111], a[24:27], v[72:75]\n"
"v_mfma_f32_16x16x32_bf16 v[76:79], a[104:107], a[12:15], v[76:79]\n"
"ds_read_b64_tr_b16 a[134:135], v22 offset:17664\n"
"v_mfma_f32_16x16x32_bf16 v[76:79], a[108:111], a[28:31], v[76:79]\n"
"s_waitcnt lgkmcnt(8)\n"
"v_mfma_f32_16x16x32_bf16 v[144:147], a[112:115], a[0:3], v[144:147]\n"
"ds_read_b64_tr_b16 a[136:137], v23 offset:4096\n"
"s_add_u32 m0, s92, 0\n"
"v_mfma_f32_16x16x32_bf16 v[144:147], a[116:119], a[16:19], v[144:147]\n"
"v_mfma_f32_16x16x32_bf16 v[148:151], a[112:115], a[4:7], v[148:151]\n"
"ds_read_b64_tr_b16 a[138:139], v23 offset:4352\n"
"v_mfma_f32_16x16x32_bf16 v[148:151], a[116:119], a[20:23], v[148:151]\n"
"buffer_load_dwordx4 v2, s[4:7], s51 offen offset:0 lds\n"
"v_mfma_f32_16x16x32_bf16 v[152:155], a[112:115], a[8:11], v[152:155]\n"
"ds_read_b64_tr_b16 a[140:141], v23 offset:17408\n"
"v_mfma_f32_16x16x32_bf16 v[152:155], a[116:119], a[24:27], v[152:155]\n"
"buffer_load_dwordx4 v3, s[4:7], s51 offen offset:1024 lds\n"
"v_mfma_f32_16x16x32_bf16 v[156:159], a[112:115], a[12:15], v[156:159]\n"
"ds_read_b64_tr_b16 a[142:143], v23 offset:17664\n"
"s_add_u32 s51, s51, 128\n"
"v_mfma_f32_16x16x32_bf16 v[156:159], a[116:119], a[28:31], v[156:159]\n"
"s_waitcnt lgkmcnt(8)\n"
"v_mfma_f32_16x16x32_bf16 v[160:163], a[120:123], a[0:3], v[160:163]\n"
"ds_read_b64_tr_b16 a[144:145], v22 offset:5120\n"
"v_mfma_f32_16x16x32_bf16 v[160:163], a[124:127], a[16:19], v[160:163]\n"
"buffer_load_dwordx4 a[196:199], v25, s[60:63], s58 offen\n"
"v_mfma_f32_16x16x32_bf16 v[164:167], a[120:123], a[4:7], v[164:167]\n"
"ds_read_b64_tr_b16 a[146:147], v22 offset:5376\n"
"s_add_u32 s58, s58, s59\n"
"v_mfma_f32_16x16x32_bf16 v[164:167], a[124:127], a[20:23], v[164:167]\n"
"buffer_load_dwordx4 a[200:203], v25, s[60:63], s58 offen\n"
"v_mfma_f32_16x16x32_bf16 v[168:171], a[120:123], a[8:11], v[168:171]\n"
"ds_read_b64_tr_b16 a[148:149], v22 offset:18432\n"
"s_add_u32 s58, s58, s59\n"
"v_mfma_f32_16x16x32_bf16 v[168:171], a[124:127], a[24:27], v[168:171]\n"
"buffer_load_dwordx4 a[204:207], v25, s[60:63], s58 offen\n"
"v_mfma_f32_16x16x32_bf16 v[172:175], a[120:123], a[12:15], v[172:175]\n"
"ds_read_b64_tr_b16 a[150:151], v22 offset:18688\n"
"s_add_u32 s58, s58, s59\n"
"v_mfma_f32_16x16x32_bf16 v[172:175], a[124:127], a[28:31], v[172:175]\n"
"s_waitcnt lgkmcnt(8)\n"
"v_mfma_f32_16x16x32_bf16 v[176:179], a[128:131], a[0:3], v[176:179]\n"
"ds_read_b64_tr_b16 a[152:153], v23 offset:5120\n"
"v_mfma_f32_16x16x32_bf16 v[176:179], a[132:135], a[16:19], v[176:179]\n"
"v_mfma_f32_16x16x32_bf16 v[180:183], a[128:131], a[4:7], v[180:183]\n"
"ds_read_b64_tr_b16 a[154:155], v23 offset:5376\n"
"v_mfma_f32_16x16x32_bf16 v[180:183], a[132:135], a[20:23], v[180:183]\n"
"buffer_load_dwordx4 v[30:33], v26, s[4:7], s73 offen offset:1024\n"
"v_mfma_f32_16x16x32_bf16 v[184:187], a[128:131], a[8:11], v[184:187]\n"
"ds_read_b64_tr_b16 a[156:157], v23 offset:18432\n"
"s_add_u32 m0, s52, 26624\n"
"v_mfma_f32_16x16x32_bf16 v[184:187], a[132:135], a[24:27], v[184:187]\n"
"s_waitcnt vmcnt(10)\n"
"v_mfma_f32_16x16x32_bf16 v[188:191], a[128:131], a[12:15], v[188:191]\n"
"ds_read_b64_tr_b16 a[158:159], v23 offset:18688\n"
"v_mfma_f32_16x16x32_bf16 v[188:191], a[132:135], a[28:31], v[188:191]\n"
"ds_read_b128 a[32:35], v17 offset:32768\n"
"s_waitcnt lgkmcnt(9)\n"
"v_mfma_f32_16x16x32_bf16 v[192:195], a[136:139], a[0:3], v[192:195]\n"
"ds_read_b64_tr_b16 a[160:161], v24 offset:6144\n"
"v_mfma_f32_16x16x32_bf16 v[192:195], a[140:143], a[16:19], v[192:195]\n"
"buffer_load_dwordx4 v10, s[8:11], s49 offen offset:0 lds\n"
"v_mfma_f32_16x16x32_bf16 v[196:199], a[136:139], a[4:7], v[196:199]\n"
"ds_read_b64_tr_b16 a[162:163], v24 offset:6400\n"
"v_mfma_f32_16x16x32_bf16 v[196:199], a[140:143], a[20:23], v[196:199]\n"
"ds_read_b128 a[48:51], v18 offset:32768\n"
"v_mfma_f32_16x16x32_bf16 v[200:203], a[136:139], a[8:11], v[200:203]\n"
"ds_read_b64_tr_b16 a[164:165], v24 offset:19456\n"
"v_mfma_f32_16x16x32_bf16 v[200:203], a[140:143], a[24:27], v[200:203]\n"
"buffer_load_dwordx4 v11, s[8:11], s49 offen offset:1024 lds\n"
"v_mfma_f32_16x16x32_bf16 v[204:207], a[136:139], a[12:15], v[204:207]\n"
"ds_read_b64_tr_b16 a[166:167], v24 offset:19712\n"
"v_mfma_f32_16x16x32_bf16 v[204:207], a[140:143], a[28:31], v[204:207]\n"
"ds_read_b128 a[36:39], v17 offset:34816\n"
"s_waitcnt lgkmcnt(11)\n"
"v_mfma_f32_16x16x32_bf16 v[208:211], a[144:147], a[0:3], v[208:211]\n"
"ds_read_b128 a[52:55], v18 offset:34816\n"
"v_mfma_f32_16x16x32_bf16 v[208:211], a[148:151], a[16:19], v[208:211]\n"
"ds_read_b128 a[40:43], v17 offset:36864\n"
"v_mfma_f32_16x16x32_bf16 v[212:215], a[144:147], a[4:7], v[212:215]\n"
"ds_read_b128 a[56:59], v18 offset:36864\n"
"v_mfma_f32_16x16x32_bf16 v[212:215], a[148:151], a[20:23], v[212:215]\n"
"buffer_load_dwordx4 v12, s[8:11], s49 offen offset:2048 lds\n"
"v_mfma_f32_16x16x32_bf16 v[216:219], a[144:147], a[8:11], v[216:219]\n"
"s_add_u32 s49, s49, 393216\n"
"ds_read_b128 a[44:47], v17 offset:38912\n"
"v_mfma_f32_16x16x32_bf16 v[216:219], a[148:151], a[24:27], v[216:219]\n"
"ds_read_b128 a[60:63], v18 offset:38912\n"
"v_mfma_f32_16x16x32_bf16 v[220:223], a[144:147], a[12:15], v[220:223]\n"
"v_mfma_f32_16x16x32_bf16 v[220:223], a[148:151], a[28:31], v[220:223]\n"
"s_waitcnt vmcnt(9) lgkmcnt(6)\n"
"s_barrier\n"
"v_mfma_f32_16x16x32_bf16 v[224:227], a[152:155], a[0:3], v[224:227]\n"
"ds_read_b64_tr_b16 a[64:65], v19 offset:1024\n"
"v_mfma_f32_16x16x32_bf16 v[224:227], a[156:159], a[16:19], v[224:227]\n"
"ds_read_b64_tr_b16 a[66:67], v19 offset:1280\n"
"v_mfma_f32_16x16x32_bf16 v[228:231], a[152:155], a[4:7], v[228:231]\n"
"ds_read_b64_tr_b16 a[68:69], v19 offset:14336\n"
"s_add_u32 m0, s93, 0\n"
"v_mfma_f32_16x16x32_bf16 v[228:231], a[156:159], a[20:23], v[228:231]\n"
"ds_read_b64_tr_b16 a[70:71], v19 offset:14592\n"
"v_mfma_f32_16x16x32_bf16 v[232:235], a[152:155], a[8:11], v[232:235]\n"
"ds_read_b64_tr_b16 a[72:73], v20 offset:1024\n"
"v_mfma_f32_16x16x32_bf16 v[232:235], a[156:159], a[24:27], v[232:235]\n"
"ds_read_b64_tr_b16 a[74:75], v20 offset:1280\n"
"v_mfma_f32_16x16x32_bf16 v[236:239], a[152:155], a[12:15], v[236:239]\n"
"ds_read_b64_tr_b16 a[76:77], v20 offset:14336\n"
"v_mfma_f32_16x16x32_bf16 v[236:239], a[156:159], a[28:31], v[236:239]\n"
"ds_read_b64_tr_b16 a[78:79], v20 offset:14592\n"
"v_mfma_f32_16x16x32_bf16 v[240:243], a[160:163], a[0:3], v[240:243]\n"
"ds_read_b64_tr_b16 a[80:81], v19 offset:2048\n"
"v_mfma_f32_16x16x32_bf16 v[240:243], a[164:167], a[16:19], v[240:243]\n"
"buffer_load_dwordx4 v4, s[4:7], s48 offen offset:0 lds\n"
"v_mfma_f32_16x16x32_bf16 v[244:247], a[160:163], a[4:7], v[244:247]\n"
"ds_read_b64_tr_b16 a[82:83], v19 offset:2304\n"
"v_mfma_f32_16x16x32_bf16 v[244:247], a[164:167], a[20:23], v[244:247]\n"
"buffer_load_dwordx4 v5, s[4:7], s48 offen offset:1024 lds\n"
"v_mfma_f32_16x16x32_bf16 v[248:251], a[160:163], a[8:11], v[248:251]\n"
"ds_read_b64_tr_b16 a[84:85], v19 offset:15360\n"
"v_mfma_f32_16x16x32_bf16 v[248:251], a[164:167], a[24:27], v[248:251]\n"
"v_mfma_f32_16x16x32_bf16 v[252:255], a[160:163], a[12:15], v[252:255]\n"
"ds_read_b64_tr_b16 a[86:87], v19 offset:15616\n"
"v_mfma_f32_16x16x32_bf16 v[252:255], a[164:167], a[28:31], v[252:255]\n"
"s_add_u32 m0, s94, 0\n"
"s_waitcnt lgkmcnt(8)\n"
"v_mfma_f32_16x16x32_bf16 v[80:83], a[64:67], a[32:35], v[80:83]\n"
"ds_read_b64_tr_b16 a[88:89], v20 offset:2048\n"
"v_mfma_f32_16x16x32_bf16 v[80:83], a[68:71], a[48:51], v[80:83]\n"
"buffer_load_dwordx4 v6, s[4:7], s48 offen offset:0 lds\n"
"v_mfma_f32_16x16x32_bf16 v[84:87], a[64:67], a[36:39], v[84:87]\n"
"ds_read_b64_tr_b16 a[90:91], v20 offset:2304\n"
"v_mfma_f32_16x16x32_bf16 v[84:87], a[68:71], a[52:55], v[84:87]\n"
"v_mfma_f32_16x16x32_bf16 v[88:91], a[64:67], a[40:43], v[88:91]\n"
"ds_read_b64_tr_b16 a[92:93], v20 offset:15360\n"
"v_mfma_f32_16x16x32_bf16 v[88:91], a[68:71], a[56:59], v[88:91]\n"
"v_mfma_f32_16x16x32_bf16 v[92:95], a[64:67], a[44:47], v[92:95]\n"
"ds_read_b64_tr_b16 a[94:95], v20 offset:15616\n"
"v_mfma_f32_16x16x32_bf16 v[92:95], a[68:71], a[60:63], v[92:95]\n"
"s_waitcnt lgkmcnt(8)\n"
"v_mfma_f32_16x16x32_bf16 v[96:99], a[72:75], a[32:35], v[96:99]\n"
"ds_read_b64_tr_b16 a[96:97], v19 offset:0\n"
"v_mfma_f32_16x16x32_bf16 v[96:99], a[76:79], a[48:51], v[96:99]\n"
"buffer_load_dwordx4 v7, s[4:7], s48 offen offset:1024 lds\n"
"v_mfma_f32_16x16x32_bf16 v[100:103], a[72:75], a[36:39], v[100:103]\n"
"ds_read_b64_tr_b16 a[98:99], v19 offset:256\n"
"v_mfma_f32_16x16x32_bf16 v[100:103], a[76:79], a[52:55], v[100:103]\n"
"v_mfma_f32_16x16x32_bf16 v[104:107], a[72:75], a[40:43], v[104:107]\n"
"ds_read_b64_tr_b16 a[100:101], v19 offset:13312\n"
"s_add_u32 m0, s95, 0\n"
"v_mfma_f32_16x16x32_bf16 v[104:107], a[76:79], a[56:59], v[104:107]\n"
"v_mfma_f32_16x16x32_bf16 v[108:111], a[72:75], a[44:47], v[108:111]\n"
"ds_read_b64_tr_b16 a[102:103], v19 offset:13568\n"
"v_mfma_f32_16x16x32_bf16 v[108:111], a[76:79], a[60:63], v[108:111]\n"
"s_waitcnt lgkmcnt(8)\n"
"v_mfma_f32_16x16x32_bf16 v[112:115], a[32:35], a[80:83], v[112:115]\n"
"ds_read_b64_tr_b16 a[104:105], v20 offset:0\n"
"v_mfma_f32_16x16x32_bf16 v[112:115], a[48:51], a[84:87], v[112:115]\n"
"buffer_load_dwordx4 v8, s[4:7], s48 offen offset:0 lds\n"
"v_mfma_f32_16x16x32_bf16 v[116:119], a[36:39], a[80:83], v[116:119]\n"
"ds_read_b64_tr_b16 a[106:107], v20 offset:256\n"
"v_mfma_f32_16x16x32_bf16 v[116:119], a[52:55], a[84:87], v[116:119]\n"
"buffer_load_dwordx4 v9, s[4:7], s48 offen offset:1024 lds\n"
"v_mfma_f32_16x16x32_bf16 v[120:123], a[40:43], a[80:83], v[120:123]\n"
"ds_read_b64_tr_b16 a[108:109], v20 offset:13312\n"
"s_add_u32 s48, s48, 128\n"
"v_mfma_f32_16x16x32_bf16 v[120:123], a[56:59], a[84:87], v[120:123]\n"
"v_mfma_f32_16x16x32_bf16 v[124:127], a[44:47], a[80:83], v[124:127]\n"
"ds_read_b64_tr_b16 a[110:111], v20 offset:13568\n"
"v_mfma_f32_16x16x32_bf16 v[124:127], a[60:63], a[84:87], v[124:127]\n"
"s_waitcnt lgkmcnt(8)\n"
"v_mfma_f32_16x16x32_bf16 v[128:131], a[32:35], a[88:91], v[128:131]\n"
"ds_read_b64_tr_b16 a[112:113], v19 offset:3072\n"
"s_add_u32 m0, s52, 29696\n"
"v_mfma_f32_16x16x32_bf16 v[128:131], a[48:51], a[92:95], v[128:131]\n"
"v_mfma_f32_16x16x32_bf16 v[132:135], a[36:39], a[88:91], v[132:135]\n"
"ds_read_b64_tr_b16 a[114:115], v19 offset:3328\n"
"v_mfma_f32_16x16x32_bf16 v[132:135], a[52:55], a[92:95], v[132:135]\n"
"buffer_load_dwordx4 v13, s[12:15], s50 offen offset:0 lds\n"
"v_mfma_f32_16x16x32_bf16 v[136:139], a[40:43], a[88:91], v[136:139]\n"
"ds_read_b64_tr_b16 a[116:117], v19 offset:16384\n"
"v_mfma_f32_16x16x32_bf16 v[136:139], a[56:59], a[92:95], v[136:139]\n"
"v_mfma_f32_16x16x32_bf16 v[140:143], a[44:47], a[88:91], v[140:143]\n"
"ds_read_b64_tr_b16 a[118:119], v19 offset:16640\n"
"v_mfma_f32_16x16x32_bf16 v[140:143], a[60:63], a[92:95], v[140:143]\n"
"s_waitcnt lgkmcnt(8)\n"
"v_mfma_f32_16x16x32_bf16 v[48:51], a[96:99], a[32:35], v[48:51]\n"
"ds_read_b64_tr_b16 a[120:121], v20 offset:3072\n"
"v_mfma_f32_16x16x32_bf16 v[48:51], a[100:103], a[48:51], v[48:51]\n"
"buffer_load_dwordx4 v14, s[12:15], s50 offen offset:1024 lds\n"
"v_mfma_f32_16x16x32_bf16 v[52:55], a[96:99], a[36:39], v[52:55]\n"
"ds_read_b64_tr_b16 a[122:123], v20 offset:3328\n"
"v_mfma_f32_16x16x32_bf16 v[52:55], a[100:103], a[52:55], v[52:55]\n"
"v_mfma_f32_16x16x32_bf16 v[56:59], a[96:99], a[40:43], v[56:59]\n"
"ds_read_b64_tr_b16 a[124:125], v20 offset:16384\n"
"v_mfma_f32_16x16x32_bf16 v[56:59], a[100:103], a[56:59], v[56:59]\n"
"buffer_load_dwordx4 v15, s[12:15], s50 offen offset:2048 lds\n"
"v_mfma_f32_16x16x32_bf16 v[60:63], a[96:99], a[44:47], v[60:63]\n"
"ds_read_b64_tr_b16 a[126:127], v20 offset:16640\n"
"v_mfma_f32_16x16x32_bf16 v[60:63], a[100:103], a[60:63], v[60:63]\n"
"s_waitcnt lgkmcnt(8)\n"
"v_mfma_f32_16x16x32_bf16 v[64:67], a[104:107], a[32:35], v[64:67]\n"
"ds_read_b64_tr_b16 a[128:129], v19 offset:4096\n"
"v_mfma_f32_16x16x32_bf16 v[64:67], a[108:111], a[48:51], v[64:67]\n"
"v_mfma_f32_16x16x32_bf16 v[68:71], a[104:107], a[36:39], v[68:71]\n"
"ds_read_b64_tr_b16 a[130:131], v19 offset:4352\n"
"v_mfma_f32_16x16x32_bf16 v[68:71], a[108:111], a[52:55], v[68:71]\n"
"s_mov_b32 exec_hi, 0\n"
"buffer_load_dwordx4 v16, s[12:15], s50 offen offset:3072 lds\n"
"s_mov_b32 exec_hi, -1\n"
"v_mfma_f32_16x16x32_bf16 v[72:75], a[104:107], a[40:43], v[72:75]\n"
"ds_read_b64_tr_b16 a[132:133], v19 offset:17408\n"
"s_add_u32 s50, s50, 458752\n"
"v_mfma_f32_16x16x32_bf16 v[72:75], a[108:111], a[56:59], v[72:75]\n"
"v_mfma_f32_16x16x32_bf16 v[76:79], a[104:107], a[44:47], v[76:79]\n"
"ds_read_b64_tr_b16 a[134:135], v19 offset:17664\n"
"v_mfma_f32_16x16x32_bf16 v[76:79], a[108:111], a[60:63], v[76:79]\n"
"s_waitcnt lgkmcnt(8)\n"
"v_mfma_f32_16x16x32_bf16 v[144:147], a[112:115], a[32:35], v[144:147]\n"
"ds_read_b64_tr_b16 a[136:137], v20 offset:4096\n"
"s_add_u32 m0, s92, 32768\n"
"v_mfma_f32_16x16x32_bf16 v[144:147], a[116:119], a[48:51], v[144:147]\n"
"s_mov_b64 exec, s[56:57]\n"
"buffer_load_dword v28, v29, s[16:19], 0 offen sc1\n"
"s_mov_b64 exec, -1\n"
"v_mfma_f32_16x16x32_bf16 v[148:151], a[112:115], a[36:39], v[148:151]\n"
"ds_read_b64_tr_b16 a[138:139], v20 offset:4352\n"
"v_lshl_add_u32 v27, v0, 5, v27\n"
"v_mfma_f32_16x16x32_bf16 v[148:151], a[116:119], a[52:55], v[148:151]\n"
"buffer_load_dwordx4 v2, s[4:7], s51 offen offset:0 lds\n"
"v_mfma_f32_16x16x32_bf16 v[152:155], a[112:115], a[40:43], v[152:155]\n"
"ds_read_b64_tr_b16 a[140:141], v20 offset:17408\n"
"s_waitcnt vmcnt(14)\n"
"v_mfma_f32_16x16x32_bf16 v[152:155], a[116:119], a[56:59], v[152:155]\n"
"buffer_load_dwordx4 v3, s[4:7], s51 offen offset:1024 lds\n"
"v_lshlrev_b32 v44, 16, v30\n"
"v_mfma_f32_16x16x32_bf16 v[156:159], a[112:115], a[44:47], v[156:159]\n"
"ds_read_b64_tr_b16 a[142:143], v20 offset:17664\n"
"s_add_u32 s51, s51, 128\n"
".Lpad2:\n"
"s_cbranch_execz .Lpad6\n"
"v_mfma_f32_16x16x32_bf16 v[156:159], a[116:119], a[60:63], v[156:159]\n"
"v_and_b32 v30, 0xffff0000, v30\n"
"s_waitcnt lgkmcnt(8)\n"
"v_mfma_f32_16x16x32_bf16 v[160:163], a[120:123], a[32:35], v[160:163]\n"
"ds_read_b64_tr_b16 a[144:145], v19 offset:5120\n"
"v_add_f32 v38, v38, v44\n"
"v_mfma_f32_16x16x32_bf16 v[160:163], a[124:127], a[48:51], v[160:163]\n"
"buffer_load_dwordx4 a[208:211], v25, s[60:63], s58 offen\n"
"v_fmac_f32 v39, v44, v44\n"
"v_mfma_f32_16x16x32_bf16 v[164:167], a[120:123], a[36:39], v[164:167]\n"
"ds_read_b64_tr_b16 a[146:147], v19 offset:5376\n"
"s_add_u32 s58, s58, s59\n"
"v_mfma_f32_16x16x32_bf16 v[164:167], a[124:127], a[52:55], v[164:167]\n"
"buffer_load_dwordx4 a[212:215], v25, s[60:63], s58 offen\n"
"v_add_f32 v38, v38, v30\n"
"v_mfma_f32_16x16x32_bf16 v[168:171], a[120:123], a[40:43], v[168:171]\n"
"ds_read_b64_tr_b16 a[148:149], v19 offset:18432\n"
"s_add_u32 s58, s58, s59\n"
"v_mfma_f32_16x16x32_bf16 v[168:171], a[124:127], a[56:59], v[168:171]\n"
"buffer_load_dwordx4 a[216:219], v25, s[60:63], s58 offen\n"
"v_fmac_f32 v39, v30, v30\n"
"v_mfma_f32_16x16x32_bf16 v[172:175], a[120:123], a[44:47], v[172:175]\n"
"ds_read_b64_tr_b16 a[150:151], v19 offset:18688\n"
"s_add_u32 s58, s58, s59\n"
"v_mfma_f32_16x16x32_bf16 v[172:175], a[124:127], a[60:63], v[172:175]\n"
"v_lshlrev_b32 v44, 16, v31\n"
"s_waitcnt lgkmcnt(8)\n"
"v_mfma_f32_16x16x32_bf16 v[176:179], a[128:131], a[32:35], v[176:179]\n"
"ds_read_b64_tr_b16 a[152:153], v20 offset:5120\n"
"v_and_b32 v31, 0xffff0000, v31\n"
"v_mfma_f32_16x16x32_bf16 v[176:179], a[132:135], a[48:51], v[176:179]\n"
"v_add_f32 v38, v38, v44\n"
"v_fmac_f32 v39, v44, v44\n"
"v_mfma_f32_16x16x32_bf16 v[180:183], a[128:131], a[36:39], v[180:183]\n"
"ds_read_b64_tr_b16 a[154:155], v20 offset:5376\n"
"v_add_f32 v38, v38, v31\n"
"v_mfma_f32_16x16x32_bf16 v[180:183], a[132:135], a[52:55], v[180:183]\n"
"buffer_load_dwordx4 v[34:37], v26, s[4:7], s73 offen offset:1536\n"
"v_fmac_f32 v39, v31, v31\n"
"v_mfma_f32_16x16x32_bf16 v[184:187], a[128:131], a[40:43], v[184:187]\n"
"ds_read_b64_tr_b16 a[156:157], v20 offset:18432\n"
"s_add_u32 m0, s52, 53248\n"
"v_mfma_f32_16x16x32_bf16 v[184:187], a[132:135], a[56:59], v[184:187]\n"
"s_waitcnt vmcnt(10)\n"
"v_lshlrev_b32 v44, 16, v32\n"
"v_mfma_f32_16x16x32_bf16 v[188:191], a[128:131], a[44:47], v[188:191]\n"
"ds_read_b64_tr_b16 a[158:159], v20 offset:18688\n"
"v_and_b32 v32, 0xffff0000, v32\n"
"v_mfma_f32_16x16x32_bf16 v[188:191], a[132:135], a[60:63], v[188:191]\n"
"ds_read_b128 a[0:3], v17 offset:0\n"
"s_waitcnt lgkmcnt(9)\n"
"v_mfma_f32_16x16x32_bf16 v[192:195], a[136:139], a[32:35], v[192:195]\n"
"ds_read_b64_tr_b16 a[160:161], v21 offset:6144\n"
"v_add_f32 v38, v38, v44\n"
"v_mfma_f32_16x16x32_bf16 v[192:195], a[140:143], a[48:51], v[192:195]\n"
"buffer_load_dwordx4 v10, s[8:11], s49 offen offset:0 lds\n"
"v_fmac_f32 v39, v44, v44\n"
"v_mfma_f32_16x16x32_bf16 v[196:199], a[136:139], a[36:39], v[196:199]\n"
"ds_read_b64_tr_b16 a[162:163], v21 offset:6400\n"
"v_add_f32 v38, v38, v32\n"
"v_mfma_f32_16x16x32_bf16 v[196:199], a[140:143], a[52:55], v[196:199]\n"
"ds_read_b128 a[16:19], v18 offset:0\n"
"v_fmac_f32 v39, v32, v32\n"
"v_mfma_f32_16x16x32_bf16 v[200:203], a[136:139], a[40:43], v[200:203]\n"
"ds_read_b64_tr_b16 a[164:165], v21 offset:19456\n"
"v_lshlrev_b32 v44, 16, v33\n"
"v_mfma_f32_16x16x32_bf16 v[200:203], a[140:143], a[56:59], v[200:203]\n"
"buffer_load_dwordx4 v11, s[8:11], s49 offen offset:1024 lds\n"
"v_and_b32 v33, 0xffff0000, v33\n"
"v_mfma_f32_16x16x32_bf16 v[204:207], a[136:139], a[44:47], v[204:207]\n"
"ds_read_b64_tr_b16 a[166:167], v21 offset:19712\n"
"v_add_f32 v38, v38, v44\n"
"v_mfma_f32_16x16x32_bf16 v[204:207], a[140:143], a[60:63], v[204:207]\n"
"ds_read_b128 a[4:7], v17 offset:2048\n"
"s_waitcnt lgkmcnt(11)\n"
"v_mfma_f32_16x16x32_bf16 v[208:211], a[144:147], a[32:35], v[208:211]\n"
"ds_read_b128 a[20:23], v18 offset:2048\n"
"v_fmac_f32 v39, v44, v44\n"
"v_mfma_f32_16x16x32_bf16 v[208:211], a[148:151], a[48:51], v[208:211]\n"
"ds_read_b128 a[8:11], v17 offset:4096\n"
"v_add_f32 v38, v38, v33\n"
"v_mfma_f32_16x16x32_bf16 v[212:215], a[144:147], a[36:39], v[212:215]\n"
"ds_read_b128 a[24:27], v18 offset:4096\n"
"v_fmac_f32 v39, v33, v33\n"
"v_mfma_f32_16x16x32_bf16 v[212:215], a[148:151], a[52:55], v[212:215]\n"
"buffer_load_dwordx4 v12, s[8:11], s49 offen offset:2048 lds\n"
"v_mfma_f32_16x16x32_bf16 v[216:219], a[144:147], a[40:43], v[216:219]\n"
"s_add_u32 s49, s49, 393216\n"
"ds_read_b128 a[12:15], v17 offset:6144\n"
"v_mfma_f32_16x16x32_bf16 v[216:219], a[148:151], a[56:59], v[216:219]\n"
"ds_read_b128 a[28:31], v18 offset:6144\n"
"v_mfma_f32_16x16x32_bf16 v[220:223], a[144:147], a[44:47], v[220:223]\n"
"v_mfma_f32_16x16x32_bf16 v[220:223], a[148:151], a[60:63], v[220:223]\n"
"s_waitcnt vmcnt(9) lgkmcnt(6)\n"
"s_barrier\n"
"v_mfma_f32_16x16x32_bf16 v[224:227], a[152:155], a[32:35], v[224:227]\n"
"ds_read_b64_tr_b16 a[64:65], v19 offset:27648\n"
"v_mfma_f32_16x16x32_bf16 v[224:227], a[156:159], a[48:51], v[224:227]\n"
"ds_read_b64_tr_b16 a[66:67], v19 offset:27904\n"
"v_mfma_f32_16x16x32_bf16 v[228:231], a[152:155], a[36:39], v[228:231]\n"
"ds_read_b64_tr_b16 a[68:69], v19 offset:40960\n"
"s_add_u32 m0, s93, 32768\n"
"v_mfma_f32_16x16x32_bf16 v[228:231], a[156:159], a[52:55], v[228:231]\n"
"ds_read_b64_tr_b16 a[70:71], v19 offset:41216\n"
"v_mfma_f32_16x16x32_bf16 v[232:235], a[152:155], a[40:43], v[232:235]\n"
"ds_read_b64_tr_b16 a[72:73], v20 offset:27648\n"
"v_mfma_f32_16x16x32_bf16 v[232:235], a[156:159], a[56:59], v[232:235]\n"
"ds_read_b64_tr_b16 a[74:75], v20 offset:27904\n"
"v_mfma_f32_16x16x32_bf16 v[236:239], a[152:155], a[44:47], v[236:239]\n"
"ds_read_b64_tr_b16 a[76:77], v20 offset:40960\n"
"v_mfma_f32_16x16x32_bf16 v[236:239], a[156:159], a[60:63], v[236:239]\n"
"ds_read_b64_tr_b16 a[78:79], v20 offset:41216\n"
"v_mfma_f32_16x16x32_bf16 v[240:243], a[160:163], a[32:35], v[240:243]\n"
"ds_read_b64_tr_b16 a[80:81], v19 offset:28672\n"
"v_mfma_f32_16x16x32_bf16 v[240:243], a[164:167], a[48:51], v[240:243]\n"
"buffer_load_dwordx4 v4, s[4:7], s48 offen offset:0 lds\n"
"v_mfma_f32_16x16x32_bf16 v[244:247], a[160:163], a[36:39], v[244:247]\n"
"ds_read_b64_tr_b16 a[82:83], v19 offset:28928\n"
"v_mfma_f32_16x16x32_bf16 v[244:247], a[164:167], a[52:55], v[244:247]\n"
"buffer_load_dwordx4 v5, s[4:7], s48 offen offset:1024 lds\n"
"v_mfma_f32_16x16x32_bf16 v[248:251], a[160:163], a[40:43], v[248:251]\n"
"ds_read_b64_tr_b16 a[84:85], v19 offset:41984\n"
"v_mfma_f32_16x16x32_bf16 v[248:251], a[164:167], a[56:59], v[248:251]\n"
"v_mfma_f32_16x16x32_bf16 v[252:255], a[160:163], a[44:47], v[252:255]\n"
"ds_read_b64_tr_b16 a[86:87], v19 offset:42240\n"
"v_mfma_f32_16x16x32_bf16 v[252:255], a[164:167], a[60:63], v[252:255]\n"
"s_add_u32 m0, s94, 32768\n"
"s_waitcnt lgkmcnt(8)\n"
"v_mfma_f32_16x16x32_bf16 v[80:83], a[64:67], a[0:3], v[80:83]\n"
"ds_read_b64_tr_b16 a[88:89], v20 offset:28672\n"
"v_mfma_f32_16x16x32_bf16 v[80:83], a[68:71], a[16:19], v[80:83]\n"
"buffer_load_dwordx4 v6, s[4:7], s48 offen offset:0 lds\n"
"v_mfma_f32_16x16x32_bf16 v[84:87], a[64:67], a[4:7], v[84:87]\n"
"ds_read_b64_tr_b16 a[90:91], v20 offset:28928\n"
"v_mfma_f32_16x16x32_bf16 v[84:87], a[68:71], a[20:23], v[84:87]\n"
"v_mfma_f32_16x16x32_bf16 v[88:91], a[64:67], a[8:11], v[88:91]\n"
"ds_read_b64_tr_b16 a[92:93], v20 offset:41984\n"
"v_mfma_f32_16x16x32_bf16 v[88:91], a[68:71], a[24:27], v[88:91]\n"
"v_mfma_f32_16x16x32_bf16 v[92:95], a[64:67], a[12:15], v[92:95]\n"
"ds_read_b64_tr_b16 a[94:95], v20 offset:42240\n"
"v_mfma_f32_16x16x32_bf16 v[92:95], a[68:71], a[28:31], v[92:95]\n"
"s_waitcnt lgkmcnt(8)\n"
"v_mfma_f32_16x16x32_bf16 v[96:99], a[72:75], a[0:3], v[96:99]\n"
"ds_read_b64_tr_b16 a[96:97], v19 offset:26624\n"
"v_mfma_f32_16x16x32_bf16 v[96:99], a[76:79], a[16:19], v[96:99]\n"
"buffer_load_dwordx4 v7, s[4:7], s48 offen offset:1024 lds\n"
"v_mfma_f32_16x16x32_bf16 v[100:103], a[72:75], a[4:7], v[100:103]\n"
"ds_read_b64_tr_b16 a[98:99], v19 offset:26880\n"
"v_mfma_f32_16x16x32_bf16 v[100:103], a[76:79], a[20:23], v[100:103]\n"
"v_mfma_f32_16x16x32_bf16 v[104:107], a[72:75], a[8:11], v[104:107]\n"
"ds_read_b64_tr_b16 a[100:101], v19 offset:39936\n"
"s_add_u32 m0, s95, 32768\n"
"v_mfma_f32_16x16x32_bf16 v[104:107], a[76:79], a[24:27], v[104:107]\n"
"v_mfma_f32_16x16x32_bf16 v[108:111], a[72:75], a[12:15], v[108:111]\n"
"ds_read_b64_tr_b16 a[102:103], v19 offset:40192\n"
"v_mfma_f32_16x16x32_bf16 v[108:111], a[76:79], a[28:31], v[108:111]\n"
"s_waitcnt lgkmcnt(8)\n"
"v_mfma_f32_16x16x32_bf16 v[112:115], a[0:3], a[80:83], v[112:115]\n"
"ds_read_b64_tr_b16 a[104:105], v20 offset:26624\n"
"v_mfma_f32_16x16x32_bf16 v[112:115], a[16:19], a[84:87], v[112:115]\n"
"buffer_load_dwordx4 v8, s[4:7], s48 offen offset:0 lds\n"
"v_mfma_f32_16x16x32_bf16 v[116:119], a[4:7], a[80:83], v[116:119]\n"
"ds_read_b64_tr_b16 a[106:107], v20 offset:26880\n"
"v_mfma_f32_16x16x32_bf16 v[116:119], a[20:23], a[84:87], v[116:119]\n"
"buffer_load_dwordx4 v9, s[4:7], s48 offen offset:1024 lds\n"
"v_mfma_f32_16x16x32_bf16 v[120:123], a[8:11], a[80:83], v[120:123]\n"
"ds_read_b64_tr_b16 a[108:109], v20 offset:39936\n"
"s_add_u32 s48, s48, 128\n"
"v_mfma_f32_16x16x32_bf16 v[120:123], a[24:27], a[84:87], v[120:123]\n"
"v_mfma_f32_16x16x32_bf16 v[124:127], a[12:15], a[80:83], v[124:127]\n"
"ds_read_b64_tr_b16 a[110:111], v20 offset:40192\n"
"v_mfma_f32_16x16x32_bf16 v[124:127], a[28:31], a[84:87], v[124:127]\n"
"s_waitcnt lgkmcnt(8)\n"
"v_mfma_f32_16x16x32_bf16 v[128:131], a[0:3], a[88:91], v[128:131]\n"
"ds_read_b64_tr_b16 a[112:113], v19 offset:29696\n"
"s_add_u32 m0, s52, 56320\n"
"v_mfma_f32_16x16x32_bf16 v[128:131], a[16:19], a[92:95], v[128:131]\n"
"v_mfma_f32_16x16x32_bf16 v[132:135], a[4:7], a[88:91], v[132:135]\n"
"ds_read_b64_tr_b16 a[114:115], v19 offset:29952\n"
"v_mfma_f32_16x16x32_bf16 v[132:135], a[20:23], a[92:95], v[132:135]\n"
"buffer_load_dwordx4 v13, s[12:15], s50 offen offset:0 lds\n"
"v_mfma_f32_16x16x32_bf16 v[136:139], a[8:11], a[88:91], v[136:139]\n"
"ds_read_b64_tr_b16 a[116:117], v19 offset:43008\n"
"v_mfma_f32_16x16x32_bf16 v[136:139], a[24:27], a[92:95], v[136:139]\n"
"v_mfma_f32_16x16x32_bf16 v[140:143], a[12:15], a[88:91], v[140:143]\n"
"ds_read_b64_tr_b16 a[118:119], v19 offset:43264\n"
"v_mfma_f32_16x16x32_bf16 v[140:143], a[28:31], a[92:95], v[140:143]\n"
"s_waitcnt lgkmcnt(8)\n"
"v_mfma_f32_16x16x32_bf16 v[48:51], a[96:99], a[0:3], v[48:51]\n"
"ds_read_b64_tr_b16 a[120:121], v20 offset:29696\n"
"v_mfma_f32_16x16x32_bf16 v[48:51], a[100:103], a[16:19], v[48:51]\n"
"buffer_load_dwordx4 v14, s[12:15], s50 offen offset:1024 lds\n"
"v_mfma_f32_16x16x32_bf16 v[52:55], a[96:99], a[4:7], v[52:55]\n"
"ds_read_b64_tr_b16 a[122:123], v20 offset:29952\n"
"v_mfma_f32_16x16x32_bf16 v[52:55], a[100:103], a[20:23], v[52:55]\n"
"v_mfma_f32_16x16x32_bf16 v[56:59], a[96:99], a[8:11], v[56:59]\n"
"ds_read_b64_tr_b16 a[124:125], v20 offset:43008\n"
"v_mfma_f32_16x16x32_bf16 v[56:59], a[100:103], a[24:27], v[56:59]\n"
"buffer_load_dwordx4 v15, s[12:15], s50 offen offset:2048 lds\n"
"v_mfma_f32_16x16x32_bf16 v[60:63], a[96:99], a[12:15], v[60:63]\n"
"ds_read_b64_tr_b16 a[126:127], v20 offset:43264\n"
"v_mfma_f32_16x16x32_bf16 v[60:63], a[100:103], a[28:31], v[60:63]\n"
"s_waitcnt lgkmcnt(8)\n"
"v_mfma_f32_16x16x32_bf16 v[64:67], a[104:107], a[0:3], v[64:67]\n"
"ds_read_b64_tr_b16 a[128:129], v19 offset:30720\n"
"v_mfma_f32_16x16x32_bf16 v[64:67], a[108:111], a[16:19], v[64:67]\n"
"v_mfma_f32_16x16x32_bf16 v[68:71], a[104:107], a[4:7], v[68:71]\n"
"ds_read_b64_tr_b16 a[130:131], v19 offset:30976\n"
"v_mfma_f32_16x16x32_bf16 v[68:71], a[108:111], a[20:23], v[68:71]\n"
"s_mov_b32 exec_hi, 0\n"
"buffer_load_dwordx4 v16, s[12:15], s50 offen offset:3072 lds\n"
"s_mov_b32 exec_hi, -1\n"
"v_mfma_f32_16x16x32_bf16 v[72:75], a[104:107], a[8:11], v[72:75]\n"
"ds_read_b64_tr_b16 a[132:133], v19 offset:44032\n"
"s_add_u32 s50, s50, 458752\n"
"v_mfma_f32_16x16x32_bf16 v[72:75], a[108:111], a[24:27], v[72:75]\n"
"v_mfma_f32_16x16x32_bf16 v[76:79], a[104:107], a[12:15], v[76:79]\n"
"ds_read_b64_tr_b16 a[134:135], v19 offset:44288\n"
"s_waitcnt vmcnt(19)\n"
"v_sub_u32 v28, v28, v27\n"
"v_mfma_f32_16x16x32_bf16 v[76:79], a[108:111], a[28:31], v[76:79]\n"
"v_mov_b32 v44, 0x400\n"
"s_waitcnt lgkmcnt(8)\n"
"v_mfma_f32_16x16x32_bf16 v[144:147], a[112:115], a[0:3], v[144:147]\n"
"ds_read_b64_tr_b16 a[136:137], v20 offset:30720\n"
"s_add_u32 m0, s92, 0\n"
"v_mfma_f32_16x16x32_bf16 v[144:147], a[116:119], a[16:19], v[144:147]\n"
"v_cmp_eq_u32 vcc, 32, v28\n"
"v_mfma_f32_16x16x32_bf16 v[148:151], a[112:115], a[4:7], v[148:151]\n"
"ds_read_b64_tr_b16 a[138:139], v20 offset:30976\n"
"v_mfma_f32_16x16x32_bf16 v[148:151], a[116:119], a[20:23], v[148:151]\n"
"buffer_load_dwordx4 v2, s[4:7], s51 offen offset:0 lds\n"
"v_mfma_f32_16x16x32_bf16 v[152:155], a[112:115], a[8:11], v[152:155]\n"
"ds_read_b64_tr_b16 a[140:141], v20 offset:44032\n"
"v_mfma_f32_16x16x32_bf16 v[152:155], a[116:119], a[24:27], v[152:155]\n"
"buffer_load_dwordx4 v3, s[4:7], s51 offen offset:1024 lds\n"
"v_mfma_f32_16x16x32_bf16 v[156:159], a[112:115], a[12:15], v[156:159]\n"
"ds_read_b64_tr_b16 a[142:143], v20 offset:44288\n"
"s_add_u32 s51, s51, 128\n"
"v_mfma_f32_16x16x32_bf16 v[156:159], a[116:119], a[28:31], v[156:159]\n"
"s_waitcnt lgkmcnt(8)\n"
"v_mfma_f32_16x16x32_bf16 v[160:163], a[120:123], a[0:3], v[160:163]\n"
"ds_read_b64_tr_b16 a[144:145], v19 offset:31744\n"
"v_mfma_f32_16x16x32_bf16 v[160:163], a[124:127], a[16:19], v[160:163]\n"
"buffer_load_dwordx4 a[220:223], v25, s[60:63], s58 offen\n"
"v_mfma_f32_16x16x32_bf16 v[164:167], a[120:123], a[4:7], v[164:167]\n"
"ds_read_b64_tr_b16 a[146:147], v19 offset:32000\n"
"s_add_u32 s58, s58, s59\n"
"v_mfma_f32_16x16x32_bf16 v[164:167], a[124:127], a[20:23], v[164:167]\n"
"buffer_load_dwordx4 a[224:227], v25, s[60:63], s58 offen\n"
"v_mfma_f32_16x16x32_bf16 v[168:171], a[120:123], a[8:11], v[168:171]\n"
"ds_read_b64_tr_b16 a[148:149], v19 offset:45056\n"
"s_add_u32 s58, s58, s59\n"
"v_mfma_f32_16x16x32_bf16 v[168:171], a[124:127], a[24:27], v[168:171]\n"
"buffer_load_dwordx4 a[228:231], v25, s[60:63], s58 offen\n"
"v_mfma_f32_16x16x32_bf16 v[172:175], a[120:123], a[12:15], v[172:175]\n"
"ds_read_b64_tr_b16 a[150:151], v19 offset:45312\n"
"s_add_u32 s58, s58, s59\n"
"v_mfma_f32_16x16x32_bf16 v[172:175], a[124:127], a[28:31], v[172:175]\n"
"s_waitcnt lgkmcnt(8)\n"
"v_mfma_f32_16x16x32_bf16 v[176:179], a[128:131], a[0:3], v[176:179]\n"
"ds_read_b64_tr_b16 a[152:153], v20 offset:31744\n"
"v_mfma_f32_16x16x32_bf16 v[176:179], a[132:135], a[16:19], v[176:179]\n"
"s_and_b64 exec, vcc, s[56:57]\n"
"buffer_atomic_add v44, v29, s[16:19], 0 offen sc1\n"
"s_mov_b64 exec, -1\n"
"v_mfma_f32_16x16x32_bf16 v[180:183], a[128:131], a[4:7], v[180:183]\n"
"ds_read_b64_tr_b16 a[154:155], v20 offset:32000\n"
"s_waitcnt vmcnt(18)\n"
"v_mfma_f32_16x16x32_bf16 v[180:183], a[132:135], a[20:23], v[180:183]\n"
"s_waitcnt vmcnt(9)\n"
"v_lshlrev_b32 v44, 16, v34\n"
"v_mfma_f32_16x16x32_bf16 v[184:187], a[128:131], a[8:11], v[184:187]\n"
"ds_read_b64_tr_b16 a[156:157], v20 offset:45056\n"
"s_add_u32 m0, s52, 0\n"
"v_mfma_f32_16x16x32_bf16 v[184:187], a[132:135], a[24:27], v[184:187]\n"
"ds_read_b128 a[32:35], v17 offset:32768\n"
"v_and_b32 v34, 0xffff0000, v34\n"
"v_mfma_f32_16x16x32_bf16 v[188:191], a[128:131], a[12:15], v[188:191]\n"
"ds_read_b64_tr_b16 a[158:159], v20 offset:45312\n"
"v_add_f32 v38, v38, v44\n"
"v_mfma_f32_16x16x32_bf16 v[188:191], a[132:135], a[28:31], v[188:191]\n"
"ds_read_b128 a[48:51], v18 offset:32768\n"
"s_waitcnt lgkmcnt(10)\n"
"v_mfma_f32_16x16x32_bf16 v[192:195], a[136:139], a[0:3], v[192:195]\n"
"ds_read_b64_tr_b16 a[160:161], v21 offset:32768\n"
"v_fmac_f32 v39, v44, v44\n"
"v_mfma_f32_16x16x32_bf16 v[192:195], a[140:143], a[16:19], v[192:195]\n"
"buffer_load_dwordx4 v10, s[8:11], s49 offen offset:0 lds\n"
"v_add_f32 v38, v38, v34\n"
"v_mfma_f32_16x16x32_bf16 v[196:199], a[136:139], a[4:7], v[196:199]\n"
"ds_read_b64_tr_b16 a[162:163], v21 offset:33024\n"
"v_fmac_f32 v39, v34, v34\n"
"v_mfma_f32_16x16x32_bf16 v[196:199], a[140:143], a[20:23], v[196:199]\n"
"ds_read_b128 a[36:39], v17 offset:34816\n"
"v_lshlrev_b32 v44, 16, v35\n"
"v_mfma_f32_16x16x32_bf16 v[200:203], a[136:139], a[8:11], v[200:203]\n"
"ds_read_b64_tr_b16 a[164:165], v21 offset:46080\n"
"v_and_b32 v35, 0xffff0000, v35\n"
"v_mfma_f32_16x16x32_bf16 v[200:203], a[140:143], a[24:27], v[200:203]\n"
"buffer_load_dwordx4 v11, s[8:11], s49 offen offset:1024 lds\n"
"v_add_f32 v38, v38, v44\n"
"v_mfma_f32_16x16x32_bf16 v[204:207], a[136:139], a[12:15], v[204:207]\n"
"ds_read_b64_tr_b16 a[166:167], v21 offset:46336\n"
"v_fmac_f32 v39, v44, v44\n"
"v_mfma_f32_16x16x32_bf16 v[204:207], a[140:143], a[28:31], v[204:207]\n"
"ds_read_b128 a[52:55], v18 offset:34816\n"
"s_waitcnt lgkmcnt(12)\n"
"v_mfma_f32_16x16x32_bf16 v[208:211], a[144:147], a[0:3], v[208:211]\n"
"ds_read_b128 a[40:43], v17 offset:36864\n"
"v_add_f32 v38, v38, v35\n"
"v_mfma_f32_16x16x32_bf16 v[208:211], a[148:151], a[16:19], v[208:211]\n"
"ds_read_b128 a[56:59], v18 offset:36864\n"
"v_fmac_f32 v39, v35, v35\n"
"v_mfma_f32_16x16x32_bf16 v[212:215], a[144:147], a[4:7], v[212:215]\n"
"ds_read_b128 a[44:47], v17 offset:38912\n"
"v_lshlrev_b32 v44, 16, v36\n"
"v_mfma_f32_16x16x32_bf16 v[212:215], a[148:151], a[20:23], v[212:215]\n"
"buffer_load_dwordx4 v12, s[8:11], s49 offen offset:2048 lds\n"
"v_and_b32 v36, 0xffff0000, v36\n"
"v_mfma_f32_16x16x32_bf16 v[216:219], a[144:147], a[8:11], v[216:219]\n"
"s_add_u32 s49, s49, 393216\n"
"ds_read_b128 a[60:63], v18 offset:38912\n"
"v_mfma_f32_16x16x32_bf16 v[216:219], a[148:151], a[24:27], v[216:219]\n"
"v_add_f32 v38, v38, v44\n"
"v_fmac_f32 v39, v44, v44\n"
"v_mfma_f32_16x16x32_bf16 v[220:223], a[144:147], a[12:15], v[220:223]\n"
"v_add_f32 v38, v38, v36\n"
"v_fmac_f32 v39, v36, v36\n"
"v_mfma_f32_16x16x32_bf16 v[220:223], a[148:151], a[28:31], v[220:223]\n"
"s_waitcnt vmcnt(8) lgkmcnt(5)\n"
"s_barrier\n"
"v_mfma_f32_16x16x32_bf16 v[224:227], a[152:155], a[0:3], v[224:227]\n"
"ds_read_b64_tr_b16 a[64:65], v22 offset:1024\n"
"v_lshlrev_b32_e64 v44, 16, v37\n"
"v_mfma_f32_16x16x32_bf16 v[224:227], a[156:159], a[16:19], v[224:227]\n"
"ds_read_b64_tr_b16 a[66:67], v22 offset:1280\n"
"v_and_b32 v37, 0xffff0000, v37\n"
"v_mfma_f32_16x16x32_bf16 v[228:231], a[152:155], a[4:7], v[228:231]\n"
"ds_read_b64_tr_b16 a[68:69], v22 offset:14336\n"
"s_add_u32 m0, s93, 0\n"
"v_mfma_f32_16x16x32_bf16 v[228:231], a[156:159], a[20:23], v[228:231]\n"
"ds_read_b64_tr_b16 a[70:71], v22 offset:14592\n"
"v_add_f32 v38, v38, v44\n"
"v_mfma_f32_16x16x32_bf16 v[232:235], a[152:155], a[8:11], v[232:235]\n"
"ds_read_b64_tr_b16 a[72:73], v23 offset:1024\n"
"v_fmac_f32 v39, v44, v44\n"
"v_mfma_f32_16x16x32_bf16 v[232:235], a[156:159], a[24:27], v[232:235]\n"
"ds_read_b64_tr_b16 a[74:75], v23 offset:1280\n"
"v_add_f32 v38, v38, v37\n"
"v_mfma_f32_16x16x32_bf16 v[236:239], a[152:155], a[12:15], v[236:239]\n"
"ds_read_b64_tr_b16 a[76:77], v23 offset:14336\n"
"v_fmac_f32 v39, v37, v37\n"
"v_mfma_f32_16x16x32_bf16 v[236:239], a[156:159], a[28:31], v[236:239]\n"
"ds_read_b64_tr_b16 a[78:79], v23 offset:14592\n"
"v_mfma_f32_16x16x32_bf16 v[240:243], a[160:163], a[0:3], v[240:243]\n"
"ds_read_b64_tr_b16 a[80:81], v22 offset:2048\n"
"v_mfma_f32_16x16x32_bf16 v[240:243], a[164:167], a[16:19], v[240:243]\n"
"buffer_load_dwordx4 v4, s[4:7], s48 offen offset:0 lds\n"
"v_mfma_f32_16x16x32_bf16 v[244:247], a[160:163], a[4:7], v[244:247]\n"
"ds_read_b64_tr_b16 a[82:83], v22 offset:2304\n"
"v_mfma_f32_16x16x32_bf16 v[244:247], a[164:167], a[20:23], v[244:247]\n"
"buffer_load_dwordx4 v5, s[4:7], s48 offen offset:1024 lds\n"
"v_mfma_f32_16x16x32_bf16 v[248:251], a[160:163], a[8:11], v[248:251]\n"
"ds_read_b64_tr_b16 a[84:85], v22 offset:15360\n"
"v_mfma_f32_16x16x32_bf16 v[248:251], a[164:167], a[24:27], v[248:251]\n"
"v_permlane32_swap_b32 v38, v39\n"
"v_mfma_f32_16x16x32_bf16 v[252:255], a[160:163], a[12:15], v[252:255]\n"
"ds_read_b64_tr_b16 a[86:87], v22 offset:15616\n"
"v_add_f32 v38, v38, v39\n"
"v_mfma_f32_16x16x32_bf16 v[252:255], a[164:167], a[28:31], v[252:255]\n"
"s_add_u32 m0, s94, 0\n"
"s_waitcnt lgkmcnt(8)\n"
"v_mfma_f32_16x16x32_bf16 v[80:83], a[64:67], a[32:35], v[80:83]\n"
"ds_read_b64_tr_b16 a[88:89], v23 offset:2048\n"
"v_mov_b32 v44, v38\n"
"v_mfma_f32_16x16x32_bf16 v[80:83], a[68:71], a[48:51], v[80:83]\n"
"buffer_load_dwordx4 v6, s[4:7], s48 offen offset:0 lds\n"
"v_accvgpr_read_b32 v31, a240\n"
"v_mfma_f32_16x16x32_bf16 v[84:87], a[64:67], a[36:39], v[84:87]\n"
"ds_read_b64_tr_b16 a[90:91], v23 offset:2304\n"
"v_mfma_f32_16x16x32_bf16 v[84:87], a[68:71], a[52:55], v[84:87]\n"
"v_permlane16_swap_b32 v44, v38\n"
"v_mfma_f32_16x16x32_bf16 v[88:91], a[64:67], a[40:43], v[88:91]\n"
"ds_read_b64_tr_b16 a[92:93], v23 offset:15360\n"
"v_add_f32 v38, v38, v44\n"
"v_mfma_f32_16x16x32_bf16 v[88:91], a[68:71], a[56:59], v[88:91]\n"
"v_mfma_f32_16x16x32_bf16 v[92:95], a[64:67], a[44:47], v[92:95]\n"
"ds_read_b64_tr_b16 a[94:95], v23 offset:15616\n"
"v_add_f32_dpp v38, v38, v38 quad_perm:[1,0,3,2] row_mask:0xf bank_mask:0xf\n"
"v_mfma_f32_16x16x32_bf16 v[92:95], a[68:71], a[60:63], v[92:95]\n"
"s_waitcnt lgkmcnt(8)\n"
"v_mfma_f32_16x16x32_bf16 v[96:99], a[72:75], a[32:35], v[96:99]\n"
"ds_read_b64_tr_b16 a[96:97], v22 offset:0\n"
"v_add_f32_dpp v38, v38, v38 quad_perm:[2,3,0,1] row_mask:0xf bank_mask:0xf\n"
"v_mfma_f32_16x16x32_bf16 v[96:99], a[76:79], a[48:51], v[96:99]\n"
"buffer_load_dwordx4 v7, s[4:7], s48 offen offset:1024 lds\n"
"v_mfma_f32_16x16x32_bf16 v[100:103], a[72:75], a[36:39], v[100:103]\n"
"ds_read_b64_tr_b16 a[98:99], v22 offset:256\n"
"v_add_f32_dpp v38, v38, v38 row_half_mirror row_mask:0xf bank_mask:0xf\n"
"v_mfma_f32_16x16x32_bf16 v[100:103], a[76:79], a[52:55], v[100:103]\n"
"v_mfma_f32_16x16x32_bf16 v[104:107], a[72:75], a[40:43], v[104:107]\n"
"ds_read_b64_tr_b16 a[100:101], v22 offset:13312\n"
"s_add_u32 m0, s95, 0\n"
"v_mfma_f32_16x16x32_bf16 v[104:107], a[76:79], a[56:59], v[104:107]\n"
"v_mfma_f32_16x16x32_bf16 v[108:111], a[72:75], a[44:47], v[108:111]\n"
"ds_read_b64_tr_b16 a[102:103], v22 offset:13568\n"
"v_mfma_f32_16x16x32_bf16 v[108:111], a[76:79], a[60:63], v[108:111]\n"
"s_waitcnt lgkmcnt(8)\n"
"v_mfma_f32_16x16x32_bf16 v[112:115], a[32:35], a[80:83], v[112:115]\n"
"ds_read_b64_tr_b16 a[104:105], v23 offset:0\n"
"v_mfma_f32_16x16x32_bf16 v[112:115], a[48:51], a[84:87], v[112:115]\n"
"buffer_load_dwordx4 v8, s[4:7], s48 offen offset:0 lds\n"
"v_mfma_f32_16x16x32_bf16 v[116:119], a[36:39], a[80:83], v[116:119]\n"
"ds_read_b64_tr_b16 a[106:107], v23 offset:256\n"
"v_mfma_f32_16x16x32_bf16 v[116:119], a[52:55], a[84:87], v[116:119]\n"
"buffer_load_dwordx4 v9, s[4:7], s48 offen offset:1024 lds\n"
"v_mfma_f32_16x16x32_bf16 v[120:123], a[40:43], a[80:83], v[120:123]\n"
"ds_read_b64_tr_b16 a[108:109], v23 offset:13312\n"
"s_add_u32 s48, s48, 128\n"
"v_mfma_f32_16x16x32_bf16 v[120:123], a[56:59], a[84:87], v[120:123]\n"
"v_mfma_f32_16x16x32_bf16 v[124:127], a[44:47], a[80:83], v[124:127]\n"
"ds_read_b64_tr_b16 a[110:111], v23 offset:13568\n"
"v_mfma_f32_16x16x32_bf16 v[124:127], a[60:63], a[84:87], v[124:127]\n"
"s_waitcnt lgkmcnt(8)\n"
"v_mfma_f32_16x16x32_bf16 v[128:131], a[32:35], a[88:91], v[128:131]\n"
"ds_read_b64_tr_b16 a[112:113], v22 offset:3072\n"
"s_add_u32 m0, s52, 3072\n"
"v_mfma_f32_16x16x32_bf16 v[128:131], a[48:51], a[92:95], v[128:131]\n"
"v_mfma_f32_16x16x32_bf16 v[132:135], a[36:39], a[88:91], v[132:135]\n"
"ds_read_b64_tr_b16 a[114:115], v22 offset:3328\n"
"v_mfma_f32_16x16x32_bf16 v[132:135], a[52:55], a[92:95], v[132:135]\n"
"buffer_load_dwordx4 v13, s[12:15], s50 offen offset:0 lds\n"
"v_mfma_f32_16x16x32_bf16 v[136:139], a[40:43], a[88:91], v[136:139]\n"
"ds_read_b64_tr_b16 a[116:117], v22 offset:16384\n"
"v_mfma_f32_16x16x32_bf16 v[136:139], a[56:59], a[92:95], v[136:139]\n"
"v_mfma_f32_16x16x32_bf16 v[140:143], a[44:47], a[88:91], v[140:143]\n"
"ds_read_b64_tr_b16 a[118:119], v22 offset:16640\n"
"v_mfma_f32_16x16x32_bf16 v[140:143], a[60:63], a[92:95], v[140:143]\n"
"s_waitcnt lgkmcnt(8)\n"
"v_mfma_f32_16x16x32_bf16 v[48:51], a[96:99], a[32:35], v[48:51]\n"
"ds_read_b64_tr_b16 a[120:121], v23 offset:3072\n"
"v_mfma_f32_16x16x32_bf16 v[48:51], a[100:103], a[48:51], v[48:51]\n"
"buffer_load_dwordx4 v14, s[12:15], s50 offen offset:1024 lds\n"
"v_mfma_f32_16x16x32_bf16 v[52:55], a[96:99], a[36:39], v[52:55]\n"
"ds_read_b64_tr_b16 a[122:123], v23 offset:3328\n"
"v_mfma_f32_16x16x32_bf16 v[52:55], a[100:103], a[52:55], v[52:55]\n"
"v_mfma_f32_16x16x32_bf16 v[56:59], a[96:99], a[40:43], v[56:59]\n"
"ds_read_b64_tr_b16 a[124:125], v23 offset:16384\n"
"v_mfma_f32_16x16x32_bf16 v[56:59], a[100:103], a[56:59], v[56:59]\n"
"buffer_load_dwordx4 v15, s[12:15], s50 offen offset:2048 lds\n"
"v_mfma_f32_16x16x32_bf16 v[60:63], a[96:99], a[44:47], v[60:63]\n"
"ds_read_b64_tr_b16 a[126:127], v23 offset:16640\n"
"v_mfma_f32_16x16x32_bf16 v[60:63], a[100:103], a[60:63], v[60:63]\n"
"s_waitcnt lgkmcnt(8)\n"
"v_mfma_f32_16x16x32_bf16 v[64:67], a[104:107], a[32:35], v[64:67]\n"
"ds_read_b64_tr_b16 a[128:129], v22 offset:4096\n"
"v_mfma_f32_16x16x32_bf16 v[64:67], a[108:111], a[48:51], v[64:67]\n"
"v_mfma_f32_16x16x32_bf16 v[68:71], a[104:107], a[36:39], v[68:71]\n"
"ds_read_b64_tr_b16 a[130:131], v22 offset:4352\n"
"v_mfma_f32_16x16x32_bf16 v[68:71], a[108:111], a[52:55], v[68:71]\n"
"s_mov_b32 exec_hi, 0\n"
"buffer_load_dwordx4 v16, s[12:15], s50 offen offset:3072 lds\n"
"s_mov_b32 exec_hi, -1\n"
"v_mfma_f32_16x16x32_bf16 v[72:75], a[104:107], a[40:43], v[72:75]\n"
"ds_read_b64_tr_b16 a[132:133], v22 offset:17408\n"
"s_add_u32 s50, s50, 458752\n"
"v_mfma_f32_16x16x32_bf16 v[72:75], a[108:111], a[56:59], v[72:75]\n"
"v_mfma_f32_16x16x32_bf16 v[76:79], a[104:107], a[44:47], v[76:79]\n"
"ds_read_b64_tr_b16 a[134:135], v22 offset:17664\n"
"v_lshlrev_b32 v46, 7, v0\n"
"v_mfma_f32_16x16x32_bf16 v[76:79], a[108:111], a[60:63], v[76:79]\n"
"s_waitcnt lgkmcnt(8)\n"
"v_mfma_f32_16x16x32_bf16 v[144:147], a[112:115], a[32:35], v[144:147]\n"
"ds_read_b64_tr_b16 a[136:137], v23 offset:4096\n"
"s_add_u32 m0, s92, 32768\n"
"v_mfma_f32_16x16x32_bf16 v[144:147], a[116:119], a[48:51], v[144:147]\n"
"buffer_store_dword v38, v31, s[16:19], 0 offen sc1\n"
"v_mfma_f32_16x16x32_bf16 v[148:151], a[112:115], a[36:39], v[148:151]\n"
"ds_read_b64_tr_b16 a[138:139], v23 offset:4352\n"
"v_mfma_f32_16x16x32_bf16 v[148:151], a[116:119], a[52:55], v[148:151]\n"
"buffer_load_dwordx4 v2, s[4:7], s51 offen offset:0 lds\n"
"v_mfma_f32_16x16x32_bf16 v[152:155], a[112:115], a[40:43], v[152:155]\n"
"ds_read_b64_tr_b16 a[140:141], v23 offset:17408\n"
"v_mfma_f32_16x16x32_bf16 v[152:155], a[116:119], a[56:59], v[152:155]\n"
"buffer_load_dwordx4 v3, s[4:7], s51 offen offset:1024 lds\n"
"v_mfma_f32_16x16x32_bf16 v[156:159], a[112:115], a[44:47], v[156:159]\n"
"ds_read_b64_tr_b16 a[142:143], v23 offset:17664\n"
"s_add_u32 s51, s51, 128\n"
"v_mfma_f32_16x16x32_bf16 v[156:159], a[116:119], a[60:63], v[156:159]\n"
"s_lshl_b32 s88, s96, 2\n"
"s_and_b32 s88, s88, 4\n"
"s_waitcnt lgkmcnt(8)\n"
"v_mfma_f32_16x16x32_bf16 v[160:163], a[120:123], a[32:35], v[160:163]\n"
"ds_read_b64_tr_b16 a[144:145], v22 offset:5120\n"
"s_lshr_b32 s44, s43, 1\n"
"v_mfma_f32_16x16x32_bf16 v[160:163], a[124:127], a[48:51], v[160:163]\n"
"s_or_b32 s44, s44, s88\n"
"s_bfe_u32 s41, s96, 0x30003\n"
"s_xor_b32 s41, s41, 0\n"
"v_mfma_f32_16x16x32_bf16 v[164:167], a[120:123], a[36:39], v[164:167]\n"
"ds_read_b64_tr_b16 a[146:147], v22 offset:5376\n"
"v_bfe_u32 v26, v0, 1, 2\n"
"v_mfma_f32_16x16x32_bf16 v[164:167], a[124:127], a[52:55], v[164:167]\n"
"v_and_b32 v25, 1, v0\n"
"v_lshlrev_b32 v25, 7, v25\n"
"v_mfma_f32_16x16x32_bf16 v[168:171], a[120:123], a[40:43], v[168:171]\n"
"ds_read_b64_tr_b16 a[148:149], v22 offset:18432\n"
"v_lshl_add_u32 v25, v26, 11, v25\n"
"v_mfma_f32_16x16x32_bf16 v[168:171], a[124:127], a[56:59], v[168:171]\n"
"s_mov_b64 s[82:83], 0xff\n"
"s_lshl2_add_u32 s88, s44, s40\n"
"s_lshl_b32 s88, s88, 13\n"
"v_mfma_f32_16x16x32_bf16 v[172:175], a[120:123], a[44:47], v[172:175]\n"
"ds_read_b64_tr_b16 a[150:151], v22 offset:18688\n"
"s_lshl_b32 s89, s41, 8\n"
"v_mfma_f32_16x16x32_bf16 v[172:175], a[124:127], a[60:63], v[172:175]\n"
"s_add_u32 s88, s88, s89\n"
"s_mul_i32 s58, s45, 1835008\n"
"s_waitcnt lgkmcnt(8)\n"
"v_mfma_f32_16x16x32_bf16 v[176:179], a[128:131], a[32:35], v[176:179]\n"
"ds_read_b64_tr_b16 a[152:153], v23 offset:5120\n"
"s_add_u32 s58, s58, s88\n"
"v_mfma_f32_16x16x32_bf16 v[176:179], a[132:135], a[48:51], v[176:179]\n"
"s_lshl_b32 s59, s45, 19\n"
"s_add_u32 s59, s59, s88\n"
"v_mfma_f32_16x16x32_bf16 v[180:183], a[128:131], a[36:39], v[180:183]\n"
"ds_read_b64_tr_b16 a[154:155], v23 offset:5376\n"
"v_mfma_f32_16x16x32_bf16 v[180:183], a[132:135], a[52:55], v[180:183]\n"
"s_mov_b64 exec, s[82:83]\n"
"buffer_load_dword a252, v25, s[20:23], s58 offen\n"
"s_mov_b64 exec, -1\n"
"v_mfma_f32_16x16x32_bf16 v[184:187], a[128:131], a[40:43], v[184:187]\n"
"ds_read_b64_tr_b16 a[156:157], v23 offset:18432\n"
"s_add_u32 s58, s58, 262144\n"
"v_mfma_f32_16x16x32_bf16 v[184:187], a[132:135], a[56:59], v[184:187]\n"
"s_mov_b64 exec, s[82:83]\n"
"buffer_load_dword a252, v25, s[20:23], s58 offen\n"
"s_mov_b64 exec, -1\n"
"v_mfma_f32_16x16x32_bf16 v[188:191], a[128:131], a[44:47], v[188:191]\n"
"ds_read_b64_tr_b16 a[158:159], v23 offset:18688\n"
"s_add_u32 s58, s58, 262144\n"
"v_mfma_f32_16x16x32_bf16 v[188:191], a[132:135], a[60:63], v[188:191]\n"
"s_add_u32 m0, s52, 26624\n"
"s_waitcnt vmcnt(9)\n"
"s_waitcnt lgkmcnt(8)\n"
"v_mfma_f32_16x16x32_bf16 v[192:195], a[136:139], a[32:35], v[192:195]\n"
"ds_read_b64_tr_b16 a[160:161], v24 offset:6144\n"
"v_mfma_f32_16x16x32_bf16 v[192:195], a[140:143], a[48:51], v[192:195]\n"
"buffer_load_dwordx4 v10, s[8:11], s49 offen offset:0 lds\n"
"v_mfma_f32_16x16x32_bf16 v[196:199], a[136:139], a[36:39], v[196:199]\n"
"ds_read_b64_tr_b16 a[162:163], v24 offset:6400\n"
"v_mfma_f32_16x16x32_bf16 v[196:199], a[140:143], a[52:55], v[196:199]\n"
"ds_read_b128 a[0:3], v17 offset:0\n"
"v_mfma_f32_16x16x32_bf16 v[200:203], a[136:139], a[40:43], v[200:203]\n"
"ds_read_b64_tr_b16 a[164:165], v24 offset:19456\n"
"v_mfma_f32_16x16x32_bf16 v[200:203], a[140:143], a[56:59], v[200:203]\n"
"buffer_load_dwordx4 v11, s[8:11], s49 offen offset:1024 lds\n"
"v_mfma_f32_16x16x32_bf16 v[204:207], a[136:139], a[44:47], v[204:207]\n"
"ds_read_b64_tr_b16 a[166:167], v24 offset:19712\n"
"v_mfma_f32_16x16x32_bf16 v[204:207], a[140:143], a[60:63], v[204:207]\n"
"ds_read_b128 a[16:19], v18 offset:0\n"
"s_waitcnt lgkmcnt(10)\n"
"v_mfma_f32_16x16x32_bf16 v[208:211], a[144:147], a[32:35], v[208:211]\n"
"ds_read_b128 a[4:7], v17 offset:2048\n"
"v_mfma_f32_16x16x32_bf16 v[208:211], a[148:151], a[48:51], v[208:211]\n"
"ds_read_b128 a[20:23], v18 offset:2048\n"
"v_mfma_f32_16x16x32_bf16 v[212:215], a[144:147], a[36:39], v[212:215]\n"
"ds_read_b128 a[8:11], v17 offset:4096\n"
"v_mfma_f32_16x16x32_bf16 v[212:215], a[148:151], a[52:55], v[212:215]\n"
".Lpad3:\n"
"s_cbranch_execz .Lpad7\n"
"buffer_load_dwordx4 v12, s[8:11], s49 offen offset:2048 lds\n"
"v_mfma_f32_16x16x32_bf16 v[216:219], a[144:147], a[40:43], v[216:219]\n"
"s_add_u32 s49, s49, 393216\n"
"ds_read_b128 a[24:27], v18 offset:4096\n"
"v_mfma_f32_16x16x32_bf16 v[216:219], a[148:151], a[56:59], v[216:219]\n"
"ds_read_b128 a[12:15], v17 offset:6144\n"
"ds_read_b128 a[28:31], v18 offset:6144\n"
"v_mfma_f32_16x16x32_bf16 v[220:223], a[144:147], a[44:47], v[220:223]\n"
"s_mov_b64 exec, 31\n"
"buffer_load_dword a252, v46, s[0:3], s97 offen\n"
"s_mov_b64 exec, -1\n"
"v_mfma_f32_16x16x32_bf16 v[220:223], a[148:151], a[60:63], v[220:223]\n"
"s_waitcnt vmcnt(9) lgkmcnt(7)\n"
"s_barrier\n"
"v_mfma_f32_16x16x32_bf16 v[224:227], a[152:155], a[32:35], v[224:227]\n"
"ds_read_b64_tr_b16 a[64:65], v19 offset:1024\n"
"v_mfma_f32_16x16x32_bf16 v[224:227], a[156:159], a[48:51], v[224:227]\n"
"ds_read_b64_tr_b16 a[66:67], v19 offset:1280\n"
"v_mfma_f32_16x16x32_bf16 v[228:231], a[152:155], a[36:39], v[228:231]\n"
"ds_read_b64_tr_b16 a[68:69], v19 offset:14336\n"
"s_add_u32 m0, s93, 32768\n"
"v_mfma_f32_16x16x32_bf16 v[228:231], a[156:159], a[52:55], v[228:231]\n"
"ds_read_b64_tr_b16 a[70:71], v19 offset:14592\n"
"v_mfma_f32_16x16x32_bf16 v[232:235], a[152:155], a[40:43], v[232:235]\n"
"ds_read_b64_tr_b16 a[72:73], v20 offset:1024\n"
"v_mfma_f32_16x16x32_bf16 v[232:235], a[156:159], a[56:59], v[232:235]\n"
"ds_read_b64_tr_b16 a[74:75], v20 offset:1280\n"
"v_mfma_f32_16x16x32_bf16 v[236:239], a[152:155], a[44:47], v[236:239]\n"
"ds_read_b64_tr_b16 a[76:77], v20 offset:14336\n"
"v_mfma_f32_16x16x32_bf16 v[236:239], a[156:159], a[60:63], v[236:239]\n"
"ds_read_b64_tr_b16 a[78:79], v20 offset:14592\n"
"v_mfma_f32_16x16x32_bf16 v[240:243], a[160:163], a[32:35], v[240:243]\n"
"ds_read_b64_tr_b16 a[80:81], v19 offset:2048\n"
"v_mfma_f32_16x16x32_bf16 v[240:243], a[164:167], a[48:51], v[240:243]\n"
"buffer_load_dwordx4 v4, s[4:7], s48 offen offset:0 lds\n"
"v_mfma_f32_16x16x32_bf16 v[244:247], a[160:163], a[36:39], v[244:247]\n"
"ds_read_b64_tr_b16 a[82:83], v19 offset:2304\n"
"v_mfma_f32_16x16x32_bf16 v[244:247], a[164:167], a[52:55], v[244:247]\n"
"buffer_load_dwordx4 v5, s[4:7], s48 offen offset:1024 lds\n"
"v_mfma_f32_16x16x32_bf16 v[248:251], a[160:163], a[40:43], v[248:251]\n"
"ds_read_b64_tr_b16 a[84:85], v19 offset:15360\n"
"v_mfma_f32_16x16x32_bf16 v[248:251], a[164:167], a[56:59], v[248:251]\n"
"s_mov_b64 exec, s[56:57]\n"
"buffer_load_dword v28, v29, s[16:19], 0 offen sc1\n"
"s_mov_b64 exec, -1\n"
"v_mfma_f32_16x16x32_bf16 v[252:255], a[160:163], a[44:47], v[252:255]\n"
"ds_read_b64_tr_b16 a[86:87], v19 offset:15616\n"
"v_mfma_f32_16x16x32_bf16 v[252:255], a[164:167], a[60:63], v[252:255]\n"
"s_add_u32 m0, s94, 32768\n"
"v_mfma_f32_16x16x32_bf16 v[40:43], a[232:235], a[168:171], 0\n"
"v_mfma_f32_16x16x32_bf16 v[40:43], a[232:235], a[172:175], v[40:43]\n"
"v_mfma_f32_16x16x32_bf16 v[40:43], a[232:235], a[176:179], v[40:43]\n"
"v_mfma_f32_16x16x32_bf16 v[40:43], a[232:235], a[180:183], v[40:43]\n"
"v_mfma_f32_16x16x32_bf16 v[40:43], a[232:235], a[184:187], v[40:43]\n"
"v_mfma_f32_16x16x32_bf16 v[40:43], a[232:235], a[188:191], v[40:43]\n"
"v_mfma_f32_16x16x32_bf16 v[40:43], a[232:235], a[192:195], v[40:43]\n"
"v_mfma_f32_16x16x32_bf16 v[40:43], a[232:235], a[196:199], v[40:43]\n"
"v_mfma_f32_16x16x32_bf16 v[40:43], a[232:235], a[200:203], v[40:43]\n"
"v_mfma_f32_16x16x32_bf16 v[40:43], a[232:235], a[204:207], v[40:43]\n"
"v_mfma_f32_16x16x32_bf16 v[40:43], a[232:235], a[208:211], v[40:43]\n"
"v_mfma_f32_16x16x32_bf16 v[40:43], a[232:235], a[212:215], v[40:43]\n"
"v_mfma_f32_16x16x32_bf16 v[40:43], a[232:235], a[216:219], v[40:43]\n"
"v_mfma_f32_16x16x32_bf16 v[40:43], a[232:235], a[220:223], v[40:43]\n"
"v_mfma_f32_16x16x32_bf16 v[40:43], a[232:235], a[224:227], v[40:43]\n"
"v_mfma_f32_16x16x32_bf16 v[40:43], a[232:235], a[228:231], v[40:43]\n"
"s_waitcnt lgkmcnt(8)\n"
"v_mfma_f32_16x16x32_bf16 v[80:83], a[64:67], a[0:3], v[80:83]\n"
"ds_read_b64_tr_b16 a[88:89], v20 offset:2048\n"
"v_mfma_f32_16x16x32_bf16 v[80:83], a[68:71], a[16:19], v[80:83]\n"
"buffer_load_dwordx4 v6, s[4:7], s48 offen offset:0 lds\n"
"v_mfma_f32_16x16x32_bf16 v[84:87], a[64:67], a[4:7], v[84:87]\n"
"ds_read_b64_tr_b16 a[90:91], v20 offset:2304\n"
"v_mfma_f32_16x16x32_bf16 v[84:87], a[68:71], a[20:23], v[84:87]\n"
"v_accvgpr_read_b32 v30, a241\n"
"v_add_f32_dpp v40, v40, v40 row_ror:8 row_mask:0xf bank_mask:0xf\n"
"v_mfma_f32_16x16x32_bf16 v[88:91], a[64:67], a[8:11], v[88:91]\n"
"ds_read_b64_tr_b16 a[92:93], v20 offset:15360\n"
"v_add_f32_dpp v41, v41, v41 row_ror:8 row_mask:0xf bank_mask:0xf\n"
"v_mfma_f32_16x16x32_bf16 v[88:91], a[68:71], a[24:27], v[88:91]\n"
"v_add_f32_dpp v42, v42, v42 row_ror:8 row_mask:0xf bank_mask:0xf\n"
"v_add_f32_dpp v43, v43, v43 row_ror:8 row_mask:0xf bank_mask:0xf\n"
"v_mfma_f32_16x16x32_bf16 v[92:95], a[64:67], a[12:15], v[92:95]\n"
"ds_read_b64_tr_b16 a[94:95], v20 offset:15616\n"
"v_mfma_f32_16x16x32_bf16 v[92:95], a[68:71], a[28:31], v[92:95]\n"
"s_waitcnt lgkmcnt(8)\n"
"v_mfma_f32_16x16x32_bf16 v[96:99], a[72:75], a[0:3], v[96:99]\n"
"ds_read_b64_tr_b16 a[96:97], v19 offset:0\n"
"v_mfma_f32_16x16x32_bf16 v[96:99], a[76:79], a[16:19], v[96:99]\n"
"buffer_load_dwordx4 v7, s[4:7], s48 offen offset:1024 lds\n"
"v_mfma_f32_16x16x32_bf16 v[100:103], a[72:75], a[4:7], v[100:103]\n"
"ds_read_b64_tr_b16 a[98:99], v19 offset:256\n"
"v_mfma_f32_16x16x32_bf16 v[100:103], a[76:79], a[20:23], v[100:103]\n"
"v_mfma_f32_16x16x32_bf16 v[104:107], a[72:75], a[8:11], v[104:107]\n"
"ds_read_b64_tr_b16 a[100:101], v19 offset:13312\n"
"s_add_u32 m0, s95, 32768\n"
"v_mfma_f32_16x16x32_bf16 v[104:107], a[76:79], a[24:27], v[104:107]\n"
"v_mfma_f32_16x16x32_bf16 v[108:111], a[72:75], a[12:15], v[108:111]\n"
"ds_read_b64_tr_b16 a[102:103], v19 offset:13568\n"
"v_mfma_f32_16x16x32_bf16 v[108:111], a[76:79], a[28:31], v[108:111]\n"
"s_waitcnt lgkmcnt(8)\n"
"v_mfma_f32_16x16x32_bf16 v[112:115], a[0:3], a[80:83], v[112:115]\n"
"ds_read_b64_tr_b16 a[104:105], v20 offset:0\n"
"v_mfma_f32_16x16x32_bf16 v[112:115], a[16:19], a[84:87], v[112:115]\n"
"buffer_load_dwordx4 v8, s[4:7], s48 offen offset:0 lds\n"
"v_mfma_f32_16x16x32_bf16 v[116:119], a[4:7], a[80:83], v[116:119]\n"
"ds_read_b64_tr_b16 a[106:107], v20 offset:256\n"
"v_mfma_f32_16x16x32_bf16 v[116:119], a[20:23], a[84:87], v[116:119]\n"
"buffer_load_dwordx4 v9, s[4:7], s48 offen offset:1024 lds\n"
"v_mfma_f32_16x16x32_bf16 v[120:123], a[8:11], a[80:83], v[120:123]\n"
"ds_read_b64_tr_b16 a[108:109], v20 offset:13312\n"
"s_add_u32 s48, s48, 128\n"
"v_mfma_f32_16x16x32_bf16 v[120:123], a[24:27], a[84:87], v[120:123]\n"
"v_mfma_f32_16x16x32_bf16 v[124:127], a[12:15], a[80:83], v[124:127]\n"
"ds_read_b64_tr_b16 a[110:111], v20 offset:13568\n"
"v_mfma_f32_16x16x32_bf16 v[124:127], a[28:31], a[84:87], v[124:127]\n"
"s_waitcnt lgkmcnt(8)\n"
"v_mfma_f32_16x16x32_bf16 v[128:131], a[0:3], a[88:91], v[128:131]\n"
"ds_read_b64_tr_b16 a[112:113], v19 offset:3072\n"
"s_add_u32 m0, s52, 29696\n"
"v_mfma_f32_16x16x32_bf16 v[128:131], a[16:19], a[92:95], v[128:131]\n"
"v_mfma_f32_16x16x32_bf16 v[132:135], a[4:7], a[88:91], v[132:135]\n"
"ds_read_b64_tr_b16 a[114:115], v19 offset:3328\n"
"v_mfma_f32_16x16x32_bf16 v[132:135], a[20:23], a[92:95], v[132:135]\n"
"buffer_load_dwordx4 v13, s[12:15], s50 offen offset:0 lds\n"
"v_mfma_f32_16x16x32_bf16 v[136:139], a[8:11], a[88:91], v[136:139]\n"
"ds_read_b64_tr_b16 a[116:117], v19 offset:16384\n"
"v_mfma_f32_16x16x32_bf16 v[136:139], a[24:27], a[92:95], v[136:139]\n"
"v_mfma_f32_16x16x32_bf16 v[140:143], a[12:15], a[88:91], v[140:143]\n"
"ds_read_b64_tr_b16 a[118:119], v19 offset:16640\n"
"v_mfma_f32_16x16x32_bf16 v[140:143], a[28:31], a[92:95], v[140:143]\n"
"s_waitcnt lgkmcnt(8)\n"
"v_mfma_f32_16x16x32_bf16 v[48:51], a[96:99], a[0:3], v[48:51]\n"
"ds_read_b64_tr_b16 a[120:121], v20 offset:3072\n"
"v_mfma_f32_16x16x32_bf16 v[48:51], a[100:103], a[16:19], v[48:51]\n"
"buffer_load_dwordx4 v14, s[12:15], s50 offen offset:1024 lds\n"
"v_mfma_f32_16x16x32_bf16 v[52:55], a[96:99], a[4:7], v[52:55]\n"
"ds_read_b64_tr_b16 a[122:123], v20 offset:3328\n"
"v_mfma_f32_16x16x32_bf16 v[52:55], a[100:103], a[20:23], v[52:55]\n"
"v_mfma_f32_16x16x32_bf16 v[56:59], a[96:99], a[8:11], v[56:59]\n"
"ds_read_b64_tr_b16 a[124:125], v20 offset:16384\n"
"v_mfma_f32_16x16x32_bf16 v[56:59], a[100:103], a[24:27], v[56:59]\n"
"buffer_load_dwordx4 v15, s[12:15], s50 offen offset:2048 lds\n"
"v_mfma_f32_16x16x32_bf16 v[60:63], a[96:99], a[12:15], v[60:63]\n"
"ds_read_b64_tr_b16 a[126:127], v20 offset:16640\n"
"v_mfma_f32_16x16x32_bf16 v[60:63], a[100:103], a[28:31], v[60:63]\n"
"s_waitcnt lgkmcnt(8)\n"
"v_mfma_f32_16x16x32_bf16 v[64:67], a[104:107], a[0:3], v[64:67]\n"
"ds_read_b64_tr_b16 a[128:129], v19 offset:4096\n"
"v_mfma_f32_16x16x32_bf16 v[64:67], a[108:111], a[16:19], v[64:67]\n"
"v_mfma_f32_16x16x32_bf16 v[68:71], a[104:107], a[4:7], v[68:71]\n"
"ds_read_b64_tr_b16 a[130:131], v19 offset:4352\n"
"v_mfma_f32_16x16x32_bf16 v[68:71], a[108:111], a[20:23], v[68:71]\n"
"s_mov_b32 exec_hi, 0\n"
"buffer_load_dwordx4 v16, s[12:15], s50 offen offset:3072 lds\n"
"s_mov_b32 exec_hi, -1\n"
"v_mfma_f32_16x16x32_bf16 v[72:75], a[104:107], a[8:11], v[72:75]\n"
"ds_read_b64_tr_b16 a[132:133], v19 offset:17408\n"
"s_add_u32 s50, s50, 458752\n"
"v_mfma_f32_16x16x32_bf16 v[72:75], a[108:111], a[24:27], v[72:75]\n"
"v_mfma_f32_16x16x32_bf16 v[76:79], a[104:107], a[12:15], v[76:79]\n"
"ds_read_b64_tr_b16 a[134:135], v19 offset:17664\n"
"v_mfma_f32_16x16x32_bf16 v[76:79], a[108:111], a[28:31], v[76:79]\n"
"s_waitcnt lgkmcnt(8)\n"
"v_mfma_f32_16x16x32_bf16 v[144:147], a[112:115], a[0:3], v[144:147]\n"
"ds_read_b64_tr_b16 a[136:137], v20 offset:4096\n"
"s_add_u32 m0, s92, 0\n"
"v_mfma_f32_16x16x32_bf16 v[144:147], a[116:119], a[16:19], v[144:147]\n"
"buffer_store_dwordx4 v[40:43], v30, s[16:19], 0 offen sc1\n"
"v_mfma_f32_16x16x32_bf16 v[148:151], a[112:115], a[4:7], v[148:151]\n"
"ds_read_b64_tr_b16 a[138:139], v20 offset:4352\n"
"v_mfma_f32_16x16x32_bf16 v[148:151], a[116:119], a[20:23], v[148:151]\n"
"buffer_load_dwordx4 v2, s[4:7], s51 offen offset:0 lds\n"
"v_mfma_f32_16x16x32_bf16 v[152:155], a[112:115], a[8:11], v[152:155]\n"
"ds_read_b64_tr_b16 a[140:141], v20 offset:17408\n"
"v_mfma_f32_16x16x32_bf16 v[152:155], a[116:119], a[24:27], v[152:155]\n"
"buffer_load_dwordx4 v3, s[4:7], s51 offen offset:1024 lds\n"
"v_mfma_f32_16x16x32_bf16 v[156:159], a[112:115], a[12:15], v[156:159]\n"
"ds_read_b64_tr_b16 a[142:143], v20 offset:17664\n"
"s_add_u32 s51, s51, 128\n"
"v_mfma_f32_16x16x32_bf16 v[156:159], a[116:119], a[28:31], v[156:159]\n"
"s_waitcnt lgkmcnt(8)\n"
"v_mfma_f32_16x16x32_bf16 v[160:163], a[120:123], a[0:3], v[160:163]\n"
"ds_read_b64_tr_b16 a[144:145], v19 offset:5120\n"
"v_mfma_f32_16x16x32_bf16 v[160:163], a[124:127], a[16:19], v[160:163]\n"
"s_mov_b64 exec, s[82:83]\n"
"buffer_load_dword a252, v25, s[20:23], s58 offen\n"
"s_mov_b64 exec, -1\n"
"v_mfma_f32_16x16x32_bf16 v[164:167], a[120:123], a[4:7], v[164:167]\n"
"ds_read_b64_tr_b16 a[146:147], v19 offset:5376\n"
"s_add_u32 s58, s58, 262144\n"
"v_mfma_f32_16x16x32_bf16 v[164:167], a[124:127], a[20:23], v[164:167]\n"
"v_mfma_f32_16x16x32_bf16 v[168:171], a[120:123], a[8:11], v[168:171]\n"
"ds_read_b64_tr_b16 a[148:149], v19 offset:18432\n"
"v_mfma_f32_16x16x32_bf16 v[168:171], a[124:127], a[24:27], v[168:171]\n"
"s_mov_b64 exec, s[82:83]\n"
"buffer_load_dword a252, v25, s[20:23], s58 offen\n"
"s_mov_b64 exec, -1\n"
"v_mfma_f32_16x16x32_bf16 v[172:175], a[120:123], a[12:15], v[172:175]\n"
"ds_read_b64_tr_b16 a[150:151], v19 offset:18688\n"
"s_add_u32 s58, s58, 262144\n"
"v_mfma_f32_16x16x32_bf16 v[172:175], a[124:127], a[28:31], v[172:175]\n"
"s_waitcnt lgkmcnt(8)\n"
"v_mfma_f32_16x16x32_bf16 v[176:179], a[128:131], a[0:3], v[176:179]\n"
"ds_read_b64_tr_b16 a[152:153], v20 offset:5120\n"
"v_mfma_f32_16x16x32_bf16 v[176:179], a[132:135], a[16:19], v[176:179]\n"
"v_mfma_f32_16x16x32_bf16 v[180:183], a[128:131], a[4:7], v[180:183]\n"
"ds_read_b64_tr_b16 a[154:155], v20 offset:5376\n"
"v_mfma_f32_16x16x32_bf16 v[180:183], a[132:135], a[20:23], v[180:183]\n"
"s_waitcnt vmcnt(9)\n"
"v_mfma_f32_16x16x32_bf16 v[184:187], a[128:131], a[8:11], v[184:187]\n"
"ds_read_b64_tr_b16 a[156:157], v20 offset:18432\n"
"s_add_u32 m0, s52, 53248\n"
"v_mfma_f32_16x16x32_bf16 v[184:187], a[132:135], a[24:27], v[184:187]\n"
"ds_read_b128 a[32:35], v17 offset:32768\n"
"v_mfma_f32_16x16x32_bf16 v[188:191], a[128:131], a[12:15], v[188:191]\n"
"ds_read_b64_tr_b16 a[158:159], v20 offset:18688\n"
"v_mfma_f32_16x16x32_bf16 v[188:191], a[132:135], a[28:31], v[188:191]\n"
"ds_read_b128 a[48:51], v18 offset:32768\n"
"s_waitcnt lgkmcnt(10)\n"
"v_mfma_f32_16x16x32_bf16 v[192:195], a[136:139], a[0:3], v[192:195]\n"
"ds_read_b64_tr_b16 a[160:161], v21 offset:6144\n"
"v_mfma_f32_16x16x32_bf16 v[192:195], a[140:143], a[16:19], v[192:195]\n"
"buffer_load_dwordx4 v10, s[8:11], s49 offen offset:0 lds\n"
"v_mfma_f32_16x16x32_bf16 v[196:199], a[136:139], a[4:7], v[196:199]\n"
"ds_read_b64_tr_b16 a[162:163], v21 offset:6400\n"
"v_mfma_f32_16x16x32_bf16 v[196:199], a[140:143], a[20:23], v[196:199]\n"
"ds_read_b128 a[36:39], v17 offset:34816\n"
"v_mfma_f32_16x16x32_bf16 v[200:203], a[136:139], a[8:11], v[200:203]\n"
"ds_read_b64_tr_b16 a[164:165], v21 offset:19456\n"
"v_mfma_f32_16x16x32_bf16 v[200:203], a[140:143], a[24:27], v[200:203]\n"
"buffer_load_dwordx4 v11, s[8:11], s49 offen offset:1024 lds\n"
"v_mfma_f32_16x16x32_bf16 v[204:207], a[136:139], a[12:15], v[204:207]\n"
"ds_read_b64_tr_b16 a[166:167], v21 offset:19712\n"
"v_mfma_f32_16x16x32_bf16 v[204:207], a[140:143], a[28:31], v[204:207]\n"
"ds_read_b128 a[52:55], v18 offset:34816\n"
"s_waitcnt lgkmcnt(12)\n"
"v_mfma_f32_16x16x32_bf16 v[208:211], a[144:147], a[0:3], v[208:211]\n"
"ds_read_b128 a[40:43], v17 offset:36864\n"
"v_mfma_f32_16x16x32_bf16 v[208:211], a[148:151], a[16:19], v[208:211]\n"
"ds_read_b128 a[56:59], v18 offset:36864\n"
"v_mfma_f32_16x16x32_bf16 v[212:215], a[144:147], a[4:7], v[212:215]\n"
"ds_read_b128 a[44:47], v17 offset:38912\n"
"v_mfma_f32_16x16x32_bf16 v[212:215], a[148:151], a[20:23], v[212:215]\n"
"buffer_load_dwordx4 v12, s[8:11], s49 offen offset:2048 lds\n"
"v_mfma_f32_16x16x32_bf16 v[216:219], a[144:147], a[8:11], v[216:219]\n"
"s_add_u32 s49, s49, 393216\n"
"ds_read_b128 a[60:63], v18 offset:38912\n"
"v_mfma_f32_16x16x32_bf16 v[216:219], a[148:151], a[24:27], v[216:219]\n"
"v_mfma_f32_16x16x32_bf16 v[220:223], a[144:147], a[12:15], v[220:223]\n"
"v_mfma_f32_16x16x32_bf16 v[220:223], a[148:151], a[28:31], v[220:223]\n"
"s_waitcnt vmcnt(8) lgkmcnt(5)\n"
"s_barrier\n"
"v_mfma_f32_16x16x32_bf16 v[224:227], a[152:155], a[0:3], v[224:227]\n"
"ds_read_b64_tr_b16 a[64:65], v19 offset:27648\n"
"v_mfma_f32_16x16x32_bf16 v[224:227], a[156:159], a[16:19], v[224:227]\n"
"ds_read_b64_tr_b16 a[66:67], v19 offset:27904\n"
"v_mfma_f32_16x16x32_bf16 v[228:231], a[152:155], a[4:7], v[228:231]\n"
"ds_read_b64_tr_b16 a[68:69], v19 offset:40960\n"
"s_add_u32 m0, s93, 0\n"
"v_mfma_f32_16x16x32_bf16 v[228:231], a[156:159], a[20:23], v[228:231]\n"
"ds_read_b64_tr_b16 a[70:71], v19 offset:41216\n"
"v_mfma_f32_16x16x32_bf16 v[232:235], a[152:155], a[8:11], v[232:235]\n"
"ds_read_b64_tr_b16 a[72:73], v20 offset:27648\n"
"v_mfma_f32_16x16x32_bf16 v[232:235], a[156:159], a[24:27], v[232:235]\n"
"ds_read_b64_tr_b16 a[74:75], v20 offset:27904\n"
"v_mfma_f32_16x16x32_bf16 v[236:239], a[152:155], a[12:15], v[236:239]\n"
"ds_read_b64_tr_b16 a[76:77], v20 offset:40960\n"
"v_mfma_f32_16x16x32_bf16 v[236:239], a[156:159], a[28:31], v[236:239]\n"
"ds_read_b64_tr_b16 a[78:79], v20 offset:41216\n"
"v_mfma_f32_16x16x32_bf16 v[240:243], a[160:163], a[0:3], v[240:243]\n"
"ds_read_b64_tr_b16 a[80:81], v19 offset:28672\n"
"v_sub_u32 v28, v28, v27\n"
"v_mfma_f32_16x16x32_bf16 v[240:243], a[164:167], a[16:19], v[240:243]\n"
"buffer_load_dwordx4 v4, s[4:7], s48 offen offset:0 lds\n"
"v_cmp_ge_i32 vcc, 32, v28\n"
"v_mfma_f32_16x16x32_bf16 v[244:247], a[160:163], a[4:7], v[244:247]\n"
"ds_read_b64_tr_b16 a[82:83], v19 offset:28928\n"
"v_mfma_f32_16x16x32_bf16 v[244:247], a[164:167], a[20:23], v[244:247]\n"
"buffer_load_dwordx4 v5, s[4:7], s48 offen offset:1024 lds\n"
"v_mfma_f32_16x16x32_bf16 v[248:251], a[160:163], a[8:11], v[248:251]\n"
"ds_read_b64_tr_b16 a[84:85], v19 offset:41984\n"
"v_mfma_f32_16x16x32_bf16 v[248:251], a[164:167], a[24:27], v[248:251]\n"
"s_and_b64 vcc, vcc, s[56:57]\n"
"s_cbranch_vccnz .Ly4_sl\n"
".Ly4_ok:\n"
"s_lshl_b32 s74, s96, 15\n"
"v_mfma_f32_16x16x32_bf16 v[252:255], a[160:163], a[12:15], v[252:255]\n"
"ds_read_b64_tr_b16 a[86:87], v19 offset:42240\n"
"s_lshl_b32 s75, s40, 12\n"
"v_mfma_f32_16x16x32_bf16 v[252:255], a[164:167], a[28:31], v[252:255]\n"
"s_add_u32 m0, s94, 0\n"
"s_add_u32 s74, s74, s75\n"
"s_waitcnt lgkmcnt(8)\n"
"v_mfma_f32_16x16x32_bf16 v[80:83], a[64:67], a[32:35], v[80:83]\n"
"ds_read_b64_tr_b16 a[88:89], v20 offset:28672\n"
"s_add_u32 s98, s74, 0\n"
"v_mfma_f32_16x16x32_bf16 v[80:83], a[68:71], a[48:51], v[80:83]\n"
"buffer_load_dwordx4 v6, s[4:7], s48 offen offset:0 lds\n"
"v_mfma_f32_16x16x32_bf16 v[84:87], a[64:67], a[36:39], v[84:87]\n"
"ds_read_b64_tr_b16 a[90:91], v20 offset:28928\n"
"s_add_u32 s74, s74, 1048576\n"
"v_mfma_f32_16x16x32_bf16 v[84:87], a[68:71], a[52:55], v[84:87]\n"
"s_cmp_lt_u32 s43, 7\n"
"s_cselect_b32 s74, s74, 2130706432\n"
"s_add_u32 s75, s74, 16384\n"
"v_mfma_f32_16x16x32_bf16 v[88:91], a[64:67], a[40:43], v[88:91]\n"
"ds_read_b64_tr_b16 a[92:93], v20 offset:41984\n"
"s_lshl_b32 s91, s40, 1\n"
"v_mfma_f32_16x16x32_bf16 v[88:91], a[68:71], a[56:59], v[88:91]\n"
"s_sub_u32 s91, 7, s91\n"
"s_sub_u32 s56, s76, 512\n"
"v_mfma_f32_16x16x32_bf16 v[92:95], a[64:67], a[44:47], v[92:95]\n"
"ds_read_b64_tr_b16 a[94:95], v20 offset:42240\n"
"v_mfma_f32_16x16x32_bf16 v[92:95], a[68:71], a[60:63], v[92:95]\n"
"s_waitcnt lgkmcnt(8)\n"
"v_mfma_f32_16x16x32_bf16 v[96:99], a[72:75], a[32:35], v[96:99]\n"
"ds_read_b64_tr_b16 a[96:97], v19 offset:26624\n"
"v_mfma_f32_16x16x32_bf16 v[96:99], a[76:79], a[48:51], v[96:99]\n"
"buffer_load_dwordx4 v7, s[4:7], s48 offen offset:1024 lds\n"
"v_mfma_f32_16x16x32_bf16 v[100:103], a[72:75], a[36:39], v[100:103]\n"
"ds_read_b64_tr_b16 a[98:99], v19 offset:26880\n"
"v_mfma_f32_16x16x32_bf16 v[100:103], a[76:79], a[52:55], v[100:103]\n"
"s_cmp_gt_u32 s43, 0\n"
"s_cselect_b32 s98, s98, 2130706432\n"
"s_cselect_b32 s91, -1, s91\n"
"v_mfma_f32_16x16x32_bf16 v[104:107], a[72:75], a[40:43], v[104:107]\n"
"ds_read_b64_tr_b16 a[100:101], v19 offset:39936\n"
"s_add_u32 m0, s95, 0\n"
"v_mfma_f32_16x16x32_bf16 v[104:107], a[76:79], a[56:59], v[104:107]\n"
"s_cmp_gt_u32 s43, 0\n"
"s_cselect_b32 s56, s56, 2130706432\n"
"s_add_u32 s99, s98, 16384\n"
"v_mfma_f32_16x16x32_bf16 v[108:111], a[72:75], a[44:47], v[108:111]\n"
"ds_read_b64_tr_b16 a[102:103], v19 offset:40192\n"
"s_lshr_b32 s64, s46, 1\n"
"v_mfma_f32_16x16x32_bf16 v[108:111], a[76:79], a[60:63], v[108:111]\n"
"s_mul_i32 s64, s64, 114688\n"
"s_add_u32 s64, s64, s70\n"
"s_waitcnt lgkmcnt(8)\n"
"v_mfma_f32_16x16x32_bf16 v[112:115], a[32:35], a[80:83], v[112:115]\n"
"ds_read_b64_tr_b16 a[104:105], v20 offset:26624\n"
"s_lshl_b32 s65, s47, 16\n"
"v_mfma_f32_16x16x32_bf16 v[112:115], a[48:51], a[84:87], v[112:115]\n"
"buffer_load_dwordx4 v8, s[4:7], s48 offen offset:0 lds\n"
"v_mfma_f32_16x16x32_bf16 v[116:119], a[36:39], a[80:83], v[116:119]\n"
"ds_read_b64_tr_b16 a[106:107], v20 offset:26880\n"
"s_add_u32 s60, s65, s64\n"
"v_mfma_f32_16x16x32_bf16 v[116:119], a[52:55], a[84:87], v[116:119]\n"
"buffer_load_dwordx4 v9, s[4:7], s48 offen offset:1024 lds\n"
"v_mfma_f32_16x16x32_bf16 v[120:123], a[40:43], a[80:83], v[120:123]\n"
"ds_read_b64_tr_b16 a[108:109], v20 offset:39936\n"
"s_add_u32 s48, s48, 128\n"
"v_mfma_f32_16x16x32_bf16 v[120:123], a[56:59], a[84:87], v[120:123]\n"
"s_add_u32 s61, s60, 16384\n"
"s_add_u32 s62, s60, 32768\n"
"s_lshl_b32 s65, s47, 15\n"
"v_mfma_f32_16x16x32_bf16 v[124:127], a[44:47], a[80:83], v[124:127]\n"
"ds_read_b64_tr_b16 a[110:111], v20 offset:40192\n"
"s_add_u32 s60, s60, s65\n"
"v_mfma_f32_16x16x32_bf16 v[124:127], a[60:63], a[84:87], v[124:127]\n"
"s_sub_u32 s62, s62, s65\n"
"s_lshl_b32 s65, s47, 9\n"
"s_waitcnt lgkmcnt(8)\n"
"v_mfma_f32_16x16x32_bf16 v[128:131], a[32:35], a[88:91], v[128:131]\n"
"ds_read_b64_tr_b16 a[112:113], v19 offset:29696\n"
"s_add_u32 m0, s52, 56320\n"
"v_mfma_f32_16x16x32_bf16 v[128:131], a[48:51], a[92:95], v[128:131]\n"
"s_add_u32 s64, s64, s65\n"
"s_add_u32 s63, s64, 49152\n"
"s_add_u32 s73, s46, 28\n"
"v_mfma_f32_16x16x32_bf16 v[132:135], a[36:39], a[88:91], v[132:135]\n"
"ds_read_b64_tr_b16 a[114:115], v19 offset:29952\n"
"s_lshl_b32 s73, s73, 14\n"
"v_mfma_f32_16x16x32_bf16 v[132:135], a[52:55], a[92:95], v[132:135]\n"
"buffer_load_dwordx4 v13, s[12:15], s50 offen offset:0 lds\n"
"v_mfma_f32_16x16x32_bf16 v[136:139], a[40:43], a[88:91], v[136:139]\n"
"ds_read_b64_tr_b16 a[116:117], v19 offset:43008\n"
"s_add_u32 s73, s73, s70\n"
"v_mfma_f32_16x16x32_bf16 v[136:139], a[56:59], a[92:95], v[136:139]\n"
"v_mov_b32 v44, 0x3f803f80\n"
"v_accvgpr_write_b32 a184, v44\n"
"v_mfma_f32_16x16x32_bf16 v[140:143], a[44:47], a[88:91], v[140:143]\n"
"ds_read_b64_tr_b16 a[118:119], v19 offset:43264\n"
"v_accvgpr_write_b32 a185, v44\n"
"v_mfma_f32_16x16x32_bf16 v[140:143], a[60:63], a[92:95], v[140:143]\n"
"v_accvgpr_write_b32 a186, v44\n"
"s_waitcnt lgkmcnt(8)\n"
"v_mfma_f32_16x16x32_bf16 v[48:51], a[96:99], a[32:35], v[48:51]\n"
"ds_read_b64_tr_b16 a[120:121], v20 offset:29696\n"
"v_accvgpr_write_b32 a187, v44\n"
"v_mfma_f32_16x16x32_bf16 v[48:51], a[100:103], a[48:51], v[48:51]\n"
"buffer_load_dwordx4 v14, s[12:15], s50 offen offset:1024 lds\n"
"v_mfma_f32_16x16x32_bf16 v[52:55], a[96:99], a[36:39], v[52:55]\n"
"ds_read_b64_tr_b16 a[122:123], v20 offset:29952\n"
"v_mfma_f32_16x16x32_bf16 v[52:55], a[100:103], a[52:55], v[52:55]\n"
"v_mfma_f32_16x16x32_bf16 v[56:59], a[96:99], a[40:43], v[56:59]\n"
"ds_read_b64_tr_b16 a[124:125], v20 offset:43008\n"
"v_mfma_f32_16x16x32_bf16 v[56:59], a[100:103], a[56:59], v[56:59]\n"
"buffer_load_dwordx4 v15, s[12:15], s50 offen offset:2048 lds\n"
"v_mfma_f32_16x16x32_bf16 v[60:63], a[96:99], a[44:47], v[60:63]\n"
"ds_read_b64_tr_b16 a[126:127], v20 offset:43264\n"
"v_mfma_f32_16x16x32_bf16 v[60:63], a[100:103], a[60:63], v[60:63]\n"
"s_waitcnt lgkmcnt(8)\n"
"v_mfma_f32_16x16x32_bf16 v[64:67], a[104:107], a[32:35], v[64:67]\n"
"ds_read_b64_tr_b16 a[128:129], v19 offset:30720\n"
"v_mfma_f32_16x16x32_bf16 v[64:67], a[108:111], a[48:51], v[64:67]\n"
"v_mfma_f32_16x16x32_bf16 v[68:71], a[104:107], a[36:39], v[68:71]\n"
"ds_read_b64_tr_b16 a[130:131], v19 offset:30976\n"
"v_mfma_f32_16x16x32_bf16 v[68:71], a[108:111], a[52:55], v[68:71]\n"
"s_mov_b32 exec_hi, 0\n"
"buffer_load_dwordx4 v16, s[12:15], s50 offen offset:3072 lds\n"
"s_mov_b32 exec_hi, -1\n"
"v_mfma_f32_16x16x32_bf16 v[72:75], a[104:107], a[40:43], v[72:75]\n"
"ds_read_b64_tr_b16 a[132:133], v19 offset:44032\n"
"s_add_u32 s50, s50, 458752\n"
"v_mfma_f32_16x16x32_bf16 v[72:75], a[108:111], a[56:59], v[72:75]\n"
"v_mfma_f32_16x16x32_bf16 v[76:79], a[104:107], a[44:47], v[76:79]\n"
"ds_read_b64_tr_b16 a[134:135], v19 offset:44288\n"
"v_mfma_f32_16x16x32_bf16 v[76:79], a[108:111], a[60:63], v[76:79]\n"
"s_waitcnt lgkmcnt(8)\n"
"v_mfma_f32_16x16x32_bf16 v[144:147], a[112:115], a[32:35], v[144:147]\n"
"ds_read_b64_tr_b16 a[136:137], v20 offset:30720\n"
"s_add_u32 m0, s92, 32768\n"
"v_mfma_f32_16x16x32_bf16 v[144:147], a[116:119], a[48:51], v[144:147]\n"
"v_mfma_f32_16x16x32_bf16 v[148:151], a[112:115], a[36:39], v[148:151]\n"
"ds_read_b64_tr_b16 a[138:139], v20 offset:30976\n"
"v_mfma_f32_16x16x32_bf16 v[148:151], a[116:119], a[52:55], v[148:151]\n"
"buffer_load_dwordx4 v2, s[4:7], s51 offen offset:0 lds\n"
"v_mfma_f32_16x16x32_bf16 v[152:155], a[112:115], a[40:43], v[152:155]\n"
"ds_read_b64_tr_b16 a[140:141], v20 offset:44032\n"
"v_mfma_f32_16x16x32_bf16 v[152:155], a[116:119], a[56:59], v[152:155]\n"
"buffer_load_dwordx4 v3, s[4:7], s51 offen offset:1024 lds\n"
"v_mfma_f32_16x16x32_bf16 v[156:159], a[112:115], a[44:47], v[156:159]\n"
"ds_read_b64_tr_b16 a[142:143], v20 offset:44288\n"
"s_add_u32 s51, s51, 128\n"
"v_mfma_f32_16x16x32_bf16 v[156:159], a[116:119], a[60:63], v[156:159]\n"
"s_waitcnt lgkmcnt(8)\n"
"v_mfma_f32_16x16x32_bf16 v[160:163], a[120:123], a[32:35], v[160:163]\n"
"ds_read_b64_tr_b16 a[144:145], v19 offset:31744\n"
"v_mfma_f32_16x16x32_bf16 v[160:163], a[124:127], a[48:51], v[160:163]\n"
"s_mov_b64 exec, s[82:83]\n"
"buffer_load_dword a252, v25, s[20:23], s58 offen\n"
"s_mov_b64 exec, -1\n"
"v_mfma_f32_16x16x32_bf16 v[164:167], a[120:123], a[36:39], v[164:167]\n"
"ds_read_b64_tr_b16 a[146:147], v19 offset:32000\n"
"s_add_u32 s58, s58, 262144\n"
"v_mfma_f32_16x16x32_bf16 v[164:167], a[124:127], a[52:55], v[164:167]\n"
"v_mfma_f32_16x16x32_bf16 v[168:171], a[120:123], a[40:43], v[168:171]\n"
"ds_read_b64_tr_b16 a[148:149], v19 offset:45056\n"
"v_mfma_f32_16x16x32_bf16 v[168:171], a[124:127], a[56:59], v[168:171]\n"
"s_mov_b64 exec, s[82:83]\n"
"buffer_load_dword a252, v25, s[20:23], s58 offen\n"
"s_mov_b64 exec, -1\n"
"v_mfma_f32_16x16x32_bf16 v[172:175], a[120:123], a[44:47], v[172:175]\n"
"ds_read_b64_tr_b16 a[150:151], v19 offset:45312\n"
"s_add_u32 s58, s58, 262144\n"
"v_mfma_f32_16x16x32_bf16 v[172:175], a[124:127], a[60:63], v[172:175]\n"
"s_waitcnt lgkmcnt(8)\n"
"v_mfma_f32_16x16x32_bf16 v[176:179], a[128:131], a[32:35], v[176:179]\n"
"ds_read_b64_tr_b16 a[152:153], v20 offset:31744\n"
"v_mfma_f32_16x16x32_bf16 v[176:179], a[132:135], a[48:51], v[176:179]\n"
"v_mfma_f32_16x16x32_bf16 v[180:183], a[128:131], a[36:39], v[180:183]\n"
"ds_read_b64_tr_b16 a[154:155], v20 offset:32000\n"
"v_mfma_f32_16x16x32_bf16 v[180:183], a[132:135], a[52:55], v[180:183]\n"
"s_waitcnt vmcnt(8)\n"
"v_mfma_f32_16x16x32_bf16 v[184:187], a[128:131], a[40:43], v[184:187]\n"
"ds_read_b64_tr_b16 a[156:157], v20 offset:45056\n"
"s_add_u32 m0, s52, 0\n"
"v_mfma_f32_16x16x32_bf16 v[184:187], a[132:135], a[56:59], v[184:187]\n"
"ds_read_b128 a[0:3], v17 offset:0\n"
"v_mfma_f32_16x16x32_bf16 v[188:191], a[128:131], a[44:47], v[188:191]\n"
"ds_read_b64_tr_b16 a[158:159], v20 offset:45312\n"
"v_mfma_f32_16x16x32_bf16 v[188:191], a[132:135], a[60:63], v[188:191]\n"
"ds_read_b128 a[16:19], v18 offset:0\n"
"s_waitcnt lgkmcnt(10)\n"
"v_mfma_f32_16x16x32_bf16 v[192:195], a[136:139], a[32:35], v[192:195]\n"
"ds_read_b64_tr_b16 a[160:161], v21 offset:32768\n"
"v_mfma_f32_16x16x32_bf16 v[192:195], a[140:143], a[48:51], v[192:195]\n"
"buffer_load_dwordx4 v10, s[8:11], s49 offen offset:0 lds\n"
"v_mfma_f32_16x16x32_bf16 v[196:199], a[136:139], a[36:39], v[196:199]\n"
"ds_read_b64_tr_b16 a[162:163], v21 offset:33024\n"
"v_mfma_f32_16x16x32_bf16 v[196:199], a[140:143], a[52:55], v[196:199]\n"
"ds_read_b128 a[4:7], v17 offset:2048\n"
"v_mfma_f32_16x16x32_bf16 v[200:203], a[136:139], a[40:43], v[200:203]\n"
"ds_read_b64_tr_b16 a[164:165], v21 offset:46080\n"
"v_mfma_f32_16x16x32_bf16 v[200:203], a[140:143], a[56:59], v[200:203]\n"
"buffer_load_dwordx4 v11, s[8:11], s49 offen offset:1024 lds\n"
"v_mfma_f32_16x16x32_bf16 v[204:207], a[136:139], a[44:47], v[204:207]\n"
"ds_read_b64_tr_b16 a[166:167], v21 offset:46336\n"
"v_mfma_f32_16x16x32_bf16 v[204:207], a[140:143], a[60:63], v[204:207]\n"
"ds_read_b128 a[20:23], v18 offset:2048\n"
"s_waitcnt lgkmcnt(12)\n"
"v_mfma_f32_16x16x32_bf16 v[208:211], a[144:147], a[32:35], v[208:211]\n"
"ds_read_b128 a[8:11], v17 offset:4096\n"
"v_mfma_f32_16x16x32_bf16 v[208:211], a[148:151], a[48:51], v[208:211]\n"
"ds_read_b128 a[24:27], v18 offset:4096\n"
"v_mfma_f32_16x16x32_bf16 v[212:215], a[144:147], a[36:39], v[212:215]\n"
"ds_read_b128 a[12:15], v17 offset:6144\n"
"v_mfma_f32_16x16x32_bf16 v[212:215], a[148:151], a[52:55], v[212:215]\n"
"buffer_load_dwordx4 v12, s[8:11], s49 offen offset:2048 lds\n"
"v_mfma_f32_16x16x32_bf16 v[216:219], a[144:147], a[40:43], v[216:219]\n"
"s_add_u32 s49, s49, 393216\n"
"ds_read_b128 a[28:31], v18 offset:6144\n"
"v_mfma_f32_16x16x32_bf16 v[216:219], a[148:151], a[56:59], v[216:219]\n"
"v_mfma_f32_16x16x32_bf16 v[220:223], a[144:147], a[44:47], v[220:223]\n"
"v_mfma_f32_16x16x32_bf16 v[220:223], a[148:151], a[60:63], v[220:223]\n"
"s_waitcnt vmcnt(7) lgkmcnt(5)\n"
"s_barrier\n"
"v_mfma_f32_16x16x32_bf16 v[224:227], a[152:155], a[32:35], v[224:227]\n"
"ds_read_b64_tr_b16 a[64:65], v22 offset:1024\n"
"v_mfma_f32_16x16x32_bf16 v[224:227], a[156:159], a[48:51], v[224:227]\n"
"ds_read_b64_tr_b16 a[66:67], v22 offset:1280\n"
"v_mfma_f32_16x16x32_bf16 v[228:231], a[152:155], a[36:39], v[228:231]\n"
"ds_read_b64_tr_b16 a[68:69], v22 offset:14336\n"
"s_add_u32 m0, s93, 32768\n"
"v_mfma_f32_16x16x32_bf16 v[228:231], a[156:159], a[52:55], v[228:231]\n"
"ds_read_b64_tr_b16 a[70:71], v22 offset:14592\n"
"v_mfma_f32_16x16x32_bf16 v[232:235], a[152:155], a[40:43], v[232:235]\n"
"ds_read_b64_tr_b16 a[72:73], v23 offset:1024\n"
"v_mfma_f32_16x16x32_bf16 v[232:235], a[156:159], a[56:59], v[232:235]\n"
"ds_read_b64_tr_b16 a[74:75], v23 offset:1280\n"
"v_mfma_f32_16x16x32_bf16 v[236:239], a[152:155], a[44:47], v[236:239]\n"
"ds_read_b64_tr_b16 a[76:77], v23 offset:14336\n"
"v_mfma_f32_16x16x32_bf16 v[236:239], a[156:159], a[60:63], v[236:239]\n"
"ds_read_b64_tr_b16 a[78:79], v23 offset:14592\n"
"v_mfma_f32_16x16x32_bf16 v[240:243], a[160:163], a[32:35], v[240:243]\n"
"ds_read_b64_tr_b16 a[80:81], v22 offset:2048\n"
"v_mfma_f32_16x16x32_bf16 v[240:243], a[164:167], a[48:51], v[240:243]\n"
"buffer_load_dwordx4 v4, s[4:7], s48 offen offset:0 lds\n"
"v_mfma_f32_16x16x32_bf16 v[244:247], a[160:163], a[36:39], v[244:247]\n"
"ds_read_b64_tr_b16 a[82:83], v22 offset:2304\n"
"v_mfma_f32_16x16x32_bf16 v[244:247], a[164:167], a[52:55], v[244:247]\n"
"buffer_load_dwordx4 v5, s[4:7], s48 offen offset:1024 lds\n"
"v_mfma_f32_16x16x32_bf16 v[248:251], a[160:163], a[40:43], v[248:251]\n"
"ds_read_b64_tr_b16 a[84:85], v22 offset:15360\n"
"v_mfma_f32_16x16x32_bf16 v[248:251], a[164:167], a[56:59], v[248:251]\n"
"v_mfma_f32_16x16x32_bf16 v[252:255], a[160:163], a[44:47], v[252:255]\n"
"ds_read_b64_tr_b16 a[86:87], v22 offset:15616\n"
"v_mfma_f32_16x16x32_bf16 v[252:255], a[164:167], a[60:63], v[252:255]\n"
"s_add_u32 m0, s94, 32768\n"
"s_waitcnt lgkmcnt(8)\n"
"v_mfma_f32_16x16x32_bf16 v[80:83], a[64:67], a[0:3], v[80:83]\n"
"ds_read_b64_tr_b16 a[88:89], v23 offset:2048\n"
"v_mfma_f32_16x16x32_bf16 v[80:83], a[68:71], a[16:19], v[80:83]\n"
"buffer_load_dwordx4 v6, s[4:7], s48 offen offset:0 lds\n"
"v_mfma_f32_16x16x32_bf16 v[84:87], a[64:67], a[4:7], v[84:87]\n"
"ds_read_b64_tr_b16 a[90:91], v23 offset:2304\n"
"v_mfma_f32_16x16x32_bf16 v[84:87], a[68:71], a[20:23], v[84:87]\n"
"v_mfma_f32_16x16x32_bf16 v[88:91], a[64:67], a[8:11], v[88:91]\n"
"ds_read_b64_tr_b16 a[92:93], v23 offset:15360\n"
"v_mfma_f32_16x16x32_bf16 v[88:91], a[68:71], a[24:27], v[88:91]\n"
".Lpad4:\n"
"s_cbranch_execz .Lpad8\n"
"v_mfma_f32_16x16x32_bf16 v[92:95], a[64:67], a[12:15], v[92:95]\n"
"ds_read_b64_tr_b16 a[94:95], v23 offset:15616\n"
"v_mfma_f32_16x16x32_bf16 v[92:95], a[68:71], a[28:31], v[92:95]\n"
"s_waitcnt lgkmcnt(8)\n"
"v_mfma_f32_16x16x32_bf16 v[96:99], a[72:75], a[0:3], v[96:99]\n"
"ds_read_b64_tr_b16 a[96:97], v22 offset:0\n"
"v_mfma_f32_16x16x32_bf16 v[96:99], a[76:79], a[16:19], v[96:99]\n"
"buffer_load_dwordx4 v7, s[4:7], s48 offen offset:1024 lds\n"
"v_mfma_f32_16x16x32_bf16 v[100:103], a[72:75], a[4:7], v[100:103]\n"
"ds_read_b64_tr_b16 a[98:99], v22 offset:256\n"
"v_mfma_f32_16x16x32_bf16 v[100:103], a[76:79], a[20:23], v[100:103]\n"
"v_mfma_f32_16x16x32_bf16 v[104:107], a[72:75], a[8:11], v[104:107]\n"
"ds_read_b64_tr_b16 a[100:101], v22 offset:13312\n"
"s_add_u32 m0, s95, 32768\n"
"v_mfma_f32_16x16x32_bf16 v[104:107], a[76:79], a[24:27], v[104:107]\n"
"v_mfma_f32_16x16x32_bf16 v[108:111], a[72:75], a[12:15], v[108:111]\n"
"ds_read_b64_tr_b16 a[102:103], v22 offset:13568\n"
"v_mfma_f32_16x16x32_bf16 v[108:111], a[76:79], a[28:31], v[108:111]\n"
"s_waitcnt lgkmcnt(8)\n"
"v_mfma_f32_16x16x32_bf16 v[112:115], a[0:3], a[80:83], v[112:115]\n"
"ds_read_b64_tr_b16 a[104:105], v23 offset:0\n"
"v_mfma_f32_16x16x32_bf16 v[112:115], a[16:19], a[84:87], v[112:115]\n"
"buffer_load_dwordx4 v8, s[4:7], s48 offen offset:0 lds\n"
"v_mfma_f32_16x16x32_bf16 v[116:119], a[4:7], a[80:83], v[116:119]\n"
"ds_read_b64_tr_b16 a[106:107], v23 offset:256\n"
"v_mfma_f32_16x16x32_bf16 v[116:119], a[20:23], a[84:87], v[116:119]\n"
"buffer_load_dwordx4 v9, s[4:7], s48 offen offset:1024 lds\n"
"v_mfma_f32_16x16x32_bf16 v[120:123], a[8:11], a[80:83], v[120:123]\n"
"ds_read_b64_tr_b16 a[108:109], v23 offset:13312\n"
"s_add_u32 s48, s48, 128\n"
"v_mfma_f32_16x16x32_bf16 v[120:123], a[24:27], a[84:87], v[120:123]\n"
"v_mfma_f32_16x16x32_bf16 v[124:127], a[12:15], a[80:83], v[124:127]\n"
"ds_read_b64_tr_b16 a[110:111], v23 offset:13568\n"
"v_mfma_f32_16x16x32_bf16 v[124:127], a[28:31], a[84:87], v[124:127]\n"
"s_waitcnt lgkmcnt(8)\n"
"v_mfma_f32_16x16x32_bf16 v[128:131], a[0:3], a[88:91], v[128:131]\n"
"ds_read_b64_tr_b16 a[112:113], v22 offset:3072\n"
"s_add_u32 m0, s52, 3072\n"
"v_mfma_f32_16x16x32_bf16 v[128:131], a[16:19], a[92:95], v[128:131]\n"
"v_mfma_f32_16x16x32_bf16 v[132:135], a[4:7], a[88:91], v[132:135]\n"
"ds_read_b64_tr_b16 a[114:115], v22 offset:3328\n"
"v_mfma_f32_16x16x32_bf16 v[132:135], a[20:23], a[92:95], v[132:135]\n"
"buffer_load_dwordx4 v13, s[12:15], s50 offen offset:0 lds\n"
"v_mfma_f32_16x16x32_bf16 v[136:139], a[8:11], a[88:91], v[136:139]\n"
"ds_read_b64_tr_b16 a[116:117], v22 offset:16384\n"
"v_mfma_f32_16x16x32_bf16 v[136:139], a[24:27], a[92:95], v[136:139]\n"
"v_mfma_f32_16x16x32_bf16 v[140:143], a[12:15], a[88:91], v[140:143]\n"
"ds_read_b64_tr_b16 a[118:119], v22 offset:16640\n"
"v_mfma_f32_16x16x32_bf16 v[140:143], a[28:31], a[92:95], v[140:143]\n"
"s_waitcnt lgkmcnt(8)\n"
"v_mfma_f32_16x16x32_bf16 v[48:51], a[96:99], a[0:3], v[48:51]\n"
"ds_read_b64_tr_b16 a[120:121], v23 offset:3072\n"
"v_mfma_f32_16x16x32_bf16 v[48:51], a[100:103], a[16:19], v[48:51]\n"
"buffer_load_dwordx4 v14, s[12:15], s50 offen offset:1024 lds\n"
"v_mfma_f32_16x16x32_bf16 v[52:55], a[96:99], a[4:7], v[52:55]\n"
"ds_read_b64_tr_b16 a[122:123], v23 offset:3328\n"
"v_mfma_f32_16x16x32_bf16 v[52:55], a[100:103], a[20:23], v[52:55]\n"
"v_mfma_f32_16x16x32_bf16 v[56:59], a[96:99], a[8:11], v[56:59]\n"
"ds_read_b64_tr_b16 a[124:125], v23 offset:16384\n"
"v_mfma_f32_16x16x32_bf16 v[56:59], a[100:103], a[24:27], v[56:59]\n"
"buffer_load_dwordx4 v15, s[12:15], s50 offen offset:2048 lds\n"
"v_mfma_f32_16x16x32_bf16 v[60:63], a[96:99], a[12:15], v[60:63]\n"
"ds_read_b64_tr_b16 a[126:127], v23 offset:16640\n"
"v_mfma_f32_16x16x32_bf16 v[60:63], a[100:103], a[28:31], v[60:63]\n"
"s_waitcnt lgkmcnt(8)\n"
"v_mfma_f32_16x16x32_bf16 v[64:67], a[104:107], a[0:3], v[64:67]\n"
"ds_read_b64_tr_b16 a[128:129], v22 offset:4096\n"
"v_accvgpr_read_b32 v41, a242\n"
"v_mfma_f32_16x16x32_bf16 v[64:67], a[108:111], a[16:19], v[64:67]\n"
"v_accvgpr_read_b32 v42, a243\n"
"v_accvgpr_read_b32 v43, a244\n"
"v_mfma_f32_16x16x32_bf16 v[68:71], a[104:107], a[4:7], v[68:71]\n"
"ds_read_b64_tr_b16 a[130:131], v22 offset:4352\n"
"v_mfma_f32_16x16x32_bf16 v[68:71], a[108:111], a[20:23], v[68:71]\n"
"s_mov_b32 exec_hi, 0\n"
"buffer_load_dwordx4 v16, s[12:15], s50 offen offset:3072 lds\n"
"s_mov_b32 exec_hi, -1\n"
"v_mfma_f32_16x16x32_bf16 v[72:75], a[104:107], a[8:11], v[72:75]\n"
"ds_read_b64_tr_b16 a[132:133], v22 offset:17408\n"
"s_add_u32 s50, s50, 458752\n"
"v_mfma_f32_16x16x32_bf16 v[72:75], a[108:111], a[24:27], v[72:75]\n"
"buffer_load_dwordx2 v[38:39], v41, s[16:19], 0 offen sc1\n"
"v_mfma_f32_16x16x32_bf16 v[76:79], a[104:107], a[12:15], v[76:79]\n"
"ds_read_b64_tr_b16 a[134:135], v22 offset:17664\n"
"s_mov_b32 s53, 0\n"
"v_mfma_f32_16x16x32_bf16 v[76:79], a[108:111], a[28:31], v[76:79]\n"
"s_waitcnt lgkmcnt(8)\n"
"v_mfma_f32_16x16x32_bf16 v[144:147], a[112:115], a[0:3], v[144:147]\n"
"ds_read_b64_tr_b16 a[136:137], v23 offset:4096\n"
"s_add_u32 m0, s92, 0\n"
"v_mfma_f32_16x16x32_bf16 v[144:147], a[116:119], a[16:19], v[144:147]\n"
"buffer_load_dword v30, v42, s[16:19], s53 offen sc1\n"
"v_mfma_f32_16x16x32_bf16 v[148:151], a[112:115], a[4:7], v[148:151]\n"
"ds_read_b64_tr_b16 a[138:139], v23 offset:4352\n"
"v_mfma_f32_16x16x32_bf16 v[148:151], a[116:119], a[20:23], v[148:151]\n"
"buffer_load_dwordx4 v2, s[4:7], s51 offen offset:0 lds\n"
"v_mfma_f32_16x16x32_bf16 v[152:155], a[112:115], a[8:11], v[152:155]\n"
"ds_read_b64_tr_b16 a[140:141], v23 offset:17408\n"
"v_mfma_f32_16x16x32_bf16 v[152:155], a[116:119], a[24:27], v[152:155]\n"
"buffer_load_dwordx4 v3, s[4:7], s51 offen offset:1024 lds\n"
"v_mfma_f32_16x16x32_bf16 v[156:159], a[112:115], a[12:15], v[156:159]\n"
"ds_read_b64_tr_b16 a[142:143], v23 offset:17664\n"
"s_add_u32 s51, s51, 128\n"
"v_mfma_f32_16x16x32_bf16 v[156:159], a[116:119], a[28:31], v[156:159]\n"
"s_waitcnt lgkmcnt(8)\n"
"v_mfma_f32_16x16x32_bf16 v[160:163], a[120:123], a[0:3], v[160:163]\n"
"ds_read_b64_tr_b16 a[144:145], v22 offset:5120\n"
"v_mfma_f32_16x16x32_bf16 v[160:163], a[124:127], a[16:19], v[160:163]\n"
"s_mov_b64 exec, s[82:83]\n"
"buffer_load_dword a252, v25, s[20:23], s58 offen\n"
"s_mov_b64 exec, -1\n"
"v_mfma_f32_16x16x32_bf16 v[164:167], a[120:123], a[4:7], v[164:167]\n"
"ds_read_b64_tr_b16 a[146:147], v22 offset:5376\n"
"s_mov_b32 s58, s59\n"
"v_mfma_f32_16x16x32_bf16 v[164:167], a[124:127], a[20:23], v[164:167]\n"
"buffer_load_dword v31, v42, s[16:19], s53 offen offset:3328 sc1\n"
"v_mfma_f32_16x16x32_bf16 v[168:171], a[120:123], a[8:11], v[168:171]\n"
"ds_read_b64_tr_b16 a[148:149], v22 offset:18432\n"
"s_add_u32 s53, s53, 6656\n"
"v_mfma_f32_16x16x32_bf16 v[168:171], a[124:127], a[24:27], v[168:171]\n"
"buffer_load_dword v32, v42, s[16:19], s53 offen sc1\n"
"v_mfma_f32_16x16x32_bf16 v[172:175], a[120:123], a[12:15], v[172:175]\n"
"ds_read_b64_tr_b16 a[150:151], v22 offset:18688\n"
"v_mfma_f32_16x16x32_bf16 v[172:175], a[124:127], a[28:31], v[172:175]\n"
"s_waitcnt lgkmcnt(8)\n"
"v_mfma_f32_16x16x32_bf16 v[176:179], a[128:131], a[0:3], v[176:179]\n"
"ds_read_b64_tr_b16 a[152:153], v23 offset:5120\n"
"v_mfma_f32_16x16x32_bf16 v[176:179], a[132:135], a[16:19], v[176:179]\n"
"buffer_load_dword v33, v42, s[16:19], s53 offen offset:3328 sc1\n"
"v_mfma_f32_16x16x32_bf16 v[180:183], a[128:131], a[4:7], v[180:183]\n"
"ds_read_b64_tr_b16 a[154:155], v23 offset:5376\n"
"s_add_u32 s53, s53, 6656\n"
"v_mfma_f32_16x16x32_bf16 v[180:183], a[132:135], a[20:23], v[180:183]\n"
"s_waitcnt vmcnt(12)\n"
"v_mfma_f32_16x16x32_bf16 v[184:187], a[128:131], a[8:11], v[184:187]\n"
"ds_read_b64_tr_b16 a[156:157], v23 offset:18432\n"
"s_add_u32 m0, s52, 26624\n"
"v_mfma_f32_16x16x32_bf16 v[184:187], a[132:135], a[24:27], v[184:187]\n"
"ds_read_b128 a[32:35], v17 offset:32768\n"
"v_mfma_f32_16x16x32_bf16 v[188:191], a[128:131], a[12:15], v[188:191]\n"
"ds_read_b64_tr_b16 a[158:159], v23 offset:18688\n"
"v_mfma_f32_16x16x32_bf16 v[188:191], a[132:135], a[28:31], v[188:191]\n"
"ds_read_b128 a[48:51], v18 offset:32768\n"
"s_waitcnt lgkmcnt(10)\n"
"v_mfma_f32_16x16x32_bf16 v[192:195], a[136:139], a[0:3], v[192:195]\n"
"ds_read_b64_tr_b16 a[160:161], v24 offset:6144\n"
"v_mfma_f32_16x16x32_bf16 v[192:195], a[140:143], a[16:19], v[192:195]\n"
"buffer_load_dwordx4 v10, s[8:11], s49 offen offset:0 lds\n"
"v_mfma_f32_16x16x32_bf16 v[196:199], a[136:139], a[4:7], v[196:199]\n"
"ds_read_b64_tr_b16 a[162:163], v24 offset:6400\n"
"v_mfma_f32_16x16x32_bf16 v[196:199], a[140:143], a[20:23], v[196:199]\n"
"ds_read_b128 a[36:39], v17 offset:34816\n"
"v_mfma_f32_16x16x32_bf16 v[200:203], a[136:139], a[8:11], v[200:203]\n"
"ds_read_b64_tr_b16 a[164:165], v24 offset:19456\n"
"v_mfma_f32_16x16x32_bf16 v[200:203], a[140:143], a[24:27], v[200:203]\n"
"buffer_load_dwordx4 v11, s[8:11], s49 offen offset:1024 lds\n"
"v_mfma_f32_16x16x32_bf16 v[204:207], a[136:139], a[12:15], v[204:207]\n"
"ds_read_b64_tr_b16 a[166:167], v24 offset:19712\n"
"v_mfma_f32_16x16x32_bf16 v[204:207], a[140:143], a[28:31], v[204:207]\n"
"ds_read_b128 a[52:55], v18 offset:34816\n"
"s_waitcnt lgkmcnt(12)\n"
"v_mfma_f32_16x16x32_bf16 v[208:211], a[144:147], a[0:3], v[208:211]\n"
"ds_read_b128 a[40:43], v17 offset:36864\n"
"v_mfma_f32_16x16x32_bf16 v[208:211], a[148:151], a[16:19], v[208:211]\n"
"ds_read_b128 a[56:59], v18 offset:36864\n"
"v_mfma_f32_16x16x32_bf16 v[212:215], a[144:147], a[4:7], v[212:215]\n"
"ds_read_b128 a[44:47], v17 offset:38912\n"
"v_mfma_f32_16x16x32_bf16 v[212:215], a[148:151], a[20:23], v[212:215]\n"
"buffer_load_dwordx4 v12, s[8:11], s49 offen offset:2048 lds\n"
"v_mfma_f32_16x16x32_bf16 v[216:219], a[144:147], a[8:11], v[216:219]\n"
"s_add_u32 s49, s49, 393216\n"
"ds_read_b128 a[60:63], v18 offset:38912\n"
"v_mfma_f32_16x16x32_bf16 v[216:219], a[148:151], a[24:27], v[216:219]\n"
"buffer_load_dword v34, v42, s[16:19], s53 offen sc1\n"
"v_mfma_f32_16x16x32_bf16 v[220:223], a[144:147], a[12:15], v[220:223]\n"
"buffer_load_dword v35, v42, s[16:19], s53 offen offset:3328 sc1\n"
"v_mfma_f32_16x16x32_bf16 v[220:223], a[148:151], a[28:31], v[220:223]\n"
"s_add_u32 s53, s53, 6656\n"
"s_waitcnt vmcnt(13) lgkmcnt(5)\n"
"s_barrier\n"
"v_mfma_f32_16x16x32_bf16 v[224:227], a[152:155], a[0:3], v[224:227]\n"
"ds_read_b64_tr_b16 a[64:65], v19 offset:1024\n"
"v_mfma_f32_16x16x32_bf16 v[224:227], a[156:159], a[16:19], v[224:227]\n"
"ds_read_b64_tr_b16 a[66:67], v19 offset:1280\n"
"v_mfma_f32_16x16x32_bf16 v[228:231], a[152:155], a[4:7], v[228:231]\n"
"ds_read_b64_tr_b16 a[68:69], v19 offset:14336\n"
"s_add_u32 m0, s93, 0\n"
"v_mfma_f32_16x16x32_bf16 v[228:231], a[156:159], a[20:23], v[228:231]\n"
"ds_read_b64_tr_b16 a[70:71], v19 offset:14592\n"
"v_mfma_f32_16x16x32_bf16 v[232:235], a[152:155], a[8:11], v[232:235]\n"
"ds_read_b64_tr_b16 a[72:73], v20 offset:1024\n"
"v_mfma_f32_16x16x32_bf16 v[232:235], a[156:159], a[24:27], v[232:235]\n"
"ds_read_b64_tr_b16 a[74:75], v20 offset:1280\n"
"v_mfma_f32_16x16x32_bf16 v[236:239], a[152:155], a[12:15], v[236:239]\n"
"ds_read_b64_tr_b16 a[76:77], v20 offset:14336\n"
"v_mfma_f32_16x16x32_bf16 v[236:239], a[156:159], a[28:31], v[236:239]\n"
"ds_read_b64_tr_b16 a[78:79], v20 offset:14592\n"
"v_mfma_f32_16x16x32_bf16 v[240:243], a[160:163], a[0:3], v[240:243]\n"
"ds_read_b64_tr_b16 a[80:81], v19 offset:2048\n"
"v_mfma_f32_16x16x32_bf16 v[240:243], a[164:167], a[16:19], v[240:243]\n"
"buffer_load_dwordx4 v4, s[4:7], s48 offen offset:0 lds\n"
"v_mfma_f32_16x16x32_bf16 v[244:247], a[160:163], a[4:7], v[244:247]\n"
"ds_read_b64_tr_b16 a[82:83], v19 offset:2304\n"
"v_mfma_f32_16x16x32_bf16 v[244:247], a[164:167], a[20:23], v[244:247]\n"
"buffer_load_dwordx4 v5, s[4:7], s48 offen offset:1024 lds\n"
"v_mfma_f32_16x16x32_bf16 v[248:251], a[160:163], a[8:11], v[248:251]\n"
"ds_read_b64_tr_b16 a[84:85], v19 offset:15360\n"
"v_mfma_f32_16x16x32_bf16 v[248:251], a[164:167], a[24:27], v[248:251]\n"
"buffer_load_dword v36, v42, s[16:19], s53 offen sc1\n"
"v_mfma_f32_16x16x32_bf16 v[252:255], a[160:163], a[12:15], v[252:255]\n"
"ds_read_b64_tr_b16 a[86:87], v19 offset:15616\n"
"v_mfma_f32_16x16x32_bf16 v[252:255], a[164:167], a[28:31], v[252:255]\n"
"s_add_u32 m0, s94, 0\n"
"s_waitcnt lgkmcnt(8)\n"
"v_mfma_f32_16x16x32_bf16 v[80:83], a[64:67], a[32:35], v[80:83]\n"
"ds_read_b64_tr_b16 a[88:89], v20 offset:2048\n"
"v_mfma_f32_16x16x32_bf16 v[80:83], a[68:71], a[48:51], v[80:83]\n"
"buffer_load_dwordx4 v6, s[4:7], s48 offen offset:0 lds\n"
"v_mfma_f32_16x16x32_bf16 v[84:87], a[64:67], a[36:39], v[84:87]\n"
"ds_read_b64_tr_b16 a[90:91], v20 offset:2304\n"
"v_mfma_f32_16x16x32_bf16 v[84:87], a[68:71], a[52:55], v[84:87]\n"
"buffer_load_dword v37, v42, s[16:19], s53 offen offset:3328 sc1\n"
"v_mfma_f32_16x16x32_bf16 v[88:91], a[64:67], a[40:43], v[88:91]\n"
"ds_read_b64_tr_b16 a[92:93], v20 offset:15360\n"
"v_mfma_f32_16x16x32_bf16 v[88:91], a[68:71], a[56:59], v[88:91]\n"
"buffer_load_ushort v40, v43, s[28:31], 0 offen\n"
"v_bfe_u32 v26, v0, 1, 2\n"
"v_mfma_f32_16x16x32_bf16 v[92:95], a[64:67], a[44:47], v[92:95]\n"
"ds_read_b64_tr_b16 a[94:95], v20 offset:15616\n"
"v_lshrrev_b32 v27, 4, v0\n"
"v_mfma_f32_16x16x32_bf16 v[92:95], a[68:71], a[60:63], v[92:95]\n"
"v_xor_b32 v26, v26, v27\n"
"s_waitcnt lgkmcnt(8)\n"
"v_mfma_f32_16x16x32_bf16 v[96:99], a[72:75], a[32:35], v[96:99]\n"
"ds_read_b64_tr_b16 a[96:97], v19 offset:0\n"
"v_lshlrev_b32 v26, 5, v26\n"
"v_mfma_f32_16x16x32_bf16 v[96:99], a[76:79], a[48:51], v[96:99]\n"
"buffer_load_dwordx4 v7, s[4:7], s48 offen offset:1024 lds\n"
"v_lshrrev_b32 v27, 3, v0\n"
"v_mfma_f32_16x16x32_bf16 v[100:103], a[72:75], a[36:39], v[100:103]\n"
"ds_read_b64_tr_b16 a[98:99], v19 offset:256\n"
"v_lshl_add_u32 v26, v27, 11, v26\n"
"v_mfma_f32_16x16x32_bf16 v[100:103], a[76:79], a[52:55], v[100:103]\n"
"v_and_b32 v27, 16, v1\n"
"v_add_u32 v26, v26, v27\n"
"v_mfma_f32_16x16x32_bf16 v[104:107], a[72:75], a[40:43], v[104:107]\n"
"ds_read_b64_tr_b16 a[100:101], v19 offset:13312\n"
"s_add_u32 m0, s95, 0\n"
"v_mfma_f32_16x16x32_bf16 v[104:107], a[76:79], a[56:59], v[104:107]\n"
"s_mov_b32 s55, 0\n"
"s_lshr_b32 s88, s40, 1\n"
"s_xor_b32 s30, s88, s55\n"
"v_mfma_f32_16x16x32_bf16 v[108:111], a[72:75], a[44:47], v[108:111]\n"
"ds_read_b64_tr_b16 a[102:103], v19 offset:13568\n"
"s_lshl_b32 s88, s30, 16\n"
"v_mfma_f32_16x16x32_bf16 v[108:111], a[76:79], a[60:63], v[108:111]\n"
"s_and_b32 s28, s40, 1\n"
"s_lshl_b32 s28, s28, 14\n"
"s_waitcnt lgkmcnt(8)\n"
"v_mfma_f32_16x16x32_bf16 v[112:115], a[32:35], a[80:83], v[112:115]\n"
"ds_read_b64_tr_b16 a[104:105], v20 offset:0\n"
"s_add_u32 s88, s88, s28\n"
"v_mfma_f32_16x16x32_bf16 v[112:115], a[48:51], a[84:87], v[112:115]\n"
"buffer_load_dwordx4 v8, s[4:7], s48 offen offset:0 lds\n"
"v_mfma_f32_16x16x32_bf16 v[116:119], a[36:39], a[80:83], v[116:119]\n"
"ds_read_b64_tr_b16 a[106:107], v20 offset:256\n"
"s_add_u32 s88, s88, s89\n"
"v_mfma_f32_16x16x32_bf16 v[116:119], a[52:55], a[84:87], v[116:119]\n"
"buffer_load_dwordx4 v9, s[4:7], s48 offen offset:1024 lds\n"
"v_mfma_f32_16x16x32_bf16 v[120:123], a[40:43], a[80:83], v[120:123]\n"
"ds_read_b64_tr_b16 a[108:109], v20 offset:13312\n"
"s_add_u32 s48, s48, 128\n"
"v_mfma_f32_16x16x32_bf16 v[120:123], a[56:59], a[84:87], v[120:123]\n"
"s_lshr_b32 s89, s45, 1\n"
"s_lshl_b32 s89, s89, 7\n"
"s_add_u32 s88, s88, s89\n"
"v_mfma_f32_16x16x32_bf16 v[124:127], a[44:47], a[80:83], v[124:127]\n"
"ds_read_b64_tr_b16 a[110:111], v20 offset:13568\n"
"v_add_u32_e64 v26, s88, v26\n"
"v_mfma_f32_16x16x32_bf16 v[124:127], a[60:63], a[84:87], v[124:127]\n"
"v_xor_b32 v28, 0x80, v26\n"
"s_waitcnt lgkmcnt(8)\n"
"v_mfma_f32_16x16x32_bf16 v[128:131], a[32:35], a[88:91], v[128:131]\n"
"ds_read_b64_tr_b16 a[112:113], v19 offset:3072\n"
"s_add_u32 m0, s52, 29696\n"
"v_mfma_f32_16x16x32_bf16 v[128:131], a[48:51], a[92:95], v[128:131]\n"
"v_add_u32 v27, 31744, v26\n"
"v_add_u32 v29, 31744, v28\n"
"v_mfma_f32_16x16x32_bf16 v[132:135], a[36:39], a[88:91], v[132:135]\n"
"ds_read_b64_tr_b16 a[114:115], v19 offset:3328\n"
"s_sub_u32 s29, s28, 16384\n"
"v_mfma_f32_16x16x32_bf16 v[132:135], a[52:55], a[92:95], v[132:135]\n"
"buffer_load_dwordx4 v13, s[12:15], s50 offen offset:0 lds\n"
"v_mfma_f32_16x16x32_bf16 v[136:139], a[40:43], a[88:91], v[136:139]\n"
"ds_read_b64_tr_b16 a[116:117], v19 offset:16384\n"
"v_mfma_f32_16x16x32_bf16 v[136:139], a[56:59], a[92:95], v[136:139]\n"
"s_cmp_eq_u32 s30, 0\n"
"s_cselect_b64 s[30:31], 0, s[28:29]\n"
"s_cselect_b64 s[28:29], s[28:29], 0\n"
"v_mfma_f32_16x16x32_bf16 v[140:143], a[44:47], a[88:91], v[140:143]\n"
"ds_read_b64_tr_b16 a[118:119], v19 offset:16640\n"
"v_accvgpr_write_b32 a245, v26\n"
"v_mfma_f32_16x16x32_bf16 v[140:143], a[60:63], a[92:95], v[140:143]\n"
"v_accvgpr_write_b32 a246, v27\n"
"s_waitcnt lgkmcnt(8)\n"
"v_mfma_f32_16x16x32_bf16 v[48:51], a[96:99], a[32:35], v[48:51]\n"
"ds_read_b64_tr_b16 a[120:121], v20 offset:3072\n"
"v_accvgpr_write_b32 a247, v28\n"
"v_mfma_f32_16x16x32_bf16 v[48:51], a[100:103], a[48:51], v[48:51]\n"
"buffer_load_dwordx4 v14, s[12:15], s50 offen offset:1024 lds\n"
"v_accvgpr_write_b32 a248, v29\n"
"v_mfma_f32_16x16x32_bf16 v[52:55], a[96:99], a[36:39], v[52:55]\n"
"ds_read_b64_tr_b16 a[122:123], v20 offset:3328\n"
"s_mul_i32 s70, s45, 1835008\n"
"v_mfma_f32_16x16x32_bf16 v[52:55], a[100:103], a[52:55], v[52:55]\n"
"s_mov_b32 s71, s70\n"
"s_lshl_b32 s57, s40, 11\n"
"s_lshl3_add_u32 s88, s45, s44\n"
"v_mfma_f32_16x16x32_bf16 v[56:59], a[96:99], a[40:43], v[56:59]\n"
"ds_read_b64_tr_b16 a[124:125], v20 offset:16384\n"
"s_mul_i32 s46, s88, 589824\n"
"v_mfma_f32_16x16x32_bf16 v[56:59], a[100:103], a[56:59], v[56:59]\n"
"buffer_load_dwordx4 v15, s[12:15], s50 offen offset:2048 lds\n"
"v_mfma_f32_16x16x32_bf16 v[60:63], a[96:99], a[44:47], v[60:63]\n"
"ds_read_b64_tr_b16 a[126:127], v20 offset:16640\n"
"s_lshl_b32 s89, s40, 12\n"
"v_mfma_f32_16x16x32_bf16 v[60:63], a[100:103], a[60:63], v[60:63]\n"
"s_add_u32 s46, s46, s89\n"
"s_add_u32 s46, s46, 9437184\n"
"s_waitcnt lgkmcnt(8)\n"
"v_mfma_f32_16x16x32_bf16 v[64:67], a[104:107], a[32:35], v[64:67]\n"
"ds_read_b64_tr_b16 a[128:129], v19 offset:4096\n"
"s_mov_b32 s54, s46\n"
"v_mfma_f32_16x16x32_bf16 v[64:67], a[108:111], a[48:51], v[64:67]\n"
"s_mov_b32 s47, s46\n"
"s_lshl2_add_u32 s88, s88, s40\n"
"s_lshl_b32 s88, s88, 5\n"
"v_mfma_f32_16x16x32_bf16 v[68:71], a[104:107], a[36:39], v[68:71]\n"
"ds_read_b64_tr_b16 a[130:131], v19 offset:4352\n"
"s_add_u32 s88, s88, 233472\n"
"v_mfma_f32_16x16x32_bf16 v[68:71], a[108:111], a[52:55], v[68:71]\n"
"s_mov_b32 exec_hi, 0\n"
"buffer_load_dwordx4 v16, s[12:15], s50 offen offset:3072 lds\n"
"s_mov_b32 exec_hi, -1\n"
"v_mfma_f32_16x16x32_bf16 v[72:75], a[104:107], a[40:43], v[72:75]\n"
"ds_read_b64_tr_b16 a[132:133], v19 offset:17408\n"
"s_add_u32 s50, s50, 458752\n"
"v_mfma_f32_16x16x32_bf16 v[72:75], a[108:111], a[56:59], v[72:75]\n"
"s_and_b32 s90, s45, 1\n"
"s_lshl_b32 s90, s90, 1\n"
"s_xor_b32 s90, s90, s42\n"
"v_mfma_f32_16x16x32_bf16 v[76:79], a[104:107], a[44:47], v[76:79]\n"
"ds_read_b64_tr_b16 a[134:135], v19 offset:17664\n"
"s_lshl4_add_u32 s89, s55, s90\n"
"v_mfma_f32_16x16x32_bf16 v[76:79], a[108:111], a[60:63], v[76:79]\n"
"s_lshl_b32 s89, s89, 10\n"
"s_lshl_b32 s55, s45, 19\n"
"s_waitcnt lgkmcnt(8)\n"
"v_mfma_f32_16x16x32_bf16 v[144:147], a[112:115], a[32:35], v[144:147]\n"
"ds_read_b64_tr_b16 a[136:137], v20 offset:4096\n"
"s_add_u32 m0, s92, 32768\n"
"v_mfma_f32_16x16x32_bf16 v[144:147], a[116:119], a[48:51], v[144:147]\n"
"v_mfma_f32_16x16x32_bf16 v[148:151], a[112:115], a[36:39], v[148:151]\n"
"ds_read_b64_tr_b16 a[138:139], v20 offset:4352\n"
"v_mfma_f32_16x16x32_bf16 v[148:151], a[116:119], a[52:55], v[148:151]\n"
"buffer_load_dwordx4 v2, s[4:7], s51 offen offset:0 lds\n"
"v_mfma_f32_16x16x32_bf16 v[152:155], a[112:115], a[40:43], v[152:155]\n"
"ds_read_b64_tr_b16 a[140:141], v20 offset:17408\n"
"v_mfma_f32_16x16x32_bf16 v[152:155], a[116:119], a[56:59], v[152:155]\n"
"buffer_load_dwordx4 v3, s[4:7], s51 offen offset:1024 lds\n"
"v_mfma_f32_16x16x32_bf16 v[156:159], a[112:115], a[44:47], v[156:159]\n"
"ds_read_b64_tr_b16 a[142:143], v20 offset:17664\n"
"s_add_u32 s51, s51, 128\n"
"v_mfma_f32_16x16x32_bf16 v[156:159], a[116:119], a[60:63], v[156:159]\n"
"s_waitcnt lgkmcnt(8)\n"
"v_mfma_f32_16x16x32_bf16 v[160:163], a[120:123], a[32:35], v[160:163]\n"
"ds_read_b64_tr_b16 a[144:145], v19 offset:5120\n"
"v_mfma_f32_16x16x32_bf16 v[160:163], a[124:127], a[48:51], v[160:163]\n"
"v_mfma_f32_16x16x32_bf16 v[164:167], a[120:123], a[36:39], v[164:167]\n"
"ds_read_b64_tr_b16 a[146:147], v19 offset:5376\n"
"v_mfma_f32_16x16x32_bf16 v[164:167], a[124:127], a[52:55], v[164:167]\n"
"v_mfma_f32_16x16x32_bf16 v[168:171], a[120:123], a[40:43], v[168:171]\n"
"ds_read_b64_tr_b16 a[148:149], v19 offset:18432\n"
"v_mfma_f32_16x16x32_bf16 v[168:171], a[124:127], a[56:59], v[168:171]\n"
"v_mfma_f32_16x16x32_bf16 v[172:175], a[120:123], a[44:47], v[172:175]\n"
"ds_read_b64_tr_b16 a[150:151], v19 offset:18688\n"
"v_mfma_f32_16x16x32_bf16 v[172:175], a[124:127], a[60:63], v[172:175]\n"
"s_waitcnt lgkmcnt(8)\n"
"v_mfma_f32_16x16x32_bf16 v[176:179], a[128:131], a[32:35], v[176:179]\n"
"ds_read_b64_tr_b16 a[152:153], v20 offset:5120\n"
"v_mfma_f32_16x16x32_bf16 v[176:179], a[132:135], a[48:51], v[176:179]\n"
"v_mfma_f32_16x16x32_bf16 v[180:183], a[128:131], a[36:39], v[180:183]\n"
"ds_read_b64_tr_b16 a[154:155], v20 offset:5376\n"
"v_mfma_f32_16x16x32_bf16 v[180:183], a[132:135], a[52:55], v[180:183]\n"
"s_waitcnt vmcnt(6)\n"
"v_mfma_f32_16x16x32_bf16 v[184:187], a[128:131], a[40:43], v[184:187]\n"
"ds_read_b64_tr_b16 a[156:157], v20 offset:18432\n"
"s_add_u32 m0, s52, 53248\n"
"v_mfma_f32_16x16x32_bf16 v[184:187], a[132:135], a[56:59], v[184:187]\n"
"ds_read_b128 a[0:3], v17 offset:0\n"
"v_mfma_f32_16x16x32_bf16 v[188:191], a[128:131], a[44:47], v[188:191]\n"
"ds_read_b64_tr_b16 a[158:159], v20 offset:18688\n"
"v_max3_u32 v45, v30, v31, v32\n"
"v_mfma_f32_16x16x32_bf16 v[188:191], a[132:135], a[60:63], v[188:191]\n"
"ds_read_b128 a[16:19], v18 offset:0\n"
"s_waitcnt lgkmcnt(10)\n"
"v_mfma_f32_16x16x32_bf16 v[192:195], a[136:139], a[32:35], v[192:195]\n"
"ds_read_b64_tr_b16 a[160:161], v21 offset:6144\n"
"v_max3_u32 v46, v33, v34, v35\n"
"v_mfma_f32_16x16x32_bf16 v[192:195], a[140:143], a[48:51], v[192:195]\n"
"buffer_load_dwordx4 v10, s[8:11], s49 offen offset:0 lds\n"
"v_max3_u32 v45, v45, v36, v37\n"
"v_mfma_f32_16x16x32_bf16 v[196:199], a[136:139], a[36:39], v[196:199]\n"
"ds_read_b64_tr_b16 a[162:163], v21 offset:6400\n"
"v_max3_u32 v46, v46, v38, v39\n"
"v_mfma_f32_16x16x32_bf16 v[196:199], a[140:143], a[52:55], v[196:199]\n"
"ds_read_b128 a[4:7], v17 offset:2048\n"
"v_max_u32 v45, v45, v46\n"
"v_mfma_f32_16x16x32_bf16 v[200:203], a[136:139], a[40:43], v[200:203]\n"
"ds_read_b64_tr_b16 a[164:165], v21 offset:19456\n"
"v_cmp_eq_u32 vcc, -1, v45\n"
"v_mfma_f32_16x16x32_bf16 v[200:203], a[140:143], a[56:59], v[200:203]\n"
"buffer_load_dwordx4 v11, s[8:11], s49 offen offset:1024 lds\n"
"v_mfma_f32_16x16x32_bf16 v[204:207], a[136:139], a[44:47], v[204:207]\n"
"ds_read_b64_tr_b16 a[166:167], v21 offset:19712\n"
"s_cbranch_vccnz .Ltb_sl\n"
".Ltb_ok:\n"
"v_mfma_f32_16x16x32_bf16 v[204:207], a[140:143], a[60:63], v[204:207]\n"
"ds_read_b128 a[20:23], v18 offset:2048\n"
"s_waitcnt lgkmcnt(12)\n"
"v_mfma_f32_16x16x32_bf16 v[208:211], a[144:147], a[32:35], v[208:211]\n"
"ds_read_b128 a[8:11], v17 offset:4096\n"
"v_mul_f32 v38, 0xba800000, v38\n"
"v_mfma_f32_16x16x32_bf16 v[208:211], a[148:151], a[48:51], v[208:211]\n"
"ds_read_b128 a[24:27], v18 offset:4096\n"
"v_mul_f32 v39, 0x3a800000, v39\n"
"v_mfma_f32_16x16x32_bf16 v[212:215], a[144:147], a[36:39], v[212:215]\n"
"ds_read_b128 a[12:15], v17 offset:6144\n"
"v_add_f32 v30, v30, v31\n"
"v_mfma_f32_16x16x32_bf16 v[212:215], a[148:151], a[52:55], v[212:215]\n"
"buffer_load_dwordx4 v12, s[8:11], s49 offen offset:2048 lds\n"
"v_fma_f32 v39, -v38, v38, v39\n"
"v_mfma_f32_16x16x32_bf16 v[216:219], a[144:147], a[40:43], v[216:219]\n"
"s_add_u32 s49, s49, 393216\n"
"ds_read_b128 a[28:31], v18 offset:6144\n"
"v_mfma_f32_16x16x32_bf16 v[216:219], a[148:151], a[56:59], v[216:219]\n"
"v_add_f32 v32, v32, v33\n"
"v_max_f32 v39, 0, v39\n"
"v_mfma_f32_16x16x32_bf16 v[220:223], a[144:147], a[44:47], v[220:223]\n"
"v_add_f32 v34, v34, v35\n"
"v_add_f32 v45, 0x3727c5ac, v39\n"
"v_mfma_f32_16x16x32_bf16 v[220:223], a[148:151], a[60:63], v[220:223]\n"
"s_waitcnt vmcnt(5) lgkmcnt(5)\n"
"s_barrier\n"
"v_mfma_f32_16x16x32_bf16 v[224:227], a[152:155], a[32:35], v[224:227]\n"
"ds_read_b64_tr_b16 a[64:65], v19 offset:27648\n"
"v_add_f32_e64 v36, v36, v37\n"
"v_mfma_f32_16x16x32_bf16 v[224:227], a[156:159], a[48:51], v[224:227]\n"
"ds_read_b64_tr_b16 a[66:67], v19 offset:27904\n"
"v_add_f32 v30, v30, v32\n"
"v_mfma_f32_16x16x32_bf16 v[228:231], a[152:155], a[36:39], v[228:231]\n"
"ds_read_b64_tr_b16 a[68:69], v19 offset:40960\n"
"s_add_u32 m0, s93, 32768\n"
"v_mfma_f32_16x16x32_bf16 v[228:231], a[156:159], a[52:55], v[228:231]\n"
"ds_read_b64_tr_b16 a[70:71], v19 offset:41216\n"
"v_add_f32 v31, v34, v36\n"
"v_mfma_f32_16x16x32_bf16 v[232:235], a[152:155], a[40:43], v[232:235]\n"
"ds_read_b64_tr_b16 a[72:73], v20 offset:27648\n"
"v_mfma_f32_16x16x32_bf16 v[232:235], a[156:159], a[56:59], v[232:235]\n"
"ds_read_b64_tr_b16 a[74:75], v20 offset:27904\n"
"v_mfma_f32_16x16x32_bf16 v[236:239], a[152:155], a[44:47], v[236:239]\n"
"ds_read_b64_tr_b16 a[76:77], v20 offset:40960\n"
"v_mfma_f32_16x16x32_bf16 v[236:239], a[156:159], a[60:63], v[236:239]\n"
"ds_read_b64_tr_b16 a[78:79], v20 offset:41216\n"
"v_mfma_f32_16x16x32_bf16 v[240:243], a[160:163], a[32:35], v[240:243]\n"
"ds_read_b64_tr_b16 a[80:81], v19 offset:28672\n"
"v_mfma_f32_16x16x32_bf16 v[240:243], a[164:167], a[48:51], v[240:243]\n"
"buffer_load_dwordx4 v4, s[4:7], s48 offen offset:0 lds\n"
"v_mfma_f32_16x16x32_bf16 v[244:247], a[160:163], a[36:39], v[244:247]\n"
"ds_read_b64_tr_b16 a[82:83], v19 offset:28928\n"
"v_mfma_f32_16x16x32_bf16 v[244:247], a[164:167], a[52:55], v[244:247]\n"
"buffer_load_dwordx4 v5, s[4:7], s48 offen offset:1024 lds\n"
"v_mfma_f32_16x16x32_bf16 v[248:251], a[160:163], a[40:43], v[248:251]\n"
"ds_read_b64_tr_b16 a[84:85], v19 offset:41984\n"
"v_mfma_f32_16x16x32_bf16 v[248:251], a[164:167], a[56:59], v[248:251]\n"
"v_rsq_f32 v37, v45\n"
"v_mfma_f32_16x16x32_bf16 v[252:255], a[160:163], a[44:47], v[252:255]\n"
"ds_read_b64_tr_b16 a[86:87], v19 offset:42240\n"
"v_add_f32 v30, v30, v31\n"
"v_mfma_f32_16x16x32_bf16 v[252:255], a[164:167], a[60:63], v[252:255]\n"
"s_add_u32 m0, s94, 32768\n"
"s_waitcnt lgkmcnt(8)\n"
"v_mfma_f32_16x16x32_bf16 v[80:83], a[64:67], a[0:3], v[80:83]\n"
"ds_read_b64_tr_b16 a[88:89], v20 offset:28672\n"
"v_mul_f32 v45, v37, v39\n"
"v_mfma_f32_16x16x32_bf16 v[80:83], a[68:71], a[16:19], v[80:83]\n"
"buffer_load_dwordx4 v6, s[4:7], s48 offen offset:0 lds\n"
"v_fmaak_f32 v45, v45, v37, 0x3727c5ac\n"
"v_mfma_f32_16x16x32_bf16 v[84:87], a[64:67], a[4:7], v[84:87]\n"
"ds_read_b64_tr_b16 a[90:91], v20 offset:28928\n"
"v_mfma_f32_16x16x32_bf16 v[84:87], a[68:71], a[20:23], v[84:87]\n"
"v_rsq_f32 v45, v45\n"
"v_mfma_f32_16x16x32_bf16 v[88:91], a[64:67], a[8:11], v[88:91]\n"
"ds_read_b64_tr_b16 a[92:93], v20 offset:41984\n"
"v_lshlrev_b32 v40, 16, v40\n"
"v_mfma_f32_16x16x32_bf16 v[88:91], a[68:71], a[24:27], v[88:91]\n"
"v_mul_f32 v45, v37, v45\n"
"v_mfma_f32_16x16x32_bf16 v[92:95], a[64:67], a[12:15], v[92:95]\n"
"ds_read_b64_tr_b16 a[94:95], v20 offset:42240\n"
"v_mfma_f32_16x16x32_bf16 v[92:95], a[68:71], a[28:31], v[92:95]\n"
"s_waitcnt lgkmcnt(8)\n"
"v_mfma_f32_16x16x32_bf16 v[96:99], a[72:75], a[0:3], v[96:99]\n"
"ds_read_b64_tr_b16 a[96:97], v19 offset:26624\n"
"v_mfma_f32_16x16x32_bf16 v[96:99], a[76:79], a[16:19], v[96:99]\n"
"buffer_load_dwordx4 v7, s[4:7], s48 offen offset:1024 lds\n"
"v_mfma_f32_16x16x32_bf16 v[100:103], a[72:75], a[4:7], v[100:103]\n"
"ds_read_b64_tr_b16 a[98:99], v19 offset:26880\n"
"v_mfma_f32_16x16x32_bf16 v[100:103], a[76:79], a[20:23], v[100:103]\n"
"v_rcp_f32 v36, v45\n"
"v_mfma_f32_16x16x32_bf16 v[104:107], a[72:75], a[8:11], v[104:107]\n"
"ds_read_b64_tr_b16 a[100:101], v19 offset:39936\n"
"s_add_u32 m0, s95, 32768\n"
"v_mfma_f32_16x16x32_bf16 v[104:107], a[76:79], a[24:27], v[104:107]\n"
"v_mul_f32 v35, 0xc0135761, v45\n"
"v_mul_f32 v34, v45, v45\n"
"v_mfma_f32_16x16x32_bf16 v[108:111], a[72:75], a[12:15], v[108:111]\n"
"ds_read_b64_tr_b16 a[102:103], v19 offset:40192\n"
"v_mul_f32 v34, v34, v45\n"
"v_mfma_f32_16x16x32_bf16 v[108:111], a[76:79], a[28:31], v[108:111]\n"
"v_mul_f32 v34, 0xbdd2d3e8, v34\n"
"s_waitcnt lgkmcnt(8)\n"
"v_mfma_f32_16x16x32_bf16 v[112:115], a[0:3], a[80:83], v[112:115]\n"
"ds_read_b64_tr_b16 a[104:105], v20 offset:26624\n"
"s_lshl_b32 s64, s40, 10\n"
"v_mfma_f32_16x16x32_bf16 v[112:115], a[16:19], a[84:87], v[112:115]\n"
"buffer_load_dwordx4 v8, s[4:7], s48 offen offset:0 lds\n"
"v_mfma_f32_16x16x32_bf16 v[116:119], a[4:7], a[80:83], v[116:119]\n"
"ds_read_b64_tr_b16 a[106:107], v20 offset:26880\n"
"s_add_u32 s64, s64, 145408\n"
"v_mfma_f32_16x16x32_bf16 v[116:119], a[20:23], a[84:87], v[116:119]\n"
"buffer_load_dwordx4 v9, s[4:7], s48 offen offset:1024 lds\n"
"v_add_u32 v45, s64, v1\n"
"v_mfma_f32_16x16x32_bf16 v[120:123], a[8:11], a[80:83], v[120:123]\n"
"ds_read_b64_tr_b16 a[108:109], v20 offset:39936\n"
"s_add_u32 s48, s48, 128\n"
"v_mfma_f32_16x16x32_bf16 v[120:123], a[24:27], a[84:87], v[120:123]\n"
"s_lshl_b32 s65, s40, 8\n"
"s_add_u32 s65, s65, 149504\n"
"v_mfma_f32_16x16x32_bf16 v[124:127], a[12:15], a[80:83], v[124:127]\n"
"ds_read_b64_tr_b16 a[110:111], v20 offset:40192\n"
"v_lshl_add_u32 v46, v0, 2, s65\n"
"v_mfma_f32_16x16x32_bf16 v[124:127], a[28:31], a[84:87], v[124:127]\n"
"v_mov_b32 v39, v36\n"
"s_waitcnt lgkmcnt(8)\n"
"v_mfma_f32_16x16x32_bf16 v[128:131], a[0:3], a[88:91], v[128:131]\n"
"ds_read_b64_tr_b16 a[112:113], v19 offset:29696\n"
"s_add_u32 m0, s52, 56320\n"
"v_mfma_f32_16x16x32_bf16 v[128:131], a[16:19], a[92:95], v[128:131]\n"
"s_lshl_b32 s67, s40, 9\n"
"s_add_u32 s67, s67, 153600\n"
"v_mfma_f32_16x16x32_bf16 v[132:135], a[4:7], a[88:91], v[132:135]\n"
"ds_read_b64_tr_b16 a[114:115], v19 offset:29952\n"
"v_lshl_add_u32 v44, v0, 3, s67\n"
".Lpad5:\n"
"s_cbranch_execz .Lwr0\n"
"v_mfma_f32_16x16x32_bf16 v[132:135], a[20:23], a[92:95], v[132:135]\n"
"buffer_load_dwordx4 v13, s[12:15], s50 offen offset:0 lds\n"
"v_mfma_f32_16x16x32_bf16 v[136:139], a[8:11], a[88:91], v[136:139]\n"
"ds_read_b64_tr_b16 a[116:117], v19 offset:43008\n"
"v_mfma_f32_16x16x32_bf16 v[136:139], a[24:27], a[92:95], v[136:139]\n"
"ds_write_b128 v45, v[34:37]\n"
"v_mfma_f32_16x16x32_bf16 v[140:143], a[12:15], a[88:91], v[140:143]\n"
"ds_read_b64_tr_b16 a[118:119], v19 offset:43264\n"
"v_mfma_f32_16x16x32_bf16 v[140:143], a[28:31], a[92:95], v[140:143]\n"
"ds_write2st64_b32 v46, v37, v38 offset0:0 offset1:4\n"
"s_waitcnt lgkmcnt(10)\n"
"v_mfma_f32_16x16x32_bf16 v[48:51], a[96:99], a[0:3], v[48:51]\n"
"ds_read_b64_tr_b16 a[120:121], v20 offset:29696\n"
"v_mfma_f32_16x16x32_bf16 v[48:51], a[100:103], a[16:19], v[48:51]\n"
"buffer_load_dwordx4 v14, s[12:15], s50 offen offset:1024 lds\n"
"v_mfma_f32_16x16x32_bf16 v[52:55], a[96:99], a[4:7], v[52:55]\n"
"ds_read_b64_tr_b16 a[122:123], v20 offset:29952\n"
"v_mfma_f32_16x16x32_bf16 v[52:55], a[100:103], a[20:23], v[52:55]\n"
"ds_write2st64_b32 v46, v30, v40 offset0:8 offset1:12\n"
"v_mfma_f32_16x16x32_bf16 v[56:59], a[96:99], a[8:11], v[56:59]\n"
"ds_read_b64_tr_b16 a[124:125], v20 offset:43008\n"
"v_mfma_f32_16x16x32_bf16 v[56:59], a[100:103], a[24:27], v[56:59]\n"
"buffer_load_dwordx4 v15, s[12:15], s50 offen offset:2048 lds\n"
"v_mfma_f32_16x16x32_bf16 v[60:63], a[96:99], a[12:15], v[60:63]\n"
"ds_read_b64_tr_b16 a[126:127], v20 offset:43264\n"
"v_mfma_f32_16x16x32_bf16 v[60:63], a[100:103], a[28:31], v[60:63]\n"
"ds_write_b64 v44, v[38:39]\n"
"s_waitcnt lgkmcnt(12)\n"
"v_mfma_f32_16x16x32_bf16 v[64:67], a[104:107], a[0:3], v[64:67]\n"
"ds_read_b64_tr_b16 a[128:129], v19 offset:30720\n"
"v_mfma_f32_16x16x32_bf16 v[64:67], a[108:111], a[16:19], v[64:67]\n"
"v_mfma_f32_16x16x32_bf16 v[68:71], a[104:107], a[4:7], v[68:71]\n"
"ds_read_b64_tr_b16 a[130:131], v19 offset:30976\n"
"v_mfma_f32_16x16x32_bf16 v[68:71], a[108:111], a[20:23], v[68:71]\n"
"s_mov_b32 exec_hi, 0\n"
"buffer_load_dwordx4 v16, s[12:15], s50 offen offset:3072 lds\n"
"s_mov_b32 exec_hi, -1\n"
"v_mfma_f32_16x16x32_bf16 v[72:75], a[104:107], a[8:11], v[72:75]\n"
"ds_read_b64_tr_b16 a[132:133], v19 offset:44032\n"
"s_add_u32 s50, s50, 458752\n"
"v_mfma_f32_16x16x32_bf16 v[72:75], a[108:111], a[24:27], v[72:75]\n"
"v_and_b32 v44, 48, v0\n"
"v_add_u32 v45, s65, v44\n"
"v_mfma_f32_16x16x32_bf16 v[76:79], a[104:107], a[12:15], v[76:79]\n"
"ds_read_b64_tr_b16 a[134:135], v19 offset:44288\n"
"v_add_u32 v44, 151552, v44\n"
"v_mfma_f32_16x16x32_bf16 v[76:79], a[108:111], a[28:31], v[76:79]\n"
"v_and_b32 v46, 15, v0\n"
"s_waitcnt lgkmcnt(11)\n"
"v_mfma_f32_16x16x32_bf16 v[144:147], a[112:115], a[0:3], v[144:147]\n"
"ds_read_b64_tr_b16 a[136:137], v20 offset:30720\n"
"s_add_u32 m0, s92, 0\n"
"v_mfma_f32_16x16x32_bf16 v[144:147], a[116:119], a[16:19], v[144:147]\n"
"v_lshlrev_b32 v42, 2, v46\n"
"v_add_u32 v42, 151552, v42\n"
"v_mfma_f32_16x16x32_bf16 v[148:151], a[112:115], a[4:7], v[148:151]\n"
"ds_read_b64_tr_b16 a[138:139], v20 offset:30976\n"
"v_lshl_add_u32 v46, v46, 3, s67\n"
"v_mfma_f32_16x16x32_bf16 v[148:151], a[116:119], a[20:23], v[148:151]\n"
"buffer_load_dwordx4 v2, s[4:7], s51 offen offset:0 lds\n"
"v_mfma_f32_16x16x32_bf16 v[152:155], a[112:115], a[8:11], v[152:155]\n"
"ds_read_b64_tr_b16 a[140:141], v20 offset:44032\n"
"v_mfma_f32_16x16x32_bf16 v[152:155], a[116:119], a[24:27], v[152:155]\n"
"buffer_load_dwordx4 v3, s[4:7], s51 offen offset:1024 lds\n"
"v_mfma_f32_16x16x32_bf16 v[156:159], a[112:115], a[12:15], v[156:159]\n"
"ds_read_b64_tr_b16 a[142:143], v20 offset:44288\n"
"s_add_u32 s51, s51, 128\n"
"v_mfma_f32_16x16x32_bf16 v[156:159], a[116:119], a[28:31], v[156:159]\n"
"s_waitcnt lgkmcnt(9)\n"
"v_mfma_f32_16x16x32_bf16 v[160:163], a[120:123], a[0:3], v[160:163]\n"
"ds_read_b64_tr_b16 a[144:145], v19 offset:31744\n"
"v_mfma_f32_16x16x32_bf16 v[160:163], a[124:127], a[16:19], v[160:163]\n"
"v_mfma_f32_16x16x32_bf16 v[164:167], a[120:123], a[4:7], v[164:167]\n"
"ds_read_b64_tr_b16 a[146:147], v19 offset:32000\n"
"v_mfma_f32_16x16x32_bf16 v[164:167], a[124:127], a[20:23], v[164:167]\n"
"v_mfma_f32_16x16x32_bf16 v[168:171], a[120:123], a[8:11], v[168:171]\n"
"ds_read_b64_tr_b16 a[148:149], v19 offset:45056\n"
"v_mfma_f32_16x16x32_bf16 v[168:171], a[124:127], a[24:27], v[168:171]\n"
"v_mfma_f32_16x16x32_bf16 v[172:175], a[120:123], a[12:15], v[172:175]\n"
"ds_read_b64_tr_b16 a[150:151], v19 offset:45312\n"
"v_mfma_f32_16x16x32_bf16 v[172:175], a[124:127], a[28:31], v[172:175]\n"
"s_waitcnt lgkmcnt(8)\n"
"v_mfma_f32_16x16x32_bf16 v[176:179], a[128:131], a[0:3], v[176:179]\n"
"ds_read_b64_tr_b16 a[152:153], v20 offset:31744\n"
"v_mfma_f32_16x16x32_bf16 v[176:179], a[132:135], a[16:19], v[176:179]\n"
"v_mfma_f32_16x16x32_bf16 v[180:183], a[128:131], a[4:7], v[180:183]\n"
"ds_read_b64_tr_b16 a[154:155], v20 offset:32000\n"
"v_mfma_f32_16x16x32_bf16 v[180:183], a[132:135], a[20:23], v[180:183]\n"
"s_waitcnt vmcnt(6)\n"
"v_mfma_f32_16x16x32_bf16 v[184:187], a[128:131], a[8:11], v[184:187]\n"
"ds_read_b64_tr_b16 a[156:157], v20 offset:45056\n"
"s_add_u32 m0, s52, 0\n"
"v_mfma_f32_16x16x32_bf16 v[184:187], a[132:135], a[24:27], v[184:187]\n"
"ds_read_b128 a[32:35], v17 offset:32768\n"
"v_mfma_f32_16x16x32_bf16 v[188:191], a[128:131], a[12:15], v[188:191]\n"
"ds_read_b64_tr_b16 a[158:159], v20 offset:45312\n"
"v_mfma_f32_16x16x32_bf16 v[188:191], a[132:135], a[28:31], v[188:191]\n"
"ds_read_b128 a[48:51], v18 offset:32768\n"
"s_waitcnt lgkmcnt(10)\n"
"v_mfma_f32_16x16x32_bf16 v[192:195], a[136:139], a[0:3], v[192:195]\n"
"ds_read_b64_tr_b16 a[160:161], v21 offset:32768\n"
"v_mfma_f32_16x16x32_bf16 v[192:195], a[140:143], a[16:19], v[192:195]\n"
"buffer_load_dwordx4 v10, s[8:11], s49 offen offset:0 lds\n"
"v_mfma_f32_16x16x32_bf16 v[196:199], a[136:139], a[4:7], v[196:199]\n"
"ds_read_b64_tr_b16 a[162:163], v21 offset:33024\n"
"v_mfma_f32_16x16x32_bf16 v[196:199], a[140:143], a[20:23], v[196:199]\n"
"ds_read_b128 a[36:39], v17 offset:34816\n"
"v_mfma_f32_16x16x32_bf16 v[200:203], a[136:139], a[8:11], v[200:203]\n"
"ds_read_b64_tr_b16 a[164:165], v21 offset:46080\n"
"v_mfma_f32_16x16x32_bf16 v[200:203], a[140:143], a[24:27], v[200:203]\n"
"buffer_load_dwordx4 v11, s[8:11], s49 offen offset:1024 lds\n"
"v_mfma_f32_16x16x32_bf16 v[204:207], a[136:139], a[12:15], v[204:207]\n"
"ds_read_b64_tr_b16 a[166:167], v21 offset:46336\n"
"v_mfma_f32_16x16x32_bf16 v[204:207], a[140:143], a[28:31], v[204:207]\n"
"ds_read_b128 a[52:55], v18 offset:34816\n"
"s_waitcnt lgkmcnt(12)\n"
"v_mfma_f32_16x16x32_bf16 v[208:211], a[144:147], a[0:3], v[208:211]\n"
"ds_read_b128 a[40:43], v17 offset:36864\n"
"v_mfma_f32_16x16x32_bf16 v[208:211], a[148:151], a[16:19], v[208:211]\n"
"ds_read_b128 a[56:59], v18 offset:36864\n"
"v_mfma_f32_16x16x32_bf16 v[212:215], a[144:147], a[4:7], v[212:215]\n"
"ds_read_b128 a[44:47], v17 offset:38912\n"
"v_mfma_f32_16x16x32_bf16 v[212:215], a[148:151], a[20:23], v[212:215]\n"
"buffer_load_dwordx4 v12, s[8:11], s49 offen offset:2048 lds\n"
"v_mfma_f32_16x16x32_bf16 v[216:219], a[144:147], a[8:11], v[216:219]\n"
"s_add_u32 s49, s49, 393216\n"
"ds_read_b128 a[60:63], v18 offset:38912\n"
"v_mfma_f32_16x16x32_bf16 v[216:219], a[148:151], a[24:27], v[216:219]\n"
"v_mfma_f32_16x16x32_bf16 v[220:223], a[144:147], a[12:15], v[220:223]\n"
"v_mfma_f32_16x16x32_bf16 v[220:223], a[148:151], a[28:31], v[220:223]\n"
"s_waitcnt vmcnt(5) lgkmcnt(5)\n"
"s_barrier\n"
"v_mfma_f32_16x16x32_bf16 v[224:227], a[152:155], a[0:3], v[224:227]\n"
"ds_read_b64_tr_b16 a[64:65], v22 offset:1024\n"
"v_mfma_f32_16x16x32_bf16 v[224:227], a[156:159], a[16:19], v[224:227]\n"
"ds_read_b64_tr_b16 a[66:67], v22 offset:1280\n"
"v_mfma_f32_16x16x32_bf16 v[228:231], a[152:155], a[4:7], v[228:231]\n"
"ds_read_b64_tr_b16 a[68:69], v22 offset:14336\n"
"s_add_u32 m0, s93, 0\n"
"v_mfma_f32_16x16x32_bf16 v[228:231], a[156:159], a[20:23], v[228:231]\n"
"ds_read_b64_tr_b16 a[70:71], v22 offset:14592\n"
"v_mfma_f32_16x16x32_bf16 v[232:235], a[152:155], a[8:11], v[232:235]\n"
"ds_read_b64_tr_b16 a[72:73], v23 offset:1024\n"
"v_mfma_f32_16x16x32_bf16 v[232:235], a[156:159], a[24:27], v[232:235]\n"
"ds_read_b64_tr_b16 a[74:75], v23 offset:1280\n"
"v_mfma_f32_16x16x32_bf16 v[236:239], a[152:155], a[12:15], v[236:239]\n"
"ds_read_b64_tr_b16 a[76:77], v23 offset:14336\n"
"v_mfma_f32_16x16x32_bf16 v[236:239], a[156:159], a[28:31], v[236:239]\n"
"ds_read_b64_tr_b16 a[78:79], v23 offset:14592\n"
"v_mfma_f32_16x16x32_bf16 v[240:243], a[160:163], a[0:3], v[240:243]\n"
"ds_read_b64_tr_b16 a[80:81], v22 offset:2048\n"
"v_mfma_f32_16x16x32_bf16 v[240:243], a[164:167], a[16:19], v[240:243]\n"
"buffer_load_dwordx4 v4, s[4:7], s48 offen offset:0 lds\n"
"v_mfma_f32_16x16x32_bf16 v[244:247], a[160:163], a[4:7], v[244:247]\n"
"ds_read_b64_tr_b16 a[82:83], v22 offset:2304\n"
"v_mfma_f32_16x16x32_bf16 v[244:247], a[164:167], a[20:23], v[244:247]\n"
"buffer_load_dwordx4 v5, s[4:7], s48 offen offset:1024 lds\n"
"v_mfma_f32_16x16x32_bf16 v[248:251], a[160:163], a[8:11], v[248:251]\n"
"ds_read_b64_tr_b16 a[84:85], v22 offset:15360\n"
"v_mfma_f32_16x16x32_bf16 v[248:251], a[164:167], a[24:27], v[248:251]\n"
"ds_read_b64 v[26:27], v46 offset:0\n"
"v_mfma_f32_16x16x32_bf16 v[252:255], a[160:163], a[12:15], v[252:255]\n"
"ds_read_b64_tr_b16 a[86:87], v22 offset:15616\n"
"v_mfma_f32_16x16x32_bf16 v[252:255], a[164:167], a[28:31], v[252:255]\n"
"s_add_u32 m0, s94, 0\n"
"s_waitcnt lgkmcnt(9)\n"
"v_mfma_f32_16x16x32_bf16 v[80:83], a[64:67], a[32:35], v[80:83]\n"
"ds_read_b64_tr_b16 a[88:89], v23 offset:2048\n"
"v_mfma_f32_16x16x32_bf16 v[80:83], a[68:71], a[48:51], v[80:83]\n"
"buffer_load_dwordx4 v6, s[4:7], s48 offen offset:0 lds\n"
"v_mfma_f32_16x16x32_bf16 v[84:87], a[64:67], a[36:39], v[84:87]\n"
"ds_read_b64_tr_b16 a[90:91], v23 offset:2304\n"
"v_mfma_f32_16x16x32_bf16 v[84:87], a[68:71], a[52:55], v[84:87]\n"
"ds_read_b64 v[28:29], v46 offset:128\n"
"v_mfma_f32_16x16x32_bf16 v[88:91], a[64:67], a[40:43], v[88:91]\n"
"ds_read_b64_tr_b16 a[92:93], v23 offset:15360\n"
"v_mfma_f32_16x16x32_bf16 v[88:91], a[68:71], a[56:59], v[88:91]\n"
"ds_read_b64 v[30:31], v46 offset:256\n"
"v_mfma_f32_16x16x32_bf16 v[92:95], a[64:67], a[44:47], v[92:95]\n"
"ds_read_b64_tr_b16 a[94:95], v23 offset:15616\n"
"v_mfma_f32_16x16x32_bf16 v[92:95], a[68:71], a[60:63], v[92:95]\n"
"ds_read_b64 v[32:33], v46 offset:384\n"
"s_waitcnt lgkmcnt(12)\n"
"v_mfma_f32_16x16x32_bf16 v[96:99], a[72:75], a[32:35], v[96:99]\n"
"ds_read_b64_tr_b16 a[96:97], v22 offset:0\n"
"v_mfma_f32_16x16x32_bf16 v[96:99], a[76:79], a[48:51], v[96:99]\n"
"buffer_load_dwordx4 v7, s[4:7], s48 offen offset:1024 lds\n"
"v_mfma_f32_16x16x32_bf16 v[100:103], a[72:75], a[36:39], v[100:103]\n"
"ds_read_b64_tr_b16 a[98:99], v22 offset:256\n"
"v_mfma_f32_16x16x32_bf16 v[100:103], a[76:79], a[52:55], v[100:103]\n"
"ds_read2_b32 v[42:43], v42 offset0:64 offset1:80\n"
"v_mfma_f32_16x16x32_bf16 v[104:107], a[72:75], a[40:43], v[104:107]\n"
"ds_read_b64_tr_b16 a[100:101], v22 offset:13312\n"
"s_add_u32 m0, s95, 0\n"
"v_mfma_f32_16x16x32_bf16 v[104:107], a[76:79], a[56:59], v[104:107]\n"
"ds_read_b128 v[34:37], v44 offset:448\n"
"v_mfma_f32_16x16x32_bf16 v[108:111], a[72:75], a[44:47], v[108:111]\n"
"ds_read_b64_tr_b16 a[102:103], v22 offset:13568\n"
"v_mfma_f32_16x16x32_bf16 v[108:111], a[76:79], a[60:63], v[108:111]\n"
"s_waitcnt lgkmcnt(13)\n"
"v_mfma_f32_16x16x32_bf16 v[112:115], a[32:35], a[80:83], v[112:115]\n"
"ds_read_b64_tr_b16 a[104:105], v23 offset:0\n"
"v_mfma_f32_16x16x32_bf16 v[112:115], a[48:51], a[84:87], v[112:115]\n"
"buffer_load_dwordx4 v8, s[4:7], s48 offen offset:0 lds\n"
"v_mfma_f32_16x16x32_bf16 v[116:119], a[36:39], a[80:83], v[116:119]\n"
"ds_read_b64_tr_b16 a[106:107], v23 offset:256\n"
"v_mfma_f32_16x16x32_bf16 v[116:119], a[52:55], a[84:87], v[116:119]\n"
"buffer_load_dwordx4 v9, s[4:7], s48 offen offset:1024 lds\n"
"v_mfma_f32_16x16x32_bf16 v[120:123], a[40:43], a[80:83], v[120:123]\n"
"ds_read_b64_tr_b16 a[108:109], v23 offset:13312\n"
"s_add_u32 s48, s48, 128\n"
"v_mfma_f32_16x16x32_bf16 v[120:123], a[56:59], a[84:87], v[120:123]\n"
"ds_read_b128 v[38:41], v44 offset:128\n"
"s_waitcnt lgkmcnt(5)\n"
"v_mfma_f32_16x16x32_bf16 v[124:127], a[44:47], a[80:83], v[124:127]\n"
"ds_read_b64_tr_b16 a[110:111], v23 offset:13568\n"
"v_fmac_f32 v160, v34, v26\n"
"v_mfma_f32_16x16x32_bf16 v[124:127], a[60:63], a[84:87], v[124:127]\n"
"v_fmac_f32 v161, v35, v26\n"
"v_mfma_f32_16x16x32_bf16 v[128:131], a[32:35], a[88:91], v[128:131]\n"
"ds_read_b64_tr_b16 a[112:113], v22 offset:3072\n"
"s_add_u32 m0, s52, 3072\n"
"v_mfma_f32_16x16x32_bf16 v[128:131], a[48:51], a[92:95], v[128:131]\n"
"v_fmac_f32 v162, v36, v26\n"
"v_fmac_f32 v163, v37, v26\n"
"v_mfma_f32_16x16x32_bf16 v[132:135], a[36:39], a[88:91], v[132:135]\n"
"ds_read_b64_tr_b16 a[114:115], v22 offset:3328\n"
"v_fmac_f32 v164, v34, v28\n"
"v_mfma_f32_16x16x32_bf16 v[132:135], a[52:55], a[92:95], v[132:135]\n"
"buffer_load_dwordx4 v13, s[12:15], s50 offen offset:0 lds\n"
"v_fmac_f32 v165, v35, v28\n"
"v_mfma_f32_16x16x32_bf16 v[136:139], a[40:43], a[88:91], v[136:139]\n"
"ds_read_b64_tr_b16 a[116:117], v22 offset:16384\n"
"v_fmac_f32 v166, v36, v28\n"
"v_mfma_f32_16x16x32_bf16 v[136:139], a[56:59], a[92:95], v[136:139]\n"
"v_fmac_f32 v167, v37, v28\n"
"v_fmac_f32 v168, v34, v30\n"
"v_mfma_f32_16x16x32_bf16 v[140:143], a[44:47], a[88:91], v[140:143]\n"
"ds_read_b64_tr_b16 a[118:119], v22 offset:16640\n"
"v_fmac_f32 v169, v35, v30\n"
"v_mfma_f32_16x16x32_bf16 v[140:143], a[60:63], a[92:95], v[140:143]\n"
"v_fmac_f32 v170, v36, v30\n"
"s_waitcnt lgkmcnt(9)\n"
"v_mfma_f32_16x16x32_bf16 v[48:51], a[96:99], a[32:35], v[48:51]\n"
"ds_read_b64_tr_b16 a[120:121], v23 offset:3072\n"
"v_fmac_f32 v171, v37, v30\n"
"v_mfma_f32_16x16x32_bf16 v[48:51], a[100:103], a[48:51], v[48:51]\n"
"buffer_load_dwordx4 v14, s[12:15], s50 offen offset:1024 lds\n"
"v_fmac_f32 v172, v34, v32\n"
"v_mfma_f32_16x16x32_bf16 v[52:55], a[96:99], a[36:39], v[52:55]\n"
"ds_read_b64_tr_b16 a[122:123], v23 offset:3328\n"
"v_fmac_f32 v173, v35, v32\n"
"v_mfma_f32_16x16x32_bf16 v[52:55], a[100:103], a[52:55], v[52:55]\n"
"v_fmac_f32 v174, v36, v32\n"
"v_fmac_f32 v175, v37, v32\n"
"v_mfma_f32_16x16x32_bf16 v[56:59], a[96:99], a[40:43], v[56:59]\n"
"ds_read_b64_tr_b16 a[124:125], v23 offset:16384\n"
"s_waitcnt lgkmcnt(8)\n"
"v_mfma_f32_16x16x32_bf16 v[56:59], a[100:103], a[56:59], v[56:59]\n"
"buffer_load_dwordx4 v15, s[12:15], s50 offen offset:2048 lds\n"
"v_fmac_f32 v80, v38, v26\n"
"v_mfma_f32_16x16x32_bf16 v[60:63], a[96:99], a[44:47], v[60:63]\n"
"ds_read_b64_tr_b16 a[126:127], v23 offset:16640\n"
"v_fmac_f32 v81, v39, v26\n"
"v_mfma_f32_16x16x32_bf16 v[60:63], a[100:103], a[60:63], v[60:63]\n"
"ds_read_b128 v[34:37], v44 offset:192\n"
"s_waitcnt lgkmcnt(9)\n"
"v_mfma_f32_16x16x32_bf16 v[64:67], a[104:107], a[32:35], v[64:67]\n"
"ds_read_b64_tr_b16 a[128:129], v22 offset:4096\n"
"v_fmac_f32 v82, v40, v26\n"
"v_mfma_f32_16x16x32_bf16 v[64:67], a[108:111], a[48:51], v[64:67]\n"
"v_fmac_f32 v83, v41, v26\n"
"v_fmac_f32 v84, v38, v28\n"
"v_mfma_f32_16x16x32_bf16 v[68:71], a[104:107], a[36:39], v[68:71]\n"
"ds_read_b64_tr_b16 a[130:131], v22 offset:4352\n"
"v_fmac_f32 v85, v39, v28\n"
"v_mfma_f32_16x16x32_bf16 v[68:71], a[108:111], a[52:55], v[68:71]\n"
"s_mov_b32 exec_hi, 0\n"
"buffer_load_dwordx4 v16, s[12:15], s50 offen offset:3072 lds\n"
"s_mov_b32 exec_hi, -1\n"
"v_mfma_f32_16x16x32_bf16 v[72:75], a[104:107], a[40:43], v[72:75]\n"
"ds_read_b64_tr_b16 a[132:133], v22 offset:17408\n"
"s_add_u32 s50, s50, 458752\n"
"v_mfma_f32_16x16x32_bf16 v[72:75], a[108:111], a[56:59], v[72:75]\n"
"v_fmac_f32 v86, v40, v28\n"
"v_fmac_f32 v87, v41, v28\n"
"v_mfma_f32_16x16x32_bf16 v[76:79], a[104:107], a[44:47], v[76:79]\n"
"ds_read_b64_tr_b16 a[134:135], v22 offset:17664\n"
"v_fmac_f32 v88, v38, v30\n"
"v_mfma_f32_16x16x32_bf16 v[76:79], a[108:111], a[60:63], v[76:79]\n"
"v_fmac_f32 v89, v39, v30\n"
"s_waitcnt lgkmcnt(9)\n"
"v_mfma_f32_16x16x32_bf16 v[144:147], a[112:115], a[32:35], v[144:147]\n"
"ds_read_b64_tr_b16 a[136:137], v23 offset:4096\n"
"s_add_u32 m0, s92, 32768\n"
"v_mfma_f32_16x16x32_bf16 v[144:147], a[116:119], a[48:51], v[144:147]\n"
"v_fmac_f32 v90, v40, v30\n"
"v_fmac_f32 v91, v41, v30\n"
"v_mfma_f32_16x16x32_bf16 v[148:151], a[112:115], a[36:39], v[148:151]\n"
"ds_read_b64_tr_b16 a[138:139], v23 offset:4352\n"
"v_fmac_f32 v92, v38, v32\n"
"v_mfma_f32_16x16x32_bf16 v[148:151], a[116:119], a[52:55], v[148:151]\n"
"buffer_load_dwordx4 v2, s[4:7], s51 offen offset:0 lds\n"
"v_fmac_f32 v93, v39, v32\n"
"v_mfma_f32_16x16x32_bf16 v[152:155], a[112:115], a[40:43], v[152:155]\n"
"ds_read_b64_tr_b16 a[140:141], v23 offset:17408\n"
"v_fmac_f32 v94, v40, v32\n"
"v_mfma_f32_16x16x32_bf16 v[152:155], a[116:119], a[56:59], v[152:155]\n"
"buffer_load_dwordx4 v3, s[4:7], s51 offen offset:1024 lds\n"
"v_fmac_f32 v95, v41, v32\n"
"v_mfma_f32_16x16x32_bf16 v[156:159], a[112:115], a[44:47], v[156:159]\n"
"ds_read_b64_tr_b16 a[142:143], v23 offset:17664\n"
"s_add_u32 s51, s51, 128\n"
"v_mfma_f32_16x16x32_bf16 v[156:159], a[116:119], a[60:63], v[156:159]\n"
"ds_read_b128 v[38:41], v45 offset:1024\n"
"s_waitcnt lgkmcnt(10)\n"
"v_mfma_f32_16x16x32_bf16 v[160:163], a[120:123], a[32:35], v[160:163]\n"
"ds_read_b64_tr_b16 a[144:145], v22 offset:5120\n"
"s_waitcnt lgkmcnt(10)\n"
"v_mfma_f32_16x16x32_bf16 v[160:163], a[124:127], a[48:51], v[160:163]\n"
"s_mov_b64 exec, s[82:83]\n"
"buffer_load_dword a252, v25, s[24:27], s58 offen\n"
"s_mov_b64 exec, -1\n"
"v_mfma_f32_16x16x32_bf16 v[164:167], a[120:123], a[36:39], v[164:167]\n"
"ds_read_b64_tr_b16 a[146:147], v22 offset:5376\n"
"s_add_u32 s58, s58, 262144\n"
"v_mfma_f32_16x16x32_bf16 v[164:167], a[124:127], a[52:55], v[164:167]\n"
"v_fmac_f32 v96, v34, v26\n"
"v_fmac_f32 v97, v35, v26\n"
"v_mfma_f32_16x16x32_bf16 v[168:171], a[120:123], a[40:43], v[168:171]\n"
"ds_read_b64_tr_b16 a[148:149], v22 offset:18432\n"
"v_fmac_f32 v98, v36, v26\n"
"v_mfma_f32_16x16x32_bf16 v[168:171], a[124:127], a[56:59], v[168:171]\n"
"s_mov_b64 exec, s[82:83]\n"
"buffer_load_dword a252, v25, s[24:27], s58 offen\n"
"s_mov_b64 exec, -1\n"
"v_mfma_f32_16x16x32_bf16 v[172:175], a[120:123], a[44:47], v[172:175]\n"
"ds_read_b64_tr_b16 a[150:151], v22 offset:18688\n"
"s_add_u32 s58, s58, 262144\n"
"v_mfma_f32_16x16x32_bf16 v[172:175], a[124:127], a[60:63], v[172:175]\n"
"v_fmac_f32 v99, v37, v26\n"
"s_waitcnt lgkmcnt(9)\n"
"v_mfma_f32_16x16x32_bf16 v[176:179], a[128:131], a[32:35], v[176:179]\n"
"ds_read_b64_tr_b16 a[152:153], v23 offset:5120\n"
"v_fmac_f32 v100, v34, v28\n"
"v_mfma_f32_16x16x32_bf16 v[176:179], a[132:135], a[48:51], v[176:179]\n"
"v_fmac_f32 v101, v35, v28\n"
"v_fmac_f32 v102, v36, v28\n"
"v_mfma_f32_16x16x32_bf16 v[180:183], a[128:131], a[36:39], v[180:183]\n"
"ds_read_b64_tr_b16 a[154:155], v23 offset:5376\n"
"v_fmac_f32 v103, v37, v28\n"
"v_mfma_f32_16x16x32_bf16 v[180:183], a[132:135], a[52:55], v[180:183]\n"
"s_waitcnt vmcnt(8)\n"
"v_fmac_f32 v104, v34, v30\n"
"v_mfma_f32_16x16x32_bf16 v[184:187], a[128:131], a[40:43], v[184:187]\n"
"ds_read_b64_tr_b16 a[156:157], v23 offset:18432\n"
"s_add_u32 m0, s52, 26624\n"
"v_mfma_f32_16x16x32_bf16 v[184:187], a[132:135], a[56:59], v[184:187]\n"
"ds_read_b128 a[0:3], v17 offset:0\n"
"v_fmac_f32 v105, v35, v30\n"
"v_mfma_f32_16x16x32_bf16 v[188:191], a[128:131], a[44:47], v[188:191]\n"
"ds_read_b64_tr_b16 a[158:159], v23 offset:18688\n"
"v_fmac_f32 v106, v36, v30\n"
"v_mfma_f32_16x16x32_bf16 v[188:191], a[132:135], a[60:63], v[188:191]\n"
"ds_read_b128 a[16:19], v18 offset:0\n"
"s_waitcnt lgkmcnt(11)\n"
"v_mfma_f32_16x16x32_bf16 v[192:195], a[136:139], a[32:35], v[192:195]\n"
"ds_read_b64_tr_b16 a[160:161], v24 offset:6144\n"
"v_fmac_f32 v107, v37, v30\n"
"v_mfma_f32_16x16x32_bf16 v[192:195], a[140:143], a[48:51], v[192:195]\n"
"buffer_load_dwordx4 v10, s[8:11], s49 offen offset:0 lds\n"
"v_fmac_f32 v108, v34, v32\n"
"v_mfma_f32_16x16x32_bf16 v[196:199], a[136:139], a[36:39], v[196:199]\n"
"ds_read_b64_tr_b16 a[162:163], v24 offset:6400\n"
"v_fmac_f32 v109, v35, v32\n"
"v_mfma_f32_16x16x32_bf16 v[196:199], a[140:143], a[52:55], v[196:199]\n"
"ds_read_b128 a[4:7], v17 offset:2048\n"
"v_fmac_f32 v110, v36, v32\n"
"v_mfma_f32_16x16x32_bf16 v[200:203], a[136:139], a[40:43], v[200:203]\n"
"ds_read_b64_tr_b16 a[164:165], v24 offset:19456\n"
"v_fmac_f32 v111, v37, v32\n"
"v_mfma_f32_16x16x32_bf16 v[200:203], a[140:143], a[56:59], v[200:203]\n"
"buffer_load_dwordx4 v11, s[8:11], s49 offen offset:1024 lds\n"
"v_mfma_f32_16x16x32_bf16 v[204:207], a[136:139], a[44:47], v[204:207]\n"
"ds_read_b64_tr_b16 a[166:167], v24 offset:19712\n"
"s_waitcnt lgkmcnt(15)\n"
"v_mfma_f32_16x16x32_bf16 v[204:207], a[140:143], a[60:63], v[204:207]\n"
"ds_read_b128 a[20:23], v18 offset:2048\n"
"s_waitcnt lgkmcnt(12)\n"
"v_mfma_f32_16x16x32_bf16 v[208:211], a[144:147], a[32:35], v[208:211]\n"
"ds_read_b128 a[8:11], v17 offset:4096\n"
"v_fmac_f32 v112, v38, v42\n"
"v_mfma_f32_16x16x32_bf16 v[208:211], a[148:151], a[48:51], v[208:211]\n"
"ds_read_b128 a[24:27], v18 offset:4096\n"
"v_fmac_f32 v113, v39, v42\n"
"v_mfma_f32_16x16x32_bf16 v[212:215], a[144:147], a[36:39], v[212:215]\n"
"ds_read_b128 a[12:15], v17 offset:6144\n"
"v_fmac_f32 v114, v40, v42\n"
"v_mfma_f32_16x16x32_bf16 v[212:215], a[148:151], a[52:55], v[212:215]\n"
"buffer_load_dwordx4 v12, s[8:11], s49 offen offset:2048 lds\n"
"v_fmac_f32 v115, v41, v42\n"
"v_mfma_f32_16x16x32_bf16 v[216:219], a[144:147], a[40:43], v[216:219]\n"
"s_add_u32 s49, s49, 393216\n"
"ds_read_b128 a[28:31], v18 offset:6144\n"
"v_mfma_f32_16x16x32_bf16 v[216:219], a[148:151], a[56:59], v[216:219]\n"
"ds_read_b128 v[34:37], v44 offset:384\n"
"v_fmac_f32 v128, v38, v43\n"
"v_mfma_f32_16x16x32_bf16 v[220:223], a[144:147], a[44:47], v[220:223]\n"
"v_fmac_f32 v129, v39, v43\n"
"v_fmac_f32 v130, v40, v43\n"
"v_mfma_f32_16x16x32_bf16 v[220:223], a[148:151], a[60:63], v[220:223]\n"
"s_waitcnt vmcnt(7) lgkmcnt(6)\n"
"s_barrier\n"
"v_mfma_f32_16x16x32_bf16 v[224:227], a[152:155], a[32:35], v[224:227]\n"
"ds_read_b64_tr_b16 a[64:65], v19 offset:1024\n"
"v_fmac_f32 v131, v41, v43\n"
"v_mfma_f32_16x16x32_bf16 v[224:227], a[156:159], a[48:51], v[224:227]\n"
"ds_read_b64_tr_b16 a[66:67], v19 offset:1280\n"
"v_mfma_f32_16x16x32_bf16 v[228:231], a[152:155], a[36:39], v[228:231]\n"
"ds_read_b64_tr_b16 a[68:69], v19 offset:14336\n"
"s_add_u32 m0, s93, 32768\n"
"v_mfma_f32_16x16x32_bf16 v[228:231], a[156:159], a[52:55], v[228:231]\n"
"ds_read_b64_tr_b16 a[70:71], v19 offset:14592\n"
"s_waitcnt lgkmcnt(4)\n"
"v_mfma_f32_16x16x32_bf16 v[232:235], a[152:155], a[40:43], v[232:235]\n"
"ds_read_b64_tr_b16 a[72:73], v20 offset:1024\n"
"v_fmac_f32 v144, v34, v26\n"
"v_mfma_f32_16x16x32_bf16 v[232:235], a[156:159], a[56:59], v[232:235]\n"
"ds_read_b64_tr_b16 a[74:75], v20 offset:1280\n"
"v_fmac_f32 v145, v35, v26\n"
"v_mfma_f32_16x16x32_bf16 v[236:239], a[152:155], a[44:47], v[236:239]\n"
"ds_read_b64_tr_b16 a[76:77], v20 offset:14336\n"
"v_fmac_f32 v146, v36, v26\n"
"v_mfma_f32_16x16x32_bf16 v[236:239], a[156:159], a[60:63], v[236:239]\n"
"ds_read_b64_tr_b16 a[78:79], v20 offset:14592\n"
"v_fmac_f32 v147, v37, v26\n"
"v_mfma_f32_16x16x32_bf16 v[240:243], a[160:163], a[32:35], v[240:243]\n"
"ds_read_b64_tr_b16 a[80:81], v19 offset:2048\n"
"v_fmac_f32 v148, v34, v28\n"
"v_mfma_f32_16x16x32_bf16 v[240:243], a[164:167], a[48:51], v[240:243]\n"
"buffer_load_dwordx4 v4, s[4:7], s48 offen offset:0 lds\n"
"v_fmac_f32 v149, v35, v28\n"
"v_mfma_f32_16x16x32_bf16 v[244:247], a[160:163], a[36:39], v[244:247]\n"
"ds_read_b64_tr_b16 a[82:83], v19 offset:2304\n"
"v_fmac_f32 v150, v36, v28\n"
"v_mfma_f32_16x16x32_bf16 v[244:247], a[164:167], a[52:55], v[244:247]\n"
"buffer_load_dwordx4 v5, s[4:7], s48 offen offset:1024 lds\n"
"v_fmac_f32 v151, v37, v28\n"
"v_mfma_f32_16x16x32_bf16 v[248:251], a[160:163], a[40:43], v[248:251]\n"
"ds_read_b64_tr_b16 a[84:85], v19 offset:15360\n"
"v_fmac_f32 v152, v34, v30\n"
"v_mfma_f32_16x16x32_bf16 v[248:251], a[164:167], a[56:59], v[248:251]\n"
"ds_read_b128 v[38:41], v44 offset:1408\n"
"v_fmac_f32 v153, v35, v30\n"
"v_mfma_f32_16x16x32_bf16 v[252:255], a[160:163], a[44:47], v[252:255]\n"
"ds_read_b64_tr_b16 a[86:87], v19 offset:15616\n"
"v_fmac_f32 v154, v36, v30\n"
"v_mfma_f32_16x16x32_bf16 v[252:255], a[164:167], a[60:63], v[252:255]\n"
"s_add_u32 m0, s94, 32768\n"
"s_waitcnt lgkmcnt(9)\n"
"v_mfma_f32_16x16x32_bf16 v[80:83], a[64:67], a[0:3], v[80:83]\n"
"ds_read_b64_tr_b16 a[88:89], v20 offset:2048\n"
"v_fmac_f32 v155, v37, v30\n"
"v_mfma_f32_16x16x32_bf16 v[80:83], a[68:71], a[16:19], v[80:83]\n"
"buffer_load_dwordx4 v6, s[4:7], s48 offen offset:0 lds\n"
"v_fmac_f32 v156, v34, v32\n"
"v_mfma_f32_16x16x32_bf16 v[84:87], a[64:67], a[4:7], v[84:87]\n"
"ds_read_b64_tr_b16 a[90:91], v20 offset:2304\n"
"v_fmac_f32 v157, v35, v32\n"
"v_mfma_f32_16x16x32_bf16 v[84:87], a[68:71], a[20:23], v[84:87]\n"
"v_fmac_f32 v158, v36, v32\n"
"v_fmac_f32 v159, v37, v32\n"
"v_mfma_f32_16x16x32_bf16 v[88:91], a[64:67], a[8:11], v[88:91]\n"
"ds_read_b64_tr_b16 a[92:93], v20 offset:15360\n"
"s_waitcnt lgkmcnt(4)\n"
"v_mfma_f32_16x16x32_bf16 v[88:91], a[68:71], a[24:27], v[88:91]\n"
"ds_read_b128 v[34:37], v44 offset:1472\n"
"v_fmac_f32 v144, v38, v27\n"
"v_mfma_f32_16x16x32_bf16 v[92:95], a[64:67], a[12:15], v[92:95]\n"
"ds_read_b64_tr_b16 a[94:95], v20 offset:15616\n"
"v_fmac_f32 v145, v39, v27\n"
"v_mfma_f32_16x16x32_bf16 v[92:95], a[68:71], a[28:31], v[92:95]\n"
"v_fmac_f32 v146, v40, v27\n"
"v_mfma_f32_16x16x32_bf16 v[96:99], a[72:75], a[0:3], v[96:99]\n"
"ds_read_b64_tr_b16 a[96:97], v19 offset:0\n"
"v_fmac_f32 v147, v41, v27\n"
"v_mfma_f32_16x16x32_bf16 v[96:99], a[76:79], a[16:19], v[96:99]\n"
"buffer_load_dwordx4 v7, s[4:7], s48 offen offset:1024 lds\n"
"v_fmac_f32 v148, v38, v29\n"
"v_mfma_f32_16x16x32_bf16 v[100:103], a[72:75], a[4:7], v[100:103]\n"
"ds_read_b64_tr_b16 a[98:99], v19 offset:256\n"
"v_fmac_f32 v149, v39, v29\n"
"v_mfma_f32_16x16x32_bf16 v[100:103], a[76:79], a[20:23], v[100:103]\n"
"v_fmac_f32 v150, v40, v29\n"
"v_fmac_f32 v151, v41, v29\n"
"v_mfma_f32_16x16x32_bf16 v[104:107], a[72:75], a[8:11], v[104:107]\n"
"ds_read_b64_tr_b16 a[100:101], v19 offset:13312\n"
"s_add_u32 m0, s95, 32768\n"
"v_mfma_f32_16x16x32_bf16 v[104:107], a[76:79], a[24:27], v[104:107]\n"
"v_fmac_f32 v152, v38, v31\n"
"v_fmac_f32 v153, v39, v31\n"
"v_mfma_f32_16x16x32_bf16 v[108:111], a[72:75], a[12:15], v[108:111]\n"
"ds_read_b64_tr_b16 a[102:103], v19 offset:13568\n"
"v_fmac_f32 v154, v40, v31\n"
"v_mfma_f32_16x16x32_bf16 v[108:111], a[76:79], a[28:31], v[108:111]\n"
"v_fmac_f32 v155, v41, v31\n"
"s_waitcnt lgkmcnt(9)\n"
"v_mfma_f32_16x16x32_bf16 v[112:115], a[0:3], a[80:83], v[112:115]\n"
"ds_read_b64_tr_b16 a[104:105], v20 offset:0\n"
"v_fmac_f32 v156, v38, v33\n"
"v_mfma_f32_16x16x32_bf16 v[112:115], a[16:19], a[84:87], v[112:115]\n"
"buffer_load_dwordx4 v8, s[4:7], s48 offen offset:0 lds\n"
"v_fmac_f32 v157, v39, v33\n"
"v_mfma_f32_16x16x32_bf16 v[116:119], a[4:7], a[80:83], v[116:119]\n"
"ds_read_b64_tr_b16 a[106:107], v20 offset:256\n"
"v_fmac_f32 v158, v40, v33\n"
"v_mfma_f32_16x16x32_bf16 v[116:119], a[20:23], a[84:87], v[116:119]\n"
"buffer_load_dwordx4 v9, s[4:7], s48 offen offset:1024 lds\n"
"v_fmac_f32 v159, v41, v33\n"
"v_mfma_f32_16x16x32_bf16 v[120:123], a[8:11], a[80:83], v[120:123]\n"
"ds_read_b64_tr_b16 a[108:109], v20 offset:13312\n"
"s_add_u32 s48, s48, 128\n"
"v_mfma_f32_16x16x32_bf16 v[120:123], a[24:27], a[84:87], v[120:123]\n"
"ds_read_b128 v[38:41], v44 offset:576\n"
"s_waitcnt lgkmcnt(9)\n"
"v_mfma_f32_16x16x32_bf16 v[124:127], a[12:15], a[80:83], v[124:127]\n"
"ds_read_b64_tr_b16 a[110:111], v20 offset:13568\n"
"v_fmac_f32 v160, v34, v27\n"
"v_mfma_f32_16x16x32_bf16 v[124:127], a[28:31], a[84:87], v[124:127]\n"
"v_fmac_f32 v161, v35, v27\n"
"s_waitcnt lgkmcnt(9)\n"
"v_mfma_f32_16x16x32_bf16 v[128:131], a[0:3], a[88:91], v[128:131]\n"
"ds_read_b64_tr_b16 a[112:113], v19 offset:3072\n"
"s_add_u32 m0, s52, 29696\n"
"v_mfma_f32_16x16x32_bf16 v[128:131], a[16:19], a[92:95], v[128:131]\n"
"v_fmac_f32 v162, v36, v27\n"
"v_fmac_f32 v163, v37, v27\n"
"v_mfma_f32_16x16x32_bf16 v[132:135], a[4:7], a[88:91], v[132:135]\n"
"ds_read_b64_tr_b16 a[114:115], v19 offset:3328\n"
"v_fmac_f32 v164, v34, v29\n"
"v_mfma_f32_16x16x32_bf16 v[132:135], a[20:23], a[92:95], v[132:135]\n"
"buffer_load_dwordx4 v13, s[12:15], s50 offen offset:0 lds\n"
"v_fmac_f32 v165, v35, v29\n"
"v_mfma_f32_16x16x32_bf16 v[136:139], a[8:11], a[88:91], v[136:139]\n"
"ds_read_b64_tr_b16 a[116:117], v19 offset:16384\n"
"v_fmac_f32 v166, v36, v29\n"
"v_mfma_f32_16x16x32_bf16 v[136:139], a[24:27], a[92:95], v[136:139]\n"
"v_fmac_f32 v167, v37, v29\n"
"v_fmac_f32 v168, v34, v31\n"
"v_mfma_f32_16x16x32_bf16 v[140:143], a[12:15], a[88:91], v[140:143]\n"
"ds_read_b64_tr_b16 a[118:119], v19 offset:16640\n"
"v_fmac_f32 v169, v35, v31\n"
"v_mfma_f32_16x16x32_bf16 v[140:143], a[28:31], a[92:95], v[140:143]\n"
"v_fmac_f32 v170, v36, v31\n"
"s_waitcnt lgkmcnt(9)\n"
"v_mfma_f32_16x16x32_bf16 v[48:51], a[96:99], a[0:3], v[48:51]\n"
"ds_read_b64_tr_b16 a[120:121], v20 offset:3072\n"
"v_fmac_f32 v171, v37, v31\n"
"v_mfma_f32_16x16x32_bf16 v[48:51], a[100:103], a[16:19], v[48:51]\n"
"buffer_load_dwordx4 v14, s[12:15], s50 offen offset:1024 lds\n"
"v_fmac_f32 v172, v34, v33\n"
"v_mfma_f32_16x16x32_bf16 v[52:55], a[96:99], a[4:7], v[52:55]\n"
"ds_read_b64_tr_b16 a[122:123], v20 offset:3328\n"
"v_fmac_f32 v173, v35, v33\n"
"v_mfma_f32_16x16x32_bf16 v[52:55], a[100:103], a[20:23], v[52:55]\n"
"v_fmac_f32 v174, v36, v33\n"
"v_fmac_f32 v175, v37, v33\n"
"v_mfma_f32_16x16x32_bf16 v[56:59], a[96:99], a[8:11], v[56:59]\n"
"ds_read_b64_tr_b16 a[124:125], v20 offset:16384\n"
"s_waitcnt lgkmcnt(8)\n"
"v_mfma_f32_16x16x32_bf16 v[56:59], a[100:103], a[24:27], v[56:59]\n"
"buffer_load_dwordx4 v15, s[12:15], s50 offen offset:2048 lds\n"
"v_fmac_f32 v192, v38, v26\n"
"v_mfma_f32_16x16x32_bf16 v[60:63], a[96:99], a[12:15], v[60:63]\n"
"ds_read_b64_tr_b16 a[126:127], v20 offset:16640\n"
"v_fmac_f32 v193, v39, v26\n"
"v_mfma_f32_16x16x32_bf16 v[60:63], a[100:103], a[28:31], v[60:63]\n"
"ds_read_b128 v[34:37], v45 offset:1088\n"
"s_waitcnt lgkmcnt(9)\n"
"v_mfma_f32_16x16x32_bf16 v[64:67], a[104:107], a[0:3], v[64:67]\n"
"ds_read_b64_tr_b16 a[128:129], v19 offset:4096\n"
"v_fmac_f32 v194, v40, v26\n"
"v_mfma_f32_16x16x32_bf16 v[64:67], a[108:111], a[16:19], v[64:67]\n"
"v_fmac_f32 v195, v41, v26\n"
"v_fmac_f32 v196, v38, v28\n"
"v_mfma_f32_16x16x32_bf16 v[68:71], a[104:107], a[4:7], v[68:71]\n"
"ds_read_b64_tr_b16 a[130:131], v19 offset:4352\n"
"v_fmac_f32 v197, v39, v28\n"
"v_mfma_f32_16x16x32_bf16 v[68:71], a[108:111], a[20:23], v[68:71]\n"
"s_mov_b32 exec_hi, 0\n"
"buffer_load_dwordx4 v16, s[12:15], s50 offen offset:3072 lds\n"
"s_mov_b32 exec_hi, -1\n"
"v_mfma_f32_16x16x32_bf16 v[72:75], a[104:107], a[8:11], v[72:75]\n"
"ds_read_b64_tr_b16 a[132:133], v19 offset:17408\n"
".Lpad6:\n"
"s_cbranch_execz .Lwr0\n"
"s_add_u32 s50, s50, 458752\n"
"v_mfma_f32_16x16x32_bf16 v[72:75], a[108:111], a[24:27], v[72:75]\n"
"v_fmac_f32 v198, v40, v28\n"
"v_fmac_f32 v199, v41, v28\n"
"v_mfma_f32_16x16x32_bf16 v[76:79], a[104:107], a[12:15], v[76:79]\n"
"ds_read_b64_tr_b16 a[134:135], v19 offset:17664\n"
"v_lshlrev_b32 v46, 7, v0\n"
"v_mfma_f32_16x16x32_bf16 v[76:79], a[108:111], a[28:31], v[76:79]\n"
"v_fmac_f32 v200, v38, v30\n"
"s_waitcnt lgkmcnt(9)\n"
"v_mfma_f32_16x16x32_bf16 v[144:147], a[112:115], a[0:3], v[144:147]\n"
"ds_read_b64_tr_b16 a[136:137], v20 offset:4096\n"
"s_add_u32 m0, s92, 0\n"
"v_mfma_f32_16x16x32_bf16 v[144:147], a[116:119], a[16:19], v[144:147]\n"
"s_mov_b64 exec, 31\n"
"buffer_load_dword a252, v46, s[0:3], s97 offen\n"
"s_mov_b64 exec, -1\n"
"v_mfma_f32_16x16x32_bf16 v[148:151], a[112:115], a[4:7], v[148:151]\n"
"ds_read_b64_tr_b16 a[138:139], v20 offset:4352\n"
"v_fmac_f32 v201, v39, v30\n"
"v_mfma_f32_16x16x32_bf16 v[148:151], a[116:119], a[20:23], v[148:151]\n"
"buffer_load_dwordx4 v2, s[4:7], s51 offen offset:0 lds\n"
"v_fmac_f32 v202, v40, v30\n"
"v_mfma_f32_16x16x32_bf16 v[152:155], a[112:115], a[8:11], v[152:155]\n"
"ds_read_b64_tr_b16 a[140:141], v20 offset:17408\n"
"v_fmac_f32 v203, v41, v30\n"
"v_mfma_f32_16x16x32_bf16 v[152:155], a[116:119], a[24:27], v[152:155]\n"
"buffer_load_dwordx4 v3, s[4:7], s51 offen offset:1024 lds\n"
"v_fmac_f32 v204, v38, v32\n"
"v_mfma_f32_16x16x32_bf16 v[156:159], a[112:115], a[12:15], v[156:159]\n"
"ds_read_b64_tr_b16 a[142:143], v20 offset:17664\n"
"s_add_u32 s51, s51, 128\n"
"v_mfma_f32_16x16x32_bf16 v[156:159], a[116:119], a[28:31], v[156:159]\n"
"v_fmac_f32 v205, v39, v32\n"
"s_waitcnt lgkmcnt(9)\n"
"v_mfma_f32_16x16x32_bf16 v[160:163], a[120:123], a[0:3], v[160:163]\n"
"ds_read_b64_tr_b16 a[144:145], v19 offset:5120\n"
"v_fmac_f32 v206, v40, v32\n"
"v_mfma_f32_16x16x32_bf16 v[160:163], a[124:127], a[16:19], v[160:163]\n"
"v_fmac_f32 v207, v41, v32\n"
"s_waitcnt lgkmcnt(9)\n"
"v_mfma_f32_16x16x32_bf16 v[164:167], a[120:123], a[4:7], v[164:167]\n"
"ds_read_b64_tr_b16 a[146:147], v19 offset:5376\n"
"v_fmac_f32 v116, v34, v42\n"
"v_mfma_f32_16x16x32_bf16 v[164:167], a[124:127], a[20:23], v[164:167]\n"
"ds_read_b128 v[38:41], v45 offset:1152\n"
"v_fmac_f32 v117, v35, v42\n"
"v_mfma_f32_16x16x32_bf16 v[168:171], a[120:123], a[8:11], v[168:171]\n"
"ds_read_b64_tr_b16 a[148:149], v19 offset:18432\n"
"v_fmac_f32 v118, v36, v42\n"
"v_mfma_f32_16x16x32_bf16 v[168:171], a[124:127], a[24:27], v[168:171]\n"
"v_fmac_f32 v119, v37, v42\n"
"v_fmac_f32 v132, v34, v43\n"
"v_mfma_f32_16x16x32_bf16 v[172:175], a[120:123], a[12:15], v[172:175]\n"
"ds_read_b64_tr_b16 a[150:151], v19 offset:18688\n"
"v_fmac_f32 v133, v35, v43\n"
"v_mfma_f32_16x16x32_bf16 v[172:175], a[124:127], a[28:31], v[172:175]\n"
"v_fmac_f32 v134, v36, v43\n"
"s_waitcnt lgkmcnt(9)\n"
"v_mfma_f32_16x16x32_bf16 v[176:179], a[128:131], a[0:3], v[176:179]\n"
"ds_read_b64_tr_b16 a[152:153], v20 offset:5120\n"
"v_fmac_f32 v135, v37, v43\n"
"v_mfma_f32_16x16x32_bf16 v[176:179], a[132:135], a[16:19], v[176:179]\n"
"ds_read_b128 v[34:37], v45 offset:1216\n"
"s_waitcnt lgkmcnt(4)\n"
"v_mfma_f32_16x16x32_bf16 v[180:183], a[128:131], a[4:7], v[180:183]\n"
"ds_read_b64_tr_b16 a[154:155], v20 offset:5376\n"
"v_fmac_f32 v120, v38, v42\n"
"v_mfma_f32_16x16x32_bf16 v[180:183], a[132:135], a[20:23], v[180:183]\n"
"s_waitcnt vmcnt(7)\n"
"v_fmac_f32 v121, v39, v42\n"
"v_mfma_f32_16x16x32_bf16 v[184:187], a[128:131], a[8:11], v[184:187]\n"
"ds_read_b64_tr_b16 a[156:157], v20 offset:18432\n"
"s_add_u32 m0, s52, 53248\n"
"v_mfma_f32_16x16x32_bf16 v[184:187], a[132:135], a[24:27], v[184:187]\n"
"ds_read_b128 a[32:35], v17 offset:32768\n"
"v_fmac_f32 v122, v40, v42\n"
"v_mfma_f32_16x16x32_bf16 v[188:191], a[128:131], a[12:15], v[188:191]\n"
"ds_read_b64_tr_b16 a[158:159], v20 offset:18688\n"
"v_fmac_f32 v123, v41, v42\n"
"v_mfma_f32_16x16x32_bf16 v[188:191], a[132:135], a[28:31], v[188:191]\n"
"ds_read_b128 a[48:51], v18 offset:32768\n"
"v_mfma_f32_16x16x32_bf16 v[192:195], a[136:139], a[0:3], v[192:195]\n"
"ds_read_b64_tr_b16 a[160:161], v21 offset:6144\n"
"v_fmac_f32 v136, v38, v43\n"
"v_mfma_f32_16x16x32_bf16 v[192:195], a[140:143], a[16:19], v[192:195]\n"
"buffer_load_dwordx4 v10, s[8:11], s49 offen offset:0 lds\n"
"v_fmac_f32 v137, v39, v43\n"
"v_mfma_f32_16x16x32_bf16 v[196:199], a[136:139], a[4:7], v[196:199]\n"
"ds_read_b64_tr_b16 a[162:163], v21 offset:6400\n"
"v_fmac_f32 v138, v40, v43\n"
"v_mfma_f32_16x16x32_bf16 v[196:199], a[140:143], a[20:23], v[196:199]\n"
"ds_read_b128 a[36:39], v17 offset:34816\n"
"v_fmac_f32 v139, v41, v43\n"
"v_mfma_f32_16x16x32_bf16 v[200:203], a[136:139], a[8:11], v[200:203]\n"
"ds_read_b64_tr_b16 a[164:165], v21 offset:19456\n"
"s_waitcnt lgkmcnt(9)\n"
"v_mfma_f32_16x16x32_bf16 v[200:203], a[140:143], a[24:27], v[200:203]\n"
"buffer_load_dwordx4 v11, s[8:11], s49 offen offset:1024 lds\n"
"v_fmac_f32 v124, v34, v42\n"
"v_mfma_f32_16x16x32_bf16 v[204:207], a[136:139], a[12:15], v[204:207]\n"
"ds_read_b64_tr_b16 a[166:167], v21 offset:19712\n"
"v_fmac_f32 v125, v35, v42\n"
"v_mfma_f32_16x16x32_bf16 v[204:207], a[140:143], a[28:31], v[204:207]\n"
"ds_read_b128 a[52:55], v18 offset:34816\n"
"v_mfma_f32_16x16x32_bf16 v[208:211], a[144:147], a[0:3], v[208:211]\n"
"ds_read_b128 a[40:43], v17 offset:36864\n"
"v_fmac_f32 v126, v36, v42\n"
"v_mfma_f32_16x16x32_bf16 v[208:211], a[148:151], a[16:19], v[208:211]\n"
"ds_read_b128 a[56:59], v18 offset:36864\n"
"v_fmac_f32 v127, v37, v42\n"
"v_mfma_f32_16x16x32_bf16 v[212:215], a[144:147], a[4:7], v[212:215]\n"
"ds_read_b128 a[44:47], v17 offset:38912\n"
"v_fmac_f32 v140, v34, v43\n"
"v_mfma_f32_16x16x32_bf16 v[212:215], a[148:151], a[20:23], v[212:215]\n"
"buffer_load_dwordx4 v12, s[8:11], s49 offen offset:2048 lds\n"
"v_fmac_f32 v141, v35, v43\n"
"v_mfma_f32_16x16x32_bf16 v[216:219], a[144:147], a[8:11], v[216:219]\n"
"s_add_u32 s49, s49, 393216\n"
"ds_read_b128 a[60:63], v18 offset:38912\n"
"v_mfma_f32_16x16x32_bf16 v[216:219], a[148:151], a[24:27], v[216:219]\n"
"ds_read_b128 v[38:41], v44 offset:512\n"
"v_fmac_f32 v142, v36, v43\n"
"v_mfma_f32_16x16x32_bf16 v[220:223], a[144:147], a[12:15], v[220:223]\n"
"v_fmac_f32 v143, v37, v43\n"
"v_mfma_f32_16x16x32_bf16 v[220:223], a[148:151], a[28:31], v[220:223]\n"
"s_waitcnt vmcnt(6) lgkmcnt(6)\n"
"s_barrier\n"
"v_mfma_f32_16x16x32_bf16 v[224:227], a[152:155], a[0:3], v[224:227]\n"
"ds_read_b64_tr_b16 a[64:65], v19 offset:27648\n"
"v_mfma_f32_16x16x32_bf16 v[224:227], a[156:159], a[16:19], v[224:227]\n"
"ds_read_b64_tr_b16 a[66:67], v19 offset:27904\n"
"v_mfma_f32_16x16x32_bf16 v[228:231], a[152:155], a[4:7], v[228:231]\n"
"ds_read_b64_tr_b16 a[68:69], v19 offset:40960\n"
"s_add_u32 m0, s93, 0\n"
"v_mfma_f32_16x16x32_bf16 v[228:231], a[156:159], a[20:23], v[228:231]\n"
"ds_read_b64_tr_b16 a[70:71], v19 offset:41216\n"
"s_waitcnt lgkmcnt(4)\n"
"v_mfma_f32_16x16x32_bf16 v[232:235], a[152:155], a[8:11], v[232:235]\n"
"ds_read_b64_tr_b16 a[72:73], v20 offset:27648\n"
"v_fmac_f32 v176, v38, v26\n"
"v_mfma_f32_16x16x32_bf16 v[232:235], a[156:159], a[24:27], v[232:235]\n"
"ds_read_b64_tr_b16 a[74:75], v20 offset:27904\n"
"v_fmac_f32 v177, v39, v26\n"
"v_mfma_f32_16x16x32_bf16 v[236:239], a[152:155], a[12:15], v[236:239]\n"
"ds_read_b64_tr_b16 a[76:77], v20 offset:40960\n"
"v_fmac_f32 v178, v40, v26\n"
"v_mfma_f32_16x16x32_bf16 v[236:239], a[156:159], a[28:31], v[236:239]\n"
"ds_read_b64_tr_b16 a[78:79], v20 offset:41216\n"
"v_fmac_f32 v179, v41, v26\n"
"v_mfma_f32_16x16x32_bf16 v[240:243], a[160:163], a[0:3], v[240:243]\n"
"ds_read_b64_tr_b16 a[80:81], v19 offset:28672\n"
"v_fmac_f32 v180, v38, v28\n"
"v_mfma_f32_16x16x32_bf16 v[240:243], a[164:167], a[16:19], v[240:243]\n"
"buffer_load_dwordx4 v4, s[4:7], s48 offen offset:0 lds\n"
"v_fmac_f32 v181, v39, v28\n"
"v_mfma_f32_16x16x32_bf16 v[244:247], a[160:163], a[4:7], v[244:247]\n"
"ds_read_b64_tr_b16 a[82:83], v19 offset:28928\n"
"v_fmac_f32 v182, v40, v28\n"
"v_mfma_f32_16x16x32_bf16 v[244:247], a[164:167], a[20:23], v[244:247]\n"
"buffer_load_dwordx4 v5, s[4:7], s48 offen offset:1024 lds\n"
"v_fmac_f32 v183, v41, v28\n"
"v_mfma_f32_16x16x32_bf16 v[248:251], a[160:163], a[8:11], v[248:251]\n"
"ds_read_b64_tr_b16 a[84:85], v19 offset:41984\n"
"s_add_u32 m0, s94, 0\n"
"v_mfma_f32_16x16x32_bf16 v[248:251], a[164:167], a[24:27], v[248:251]\n"
"buffer_load_dwordx4 v6, s[4:7], s48 offen offset:0 lds\n"
"v_fmac_f32 v184, v38, v30\n"
"v_mfma_f32_16x16x32_bf16 v[252:255], a[160:163], a[12:15], v[252:255]\n"
"ds_read_b64_tr_b16 a[86:87], v19 offset:42240\n"
"v_fmac_f32 v185, v39, v30\n"
"v_mfma_f32_16x16x32_bf16 v[252:255], a[164:167], a[28:31], v[252:255]\n"
"ds_read_b128 v[34:37], v44 offset:1536\n"
"s_waitcnt lgkmcnt(9)\n"
"v_mfma_f32_16x16x32_bf16 v[80:83], a[64:67], a[32:35], v[80:83]\n"
"ds_read_b64_tr_b16 a[88:89], v20 offset:28672\n"
"v_fmac_f32 v186, v40, v30\n"
"v_mfma_f32_16x16x32_bf16 v[80:83], a[68:71], a[48:51], v[80:83]\n"
"buffer_load_dwordx4 v7, s[4:7], s48 offen offset:1024 lds\n"
"v_fmac_f32 v187, v41, v30\n"
"v_mfma_f32_16x16x32_bf16 v[84:87], a[64:67], a[36:39], v[84:87]\n"
"ds_read_b64_tr_b16 a[90:91], v20 offset:28928\n"
"s_add_u32 m0, s95, 0\n"
"v_mfma_f32_16x16x32_bf16 v[84:87], a[68:71], a[52:55], v[84:87]\n"
"v_fmac_f32 v188, v38, v32\n"
"v_fmac_f32 v189, v39, v32\n"
"v_mfma_f32_16x16x32_bf16 v[88:91], a[64:67], a[40:43], v[88:91]\n"
"ds_read_b64_tr_b16 a[92:93], v20 offset:41984\n"
"v_fmac_f32 v190, v40, v32\n"
"v_mfma_f32_16x16x32_bf16 v[88:91], a[68:71], a[56:59], v[88:91]\n"
"buffer_load_dwordx4 v8, s[4:7], s48 offen offset:0 lds\n"
"v_fmac_f32 v191, v41, v32\n"
"v_mfma_f32_16x16x32_bf16 v[92:95], a[64:67], a[44:47], v[92:95]\n"
"ds_read_b64_tr_b16 a[94:95], v20 offset:42240\n"
"s_waitcnt lgkmcnt(4)\n"
"v_mfma_f32_16x16x32_bf16 v[92:95], a[68:71], a[60:63], v[92:95]\n"
"ds_read_b128 v[38:41], v44 offset:1600\n"
"v_mfma_f32_16x16x32_bf16 v[96:99], a[72:75], a[32:35], v[96:99]\n"
"ds_read_b64_tr_b16 a[96:97], v19 offset:26624\n"
"v_fmac_f32 v176, v34, v27\n"
"v_mfma_f32_16x16x32_bf16 v[96:99], a[76:79], a[48:51], v[96:99]\n"
"buffer_load_dwordx4 v9, s[4:7], s48 offen offset:1024 lds\n"
"v_fmac_f32 v177, v35, v27\n"
"v_mfma_f32_16x16x32_bf16 v[100:103], a[72:75], a[36:39], v[100:103]\n"
"ds_read_b64_tr_b16 a[98:99], v19 offset:26880\n"
"s_add_u32 s48, s48, 128\n"
"v_mfma_f32_16x16x32_bf16 v[100:103], a[76:79], a[52:55], v[100:103]\n"
"s_add_u32 m0, s92, 32768\n"
"v_fmac_f32 v178, v36, v27\n"
"v_mfma_f32_16x16x32_bf16 v[104:107], a[72:75], a[40:43], v[104:107]\n"
"ds_read_b64_tr_b16 a[100:101], v19 offset:39936\n"
"v_fmac_f32 v179, v37, v27\n"
"v_mfma_f32_16x16x32_bf16 v[104:107], a[76:79], a[56:59], v[104:107]\n"
"buffer_load_dwordx4 v2, s[4:7], s51 offen offset:0 lds\n"
"v_fmac_f32 v180, v34, v29\n"
"v_mfma_f32_16x16x32_bf16 v[108:111], a[72:75], a[44:47], v[108:111]\n"
"ds_read_b64_tr_b16 a[102:103], v19 offset:40192\n"
"v_fmac_f32 v181, v35, v29\n"
"v_mfma_f32_16x16x32_bf16 v[108:111], a[76:79], a[60:63], v[108:111]\n"
"v_fmac_f32 v182, v36, v29\n"
"v_mfma_f32_16x16x32_bf16 v[112:115], a[32:35], a[80:83], v[112:115]\n"
"ds_read_b64_tr_b16 a[104:105], v20 offset:26624\n"
"v_fmac_f32 v183, v37, v29\n"
"v_mfma_f32_16x16x32_bf16 v[112:115], a[48:51], a[84:87], v[112:115]\n"
"buffer_load_dwordx4 v3, s[4:7], s51 offen offset:1024 lds\n"
"v_fmac_f32 v184, v34, v31\n"
"v_mfma_f32_16x16x32_bf16 v[116:119], a[36:39], a[80:83], v[116:119]\n"
"ds_read_b64_tr_b16 a[106:107], v20 offset:26880\n"
"s_add_u32 s51, s51, 128\n"
"v_mfma_f32_16x16x32_bf16 v[116:119], a[52:55], a[84:87], v[116:119]\n"
"s_add_u32 m0, s52, 0\n"
"v_fmac_f32 v185, v35, v31\n"
"v_mfma_f32_16x16x32_bf16 v[120:123], a[40:43], a[80:83], v[120:123]\n"
"ds_read_b64_tr_b16 a[108:109], v20 offset:39936\n"
"v_fmac_f32 v186, v36, v31\n"
"v_mfma_f32_16x16x32_bf16 v[120:123], a[56:59], a[84:87], v[120:123]\n"
"buffer_load_dwordx4 v10, s[8:11], s49 offen offset:0 lds\n"
"v_fmac_f32 v187, v37, v31\n"
"v_mfma_f32_16x16x32_bf16 v[124:127], a[44:47], a[80:83], v[124:127]\n"
"ds_read_b64_tr_b16 a[110:111], v20 offset:40192\n"
"v_fmac_f32 v188, v34, v33\n"
"v_mfma_f32_16x16x32_bf16 v[124:127], a[60:63], a[84:87], v[124:127]\n"
"v_fmac_f32 v189, v35, v33\n"
"s_waitcnt lgkmcnt(9)\n"
"v_mfma_f32_16x16x32_bf16 v[128:131], a[32:35], a[88:91], v[128:131]\n"
"ds_read_b64_tr_b16 a[112:113], v19 offset:29696\n"
"v_fmac_f32 v190, v36, v33\n"
"v_mfma_f32_16x16x32_bf16 v[128:131], a[48:51], a[92:95], v[128:131]\n"
"buffer_load_dwordx4 v11, s[8:11], s49 offen offset:1024 lds\n"
"v_fmac_f32 v191, v37, v33\n"
"v_mfma_f32_16x16x32_bf16 v[132:135], a[36:39], a[88:91], v[132:135]\n"
"ds_read_b64_tr_b16 a[114:115], v19 offset:29952\n"
"s_waitcnt lgkmcnt(10)\n"
"v_mfma_f32_16x16x32_bf16 v[132:135], a[52:55], a[92:95], v[132:135]\n"
"ds_read_b128 v[34:37], v44 offset:768\n"
"v_fmac_f32 v192, v38, v27\n"
"v_mfma_f32_16x16x32_bf16 v[136:139], a[40:43], a[88:91], v[136:139]\n"
"ds_read_b64_tr_b16 a[116:117], v19 offset:43008\n"
"v_fmac_f32 v193, v39, v27\n"
"v_mfma_f32_16x16x32_bf16 v[136:139], a[56:59], a[92:95], v[136:139]\n"
"v_fmac_f32 v194, v40, v27\n"
"v_fmac_f32 v195, v41, v27\n"
"v_mfma_f32_16x16x32_bf16 v[140:143], a[44:47], a[88:91], v[140:143]\n"
"ds_read_b64_tr_b16 a[118:119], v19 offset:43264\n"
"v_fmac_f32 v196, v38, v29\n"
"v_mfma_f32_16x16x32_bf16 v[140:143], a[60:63], a[92:95], v[140:143]\n"
"v_fmac_f32 v197, v39, v29\n"
"s_waitcnt lgkmcnt(9)\n"
"v_mfma_f32_16x16x32_bf16 v[48:51], a[96:99], a[32:35], v[48:51]\n"
"ds_read_b64_tr_b16 a[120:121], v20 offset:29696\n"
"v_fmac_f32 v198, v40, v29\n"
"v_mfma_f32_16x16x32_bf16 v[48:51], a[100:103], a[48:51], v[48:51]\n"
"buffer_load_dwordx4 v12, s[8:11], s49 offen offset:2048 lds\n"
"v_fmac_f32 v199, v41, v29\n"
"v_mfma_f32_16x16x32_bf16 v[52:55], a[96:99], a[36:39], v[52:55]\n"
"ds_read_b64_tr_b16 a[122:123], v20 offset:29952\n"
"s_add_u32 s49, s49, 393216\n"
"v_mfma_f32_16x16x32_bf16 v[52:55], a[100:103], a[52:55], v[52:55]\n"
"v_fmac_f32 v200, v38, v31\n"
"v_fmac_f32 v201, v39, v31\n"
"v_mfma_f32_16x16x32_bf16 v[56:59], a[96:99], a[40:43], v[56:59]\n"
"ds_read_b64_tr_b16 a[124:125], v20 offset:43008\n"
"v_fmac_f32 v202, v40, v31\n"
"v_mfma_f32_16x16x32_bf16 v[56:59], a[100:103], a[56:59], v[56:59]\n"
"v_fmac_f32 v203, v41, v31\n"
"v_fmac_f32 v204, v38, v33\n"
"v_mfma_f32_16x16x32_bf16 v[60:63], a[96:99], a[44:47], v[60:63]\n"
"ds_read_b64_tr_b16 a[126:127], v20 offset:43264\n"
"v_fmac_f32 v205, v39, v33\n"
"v_mfma_f32_16x16x32_bf16 v[60:63], a[100:103], a[60:63], v[60:63]\n"
"v_fmac_f32 v206, v40, v33\n"
"s_waitcnt lgkmcnt(9)\n"
"v_mfma_f32_16x16x32_bf16 v[64:67], a[104:107], a[32:35], v[64:67]\n"
"ds_read_b64_tr_b16 a[128:129], v19 offset:30720\n"
"s_add_u32 m0, s93, 32768\n"
"v_mfma_f32_16x16x32_bf16 v[64:67], a[108:111], a[48:51], v[64:67]\n"
"v_fmac_f32 v207, v41, v33\n"
"s_waitcnt lgkmcnt(7)\n"
"v_mfma_f32_16x16x32_bf16 v[68:71], a[104:107], a[36:39], v[68:71]\n"
"ds_read_b64_tr_b16 a[130:131], v19 offset:30976\n"
"v_fmac_f32 v240, v34, v26\n"
"v_mfma_f32_16x16x32_bf16 v[68:71], a[108:111], a[52:55], v[68:71]\n"
"buffer_load_dwordx4 v4, s[4:7], s48 offen offset:0 lds\n"
"v_fmac_f32 v241, v35, v26\n"
"v_mfma_f32_16x16x32_bf16 v[72:75], a[104:107], a[40:43], v[72:75]\n"
"ds_read_b64_tr_b16 a[132:133], v19 offset:44032\n"
"v_fmac_f32 v242, v36, v26\n"
"v_mfma_f32_16x16x32_bf16 v[72:75], a[108:111], a[56:59], v[72:75]\n"
"ds_read_b128 v[38:41], v44 offset:1792\n"
"v_fmac_f32 v243, v37, v26\n"
"v_mfma_f32_16x16x32_bf16 v[76:79], a[104:107], a[44:47], v[76:79]\n"
"ds_read_b64_tr_b16 a[134:135], v19 offset:44288\n"
"v_fmac_f32 v244, v34, v28\n"
"v_mfma_f32_16x16x32_bf16 v[76:79], a[108:111], a[60:63], v[76:79]\n"
"v_fmac_f32 v245, v35, v28\n"
"s_waitcnt lgkmcnt(9)\n"
"v_mfma_f32_16x16x32_bf16 v[144:147], a[112:115], a[32:35], v[144:147]\n"
"ds_read_b64_tr_b16 a[136:137], v20 offset:30720\n"
"v_fmac_f32 v246, v36, v28\n"
"v_mfma_f32_16x16x32_bf16 v[144:147], a[116:119], a[48:51], v[144:147]\n"
"buffer_load_dwordx4 v5, s[4:7], s48 offen offset:1024 lds\n"
"v_fmac_f32 v247, v37, v28\n"
"v_mfma_f32_16x16x32_bf16 v[148:151], a[112:115], a[36:39], v[148:151]\n"
"ds_read_b64_tr_b16 a[138:139], v20 offset:30976\n"
"v_fmac_f32 v248, v34, v30\n"
"v_mfma_f32_16x16x32_bf16 v[148:151], a[116:119], a[52:55], v[148:151]\n"
"s_add_u32 m0, s94, 32768\n"
"v_fmac_f32 v249, v35, v30\n"
"v_mfma_f32_16x16x32_bf16 v[152:155], a[112:115], a[40:43], v[152:155]\n"
"ds_read_b64_tr_b16 a[140:141], v20 offset:44032\n"
"v_fmac_f32 v250, v36, v30\n"
"v_mfma_f32_16x16x32_bf16 v[152:155], a[116:119], a[56:59], v[152:155]\n"
"buffer_load_dwordx4 v6, s[4:7], s48 offen offset:0 lds\n"
"v_fmac_f32 v251, v37, v30\n"
"v_mfma_f32_16x16x32_bf16 v[156:159], a[112:115], a[44:47], v[156:159]\n"
"ds_read_b64_tr_b16 a[142:143], v20 offset:44288\n"
"v_fmac_f32 v252, v34, v32\n"
"v_mfma_f32_16x16x32_bf16 v[156:159], a[116:119], a[60:63], v[156:159]\n"
"v_fmac_f32 v253, v35, v32\n"
"s_waitcnt lgkmcnt(9)\n"
"v_mfma_f32_16x16x32_bf16 v[160:163], a[120:123], a[32:35], v[160:163]\n"
"ds_read_b64_tr_b16 a[144:145], v19 offset:31744\n"
"v_fmac_f32 v254, v36, v32\n"
"v_mfma_f32_16x16x32_bf16 v[160:163], a[124:127], a[48:51], v[160:163]\n"
"v_fmac_f32 v255, v37, v32\n"
"s_waitcnt lgkmcnt(6)\n"
"v_mfma_f32_16x16x32_bf16 v[164:167], a[120:123], a[36:39], v[164:167]\n"
"ds_read_b64_tr_b16 a[146:147], v19 offset:32000\n"
"v_fmac_f32 v240, v38, v27\n"
"v_mfma_f32_16x16x32_bf16 v[164:167], a[124:127], a[52:55], v[164:167]\n"
"buffer_load_dwordx4 v7, s[4:7], s48 offen offset:1024 lds\n"
"v_fmac_f32 v241, v39, v27\n"
"v_mfma_f32_16x16x32_bf16 v[168:171], a[120:123], a[40:43], v[168:171]\n"
"ds_read_b64_tr_b16 a[148:149], v19 offset:45056\n"
"v_fmac_f32 v242, v40, v27\n"
"v_mfma_f32_16x16x32_bf16 v[168:171], a[124:127], a[56:59], v[168:171]\n"
"ds_read_b128 v[34:37], v44 offset:704\n"
"v_fmac_f32 v243, v41, v27\n"
"v_mfma_f32_16x16x32_bf16 v[172:175], a[120:123], a[44:47], v[172:175]\n"
"ds_read_b64_tr_b16 a[150:151], v19 offset:45312\n"
"s_add_u32 m0, s95, 32768\n"
"v_mfma_f32_16x16x32_bf16 v[172:175], a[124:127], a[60:63], v[172:175]\n"
"v_fmac_f32 v244, v38, v29\n"
"s_waitcnt lgkmcnt(9)\n"
"v_mfma_f32_16x16x32_bf16 v[176:179], a[128:131], a[32:35], v[176:179]\n"
"ds_read_b64_tr_b16 a[152:153], v20 offset:31744\n"
"v_fmac_f32 v245, v39, v29\n"
"v_mfma_f32_16x16x32_bf16 v[176:179], a[132:135], a[48:51], v[176:179]\n"
"buffer_load_dwordx4 v8, s[4:7], s48 offen offset:0 lds\n"
"v_fmac_f32 v246, v40, v29\n"
"v_mfma_f32_16x16x32_bf16 v[180:183], a[128:131], a[36:39], v[180:183]\n"
"ds_read_b64_tr_b16 a[154:155], v20 offset:32000\n"
"v_fmac_f32 v247, v41, v29\n"
"v_mfma_f32_16x16x32_bf16 v[180:183], a[132:135], a[52:55], v[180:183]\n"
"s_waitcnt vmcnt(10)\n"
"v_fmac_f32 v248, v38, v31\n"
"v_mfma_f32_16x16x32_bf16 v[184:187], a[128:131], a[40:43], v[184:187]\n"
"ds_read_b64_tr_b16 a[156:157], v20 offset:45056\n"
"v_fmac_f32 v249, v39, v31\n"
"v_mfma_f32_16x16x32_bf16 v[184:187], a[132:135], a[56:59], v[184:187]\n"
"buffer_load_dwordx4 v9, s[4:7], s48 offen offset:1024 lds\n"
"v_fmac_f32 v250, v40, v31\n"
"v_mfma_f32_16x16x32_bf16 v[188:191], a[128:131], a[44:47], v[188:191]\n"
"ds_read_b64_tr_b16 a[158:159], v20 offset:45312\n"
"s_add_u32 s48, s48, 128\n"
"v_mfma_f32_16x16x32_bf16 v[188:191], a[132:135], a[60:63], v[188:191]\n"
"ds_read_b128 a[0:3], v17 offset:0\n"
"s_waitcnt lgkmcnt(10)\n"
"v_mfma_f32_16x16x32_bf16 v[192:195], a[136:139], a[32:35], v[192:195]\n"
"ds_read_b64_tr_b16 a[160:161], v21 offset:32768\n"
"v_fmac_f32 v251, v41, v31\n"
"v_mfma_f32_16x16x32_bf16 v[192:195], a[140:143], a[48:51], v[192:195]\n"
"ds_read_b128 a[16:19], v18 offset:0\n"
"v_fmac_f32 v252, v38, v33\n"
"v_mfma_f32_16x16x32_bf16 v[196:199], a[136:139], a[36:39], v[196:199]\n"
"ds_read_b64_tr_b16 a[162:163], v21 offset:33024\n"
"v_fmac_f32 v253, v39, v33\n"
"v_mfma_f32_16x16x32_bf16 v[196:199], a[140:143], a[52:55], v[196:199]\n"
"ds_read_b128 a[4:7], v17 offset:2048\n"
"v_fmac_f32 v254, v40, v33\n"
"v_mfma_f32_16x16x32_bf16 v[200:203], a[136:139], a[40:43], v[200:203]\n"
"ds_read_b64_tr_b16 a[164:165], v21 offset:46080\n"
"v_fmac_f32 v255, v41, v33\n"
"v_mfma_f32_16x16x32_bf16 v[200:203], a[140:143], a[56:59], v[200:203]\n"
"ds_read_b128 a[20:23], v18 offset:2048\n"
"s_waitcnt lgkmcnt(12)\n"
"v_mfma_f32_16x16x32_bf16 v[204:207], a[136:139], a[44:47], v[204:207]\n"
"ds_read_b64_tr_b16 a[166:167], v21 offset:46336\n"
"v_fmac_f32 v224, v34, v26\n"
"v_mfma_f32_16x16x32_bf16 v[204:207], a[140:143], a[60:63], v[204:207]\n"
"ds_read_b128 a[8:11], v17 offset:4096\n"
"s_waitcnt lgkmcnt(13)\n"
"v_mfma_f32_16x16x32_bf16 v[208:211], a[144:147], a[32:35], v[208:211]\n"
"ds_read_b128 a[24:27], v18 offset:4096\n"
"v_fmac_f32 v225, v35, v26\n"
"v_mfma_f32_16x16x32_bf16 v[208:211], a[148:151], a[48:51], v[208:211]\n"
"ds_read_b128 a[12:15], v17 offset:6144\n"
"v_fmac_f32 v226, v36, v26\n"
"v_mfma_f32_16x16x32_bf16 v[212:215], a[144:147], a[36:39], v[212:215]\n"
"ds_read_b128 a[28:31], v18 offset:6144\n"
"v_fmac_f32 v227, v37, v26\n"
"v_mfma_f32_16x16x32_bf16 v[212:215], a[148:151], a[52:55], v[212:215]\n"
"ds_read_b128 v[38:41], v44 offset:1728\n"
"v_fmac_f32 v228, v34, v28\n"
"v_mfma_f32_16x16x32_bf16 v[216:219], a[144:147], a[40:43], v[216:219]\n"
"v_fmac_f32 v229, v35, v28\n"
"v_fmac_f32 v230, v36, v28\n"
"v_mfma_f32_16x16x32_bf16 v[216:219], a[148:151], a[56:59], v[216:219]\n"
"v_fmac_f32 v231, v37, v28\n"
"v_fmac_f32 v232, v34, v30\n"
"v_mfma_f32_16x16x32_bf16 v[220:223], a[144:147], a[44:47], v[220:223]\n"
"v_fmac_f32 v233, v35, v30\n"
"v_fmac_f32 v234, v36, v30\n"
"v_mfma_f32_16x16x32_bf16 v[220:223], a[148:151], a[60:63], v[220:223]\n"
"s_waitcnt vmcnt(6) lgkmcnt(5)\n"
"s_barrier\n"
"v_mfma_f32_16x16x32_bf16 v[224:227], a[152:155], a[32:35], v[224:227]\n"
"ds_read_b64_tr_b16 a[64:65], v22 offset:1024\n"
"v_fmac_f32 v235, v37, v30\n"
"v_mfma_f32_16x16x32_bf16 v[224:227], a[156:159], a[48:51], v[224:227]\n"
"ds_read_b64_tr_b16 a[66:67], v22 offset:1280\n"
"v_fmac_f32 v236, v34, v32\n"
"v_mfma_f32_16x16x32_bf16 v[228:231], a[152:155], a[36:39], v[228:231]\n"
"ds_read_b64_tr_b16 a[68:69], v22 offset:14336\n"
"s_add_u32 m0, s52, 56320\n"
"v_mfma_f32_16x16x32_bf16 v[228:231], a[156:159], a[52:55], v[228:231]\n"
"ds_read_b64_tr_b16 a[70:71], v22 offset:14592\n"
"v_mfma_f32_16x16x32_bf16 v[232:235], a[152:155], a[40:43], v[232:235]\n"
"ds_read_b64_tr_b16 a[72:73], v23 offset:1024\n"
"v_mfma_f32_16x16x32_bf16 v[232:235], a[156:159], a[56:59], v[232:235]\n"
"ds_read_b64_tr_b16 a[74:75], v23 offset:1280\n"
"v_mfma_f32_16x16x32_bf16 v[236:239], a[152:155], a[44:47], v[236:239]\n"
"ds_read_b64_tr_b16 a[76:77], v23 offset:14336\n"
"v_mfma_f32_16x16x32_bf16 v[236:239], a[156:159], a[60:63], v[236:239]\n"
"ds_read_b64_tr_b16 a[78:79], v23 offset:14592\n"
"v_mfma_f32_16x16x32_bf16 v[240:243], a[160:163], a[32:35], v[240:243]\n"
"ds_read_b64_tr_b16 a[80:81], v22 offset:2048\n"
"v_mfma_f32_16x16x32_bf16 v[240:243], a[164:167], a[48:51], v[240:243]\n"
"buffer_load_dwordx4 v13, s[12:15], s50 offen offset:0 lds\n"
"v_mfma_f32_16x16x32_bf16 v[244:247], a[160:163], a[36:39], v[244:247]\n"
"ds_read_b64_tr_b16 a[82:83], v22 offset:2304\n"
"v_mfma_f32_16x16x32_bf16 v[244:247], a[164:167], a[52:55], v[244:247]\n"
"buffer_load_dwordx4 v14, s[12:15], s50 offen offset:1024 lds\n"
"v_mfma_f32_16x16x32_bf16 v[248:251], a[160:163], a[40:43], v[248:251]\n"
"ds_read_b64_tr_b16 a[84:85], v22 offset:15360\n"
"v_mfma_f32_16x16x32_bf16 v[248:251], a[164:167], a[56:59], v[248:251]\n"
"v_mfma_f32_16x16x32_bf16 v[252:255], a[160:163], a[44:47], v[252:255]\n"
"ds_read_b64_tr_b16 a[86:87], v22 offset:15616\n"
"v_fmac_f32 v237, v35, v32\n"
"v_mfma_f32_16x16x32_bf16 v[252:255], a[164:167], a[60:63], v[252:255]\n"
"v_fmac_f32 v238, v36, v32\n"
"s_waitcnt lgkmcnt(8)\n"
"v_mfma_f32_16x16x32_bf16 v[80:83], a[64:67], a[0:3], v[80:83]\n"
"ds_read_b64_tr_b16 a[88:89], v23 offset:2048\n"
"v_fmac_f32 v239, v37, v32\n"
"v_mfma_f32_16x16x32_bf16 v[80:83], a[68:71], a[16:19], v[80:83]\n"
"buffer_load_dwordx4 v15, s[12:15], s50 offen offset:2048 lds\n"
"v_mfma_f32_16x16x32_bf16 v[84:87], a[64:67], a[4:7], v[84:87]\n"
"ds_read_b64_tr_b16 a[90:91], v23 offset:2304\n"
"v_mfma_f32_16x16x32_bf16 v[84:87], a[68:71], a[20:23], v[84:87]\n"
"ds_read_b128 v[34:37], v45 offset:0\n"
"v_fmac_f32 v224, v38, v27\n"
"v_mfma_f32_16x16x32_bf16 v[88:91], a[64:67], a[8:11], v[88:91]\n"
"ds_read_b64_tr_b16 a[92:93], v23 offset:15360\n"
"v_fmac_f32 v225, v39, v27\n"
"v_mfma_f32_16x16x32_bf16 v[88:91], a[68:71], a[24:27], v[88:91]\n"
"s_mov_b32 exec_hi, 0\n"
"buffer_load_dwordx4 v16, s[12:15], s50 offen offset:3072 lds\n"
"s_mov_b32 exec_hi, -1\n"
"v_mfma_f32_16x16x32_bf16 v[92:95], a[64:67], a[12:15], v[92:95]\n"
"ds_read_b64_tr_b16 a[94:95], v23 offset:15616\n"
"s_add_u32 s50, s50, 458752\n"
"v_mfma_f32_16x16x32_bf16 v[92:95], a[68:71], a[28:31], v[92:95]\n"
"s_waitcnt vmcnt(4)\n"
"s_waitcnt lgkmcnt(9)\n"
"v_mfma_f32_16x16x32_bf16 v[96:99], a[72:75], a[0:3], v[96:99]\n"
"ds_read_b64_tr_b16 a[96:97], v19 offset:1024\n"
"v_fmac_f32 v226, v40, v27\n"
"v_mfma_f32_16x16x32_bf16 v[96:99], a[76:79], a[16:19], v[96:99]\n"
"ds_read_b128 a[32:35], v17 offset:32768\n"
"v_fmac_f32 v227, v41, v27\n"
"v_mfma_f32_16x16x32_bf16 v[100:103], a[72:75], a[4:7], v[100:103]\n"
"ds_read_b64_tr_b16 a[98:99], v19 offset:1280\n"
"s_add_u32 m0, s52, 3072\n"
"v_mfma_f32_16x16x32_bf16 v[100:103], a[76:79], a[20:23], v[100:103]\n"
"ds_read_b128 a[48:51], v18 offset:32768\n"
"v_fmac_f32 v228, v38, v29\n"
"v_mfma_f32_16x16x32_bf16 v[104:107], a[72:75], a[8:11], v[104:107]\n"
"ds_read_b64_tr_b16 a[100:101], v19 offset:14336\n"
"v_fmac_f32 v229, v39, v29\n"
"v_mfma_f32_16x16x32_bf16 v[104:107], a[76:79], a[24:27], v[104:107]\n"
"buffer_load_dwordx4 v13, s[12:15], s50 offen offset:0 lds\n"
"v_fmac_f32 v230, v40, v29\n"
"v_mfma_f32_16x16x32_bf16 v[108:111], a[72:75], a[12:15], v[108:111]\n"
"ds_read_b64_tr_b16 a[102:103], v19 offset:14592\n"
"v_fmac_f32 v231, v41, v29\n"
"v_mfma_f32_16x16x32_bf16 v[108:111], a[76:79], a[28:31], v[108:111]\n"
"ds_read_b128 a[36:39], v17 offset:34816\n"
"s_waitcnt lgkmcnt(12)\n"
"v_mfma_f32_16x16x32_bf16 v[112:115], a[0:3], a[80:83], v[112:115]\n"
"ds_read_b64_tr_b16 a[104:105], v20 offset:1024\n"
"v_fmac_f32 v232, v38, v31\n"
"v_mfma_f32_16x16x32_bf16 v[112:115], a[16:19], a[84:87], v[112:115]\n"
"buffer_load_dwordx4 v14, s[12:15], s50 offen offset:1024 lds\n"
"v_fmac_f32 v233, v39, v31\n"
"v_mfma_f32_16x16x32_bf16 v[116:119], a[4:7], a[80:83], v[116:119]\n"
"ds_read_b64_tr_b16 a[106:107], v20 offset:1280\n"
"v_fmac_f32 v234, v40, v31\n"
"v_mfma_f32_16x16x32_bf16 v[116:119], a[20:23], a[84:87], v[116:119]\n"
"ds_read_b128 a[52:55], v18 offset:34816\n"
"v_fmac_f32 v235, v41, v31\n"
"v_mfma_f32_16x16x32_bf16 v[120:123], a[8:11], a[80:83], v[120:123]\n"
"ds_read_b64_tr_b16 a[108:109], v20 offset:14336\n"
"v_fmac_f32 v236, v38, v33\n"
"v_mfma_f32_16x16x32_bf16 v[120:123], a[24:27], a[84:87], v[120:123]\n"
"ds_read_b128 a[40:43], v17 offset:36864\n"
"v_fmac_f32 v237, v39, v33\n"
"v_mfma_f32_16x16x32_bf16 v[124:127], a[12:15], a[80:83], v[124:127]\n"
"ds_read_b64_tr_b16 a[110:111], v20 offset:14592\n"
"v_fmac_f32 v238, v40, v33\n"
"v_mfma_f32_16x16x32_bf16 v[124:127], a[28:31], a[84:87], v[124:127]\n"
"ds_read_b128 a[56:59], v18 offset:36864\n"
"s_waitcnt lgkmcnt(14)\n"
"v_mfma_f32_16x16x32_bf16 v[128:131], a[0:3], a[88:91], v[128:131]\n"
"ds_read_b64_tr_b16 a[112:113], v19 offset:2048\n"
"v_fmac_f32 v239, v41, v33\n"
"v_mfma_f32_16x16x32_bf16 v[128:131], a[16:19], a[92:95], v[128:131]\n"
"buffer_load_dwordx4 v15, s[12:15], s50 offen offset:2048 lds\n"
"v_and_b32 v46, 0xf0, v1\n"
"v_mfma_f32_16x16x32_bf16 v[132:135], a[4:7], a[88:91], v[132:135]\n"
"ds_read_b64_tr_b16 a[114:115], v19 offset:2304\n"
"v_add_u32 v46, s64, v46\n"
"v_mfma_f32_16x16x32_bf16 v[132:135], a[20:23], a[92:95], v[132:135]\n"
"ds_read_b128 a[44:47], v17 offset:38912\n"
"v_mov_b32 v25, s56\n"
"v_mfma_f32_16x16x32_bf16 v[136:139], a[8:11], a[88:91], v[136:139]\n"
"ds_read_b64_tr_b16 a[116:117], v19 offset:15360\n"
"v_mfma_f32_16x16x32_bf16 v[136:139], a[24:27], a[92:95], v[136:139]\n"
"s_mov_b32 exec_hi, 0\n"
"buffer_load_dwordx4 v16, s[12:15], s50 offen offset:3072 lds\n"
"s_mov_b32 exec_hi, -1\n"
"v_mfma_f32_16x16x32_bf16 v[140:143], a[12:15], a[88:91], v[140:143]\n"
"ds_read_b64_tr_b16 a[118:119], v19 offset:15616\n"
"s_add_u32 s50, s50, 458752\n"
"v_mfma_f32_16x16x32_bf16 v[140:143], a[28:31], a[92:95], v[140:143]\n"
"ds_read_b128 a[60:63], v18 offset:38912\n"
"s_waitcnt lgkmcnt(14)\n"
"v_mfma_f32_16x16x32_bf16 v[80:83], a[96:99], a[32:35], v[80:83]\n"
"ds_read_b64_tr_b16 a[120:121], v20 offset:2048\n"
"v_mfma_f32_16x16x32_bf16 v[80:83], a[100:103], a[48:51], v[80:83]\n"
"v_accvgpr_read_b32 v42, a247\n"
"s_waitcnt lgkmcnt(14)\n"
"v_mfma_f32_16x16x32_bf16 v[84:87], a[96:99], a[36:39], v[84:87]\n"
"ds_read_b64_tr_b16 a[122:123], v20 offset:2304\n"
"v_accvgpr_read_b32 v43, a248\n"
"s_waitcnt lgkmcnt(12)\n"
"v_mfma_f32_16x16x32_bf16 v[84:87], a[100:103], a[52:55], v[84:87]\n"
"s_add_u32 m0, s57, 98304\n"
"ds_read_b128 v[2:5], v46 offset:0\n"
"s_waitcnt lgkmcnt(11)\n"
"v_mfma_f32_16x16x32_bf16 v[88:91], a[96:99], a[40:43], v[88:91]\n"
"ds_read_b64_tr_b16 a[124:125], v20 offset:15360\n"
"s_waitcnt lgkmcnt(10)\n"
"v_mfma_f32_16x16x32_bf16 v[88:91], a[100:103], a[56:59], v[88:91]\n"
"ds_read_b128 v[6:9], v46 offset:256\n"
"s_waitcnt lgkmcnt(8)\n"
"v_mfma_f32_16x16x32_bf16 v[92:95], a[96:99], a[44:47], v[92:95]\n"
"ds_read_b64_tr_b16 a[126:127], v20 offset:15616\n"
"s_waitcnt lgkmcnt(6)\n"
"v_mfma_f32_16x16x32_bf16 v[92:95], a[100:103], a[60:63], v[92:95]\n"
"ds_read_b128 v[10:13], v46 offset:512\n"
"v_mfma_f32_16x16x32_bf16 v[96:99], a[104:107], a[32:35], v[96:99]\n"
"ds_read_b64_tr_b16 a[128:129], v22 offset:0\n"
"v_mfma_f32_16x16x32_bf16 v[96:99], a[108:111], a[48:51], v[96:99]\n"
"buffer_load_dwordx4 v42, s[20:23], s71 offen offset:0 lds\n"
"v_mfma_f32_16x16x32_bf16 v[100:103], a[104:107], a[36:39], v[100:103]\n"
"ds_read_b64_tr_b16 a[130:131], v22 offset:256\n"
"v_mfma_f32_16x16x32_bf16 v[100:103], a[108:111], a[52:55], v[100:103]\n"
"ds_read_b128 v[14:17], v46 offset:768\n"
"s_lshl_b32 s66, s40, 12\n"
"v_mfma_f32_16x16x32_bf16 v[104:107], a[104:107], a[40:43], v[104:107]\n"
"ds_read_b64_tr_b16 a[132:133], v22 offset:13312\n"
"v_add_u32 v18, s66, v1\n"
"v_mfma_f32_16x16x32_bf16 v[104:107], a[108:111], a[56:59], v[104:107]\n"
"buffer_load_dwordx4 v43, s[20:23], s71 offen offset:1024 lds\n"
"v_mfma_f32_16x16x32_bf16 v[108:111], a[104:107], a[44:47], v[108:111]\n"
"ds_read_b64_tr_b16 a[134:135], v22 offset:13568\n"
"s_add_u32 s71, s71, 131072\n"
"v_mfma_f32_16x16x32_bf16 v[108:111], a[108:111], a[60:63], v[108:111]\n"
"s_add_u32 m0, s57, 106496\n"
"s_add_u32 s53, s71, s30\n"
"v_mfma_f32_16x16x32_bf16 v[112:115], a[32:35], a[112:115], v[112:115]\n"
"ds_read_b64_tr_b16 a[136:137], v19 offset:0\n"
".Lpad7:\n"
"s_cbranch_execz .Lwr0\n"
"v_mfma_f32_16x16x32_bf16 v[112:115], a[48:51], a[116:119], v[112:115]\n"
"s_waitcnt lgkmcnt(3)\n"
"v_mul_f32 v80, v80, v5\n"
"v_mfma_f32_16x16x32_bf16 v[116:119], a[36:39], a[112:115], v[116:119]\n"
"ds_read_b64_tr_b16 a[138:139], v19 offset:256\n"
"v_mul_f32 v81, v81, v5\n"
"v_mfma_f32_16x16x32_bf16 v[116:119], a[52:55], a[116:119], v[116:119]\n"
"buffer_load_dwordx4 v42, s[20:23], s53 offen offset:0 lds\n"
"v_mul_f32 v82, v82, v5\n"
"v_mfma_f32_16x16x32_bf16 v[120:123], a[40:43], a[112:115], v[120:123]\n"
"ds_read_b64_tr_b16 a[140:141], v19 offset:13312\n"
"s_add_u32 s53, s71, s31\n"
"v_mfma_f32_16x16x32_bf16 v[120:123], a[56:59], a[116:119], v[120:123]\n"
"v_mul_f32 v83, v83, v5\n"
"v_mul_f32 v96, v96, v5\n"
"v_mfma_f32_16x16x32_bf16 v[124:127], a[44:47], a[112:115], v[124:127]\n"
"ds_read_b64_tr_b16 a[142:143], v19 offset:13568\n"
"v_mul_f32 v97, v97, v5\n"
"v_mfma_f32_16x16x32_bf16 v[124:127], a[60:63], a[116:119], v[124:127]\n"
"v_mul_f32 v98, v98, v5\n"
"v_mfma_f32_16x16x32_bf16 v[128:131], a[32:35], a[120:123], v[128:131]\n"
"ds_read_b64_tr_b16 a[144:145], v23 offset:0\n"
"v_mul_f32 v99, v99, v5\n"
"v_mfma_f32_16x16x32_bf16 v[128:131], a[48:51], a[124:127], v[128:131]\n"
"buffer_load_dwordx4 v43, s[20:23], s53 offen offset:1024 lds\n"
"v_cvt_pk_bf16_f32 v80, v80, v81\n"
"v_mfma_f32_16x16x32_bf16 v[132:135], a[36:39], a[120:123], v[132:135]\n"
"ds_read_b64_tr_b16 a[146:147], v23 offset:256\n"
"s_add_u32 s71, s71, 131072\n"
"v_mfma_f32_16x16x32_bf16 v[132:135], a[52:55], a[124:127], v[132:135]\n"
"v_accvgpr_read_b32 v42, a245\n"
"v_cvt_pk_bf16_f32 v81, v82, v83\n"
"v_mfma_f32_16x16x32_bf16 v[136:139], a[40:43], a[120:123], v[136:139]\n"
"ds_read_b64_tr_b16 a[148:149], v23 offset:13312\n"
"v_accvgpr_read_b32 v43, a246\n"
"v_mfma_f32_16x16x32_bf16 v[136:139], a[56:59], a[124:127], v[136:139]\n"
"s_add_u32 m0, s57, 155648\n"
"v_cvt_pk_bf16_f32 v82, v96, v97\n"
"v_mfma_f32_16x16x32_bf16 v[140:143], a[44:47], a[120:123], v[140:143]\n"
"ds_read_b64_tr_b16 a[150:151], v23 offset:13568\n"
"v_cvt_pk_bf16_f32 v83, v98, v99\n"
"v_mfma_f32_16x16x32_bf16 v[140:143], a[60:63], a[124:127], v[140:143]\n"
"v_mul_f32 v84, v84, v9\n"
"s_waitcnt lgkmcnt(4)\n"
"v_mfma_f32_16x16x32_bf16 v[48:51], a[128:131], a[0:3], v[48:51]\n"
"ds_read_b64_tr_b16 a[152:153], v20 offset:0\n"
"v_mul_f32 v85, v85, v9\n"
"v_mfma_f32_16x16x32_bf16 v[48:51], a[132:135], a[16:19], v[48:51]\n"
"buffer_load_dwordx4 v42, s[20:23], s70 offen offset:0 lds\n"
"v_mul_f32 v86, v86, v9\n"
"v_mfma_f32_16x16x32_bf16 v[48:51], a[136:139], a[32:35], v[48:51]\n"
"ds_read_b64_tr_b16 a[154:155], v20 offset:256\n"
"v_mul_f32 v87, v87, v9\n"
"v_mfma_f32_16x16x32_bf16 v[48:51], a[140:143], a[48:51], v[48:51]\n"
"buffer_store_dwordx4 v[80:83], v1, s[16:19], s74 offen offset:0 sc1\n"
"v_mul_f32 v100, v100, v9\n"
"v_mfma_f32_16x16x32_bf16 v[52:55], a[128:131], a[4:7], v[52:55]\n"
"ds_read_b64_tr_b16 a[156:157], v20 offset:13312\n"
"v_mul_f32 v101, v101, v9\n"
"v_mfma_f32_16x16x32_bf16 v[52:55], a[132:135], a[20:23], v[52:55]\n"
"buffer_load_dwordx4 v43, s[20:23], s70 offen offset:1024 lds\n"
"v_mul_f32 v102, v102, v9\n"
"v_mfma_f32_16x16x32_bf16 v[52:55], a[136:139], a[36:39], v[52:55]\n"
"ds_read_b64_tr_b16 a[158:159], v20 offset:13568\n"
"s_add_u32 s70, s70, 131072\n"
"v_mfma_f32_16x16x32_bf16 v[52:55], a[140:143], a[52:55], v[52:55]\n"
"s_waitcnt vmcnt(7)\n"
"s_barrier\n"
"v_mfma_f32_16x16x32_bf16 v[56:59], a[128:131], a[8:11], v[56:59]\n"
"ds_read_b64_tr_b16 a[160:161], v22 offset:3072\n"
"v_mul_f32 v103, v103, v9\n"
"v_mfma_f32_16x16x32_bf16 v[56:59], a[132:135], a[24:27], v[56:59]\n"
"ds_write_b128 v18, v[80:83] offset:16384\n"
"v_cvt_pk_bf16_f32 v84, v84, v85\n"
"v_mfma_f32_16x16x32_bf16 v[56:59], a[136:139], a[40:43], v[56:59]\n"
"ds_read_b64_tr_b16 a[162:163], v22 offset:3328\n"
"v_cvt_pk_bf16_f32 v85, v86, v87\n"
"v_mfma_f32_16x16x32_bf16 v[56:59], a[140:143], a[56:59], v[56:59]\n"
"v_cvt_pk_bf16_f32 v86, v100, v101\n"
"v_cvt_pk_bf16_f32 v87, v102, v103\n"
"v_mfma_f32_16x16x32_bf16 v[60:63], a[128:131], a[12:15], v[60:63]\n"
"ds_read_b64_tr_b16 a[164:165], v22 offset:16384\n"
"v_mul_f32 v88, v88, v13\n"
"v_mfma_f32_16x16x32_bf16 v[60:63], a[132:135], a[28:31], v[60:63]\n"
"buffer_store_dwordx4 v[84:87], v1, s[16:19], s74 offen offset:1024 sc1\n"
"v_mul_f32 v89, v89, v13\n"
"v_mfma_f32_16x16x32_bf16 v[60:63], a[136:139], a[44:47], v[60:63]\n"
"ds_read_b64_tr_b16 a[166:167], v22 offset:16640\n"
"v_mul_f32 v90, v90, v13\n"
"v_mfma_f32_16x16x32_bf16 v[60:63], a[140:143], a[60:63], v[60:63]\n"
"ds_write_b128 v18, v[84:87] offset:17408\n"
"s_waitcnt lgkmcnt(6)\n"
"v_mfma_f32_16x16x32_bf16 v[64:67], a[144:147], a[0:3], v[64:67]\n"
"ds_read_b64_tr_b16 a[64:65], v19 offset:3072\n"
"v_mul_f32 v91, v91, v13\n"
"v_mfma_f32_16x16x32_bf16 v[64:67], a[148:151], a[16:19], v[64:67]\n"
"v_mul_f32 v104, v104, v13\n"
"v_mul_f32 v105, v105, v13\n"
"v_mfma_f32_16x16x32_bf16 v[64:67], a[152:155], a[32:35], v[64:67]\n"
"ds_read_b64_tr_b16 a[66:67], v19 offset:3328\n"
"v_mul_f32 v106, v106, v13\n"
"v_mfma_f32_16x16x32_bf16 v[64:67], a[156:159], a[48:51], v[64:67]\n"
"v_mul_f32 v107, v107, v13\n"
"v_cvt_pk_bf16_f32 v88, v88, v89\n"
"v_mfma_f32_16x16x32_bf16 v[68:71], a[144:147], a[4:7], v[68:71]\n"
"ds_read_b64_tr_b16 a[68:69], v19 offset:16384\n"
"v_cvt_pk_bf16_f32 v89, v90, v91\n"
"v_mfma_f32_16x16x32_bf16 v[68:71], a[148:151], a[20:23], v[68:71]\n"
"v_cvt_pk_bf16_f32 v90, v104, v105\n"
"v_cvt_pk_bf16_f32 v91, v106, v107\n"
"v_mfma_f32_16x16x32_bf16 v[68:71], a[152:155], a[36:39], v[68:71]\n"
"ds_read_b64_tr_b16 a[70:71], v19 offset:16640\n"
"v_mul_f32 v92, v92, v17\n"
"v_mfma_f32_16x16x32_bf16 v[68:71], a[156:159], a[52:55], v[68:71]\n"
"buffer_store_dwordx4 v[88:91], v1, s[16:19], s74 offen offset:2048 sc1\n"
"v_mul_f32 v93, v93, v17\n"
"v_mfma_f32_16x16x32_bf16 v[72:75], a[144:147], a[8:11], v[72:75]\n"
"ds_read_b64_tr_b16 a[72:73], v23 offset:3072\n"
"v_mul_f32 v94, v94, v17\n"
"v_mfma_f32_16x16x32_bf16 v[72:75], a[148:151], a[24:27], v[72:75]\n"
"ds_write_b128 v18, v[88:91] offset:18432\n"
"v_mul_f32 v95, v95, v17\n"
"v_mfma_f32_16x16x32_bf16 v[72:75], a[152:155], a[40:43], v[72:75]\n"
"ds_read_b64_tr_b16 a[74:75], v23 offset:3328\n"
"v_mul_f32 v108, v108, v17\n"
"v_mfma_f32_16x16x32_bf16 v[72:75], a[156:159], a[56:59], v[72:75]\n"
"v_mul_f32 v109, v109, v17\n"
"v_mul_f32 v110, v110, v17\n"
"v_mfma_f32_16x16x32_bf16 v[76:79], a[144:147], a[12:15], v[76:79]\n"
"ds_read_b64_tr_b16 a[76:77], v23 offset:16384\n"
"v_mul_f32 v111, v111, v17\n"
"v_mfma_f32_16x16x32_bf16 v[76:79], a[148:151], a[28:31], v[76:79]\n"
"v_cvt_pk_bf16_f32 v92, v92, v93\n"
"v_cvt_pk_bf16_f32 v93, v94, v95\n"
"v_mfma_f32_16x16x32_bf16 v[76:79], a[152:155], a[44:47], v[76:79]\n"
"ds_read_b64_tr_b16 a[78:79], v23 offset:16640\n"
"v_cvt_pk_bf16_f32 v94, v108, v109\n"
"v_mfma_f32_16x16x32_bf16 v[76:79], a[156:159], a[60:63], v[76:79]\n"
"v_cvt_pk_bf16_f32 v95, v110, v111\n"
"s_waitcnt lgkmcnt(5)\n"
"v_mfma_f32_16x16x32_bf16 v[144:147], a[160:163], a[0:3], v[144:147]\n"
"ds_read_b64_tr_b16 a[80:81], v20 offset:3072\n"
"v_mfma_f32_16x16x32_bf16 v[144:147], a[164:167], a[16:19], v[144:147]\n"
"buffer_store_dwordx4 v[92:95], v1, s[16:19], s74 offen offset:3072 sc1\n"
"v_mul_f32 v112, v112, v34\n"
"v_mfma_f32_16x16x32_bf16 v[144:147], a[64:67], a[32:35], v[144:147]\n"
"ds_read_b64_tr_b16 a[82:83], v20 offset:3328\n"
"v_mul_f32 v113, v113, v35\n"
"v_mfma_f32_16x16x32_bf16 v[144:147], a[68:71], a[48:51], v[144:147]\n"
"ds_write_b128 v18, v[92:95] offset:19456\n"
"v_mul_f32 v114, v114, v36\n"
"v_mfma_f32_16x16x32_bf16 v[148:151], a[160:163], a[4:7], v[148:151]\n"
"ds_read_b64_tr_b16 a[84:85], v20 offset:16384\n"
"v_mul_f32 v115, v115, v37\n"
"v_mfma_f32_16x16x32_bf16 v[148:151], a[164:167], a[20:23], v[148:151]\n"
"ds_read_b128 v[38:41], v45 offset:64\n"
"v_mul_f32 v128, v128, v34\n"
"v_mfma_f32_16x16x32_bf16 v[148:151], a[64:67], a[36:39], v[148:151]\n"
"ds_read_b64_tr_b16 a[86:87], v20 offset:16640\n"
"v_mul_f32 v129, v129, v35\n"
"v_mfma_f32_16x16x32_bf16 v[148:151], a[68:71], a[52:55], v[148:151]\n"
"v_mul_f32 v130, v130, v36\n"
"v_mul_f32 v131, v131, v37\n"
"v_mfma_f32_16x16x32_bf16 v[152:155], a[160:163], a[8:11], v[152:155]\n"
"ds_read_b64_tr_b16 a[88:89], v22 offset:4096\n"
"v_mfma_f32_16x16x32_bf16 v[152:155], a[164:167], a[24:27], v[152:155]\n"
"ds_read_b128 v[34:37], v45 offset:128\n"
"s_waitcnt lgkmcnt(3)\n"
"v_mfma_f32_16x16x32_bf16 v[152:155], a[64:67], a[40:43], v[152:155]\n"
"ds_read_b64_tr_b16 a[90:91], v22 offset:4352\n"
"v_mul_f32 v116, v116, v38\n"
"v_mfma_f32_16x16x32_bf16 v[152:155], a[68:71], a[56:59], v[152:155]\n"
"v_mul_f32 v117, v117, v39\n"
"v_mul_f32 v118, v118, v40\n"
"v_mfma_f32_16x16x32_bf16 v[156:159], a[160:163], a[12:15], v[156:159]\n"
"ds_read_b64_tr_b16 a[92:93], v22 offset:17408\n"
"v_mul_f32 v119, v119, v41\n"
"v_mfma_f32_16x16x32_bf16 v[156:159], a[164:167], a[28:31], v[156:159]\n"
"v_mul_f32 v132, v132, v38\n"
"v_mul_f32 v133, v133, v39\n"
"v_mfma_f32_16x16x32_bf16 v[156:159], a[64:67], a[44:47], v[156:159]\n"
"ds_read_b64_tr_b16 a[94:95], v22 offset:17664\n"
"v_mul_f32 v134, v134, v40\n"
"v_mfma_f32_16x16x32_bf16 v[156:159], a[68:71], a[60:63], v[156:159]\n"
"v_mul_f32 v135, v135, v41\n"
"s_waitcnt lgkmcnt(5)\n"
"v_mfma_f32_16x16x32_bf16 v[160:163], a[72:75], a[0:3], v[160:163]\n"
"ds_read_b64_tr_b16 a[96:97], v19 offset:4096\n"
"v_cvt_pk_bf16_f32 v128, v128, v129\n"
"v_mfma_f32_16x16x32_bf16 v[160:163], a[76:79], a[16:19], v[160:163]\n"
"v_cvt_pk_bf16_f32 v129, v130, v131\n"
"v_cvt_pk_bf16_f32 v130, v132, v133\n"
"v_mfma_f32_16x16x32_bf16 v[160:163], a[80:83], a[32:35], v[160:163]\n"
"ds_read_b64_tr_b16 a[98:99], v19 offset:4352\n"
"v_cvt_pk_bf16_f32 v131, v134, v135\n"
"v_mfma_f32_16x16x32_bf16 v[160:163], a[84:87], a[48:51], v[160:163]\n"
"buffer_store_dwordx4 v[128:131], v1, s[16:19], s75 offen offset:1024 sc1\n"
"v_cvt_pk_bf16_f32 v132, v112, v113\n"
"v_mfma_f32_16x16x32_bf16 v[164:167], a[72:75], a[4:7], v[164:167]\n"
"ds_read_b64_tr_b16 a[100:101], v19 offset:17408\n"
"v_cvt_pk_bf16_f32 v133, v114, v115\n"
"v_mfma_f32_16x16x32_bf16 v[164:167], a[76:79], a[20:23], v[164:167]\n"
"ds_write_b128 v18, v[128:131] offset:50176\n"
"v_cvt_pk_bf16_f32 v134, v116, v117\n"
"v_mfma_f32_16x16x32_bf16 v[164:167], a[80:83], a[36:39], v[164:167]\n"
"ds_read_b64_tr_b16 a[102:103], v19 offset:17664\n"
"v_cvt_pk_bf16_f32 v135, v118, v119\n"
"v_mfma_f32_16x16x32_bf16 v[164:167], a[84:87], a[52:55], v[164:167]\n"
"buffer_store_dwordx4 v[132:135], v1, s[16:19], s75 offen offset:0 sc1\n"
"v_mfma_f32_16x16x32_bf16 v[168:171], a[72:75], a[8:11], v[168:171]\n"
"ds_read_b64_tr_b16 a[104:105], v23 offset:4096\n"
"s_waitcnt lgkmcnt(9)\n"
"v_mfma_f32_16x16x32_bf16 v[168:171], a[76:79], a[24:27], v[168:171]\n"
"ds_write_b128 v18, v[132:135] offset:49152\n"
"v_mul_f32 v120, v120, v34\n"
"v_mfma_f32_16x16x32_bf16 v[168:171], a[80:83], a[40:43], v[168:171]\n"
"ds_read_b64_tr_b16 a[106:107], v23 offset:4352\n"
"v_mul_f32 v121, v121, v35\n"
"v_mfma_f32_16x16x32_bf16 v[168:171], a[84:87], a[56:59], v[168:171]\n"
"ds_read_b128 v[38:41], v45 offset:192\n"
"v_mul_f32 v122, v122, v36\n"
"v_mfma_f32_16x16x32_bf16 v[172:175], a[72:75], a[12:15], v[172:175]\n"
"ds_read_b64_tr_b16 a[108:109], v23 offset:17408\n"
"v_mul_f32 v123, v123, v37\n"
"v_mfma_f32_16x16x32_bf16 v[172:175], a[76:79], a[28:31], v[172:175]\n"
"v_mul_f32 v136, v136, v34\n"
"v_mul_f32 v137, v137, v35\n"
"v_mfma_f32_16x16x32_bf16 v[172:175], a[80:83], a[44:47], v[172:175]\n"
"ds_read_b64_tr_b16 a[110:111], v23 offset:17664\n"
"v_mul_f32 v138, v138, v36\n"
"v_mfma_f32_16x16x32_bf16 v[172:175], a[84:87], a[60:63], v[172:175]\n"
"v_mul_f32 v139, v139, v37\n"
"s_waitcnt lgkmcnt(6)\n"
"v_mfma_f32_16x16x32_bf16 v[176:179], a[88:91], a[0:3], v[176:179]\n"
"ds_read_b64_tr_b16 a[112:113], v20 offset:4096\n"
"s_waitcnt lgkmcnt(3)\n"
"v_mfma_f32_16x16x32_bf16 v[176:179], a[92:95], a[16:19], v[176:179]\n"
"v_mul_f32 v124, v124, v38\n"
"v_mul_f32 v125, v125, v39\n"
"v_mfma_f32_16x16x32_bf16 v[176:179], a[96:99], a[32:35], v[176:179]\n"
"ds_read_b64_tr_b16 a[114:115], v20 offset:4352\n"
"v_mul_f32 v126, v126, v40\n"
"v_mfma_f32_16x16x32_bf16 v[176:179], a[100:103], a[48:51], v[176:179]\n"
"v_mul_f32 v127, v127, v41\n"
"v_mul_f32 v140, v140, v38\n"
"v_mfma_f32_16x16x32_bf16 v[180:183], a[88:91], a[4:7], v[180:183]\n"
"ds_read_b64_tr_b16 a[116:117], v20 offset:17408\n"
"v_mul_f32 v141, v141, v39\n"
"v_mfma_f32_16x16x32_bf16 v[180:183], a[92:95], a[20:23], v[180:183]\n"
"v_mul_f32 v142, v142, v40\n"
"v_mul_f32 v143, v143, v41\n"
"v_mfma_f32_16x16x32_bf16 v[180:183], a[96:99], a[36:39], v[180:183]\n"
"ds_read_b64_tr_b16 a[118:119], v20 offset:17664\n"
"v_cvt_pk_bf16_f32 v136, v136, v137\n"
"v_mfma_f32_16x16x32_bf16 v[180:183], a[100:103], a[52:55], v[180:183]\n"
"v_cvt_pk_bf16_f32 v137, v138, v139\n"
"v_cvt_pk_bf16_f32 v138, v140, v141\n"
"v_pk_mul_f32 v[96:97], v[144:145], v[144:145]\n"
"v_pk_mul_f32 v[98:99], v[146:147], v[146:147]\n"
"v_pk_mul_f32 v[100:101], v[160:161], v[160:161]\n"
"v_pk_mul_f32 v[102:103], v[162:163], v[162:163]\n"
"v_pk_fma_f32 v[96:97], v[96:97], v[2:3], v[2:3] op_sel:[0,0,1] op_sel_hi:[1,0,1]\n"
"v_pk_fma_f32 v[98:99], v[98:99], v[2:3], v[2:3] op_sel:[0,0,1] op_sel_hi:[1,0,1]\n"
"v_pk_fma_f32 v[100:101], v[100:101], v[2:3], v[2:3] op_sel:[0,0,1] op_sel_hi:[1,0,1]\n"
"v_pk_fma_f32 v[102:103], v[102:103], v[2:3], v[2:3] op_sel:[0,0,1] op_sel_hi:[1,0,1]\n"
"v_pk_mul_f32 v[96:97], v[96:97], v[144:145]\n"
"v_pk_mul_f32 v[98:99], v[98:99], v[146:147]\n"
"v_pk_mul_f32 v[100:101], v[100:101], v[160:161]\n"
"v_pk_mul_f32 v[102:103], v[102:103], v[162:163]\n"
"v_exp_f32 v96, v96\n"
"v_exp_f32 v97, v97\n"
"v_exp_f32 v98, v98\n"
"v_exp_f32 v99, v99\n"
"v_exp_f32 v100, v100\n"
"v_exp_f32 v101, v101\n"
"v_exp_f32 v102, v102\n"
"v_exp_f32_e64 v103, v103\n"
"v_pk_fma_f32 v[96:97], v[96:97], v[4:5], v[4:5] op_sel_hi:[1,0,0]\n"
"v_pk_fma_f32 v[98:99], v[98:99], v[4:5], v[4:5] op_sel_hi:[1,0,0]\n"
"v_pk_fma_f32 v[100:101], v[100:101], v[4:5], v[4:5] op_sel_hi:[1,0,0]\n"
"v_pk_fma_f32 v[102:103], v[102:103], v[4:5], v[4:5] op_sel_hi:[1,0,0]\n"
"v_rcp_f32 v96, v96\n"
"v_rcp_f32 v97, v97\n"
"v_rcp_f32 v98, v98\n"
"v_rcp_f32 v99, v99\n"
"v_rcp_f32 v100, v100\n"
"v_rcp_f32 v101, v101\n"
"v_rcp_f32 v102, v102\n"
"v_rcp_f32 v103, v103\n"
"v_pk_mul_f32 v[96:97], v[96:97], v[144:145]\n"
"v_pk_mul_f32 v[98:99], v[98:99], v[146:147]\n"
"v_pk_mul_f32 v[100:101], v[100:101], v[160:161]\n"
"v_pk_mul_f32 v[102:103], v[102:103], v[162:163]\n"
"v_cvt_pk_bf16_f32 v144, v96, v97\n"
"v_cvt_pk_bf16_f32 v146, v100, v101\n"
"v_cvt_pk_bf16_f32 v145, v98, v99\n"
"v_cvt_pk_bf16_f32 v147, v102, v103\n"
"buffer_store_dwordx4 v[144:147], v1, s[16:19], s60 offen offset:0 sc1\n"
"v_mfma_f32_16x16x32_bf16 v[184:187], a[88:91], a[8:11], v[184:187]\n"
"ds_read_b64_tr_b16 a[120:121], v22 offset:5120\n"
"v_cvt_pk_bf16_f32 v139, v142, v143\n"
"v_mfma_f32_16x16x32_bf16 v[184:187], a[92:95], a[24:27], v[184:187]\n"
"buffer_store_dwordx4 v[136:139], v1, s[16:19], s75 offen offset:3072 sc1\n"
"v_cvt_pk_bf16_f32 v140, v120, v121\n"
"v_mfma_f32_16x16x32_bf16 v[184:187], a[96:99], a[40:43], v[184:187]\n"
"ds_read_b64_tr_b16 a[122:123], v22 offset:5376\n"
"v_cvt_pk_bf16_f32 v141, v122, v123\n"
"v_mfma_f32_16x16x32_bf16 v[184:187], a[100:103], a[56:59], v[184:187]\n"
"ds_write_b128 v18, v[136:139] offset:52224\n"
"v_cvt_pk_bf16_f32 v142, v124, v125\n"
"v_mfma_f32_16x16x32_bf16 v[188:191], a[88:91], a[12:15], v[188:191]\n"
"ds_read_b64_tr_b16 a[124:125], v22 offset:18432\n"
"v_cvt_pk_bf16_f32 v143, v126, v127\n"
"v_mfma_f32_16x16x32_bf16 v[188:191], a[92:95], a[28:31], v[188:191]\n"
"buffer_store_dwordx4 v[140:143], v1, s[16:19], s75 offen offset:2048 sc1\n"
"v_mfma_f32_16x16x32_bf16 v[188:191], a[96:99], a[44:47], v[188:191]\n"
"ds_read_b64_tr_b16 a[126:127], v22 offset:18688\n"
"v_mfma_f32_16x16x32_bf16 v[188:191], a[100:103], a[60:63], v[188:191]\n"
"ds_write_b128 v18, v[140:143] offset:51200\n"
"s_waitcnt lgkmcnt(6)\n"
"v_mfma_f32_16x16x32_bf16 v[192:195], a[104:107], a[0:3], v[192:195]\n"
"ds_read_b64_tr_b16 a[128:129], v19 offset:5120\n"
"v_mfma_f32_16x16x32_bf16 v[192:195], a[108:111], a[16:19], v[192:195]\n"
"v_mfma_f32_16x16x32_bf16 v[192:195], a[112:115], a[32:35], v[192:195]\n"
"ds_read_b64_tr_b16 a[130:131], v19 offset:5376\n"
"v_mfma_f32_16x16x32_bf16 v[192:195], a[116:119], a[48:51], v[192:195]\n"
"v_mfma_f32_16x16x32_bf16 v[196:199], a[104:107], a[4:7], v[196:199]\n"
"ds_read_b64_tr_b16 a[132:133], v19 offset:18432\n"
"v_mfma_f32_16x16x32_bf16 v[196:199], a[108:111], a[20:23], v[196:199]\n"
"v_mfma_f32_16x16x32_bf16 v[196:199], a[112:115], a[36:39], v[196:199]\n"
"ds_read_b64_tr_b16 a[134:135], v19 offset:18688\n"
"v_mfma_f32_16x16x32_bf16 v[196:199], a[116:119], a[52:55], v[196:199]\n"
"v_pk_mul_f32 v[96:97], v[148:149], v[148:149]\n"
"v_pk_mul_f32 v[98:99], v[150:151], v[150:151]\n"
"v_pk_mul_f32 v[100:101], v[164:165], v[164:165]\n"
"v_pk_mul_f32 v[102:103], v[166:167], v[166:167]\n"
"v_pk_fma_f32 v[96:97], v[96:97], v[6:7], v[6:7] op_sel:[0,0,1] op_sel_hi:[1,0,1]\n"
"v_pk_fma_f32 v[98:99], v[98:99], v[6:7], v[6:7] op_sel:[0,0,1] op_sel_hi:[1,0,1]\n"
"v_pk_fma_f32 v[100:101], v[100:101], v[6:7], v[6:7] op_sel:[0,0,1] op_sel_hi:[1,0,1]\n"
"v_pk_fma_f32 v[102:103], v[102:103], v[6:7], v[6:7] op_sel:[0,0,1] op_sel_hi:[1,0,1]\n"
"v_pk_mul_f32 v[96:97], v[96:97], v[148:149]\n"
"v_pk_mul_f32 v[98:99], v[98:99], v[150:151]\n"
"v_pk_mul_f32 v[100:101], v[100:101], v[164:165]\n"
"v_pk_mul_f32 v[102:103], v[102:103], v[166:167]\n"
"v_exp_f32 v96, v96\n"
"v_exp_f32 v97, v97\n"
"v_exp_f32 v98, v98\n"
"v_exp_f32 v99, v99\n"
"v_exp_f32 v100, v100\n"
"v_exp_f32 v101, v101\n"
"v_exp_f32 v102, v102\n"
"v_exp_f32_e64 v103, v103\n"
"v_pk_fma_f32 v[96:97], v[96:97], v[8:9], v[8:9] op_sel_hi:[1,0,0]\n"
"v_pk_fma_f32 v[98:99], v[98:99], v[8:9], v[8:9] op_sel_hi:[1,0,0]\n"
"v_pk_fma_f32 v[100:101], v[100:101], v[8:9], v[8:9] op_sel_hi:[1,0,0]\n"
"v_pk_fma_f32 v[102:103], v[102:103], v[8:9], v[8:9] op_sel_hi:[1,0,0]\n"
"v_rcp_f32 v96, v96\n"
"v_rcp_f32 v97, v97\n"
"v_rcp_f32 v98, v98\n"
"v_rcp_f32 v99, v99\n"
"v_rcp_f32 v100, v100\n"
"v_rcp_f32 v101, v101\n"
"v_rcp_f32 v102, v102\n"
"v_rcp_f32 v103, v103\n"
"v_pk_mul_f32 v[96:97], v[96:97], v[148:149]\n"
"v_pk_mul_f32 v[98:99], v[98:99], v[150:151]\n"
"v_pk_mul_f32 v[100:101], v[100:101], v[164:165]\n"
"v_pk_mul_f32 v[102:103], v[102:103], v[166:167]\n"
"v_cvt_pk_bf16_f32 v148, v96, v97\n"
"v_cvt_pk_bf16_f32 v150, v100, v101\n"
"v_cvt_pk_bf16_f32 v149, v98, v99\n"
"v_cvt_pk_bf16_f32 v151, v102, v103\n"
"buffer_store_dwordx4 v[148:151], v1, s[16:19], s60 offen offset:1024 sc1\n"
"v_mfma_f32_16x16x32_bf16 v[200:203], a[104:107], a[8:11], v[200:203]\n"
"ds_read_b64_tr_b16 a[136:137], v23 offset:5120\n"
"v_mfma_f32_16x16x32_bf16 v[200:203], a[108:111], a[24:27], v[200:203]\n"
"v_mfma_f32_16x16x32_bf16 v[200:203], a[112:115], a[40:43], v[200:203]\n"
"ds_read_b64_tr_b16 a[138:139], v23 offset:5376\n"
"v_mfma_f32_16x16x32_bf16 v[200:203], a[116:119], a[56:59], v[200:203]\n"
"v_mfma_f32_16x16x32_bf16 v[204:207], a[104:107], a[12:15], v[204:207]\n"
"ds_read_b64_tr_b16 a[140:141], v23 offset:18432\n"
"v_mfma_f32_16x16x32_bf16 v[204:207], a[108:111], a[28:31], v[204:207]\n"
"v_mfma_f32_16x16x32_bf16 v[204:207], a[112:115], a[44:47], v[204:207]\n"
"ds_read_b64_tr_b16 a[142:143], v23 offset:18688\n"
"v_mfma_f32_16x16x32_bf16 v[204:207], a[116:119], a[60:63], v[204:207]\n"
"s_waitcnt lgkmcnt(4)\n"
"v_mfma_f32_16x16x32_bf16 v[208:211], a[120:123], a[0:3], v[208:211]\n"
"ds_read_b64_tr_b16 a[144:145], v20 offset:5120\n"
"v_mfma_f32_16x16x32_bf16 v[208:211], a[124:127], a[16:19], v[208:211]\n"
"v_mfma_f32_16x16x32_bf16 v[208:211], a[128:131], a[32:35], v[208:211]\n"
"ds_read_b64_tr_b16 a[146:147], v20 offset:5376\n"
"v_mfma_f32_16x16x32_bf16 v[208:211], a[132:135], a[48:51], v[208:211]\n"
"v_pk_mul_f32 v[96:97], v[152:153], v[152:153]\n"
"v_pk_mul_f32 v[98:99], v[154:155], v[154:155]\n"
"v_pk_mul_f32 v[100:101], v[168:169], v[168:169]\n"
"v_pk_mul_f32 v[102:103], v[170:171], v[170:171]\n"
"v_pk_fma_f32 v[96:97], v[96:97], v[10:11], v[10:11] op_sel:[0,0,1] op_sel_hi:[1,0,1]\n"
"v_pk_fma_f32 v[98:99], v[98:99], v[10:11], v[10:11] op_sel:[0,0,1] op_sel_hi:[1,0,1]\n"
"v_pk_fma_f32 v[100:101], v[100:101], v[10:11], v[10:11] op_sel:[0,0,1] op_sel_hi:[1,0,1]\n"
"v_pk_fma_f32 v[102:103], v[102:103], v[10:11], v[10:11] op_sel:[0,0,1] op_sel_hi:[1,0,1]\n"
"v_pk_mul_f32 v[96:97], v[96:97], v[152:153]\n"
"v_pk_mul_f32 v[98:99], v[98:99], v[154:155]\n"
"v_pk_mul_f32 v[100:101], v[100:101], v[168:169]\n"
"v_pk_mul_f32 v[102:103], v[102:103], v[170:171]\n"
"v_exp_f32 v96, v96\n"
"v_exp_f32 v97, v97\n"
"v_exp_f32 v98, v98\n"
"v_exp_f32 v99, v99\n"
"v_exp_f32 v100, v100\n"
"v_exp_f32 v101, v101\n"
"v_exp_f32 v102, v102\n"
"v_exp_f32_e64 v103, v103\n"
"v_pk_fma_f32 v[96:97], v[96:97], v[12:13], v[12:13] op_sel_hi:[1,0,0]\n"
"v_pk_fma_f32 v[98:99], v[98:99], v[12:13], v[12:13] op_sel_hi:[1,0,0]\n"
"v_pk_fma_f32 v[100:101], v[100:101], v[12:13], v[12:13] op_sel_hi:[1,0,0]\n"
"v_pk_fma_f32 v[102:103], v[102:103], v[12:13], v[12:13] op_sel_hi:[1,0,0]\n"
"v_rcp_f32 v96, v96\n"
"v_rcp_f32 v97, v97\n"
"v_rcp_f32 v98, v98\n"
"v_rcp_f32 v99, v99\n"
"v_rcp_f32 v100, v100\n"
"v_rcp_f32 v101, v101\n"
"v_rcp_f32 v102, v102\n"
"v_rcp_f32 v103, v103\n"
"v_pk_mul_f32 v[96:97], v[96:97], v[152:153]\n"
"v_pk_mul_f32 v[98:99], v[98:99], v[154:155]\n"
"v_pk_mul_f32 v[100:101], v[100:101], v[168:169]\n"
"v_pk_mul_f32 v[102:103], v[102:103], v[170:171]\n"
"v_cvt_pk_bf16_f32 v152, v96, v97\n"
"v_cvt_pk_bf16_f32 v154, v100, v101\n"
"v_cvt_pk_bf16_f32 v153, v98, v99\n"
"v_cvt_pk_bf16_f32 v155, v102, v103\n"
"buffer_store_dwordx4 v[152:155], v1, s[16:19], s60 offen offset:2048 sc1\n"
"v_mfma_f32_16x16x32_bf16 v[212:215], a[120:123], a[4:7], v[212:215]\n"
"ds_read_b64_tr_b16 a[148:149], v20 offset:18432\n"
"v_mfma_f32_16x16x32_bf16 v[212:215], a[124:127], a[20:23], v[212:215]\n"
"v_mfma_f32_16x16x32_bf16 v[212:215], a[128:131], a[36:39], v[212:215]\n"
"ds_read_b64_tr_b16 a[150:151], v20 offset:18688\n"
"v_mfma_f32_16x16x32_bf16 v[212:215], a[132:135], a[52:55], v[212:215]\n"
"v_mfma_f32_16x16x32_bf16 v[216:219], a[120:123], a[8:11], v[216:219]\n"
"ds_read_b64_tr_b16 a[152:153], v24 offset:6144\n"
"v_mfma_f32_16x16x32_bf16 v[216:219], a[124:127], a[24:27], v[216:219]\n"
"v_mfma_f32_16x16x32_bf16 v[216:219], a[128:131], a[40:43], v[216:219]\n"
"ds_read_b64_tr_b16 a[154:155], v24 offset:6400\n"
"v_mfma_f32_16x16x32_bf16 v[216:219], a[132:135], a[56:59], v[216:219]\n"
"v_mov_b32 v46, 0\n"
"s_waitcnt vmcnt(2)\n"
"s_mov_b64 exec, 1\n"
"buffer_store_dword v46, off, s[16:19], s76 sc1\n"
"s_mov_b64 exec, -1\n"
"v_mfma_f32_16x16x32_bf16 v[220:223], a[120:123], a[12:15], v[220:223]\n"
"ds_read_b64_tr_b16 a[156:157], v24 offset:19456\n"
"v_mfma_f32_16x16x32_bf16 v[220:223], a[124:127], a[28:31], v[220:223]\n"
"v_mfma_f32_16x16x32_bf16 v[220:223], a[128:131], a[44:47], v[220:223]\n"
"ds_read_b64_tr_b16 a[158:159], v24 offset:19712\n"
"v_mfma_f32_16x16x32_bf16 v[220:223], a[132:135], a[60:63], v[220:223]\n"
"v_pk_mul_f32 v[96:97], v[156:157], v[156:157]\n"
"v_pk_mul_f32 v[98:99], v[158:159], v[158:159]\n"
"v_pk_mul_f32 v[100:101], v[172:173], v[172:173]\n"
"v_pk_mul_f32 v[102:103], v[174:175], v[174:175]\n"
"v_pk_fma_f32 v[96:97], v[96:97], v[14:15], v[14:15] op_sel:[0,0,1] op_sel_hi:[1,0,1]\n"
"v_pk_fma_f32 v[98:99], v[98:99], v[14:15], v[14:15] op_sel:[0,0,1] op_sel_hi:[1,0,1]\n"
"v_pk_fma_f32 v[100:101], v[100:101], v[14:15], v[14:15] op_sel:[0,0,1] op_sel_hi:[1,0,1]\n"
"v_pk_fma_f32 v[102:103], v[102:103], v[14:15], v[14:15] op_sel:[0,0,1] op_sel_hi:[1,0,1]\n"
"v_pk_mul_f32 v[96:97], v[96:97], v[156:157]\n"
"v_pk_mul_f32 v[98:99], v[98:99], v[158:159]\n"
"v_pk_mul_f32 v[100:101], v[100:101], v[172:173]\n"
"v_pk_mul_f32 v[102:103], v[102:103], v[174:175]\n"
"v_exp_f32 v96, v96\n"
"v_exp_f32 v97, v97\n"
"v_exp_f32 v98, v98\n"
"v_exp_f32 v99, v99\n"
"v_exp_f32 v100, v100\n"
"v_exp_f32 v101, v101\n"
"v_exp_f32 v102, v102\n"
"v_exp_f32 v103, v103\n"
"v_pk_fma_f32 v[96:97], v[96:97], v[16:17], v[16:17] op_sel_hi:[1,0,0]\n"
"v_pk_fma_f32 v[98:99], v[98:99], v[16:17], v[16:17] op_sel_hi:[1,0,0]\n"
"v_pk_fma_f32 v[100:101], v[100:101], v[16:17], v[16:17] op_sel_hi:[1,0,0]\n"
"v_pk_fma_f32 v[102:103], v[102:103], v[16:17], v[16:17] op_sel_hi:[1,0,0]\n"
"v_rcp_f32 v96, v96\n"
"v_rcp_f32 v97, v97\n"
"v_rcp_f32 v98, v98\n"
"v_rcp_f32 v99, v99\n"
"v_rcp_f32 v100, v100\n"
"v_rcp_f32 v101, v101\n"
"v_rcp_f32 v102, v102\n"
"v_rcp_f32 v103, v103\n"
"v_pk_mul_f32 v[96:97], v[96:97], v[156:157]\n"
"v_pk_mul_f32 v[98:99], v[98:99], v[158:159]\n"
"v_pk_mul_f32 v[100:101], v[100:101], v[172:173]\n"
"v_pk_mul_f32 v[102:103], v[102:103], v[174:175]\n"
"v_cvt_pk_bf16_f32 v156, v96, v97\n"
"v_cvt_pk_bf16_f32 v158, v100, v101\n"
"v_cvt_pk_bf16_f32 v157, v98, v99\n"
"v_cvt_pk_bf16_f32 v159, v102, v103\n"
"buffer_store_dwordx4 v[156:159], v1, s[16:19], s60 offen offset:3072 sc1\n"
"s_waitcnt lgkmcnt(4)\n"
"v_mfma_f32_16x16x32_bf16 v[224:227], a[136:139], a[0:3], v[224:227]\n"
"ds_read_b64_tr_b16 a[160:161], v21 offset:6144\n"
"v_add_u32 v22, s66, v1\n"
"v_mfma_f32_16x16x32_bf16 v[224:227], a[140:143], a[16:19], v[224:227]\n"
"v_mov_b32 v23, 0\n"
"v_and_b32 v24, 0x200, v1\n"
"v_mfma_f32_16x16x32_bf16 v[224:227], a[144:147], a[32:35], v[224:227]\n"
"ds_read_b64_tr_b16 a[162:163], v21 offset:6400\n"
"v_add_u32 v24, v24, v1\n"
"v_mfma_f32_16x16x32_bf16 v[224:227], a[148:151], a[48:51], v[224:227]\n"
"v_mfma_f32_16x16x32_bf16 v[228:231], a[136:139], a[4:7], v[228:231]\n"
"ds_read_b64_tr_b16 a[164:165], v21 offset:19456\n"
"v_mfma_f32_16x16x32_bf16 v[228:231], a[140:143], a[20:23], v[228:231]\n"
"v_mfma_f32_16x16x32_bf16 v[228:231], a[144:147], a[36:39], v[228:231]\n"
"ds_read_b64_tr_b16 a[166:167], v21 offset:19712\n"
"v_mfma_f32_16x16x32_bf16 v[228:231], a[148:151], a[52:55], v[228:231]\n"
"v_mfma_f32_16x16x32_bf16 v[232:235], a[136:139], a[8:11], v[232:235]\n"
"v_mfma_f32_16x16x32_bf16 v[232:235], a[140:143], a[24:27], v[232:235]\n"
"v_mfma_f32_16x16x32_bf16 v[232:235], a[144:147], a[40:43], v[232:235]\n"
"v_mfma_f32_16x16x32_bf16 v[232:235], a[148:151], a[56:59], v[232:235]\n"
"v_pk_mul_f32 v[96:97], v[176:177], v[176:177]\n"
"v_pk_mul_f32 v[98:99], v[178:179], v[178:179]\n"
"v_pk_mul_f32 v[100:101], v[192:193], v[192:193]\n"
"v_pk_mul_f32 v[102:103], v[194:195], v[194:195]\n"
"v_pk_fma_f32 v[96:97], v[96:97], v[2:3], v[2:3] op_sel:[0,0,1] op_sel_hi:[1,0,1]\n"
"v_pk_fma_f32 v[98:99], v[98:99], v[2:3], v[2:3] op_sel:[0,0,1] op_sel_hi:[1,0,1]\n"
"v_pk_fma_f32 v[100:101], v[100:101], v[2:3], v[2:3] op_sel:[0,0,1] op_sel_hi:[1,0,1]\n"
"v_pk_fma_f32 v[102:103], v[102:103], v[2:3], v[2:3] op_sel:[0,0,1] op_sel_hi:[1,0,1]\n"
"v_pk_mul_f32 v[96:97], v[96:97], v[176:177]\n"
"v_pk_mul_f32 v[98:99], v[98:99], v[178:179]\n"
"v_pk_mul_f32 v[100:101], v[100:101], v[192:193]\n"
"v_pk_mul_f32 v[102:103], v[102:103], v[194:195]\n"
"v_exp_f32 v96, v96\n"
"v_exp_f32 v97, v97\n"
"v_exp_f32 v98, v98\n"
"v_exp_f32 v99, v99\n"
"v_exp_f32 v100, v100\n"
"v_exp_f32 v101, v101\n"
"v_exp_f32 v102, v102\n"
"v_exp_f32 v103, v103\n"
"v_pk_fma_f32 v[96:97], v[96:97], v[4:5], v[4:5] op_sel_hi:[1,0,0]\n"
"v_pk_fma_f32 v[98:99], v[98:99], v[4:5], v[4:5] op_sel_hi:[1,0,0]\n"
"v_pk_fma_f32 v[100:101], v[100:101], v[4:5], v[4:5] op_sel_hi:[1,0,0]\n"
"v_pk_fma_f32 v[102:103], v[102:103], v[4:5], v[4:5] op_sel_hi:[1,0,0]\n"
"v_rcp_f32 v96, v96\n"
"v_rcp_f32 v97, v97\n"
"v_rcp_f32 v98, v98\n"
"v_rcp_f32 v99, v99\n"
"v_rcp_f32 v100, v100\n"
"v_rcp_f32 v101, v101\n"
"v_rcp_f32 v102, v102\n"
"v_rcp_f32 v103, v103\n"
"v_pk_mul_f32 v[96:97], v[96:97], v[176:177]\n"
"v_pk_mul_f32 v[98:99], v[98:99], v[178:179]\n"
"v_pk_mul_f32 v[100:101], v[100:101], v[192:193]\n"
"v_pk_mul_f32 v[102:103], v[102:103], v[194:195]\n"
"v_cvt_pk_bf16_f32 v176, v96, v97\n"
"v_cvt_pk_bf16_f32 v178, v100, v101\n"
"v_cvt_pk_bf16_f32 v177, v98, v99\n"
"v_cvt_pk_bf16_f32 v179, v102, v103\n"
"buffer_store_dwordx4 v[176:179], v1, s[16:19], s61 offen offset:0 sc1\n"
"v_mfma_f32_16x16x32_bf16 v[236:239], a[136:139], a[12:15], v[236:239]\n"
"ds_read_b128 v[104:107], v44 offset:640\n"
"v_mfma_f32_16x16x32_bf16 v[236:239], a[140:143], a[28:31], v[236:239]\n"
"v_mfma_f32_16x16x32_bf16 v[236:239], a[144:147], a[44:47], v[236:239]\n"
"ds_read_b128 v[108:111], v44 offset:1664\n"
"v_mfma_f32_16x16x32_bf16 v[236:239], a[148:151], a[60:63], v[236:239]\n"
"s_waitcnt lgkmcnt(2)\n"
"v_mfma_f32_16x16x32_bf16 v[240:243], a[152:155], a[0:3], v[240:243]\n"
"ds_read_b128 v[112:115], v44 offset:0\n"
"v_mfma_f32_16x16x32_bf16 v[240:243], a[156:159], a[16:19], v[240:243]\n"
"v_mfma_f32_16x16x32_bf16 v[240:243], a[160:163], a[32:35], v[240:243]\n"
"ds_read_b128 v[116:119], v44 offset:64\n"
"v_mfma_f32_16x16x32_bf16 v[240:243], a[164:167], a[48:51], v[240:243]\n"
"v_mfma_f32_16x16x32_bf16 v[244:247], a[152:155], a[4:7], v[244:247]\n"
"v_mfma_f32_16x16x32_bf16 v[244:247], a[156:159], a[20:23], v[244:247]\n"
"v_mfma_f32_16x16x32_bf16 v[244:247], a[160:163], a[36:39], v[244:247]\n"
"v_mfma_f32_16x16x32_bf16 v[244:247], a[164:167], a[52:55], v[244:247]\n"
"v_pk_mul_f32 v[96:97], v[180:181], v[180:181]\n"
"v_pk_mul_f32 v[98:99], v[182:183], v[182:183]\n"
"v_pk_mul_f32 v[100:101], v[196:197], v[196:197]\n"
"v_pk_mul_f32 v[102:103], v[198:199], v[198:199]\n"
"v_pk_fma_f32 v[96:97], v[96:97], v[6:7], v[6:7] op_sel:[0,0,1] op_sel_hi:[1,0,1]\n"
"v_pk_fma_f32 v[98:99], v[98:99], v[6:7], v[6:7] op_sel:[0,0,1] op_sel_hi:[1,0,1]\n"
"v_pk_fma_f32 v[100:101], v[100:101], v[6:7], v[6:7] op_sel:[0,0,1] op_sel_hi:[1,0,1]\n"
"v_pk_fma_f32 v[102:103], v[102:103], v[6:7], v[6:7] op_sel:[0,0,1] op_sel_hi:[1,0,1]\n"
"v_pk_mul_f32 v[96:97], v[96:97], v[180:181]\n"
"v_pk_mul_f32 v[98:99], v[98:99], v[182:183]\n"
"v_pk_mul_f32 v[100:101], v[100:101], v[196:197]\n"
"v_pk_mul_f32 v[102:103], v[102:103], v[198:199]\n"
"v_exp_f32 v96, v96\n"
"v_exp_f32 v97, v97\n"
"v_exp_f32 v98, v98\n"
"v_exp_f32 v99, v99\n"
"v_exp_f32 v100, v100\n"
"v_exp_f32 v101, v101\n"
"v_exp_f32 v102, v102\n"
"v_exp_f32_e64 v103, v103\n"
"v_pk_fma_f32 v[96:97], v[96:97], v[8:9], v[8:9] op_sel_hi:[1,0,0]\n"
".Lpad8:\n"
"s_cbranch_execz .Lwr0\n"
"v_pk_fma_f32 v[98:99], v[98:99], v[8:9], v[8:9] op_sel_hi:[1,0,0]\n"
"v_pk_fma_f32 v[100:101], v[100:101], v[8:9], v[8:9] op_sel_hi:[1,0,0]\n"
"v_pk_fma_f32 v[102:103], v[102:103], v[8:9], v[8:9] op_sel_hi:[1,0,0]\n"
"v_rcp_f32 v96, v96\n"
"v_rcp_f32 v97, v97\n"
"v_rcp_f32 v98, v98\n"
"v_rcp_f32 v99, v99\n"
"v_rcp_f32 v100, v100\n"
"v_rcp_f32 v101, v101\n"
"v_rcp_f32 v102, v102\n"
"v_rcp_f32 v103, v103\n"
"v_pk_mul_f32 v[96:97], v[96:97], v[180:181]\n"
"v_pk_mul_f32 v[98:99], v[98:99], v[182:183]\n"
"v_pk_mul_f32 v[100:101], v[100:101], v[196:197]\n"
"v_pk_mul_f32 v[102:103], v[102:103], v[198:199]\n"
"v_cvt_pk_bf16_f32 v180, v96, v97\n"
"v_cvt_pk_bf16_f32 v182, v100, v101\n"
"v_cvt_pk_bf16_f32 v181, v98, v99\n"
"v_cvt_pk_bf16_f32 v183, v102, v103\n"
"buffer_store_dwordx4 v[180:183], v1, s[16:19], s61 offen offset:1024 sc1\n"
"v_mfma_f32_16x16x32_bf16 v[248:251], a[152:155], a[8:11], v[248:251]\n"
"v_mfma_f32_16x16x32_bf16 v[248:251], a[156:159], a[24:27], v[248:251]\n"
"v_mfma_f32_16x16x32_bf16 v[248:251], a[160:163], a[40:43], v[248:251]\n"
"v_mfma_f32_16x16x32_bf16 v[248:251], a[164:167], a[56:59], v[248:251]\n"
"v_mfma_f32_16x16x32_bf16 v[252:255], a[152:155], a[12:15], v[252:255]\n"
"v_mfma_f32_16x16x32_bf16 v[252:255], a[156:159], a[28:31], v[252:255]\n"
"v_mfma_f32_16x16x32_bf16 v[252:255], a[160:163], a[44:47], v[252:255]\n"
"v_mfma_f32_16x16x32_bf16 v[252:255], a[164:167], a[60:63], v[252:255]\n"
"s_waitcnt lgkmcnt(3)\n"
"v_pk_fma_f32 v[208:209], v[104:105], v[26:27], v[208:209] op_sel_hi:[1,0,1]\n"
"v_pk_fma_f32 v[210:211], v[106:107], v[26:27], v[210:211] op_sel_hi:[1,0,1]\n"
"v_pk_fma_f32 v[212:213], v[104:105], v[28:29], v[212:213] op_sel_hi:[1,0,1]\n"
"v_pk_fma_f32 v[214:215], v[106:107], v[28:29], v[214:215] op_sel_hi:[1,0,1]\n"
"v_pk_fma_f32 v[216:217], v[104:105], v[30:31], v[216:217] op_sel_hi:[1,0,1]\n"
"v_pk_fma_f32 v[218:219], v[106:107], v[30:31], v[218:219] op_sel_hi:[1,0,1]\n"
"v_pk_fma_f32 v[220:221], v[104:105], v[32:33], v[220:221] op_sel_hi:[1,0,1]\n"
"v_pk_fma_f32 v[222:223], v[106:107], v[32:33], v[222:223] op_sel_hi:[1,0,1]\n"
"s_waitcnt lgkmcnt(2)\n"
"v_pk_fma_f32 v[208:209], v[108:109], v[26:27], v[208:209] op_sel:[0,1,0] op_sel_hi:[1,1,1]\n"
"v_pk_fma_f32 v[210:211], v[110:111], v[26:27], v[210:211] op_sel:[0,1,0] op_sel_hi:[1,1,1]\n"
"v_pk_fma_f32 v[212:213], v[108:109], v[28:29], v[212:213] op_sel:[0,1,0] op_sel_hi:[1,1,1]\n"
"v_pk_fma_f32 v[214:215], v[110:111], v[28:29], v[214:215] op_sel:[0,1,0] op_sel_hi:[1,1,1]\n"
"v_pk_fma_f32 v[216:217], v[108:109], v[30:31], v[216:217] op_sel:[0,1,0] op_sel_hi:[1,1,1]\n"
"v_pk_fma_f32 v[218:219], v[110:111], v[30:31], v[218:219] op_sel:[0,1,0] op_sel_hi:[1,1,1]\n"
"v_pk_fma_f32 v[220:221], v[108:109], v[32:33], v[220:221] op_sel:[0,1,0] op_sel_hi:[1,1,1]\n"
"v_pk_fma_f32 v[222:223], v[110:111], v[32:33], v[222:223] op_sel:[0,1,0] op_sel_hi:[1,1,1]\n"
"s_waitcnt lgkmcnt(1)\n"
"s_nop 0\n"
"v_pk_fma_f32 v[48:49], v[112:113], v[26:27], v[48:49] op_sel_hi:[1,0,1]\n"
"v_pk_fma_f32 v[50:51], v[114:115], v[26:27], v[50:51] op_sel_hi:[1,0,1]\n"
"v_pk_fma_f32 v[52:53], v[112:113], v[28:29], v[52:53] op_sel_hi:[1,0,1]\n"
"v_pk_fma_f32 v[54:55], v[114:115], v[28:29], v[54:55] op_sel_hi:[1,0,1]\n"
"v_pk_fma_f32 v[56:57], v[112:113], v[30:31], v[56:57] op_sel_hi:[1,0,1]\n"
"v_pk_fma_f32 v[58:59], v[114:115], v[30:31], v[58:59] op_sel_hi:[1,0,1]\n"
"v_pk_fma_f32 v[60:61], v[112:113], v[32:33], v[60:61] op_sel_hi:[1,0,1]\n"
"v_pk_fma_f32 v[62:63], v[114:115], v[32:33], v[62:63] op_sel_hi:[1,0,1]\n"
"s_waitcnt lgkmcnt(0)\n"
"s_nop 0\n"
"v_pk_fma_f32 v[64:65], v[116:117], v[26:27], v[64:65] op_sel_hi:[1,0,1]\n"
"v_pk_fma_f32 v[66:67], v[118:119], v[26:27], v[66:67] op_sel_hi:[1,0,1]\n"
"v_pk_fma_f32 v[68:69], v[116:117], v[28:29], v[68:69] op_sel_hi:[1,0,1]\n"
"v_pk_fma_f32 v[70:71], v[118:119], v[28:29], v[70:71] op_sel_hi:[1,0,1]\n"
"v_pk_fma_f32 v[72:73], v[116:117], v[30:31], v[72:73] op_sel_hi:[1,0,1]\n"
"v_pk_fma_f32 v[74:75], v[118:119], v[30:31], v[74:75] op_sel_hi:[1,0,1]\n"
"v_pk_fma_f32 v[76:77], v[116:117], v[32:33], v[76:77] op_sel_hi:[1,0,1]\n"
"v_pk_fma_f32 v[78:79], v[118:119], v[32:33], v[78:79] op_sel_hi:[1,0,1]\n"
"v_pk_mul_f32 v[96:97], v[184:185], v[184:185]\n"
"buffer_load_dword v46, v25, s[16:19], 0 offen sc1\n"
"v_pk_mul_f32 v[98:99], v[186:187], v[186:187]\n"
"v_pk_mul_f32 v[100:101], v[200:201], v[200:201]\n"
"v_pk_mul_f32 v[102:103], v[202:203], v[202:203]\n"
"v_pk_fma_f32 v[96:97], v[96:97], v[10:11], v[10:11] op_sel:[0,0,1] op_sel_hi:[1,0,1]\n"
"v_pk_fma_f32 v[98:99], v[98:99], v[10:11], v[10:11] op_sel:[0,0,1] op_sel_hi:[1,0,1]\n"
"v_pk_fma_f32 v[100:101], v[100:101], v[10:11], v[10:11] op_sel:[0,0,1] op_sel_hi:[1,0,1]\n"
"v_pk_fma_f32 v[102:103], v[102:103], v[10:11], v[10:11] op_sel:[0,0,1] op_sel_hi:[1,0,1]\n"
"v_pk_mul_f32 v[96:97], v[96:97], v[184:185]\n"
"v_pk_mul_f32 v[98:99], v[98:99], v[186:187]\n"
"v_pk_mul_f32 v[100:101], v[100:101], v[200:201]\n"
"v_pk_mul_f32 v[102:103], v[102:103], v[202:203]\n"
"v_exp_f32 v96, v96\n"
"v_exp_f32 v97, v97\n"
"v_exp_f32 v98, v98\n"
"v_exp_f32 v99, v99\n"
"v_exp_f32 v100, v100\n"
"v_exp_f32 v101, v101\n"
"v_exp_f32 v102, v102\n"
"v_exp_f32 v103, v103\n"
"v_pk_fma_f32 v[96:97], v[96:97], v[12:13], v[12:13] op_sel_hi:[1,0,0]\n"
"v_pk_fma_f32 v[98:99], v[98:99], v[12:13], v[12:13] op_sel_hi:[1,0,0]\n"
"v_pk_fma_f32 v[100:101], v[100:101], v[12:13], v[12:13] op_sel_hi:[1,0,0]\n"
"v_pk_fma_f32 v[102:103], v[102:103], v[12:13], v[12:13] op_sel_hi:[1,0,0]\n"
"v_rcp_f32 v96, v96\n"
"v_rcp_f32 v97, v97\n"
"v_rcp_f32 v98, v98\n"
"v_rcp_f32 v99, v99\n"
"v_rcp_f32 v100, v100\n"
"v_rcp_f32 v101, v101\n"
"v_rcp_f32 v102, v102\n"
"v_rcp_f32 v103, v103\n"
"v_pk_mul_f32 v[96:97], v[96:97], v[184:185]\n"
"v_pk_mul_f32 v[98:99], v[98:99], v[186:187]\n"
"v_pk_mul_f32 v[100:101], v[100:101], v[200:201]\n"
"v_pk_mul_f32 v[102:103], v[102:103], v[202:203]\n"
"v_cvt_pk_bf16_f32 v184, v96, v97\n"
"v_cvt_pk_bf16_f32 v186, v100, v101\n"
"v_cvt_pk_bf16_f32 v185, v98, v99\n"
"v_cvt_pk_bf16_f32 v187, v102, v103\n"
"v_pk_mul_f32 v[96:97], v[188:189], v[188:189]\n"
"buffer_store_dwordx4 v[184:187], v1, s[16:19], s61 offen offset:2048 sc1\n"
"v_pk_mul_f32 v[98:99], v[190:191], v[190:191]\n"
"v_pk_mul_f32 v[100:101], v[204:205], v[204:205]\n"
"v_pk_mul_f32 v[102:103], v[206:207], v[206:207]\n"
"v_pk_fma_f32 v[96:97], v[96:97], v[14:15], v[14:15] op_sel:[0,0,1] op_sel_hi:[1,0,1]\n"
"v_pk_fma_f32 v[98:99], v[98:99], v[14:15], v[14:15] op_sel:[0,0,1] op_sel_hi:[1,0,1]\n"
"v_pk_fma_f32 v[100:101], v[100:101], v[14:15], v[14:15] op_sel:[0,0,1] op_sel_hi:[1,0,1]\n"
"v_pk_fma_f32 v[102:103], v[102:103], v[14:15], v[14:15] op_sel:[0,0,1] op_sel_hi:[1,0,1]\n"
"v_pk_mul_f32 v[96:97], v[96:97], v[188:189]\n"
"v_pk_mul_f32 v[98:99], v[98:99], v[190:191]\n"
"v_pk_mul_f32 v[100:101], v[100:101], v[204:205]\n"
"v_pk_mul_f32 v[102:103], v[102:103], v[206:207]\n"
"v_exp_f32 v96, v96\n"
"v_exp_f32 v97, v97\n"
"v_exp_f32 v98, v98\n"
"v_exp_f32 v99, v99\n"
"v_exp_f32 v100, v100\n"
"v_exp_f32 v101, v101\n"
"v_exp_f32 v102, v102\n"
"v_exp_f32 v103, v103\n"
"v_pk_fma_f32 v[96:97], v[96:97], v[16:17], v[16:17] op_sel_hi:[1,0,0]\n"
"v_pk_fma_f32 v[98:99], v[98:99], v[16:17], v[16:17] op_sel_hi:[1,0,0]\n"
"v_pk_fma_f32 v[100:101], v[100:101], v[16:17], v[16:17] op_sel_hi:[1,0,0]\n"
"v_pk_fma_f32 v[102:103], v[102:103], v[16:17], v[16:17] op_sel_hi:[1,0,0]\n"
"v_rcp_f32 v96, v96\n"
"v_rcp_f32 v97, v97\n"
"v_rcp_f32 v98, v98\n"
"v_rcp_f32 v99, v99\n"
"v_rcp_f32 v100, v100\n"
"v_rcp_f32 v101, v101\n"
"v_rcp_f32 v102, v102\n"
"v_rcp_f32 v103, v103\n"
"v_pk_mul_f32 v[96:97], v[96:97], v[188:189]\n"
"v_pk_mul_f32 v[98:99], v[98:99], v[190:191]\n"
"v_pk_mul_f32 v[100:101], v[100:101], v[204:205]\n"
"v_pk_mul_f32 v[102:103], v[102:103], v[206:207]\n"
"v_cvt_pk_bf16_f32 v188, v96, v97\n"
"v_cvt_pk_bf16_f32 v190, v100, v101\n"
"v_cvt_pk_bf16_f32 v189, v98, v99\n"
"v_cvt_pk_bf16_f32 v191, v102, v103\n"
"v_pk_mul_f32 v[96:97], v[208:209], v[208:209]\n"
"buffer_store_dwordx4 v[188:191], v1, s[16:19], s61 offen offset:3072 sc1\n"
"v_pk_mul_f32 v[98:99], v[210:211], v[210:211]\n"
"v_pk_mul_f32 v[100:101], v[224:225], v[224:225]\n"
"v_pk_mul_f32 v[102:103], v[226:227], v[226:227]\n"
"v_pk_fma_f32 v[96:97], v[96:97], v[2:3], v[2:3] op_sel:[0,0,1] op_sel_hi:[1,0,1]\n"
"v_pk_fma_f32 v[98:99], v[98:99], v[2:3], v[2:3] op_sel:[0,0,1] op_sel_hi:[1,0,1]\n"
"v_pk_fma_f32 v[100:101], v[100:101], v[2:3], v[2:3] op_sel:[0,0,1] op_sel_hi:[1,0,1]\n"
"v_pk_fma_f32 v[102:103], v[102:103], v[2:3], v[2:3] op_sel:[0,0,1] op_sel_hi:[1,0,1]\n"
"v_pk_mul_f32 v[96:97], v[96:97], v[208:209]\n"
"v_pk_mul_f32 v[98:99], v[98:99], v[210:211]\n"
"v_pk_mul_f32 v[100:101], v[100:101], v[224:225]\n"
"v_pk_mul_f32 v[102:103], v[102:103], v[226:227]\n"
"v_exp_f32 v96, v96\n"
"v_exp_f32 v97, v97\n"
"v_exp_f32 v98, v98\n"
"v_exp_f32 v99, v99\n"
"v_exp_f32 v100, v100\n"
"v_exp_f32 v101, v101\n"
"v_exp_f32 v102, v102\n"
"v_exp_f32 v103, v103\n"
"v_pk_fma_f32 v[96:97], v[96:97], v[4:5], v[4:5] op_sel_hi:[1,0,0]\n"
"v_pk_fma_f32 v[98:99], v[98:99], v[4:5], v[4:5] op_sel_hi:[1,0,0]\n"
"v_pk_fma_f32 v[100:101], v[100:101], v[4:5], v[4:5] op_sel_hi:[1,0,0]\n"
"v_pk_fma_f32 v[102:103], v[102:103], v[4:5], v[4:5] op_sel_hi:[1,0,0]\n"
"v_rcp_f32 v96, v96\n"
"v_rcp_f32 v97, v97\n"
"v_rcp_f32 v98, v98\n"
"v_rcp_f32 v99, v99\n"
"v_rcp_f32 v100, v100\n"
"v_rcp_f32 v101, v101\n"
"v_rcp_f32 v102, v102\n"
"v_rcp_f32 v103, v103\n"
"v_pk_mul_f32 v[96:97], v[96:97], v[208:209]\n"
"v_pk_mul_f32 v[98:99], v[98:99], v[210:211]\n"
"v_pk_mul_f32 v[100:101], v[100:101], v[224:225]\n"
"v_pk_mul_f32 v[102:103], v[102:103], v[226:227]\n"
"v_cvt_pk_bf16_f32 v208, v96, v97\n"
"v_cvt_pk_bf16_f32 v210, v100, v101\n"
"v_cvt_pk_bf16_f32 v209, v98, v99\n"
"v_cvt_pk_bf16_f32 v211, v102, v103\n"
"v_pk_mul_f32 v[96:97], v[212:213], v[212:213]\n"
"v_mul_f32 v5, 0x3e8293ee, v5\n"
"v_pk_mul_f32 v[48:49], v[48:49], v[4:5] op_sel:[0,1] op_sel_hi:[1,1]\n"
"v_pk_mul_f32 v[50:51], v[50:51], v[4:5] op_sel:[0,1] op_sel_hi:[1,1]\n"
"v_pk_mul_f32 v[64:65], v[64:65], v[4:5] op_sel:[0,1] op_sel_hi:[1,1]\n"
"v_pk_mul_f32 v[66:67], v[66:67], v[4:5] op_sel:[0,1] op_sel_hi:[1,1]\n"
"v_cvt_pk_bf16_f32 v48, v48, v49\n"
"v_cvt_pk_bf16_f32 v49, v50, v51\n"
"v_cvt_pk_bf16_f32 v50, v64, v65\n"
"v_cvt_pk_bf16_f32 v51, v66, v67\n"
"s_waitcnt vmcnt(2)\n"
"v_cmp_ne_u32 vcc, 0, v46\n"
"s_mov_b32 s72, 0\n"
"s_nop 0\n"
"s_cbranch_vccz .Lkv_ok\n"
".Lkv_lp:\n"
"buffer_load_dword v46, v25, s[16:19], 0 offen sc1\n"
"s_add_u32 s72, s72, 1\n"
"s_waitcnt vmcnt(0)\n"
"v_cmp_ne_u32 vcc, 0, v46\n"
"s_cmp_lt_u32 s72, 262144\n"
"s_nop 0\n"
"s_cselect_b64 vcc, vcc, 0\n"
"s_cbranch_vccnz .Lkv_lp\n"
".Lkv_ok:\n"
"s_nop 0\n"
"buffer_load_dwordx4 v[160:163], v1, s[16:19], s98 offen offset:0 sc1\n"
"buffer_load_dwordx4 v[164:167], v1, s[16:19], s98 offen offset:1024 sc1\n"
"buffer_store_dwordx4 v[208:211], v1, s[16:19], s62 offen offset:0 sc1\n"
"v_pk_mul_f32 v[98:99], v[214:215], v[214:215]\n"
"v_pk_mul_f32 v[100:101], v[228:229], v[228:229]\n"
"v_pk_mul_f32 v[102:103], v[230:231], v[230:231]\n"
"v_pk_fma_f32 v[96:97], v[96:97], v[6:7], v[6:7] op_sel:[0,0,1] op_sel_hi:[1,0,1]\n"
"v_pk_fma_f32 v[98:99], v[98:99], v[6:7], v[6:7] op_sel:[0,0,1] op_sel_hi:[1,0,1]\n"
"v_pk_fma_f32 v[100:101], v[100:101], v[6:7], v[6:7] op_sel:[0,0,1] op_sel_hi:[1,0,1]\n"
"v_pk_fma_f32 v[102:103], v[102:103], v[6:7], v[6:7] op_sel:[0,0,1] op_sel_hi:[1,0,1]\n"
"v_pk_mul_f32 v[96:97], v[96:97], v[212:213]\n"
"v_pk_mul_f32 v[98:99], v[98:99], v[214:215]\n"
"v_pk_mul_f32 v[100:101], v[100:101], v[228:229]\n"
"v_pk_mul_f32 v[102:103], v[102:103], v[230:231]\n"
"v_exp_f32 v96, v96\n"
"v_exp_f32 v97, v97\n"
"v_exp_f32 v98, v98\n"
"v_exp_f32 v99, v99\n"
"v_exp_f32 v100, v100\n"
"v_exp_f32 v101, v101\n"
"v_exp_f32 v102, v102\n"
"v_exp_f32 v103, v103\n"
"v_pk_fma_f32 v[96:97], v[96:97], v[8:9], v[8:9] op_sel_hi:[1,0,0]\n"
"v_pk_fma_f32 v[98:99], v[98:99], v[8:9], v[8:9] op_sel_hi:[1,0,0]\n"
"v_pk_fma_f32 v[100:101], v[100:101], v[8:9], v[8:9] op_sel_hi:[1,0,0]\n"
"v_pk_fma_f32 v[102:103], v[102:103], v[8:9], v[8:9] op_sel_hi:[1,0,0]\n"
"v_rcp_f32 v96, v96\n"
"v_rcp_f32 v97, v97\n"
"v_rcp_f32 v98, v98\n"
"v_rcp_f32 v99, v99\n"
"v_rcp_f32 v100, v100\n"
"v_rcp_f32 v101, v101\n"
"v_rcp_f32 v102, v102\n"
"v_rcp_f32 v103, v103\n"
"v_pk_mul_f32 v[96:97], v[96:97], v[212:213]\n"
"v_pk_mul_f32 v[98:99], v[98:99], v[214:215]\n"
"v_pk_mul_f32 v[100:101], v[100:101], v[228:229]\n"
"v_pk_mul_f32 v[102:103], v[102:103], v[230:231]\n"
"v_cvt_pk_bf16_f32 v212, v96, v97\n"
"v_cvt_pk_bf16_f32 v214, v100, v101\n"
"v_cvt_pk_bf16_f32 v213, v98, v99\n"
"v_cvt_pk_bf16_f32 v215, v102, v103\n"
"v_pk_mul_f32 v[96:97], v[216:217], v[216:217]\n"
"v_mul_f32 v9, 0x3e8293ee, v9\n"
"v_pk_mul_f32 v[52:53], v[52:53], v[8:9] op_sel:[0,1] op_sel_hi:[1,1]\n"
"v_pk_mul_f32 v[54:55], v[54:55], v[8:9] op_sel:[0,1] op_sel_hi:[1,1]\n"
"v_pk_mul_f32 v[68:69], v[68:69], v[8:9] op_sel:[0,1] op_sel_hi:[1,1]\n"
"v_pk_mul_f32 v[70:71], v[70:71], v[8:9] op_sel:[0,1] op_sel_hi:[1,1]\n"
"v_cvt_pk_bf16_f32 v52, v52, v53\n"
"v_cvt_pk_bf16_f32 v53, v54, v55\n"
"v_cvt_pk_bf16_f32 v54, v68, v69\n"
"v_cvt_pk_bf16_f32 v55, v70, v71\n"
"buffer_load_dwordx4 v[168:171], v1, s[16:19], s98 offen offset:2048 sc1\n"
"buffer_load_dwordx4 v[172:175], v1, s[16:19], s98 offen offset:3072 sc1\n"
"buffer_store_dwordx4 v[212:215], v1, s[16:19], s62 offen offset:1024 sc1\n"
"v_pk_mul_f32 v[98:99], v[218:219], v[218:219]\n"
"v_pk_mul_f32 v[100:101], v[232:233], v[232:233]\n"
"v_pk_mul_f32 v[102:103], v[234:235], v[234:235]\n"
"v_pk_fma_f32 v[96:97], v[96:97], v[10:11], v[10:11] op_sel:[0,0,1] op_sel_hi:[1,0,1]\n"
"v_pk_fma_f32 v[98:99], v[98:99], v[10:11], v[10:11] op_sel:[0,0,1] op_sel_hi:[1,0,1]\n"
"v_pk_fma_f32 v[100:101], v[100:101], v[10:11], v[10:11] op_sel:[0,0,1] op_sel_hi:[1,0,1]\n"
"v_pk_fma_f32 v[102:103], v[102:103], v[10:11], v[10:11] op_sel:[0,0,1] op_sel_hi:[1,0,1]\n"
"v_pk_mul_f32 v[96:97], v[96:97], v[216:217]\n"
"v_pk_mul_f32 v[98:99], v[98:99], v[218:219]\n"
"v_pk_mul_f32 v[100:101], v[100:101], v[232:233]\n"
"v_pk_mul_f32 v[102:103], v[102:103], v[234:235]\n"
"v_exp_f32 v96, v96\n"
"v_exp_f32 v97, v97\n"
"v_exp_f32 v98, v98\n"
"v_exp_f32 v99, v99\n"
"v_exp_f32 v100, v100\n"
"v_exp_f32 v101, v101\n"
"v_exp_f32 v102, v102\n"
"v_exp_f32 v103, v103\n"
"v_pk_fma_f32 v[96:97], v[96:97], v[12:13], v[12:13] op_sel_hi:[1,0,0]\n"
"v_pk_fma_f32 v[98:99], v[98:99], v[12:13], v[12:13] op_sel_hi:[1,0,0]\n"
"v_pk_fma_f32 v[100:101], v[100:101], v[12:13], v[12:13] op_sel_hi:[1,0,0]\n"
"v_pk_fma_f32 v[102:103], v[102:103], v[12:13], v[12:13] op_sel_hi:[1,0,0]\n"
"v_rcp_f32 v96, v96\n"
"v_rcp_f32 v97, v97\n"
"v_rcp_f32 v98, v98\n"
"v_rcp_f32 v99, v99\n"
"v_rcp_f32 v100, v100\n"
"v_rcp_f32 v101, v101\n"
"v_rcp_f32 v102, v102\n"
"v_rcp_f32 v103, v103\n"
"v_pk_mul_f32 v[96:97], v[96:97], v[216:217]\n"
"v_pk_mul_f32 v[98:99], v[98:99], v[218:219]\n"
"v_pk_mul_f32 v[100:101], v[100:101], v[232:233]\n"
"v_pk_mul_f32 v[102:103], v[102:103], v[234:235]\n"
"v_cvt_pk_bf16_f32 v216, v96, v97\n"
"v_cvt_pk_bf16_f32 v218, v100, v101\n"
"v_cvt_pk_bf16_f32 v217, v98, v99\n"
"v_cvt_pk_bf16_f32 v219, v102, v103\n"
"v_pk_mul_f32 v[96:97], v[220:221], v[220:221]\n"
"v_mul_f32 v13, 0x3e8293ee, v13\n"
"v_pk_mul_f32 v[56:57], v[56:57], v[12:13] op_sel:[0,1] op_sel_hi:[1,1]\n"
"v_pk_mul_f32 v[58:59], v[58:59], v[12:13] op_sel:[0,1] op_sel_hi:[1,1]\n"
"v_pk_mul_f32 v[72:73], v[72:73], v[12:13] op_sel:[0,1] op_sel_hi:[1,1]\n"
"v_pk_mul_f32 v[74:75], v[74:75], v[12:13] op_sel:[0,1] op_sel_hi:[1,1]\n"
"v_cvt_pk_bf16_f32 v56, v56, v57\n"
"v_cvt_pk_bf16_f32 v57, v58, v59\n"
"v_cvt_pk_bf16_f32 v58, v72, v73\n"
"v_cvt_pk_bf16_f32 v59, v74, v75\n"
"buffer_load_dwordx4 v[192:195], v1, s[16:19], s99 offen offset:0 sc1\n"
"buffer_load_dwordx4 v[196:199], v1, s[16:19], s99 offen offset:1024 sc1\n"
"buffer_store_dwordx4 v[216:219], v1, s[16:19], s62 offen offset:2048 sc1\n"
"v_pk_mul_f32 v[98:99], v[222:223], v[222:223]\n"
"v_pk_mul_f32 v[100:101], v[236:237], v[236:237]\n"
"v_pk_mul_f32 v[102:103], v[238:239], v[238:239]\n"
"v_pk_fma_f32 v[96:97], v[96:97], v[14:15], v[14:15] op_sel:[0,0,1] op_sel_hi:[1,0,1]\n"
"v_pk_fma_f32 v[98:99], v[98:99], v[14:15], v[14:15] op_sel:[0,0,1] op_sel_hi:[1,0,1]\n"
"v_pk_fma_f32 v[100:101], v[100:101], v[14:15], v[14:15] op_sel:[0,0,1] op_sel_hi:[1,0,1]\n"
"v_pk_fma_f32 v[102:103], v[102:103], v[14:15], v[14:15] op_sel:[0,0,1] op_sel_hi:[1,0,1]\n"
"v_pk_mul_f32 v[96:97], v[96:97], v[220:221]\n"
"v_pk_mul_f32 v[98:99], v[98:99], v[222:223]\n"
"v_pk_mul_f32 v[100:101], v[100:101], v[236:237]\n"
"v_pk_mul_f32 v[102:103], v[102:103], v[238:239]\n"
"v_exp_f32 v96, v96\n"
"v_exp_f32 v97, v97\n"
"v_exp_f32 v98, v98\n"
"v_exp_f32 v99, v99\n"
"v_exp_f32 v100, v100\n"
"v_exp_f32 v101, v101\n"
"v_exp_f32 v102, v102\n"
"v_exp_f32 v103, v103\n"
"v_pk_fma_f32 v[96:97], v[96:97], v[16:17], v[16:17] op_sel_hi:[1,0,0]\n"
"v_pk_fma_f32 v[98:99], v[98:99], v[16:17], v[16:17] op_sel_hi:[1,0,0]\n"
"v_pk_fma_f32 v[100:101], v[100:101], v[16:17], v[16:17] op_sel_hi:[1,0,0]\n"
"v_pk_fma_f32 v[102:103], v[102:103], v[16:17], v[16:17] op_sel_hi:[1,0,0]\n"
"v_rcp_f32 v96, v96\n"
"v_rcp_f32 v97, v97\n"
"v_rcp_f32 v98, v98\n"
"v_rcp_f32 v99, v99\n"
"v_rcp_f32 v100, v100\n"
"v_rcp_f32 v101, v101\n"
"v_rcp_f32 v102, v102\n"
"v_rcp_f32 v103, v103\n"
"v_pk_mul_f32 v[96:97], v[96:97], v[220:221]\n"
"v_pk_mul_f32 v[98:99], v[98:99], v[222:223]\n"
"v_pk_mul_f32 v[100:101], v[100:101], v[236:237]\n"
"v_pk_mul_f32 v[102:103], v[102:103], v[238:239]\n"
"v_cvt_pk_bf16_f32 v220, v96, v97\n"
"v_cvt_pk_bf16_f32 v222, v100, v101\n"
"v_cvt_pk_bf16_f32 v221, v98, v99\n"
"v_cvt_pk_bf16_f32 v223, v102, v103\n"
"v_pk_mul_f32 v[96:97], v[240:241], v[240:241]\n"
"v_mul_f32 v17, 0x3e8293ee, v17\n"
"v_pk_mul_f32 v[60:61], v[60:61], v[16:17] op_sel:[0,1] op_sel_hi:[1,1]\n"
"v_pk_mul_f32 v[62:63], v[62:63], v[16:17] op_sel:[0,1] op_sel_hi:[1,1]\n"
"v_pk_mul_f32 v[76:77], v[76:77], v[16:17] op_sel:[0,1] op_sel_hi:[1,1]\n"
"v_pk_mul_f32 v[78:79], v[78:79], v[16:17] op_sel:[0,1] op_sel_hi:[1,1]\n"
"v_cvt_pk_bf16_f32 v60, v60, v61\n"
"v_cvt_pk_bf16_f32 v61, v62, v63\n"
"v_cvt_pk_bf16_f32 v62, v76, v77\n"
"v_cvt_pk_bf16_f32 v63, v78, v79\n"
"buffer_load_dwordx4 v[200:203], v1, s[16:19], s99 offen offset:2048 sc1\n"
"buffer_load_dwordx4 v[204:207], v1, s[16:19], s99 offen offset:3072 sc1\n"
"buffer_store_dwordx4 v[220:223], v1, s[16:19], s62 offen offset:3072 sc1\n"
"v_pk_mul_f32 v[98:99], v[242:243], v[242:243]\n"
"v_pk_mul_f32 v[100:101], v[244:245], v[244:245]\n"
"v_pk_mul_f32 v[102:103], v[246:247], v[246:247]\n"
"v_pk_fma_f32 v[96:97], v[96:97], v[2:3], v[2:3] op_sel:[0,0,1] op_sel_hi:[1,0,1]\n"
"v_pk_fma_f32 v[98:99], v[98:99], v[2:3], v[2:3] op_sel:[0,0,1] op_sel_hi:[1,0,1]\n"
"v_pk_fma_f32 v[100:101], v[100:101], v[6:7], v[6:7] op_sel:[0,0,1] op_sel_hi:[1,0,1]\n"
"v_pk_fma_f32 v[102:103], v[102:103], v[6:7], v[6:7] op_sel:[0,0,1] op_sel_hi:[1,0,1]\n"
"v_pk_mul_f32 v[96:97], v[96:97], v[240:241]\n"
"v_pk_mul_f32 v[98:99], v[98:99], v[242:243]\n"
"v_pk_mul_f32 v[100:101], v[100:101], v[244:245]\n"
"v_pk_mul_f32 v[102:103], v[102:103], v[246:247]\n"
"v_exp_f32 v96, v96\n"
"v_exp_f32 v97, v97\n"
"v_exp_f32 v98, v98\n"
"v_exp_f32 v99, v99\n"
"v_exp_f32 v100, v100\n"
"v_exp_f32 v101, v101\n"
"v_exp_f32 v102, v102\n"
"v_exp_f32 v103, v103\n"
"v_pk_fma_f32 v[96:97], v[96:97], v[4:5], v[4:5] op_sel_hi:[1,0,0]\n"
"v_pk_fma_f32 v[98:99], v[98:99], v[4:5], v[4:5] op_sel_hi:[1,0,0]\n"
"v_pk_fma_f32 v[100:101], v[100:101], v[8:9], v[8:9] op_sel_hi:[1,0,0]\n"
"v_pk_fma_f32 v[102:103], v[102:103], v[8:9], v[8:9] op_sel_hi:[1,0,0]\n"
"v_rcp_f32 v96, v96\n"
"v_rcp_f32 v97, v97\n"
"v_rcp_f32 v98, v98\n"
"v_rcp_f32 v99, v99\n"
"v_rcp_f32 v100, v100\n"
"v_rcp_f32 v101, v101\n"
"v_rcp_f32 v102, v102\n"
"v_rcp_f32 v103, v103\n"
"v_pk_mul_f32 v[96:97], v[96:97], v[240:241]\n"
"v_pk_mul_f32 v[98:99], v[98:99], v[242:243]\n"
"v_pk_mul_f32 v[100:101], v[100:101], v[244:245]\n"
"v_pk_mul_f32 v[102:103], v[102:103], v[246:247]\n"
"v_cvt_pk_bf16_f32 v240, v96, v97\n"
"v_cvt_pk_bf16_f32 v242, v100, v101\n"
"v_cvt_pk_bf16_f32 v241, v98, v99\n"
"v_cvt_pk_bf16_f32 v243, v102, v103\n"
"v_pk_mul_f32 v[96:97], v[248:249], v[248:249]\n"
"v_permlane32_swap_b32 v240, v242\n"
"v_permlane32_swap_b32 v241, v243\n"
"v_and_b32_e64 v34, 15, v0\n"
"v_bfe_u32 v35, v0, 4, 2\n"
"v_lshlrev_b32 v35, 2, v35\n"
"v_sub_u32 v34, v34, v35\n"
"v_mov_b32 v35, 4048155338\n"
"v_cmp_gt_i32 s[64:65], 0, v34\n"
"v_cmp_gt_i32 s[66:67], 1, v34\n"
"v_cmp_gt_i32 s[68:69], 2, v34\n"
"v_cmp_gt_i32 vcc, 3, v34\n"
"v_cndmask_b32 v30, v23, v35, s[64:65]\n"
"v_cndmask_b32 v31, v23, v35, s[66:67]\n"
"v_cndmask_b32 v32, v23, v35, s[68:69]\n"
"v_cndmask_b32 v33, v23, v35, vcc\n"
"v_cndmask_b32 v26, v35, v23, s[64:65]\n"
"v_cndmask_b32 v27, v35, v23, s[66:67]\n"
"v_cndmask_b32 v28, v35, v23, s[68:69]\n"
"v_cndmask_b32 v29, v35, v23, vcc\n"
"s_nop 0\n"
"buffer_store_dwordx4 v[240:243], v24, s[16:19], s63 offen offset:0 sc1\n"
"v_pk_mul_f32 v[98:99], v[250:251], v[250:251]\n"
"v_pk_mul_f32 v[100:101], v[252:253], v[252:253]\n"
"v_pk_mul_f32 v[102:103], v[254:255], v[254:255]\n"
"v_pk_fma_f32 v[96:97], v[96:97], v[10:11], v[10:11] op_sel:[0,0,1] op_sel_hi:[1,0,1]\n"
"v_pk_fma_f32 v[98:99], v[98:99], v[10:11], v[10:11] op_sel:[0,0,1] op_sel_hi:[1,0,1]\n"
"v_pk_fma_f32 v[100:101], v[100:101], v[14:15], v[14:15] op_sel:[0,0,1] op_sel_hi:[1,0,1]\n"
"v_pk_fma_f32 v[102:103], v[102:103], v[14:15], v[14:15] op_sel:[0,0,1] op_sel_hi:[1,0,1]\n"
"v_pk_mul_f32 v[96:97], v[96:97], v[248:249]\n"
"v_pk_mul_f32 v[98:99], v[98:99], v[250:251]\n"
"v_pk_mul_f32 v[100:101], v[100:101], v[252:253]\n"
"v_pk_mul_f32 v[102:103], v[102:103], v[254:255]\n"
"v_exp_f32 v96, v96\n"
"v_exp_f32 v97, v97\n"
"v_exp_f32 v98, v98\n"
"v_exp_f32 v99, v99\n"
"v_exp_f32 v100, v100\n"
"v_exp_f32 v101, v101\n"
"v_exp_f32 v102, v102\n"
"v_exp_f32 v103, v103\n"
"v_pk_fma_f32 v[96:97], v[96:97], v[12:13], v[12:13] op_sel_hi:[1,0,0]\n"
"v_pk_fma_f32 v[98:99], v[98:99], v[12:13], v[12:13] op_sel_hi:[1,0,0]\n"
"v_pk_fma_f32 v[100:101], v[100:101], v[16:17], v[16:17] op_sel_hi:[1,0,0]\n"
"v_pk_fma_f32 v[102:103], v[102:103], v[16:17], v[16:17] op_sel_hi:[1,0,0]\n"
"v_rcp_f32 v96, v96\n"
"v_rcp_f32 v97, v97\n"
"v_rcp_f32 v98, v98\n"
"v_rcp_f32 v99, v99\n"
"v_rcp_f32 v100, v100\n"
"v_rcp_f32 v101, v101\n"
"v_rcp_f32 v102, v102\n"
"v_rcp_f32 v103, v103\n"
"v_pk_mul_f32 v[96:97], v[96:97], v[248:249]\n"
"v_pk_mul_f32 v[98:99], v[98:99], v[250:251]\n"
"v_pk_mul_f32 v[100:101], v[100:101], v[252:253]\n"
"v_pk_mul_f32 v[102:103], v[102:103], v[254:255]\n"
"v_cvt_pk_bf16_f32 v248, v96, v97\n"
"v_cvt_pk_bf16_f32 v250, v100, v101\n"
"v_cvt_pk_bf16_f32 v249, v98, v99\n"
"v_cvt_pk_bf16_f32 v251, v102, v103\n"
"v_mfma_f32_16x16x32_bf16 v[144:147], v[88:91], v[56:59], v[30:33]\n"
"v_permlane32_swap_b32 v248, v250\n"
"v_permlane32_swap_b32 v249, v251\n"
"buffer_store_dwordx4 v[248:251], v24, s[16:19], s63 offen offset:2048 sc1\n"
"s_nop 4\n"
"v_exp_f32 v144, v144\n"
"v_mfma_f32_16x16x32_bf16 v[152:155], v[88:91], v[60:63], 0\n"
"s_waitcnt vmcnt(13)\n"
"ds_write_b128 v22, v[160:163] offset:0\n"
"v_exp_f32 v145, v145\n"
"v_mfma_f32_16x16x32_bf16 v[156:159], v[92:95], v[60:63], v[30:33]\n"
"s_waitcnt vmcnt(12)\n"
"ds_write_b128 v22, v[164:167] offset:1024\n"
"v_exp_f32 v146, v146\n"
"v_exp_f32_e64 v147, v147\n"
"v_cvt_pk_bf16_f32 v224, v144, v145\n"
"v_cvt_pk_bf16_f32 v225, v146, v147\n"
"v_mov_b64 v[226:227], 0\n"
"v_exp_f32 v152, v152\n"
"v_mfma_f32_16x16x32_bf16 v[144:147], v[80:83], v[48:51], v[30:33]\n"
"s_waitcnt vmcnt(10)\n"
"ds_write_b128 v22, v[168:171] offset:2048\n"
"v_exp_f32 v153, v153\n"
"v_exp_f32_e64 v154, v154\n"
"v_mfma_f32_16x16x32_bf16 v[112:115], v[140:143], v[224:227], 0\n"
"s_waitcnt vmcnt(9)\n"
"ds_write_b128 v22, v[172:175] offset:3072\n"
"v_exp_f32 v155, v155\n"
"v_exp_f32_e64 v156, v156\n"
"v_mfma_f32_16x16x32_bf16 v[116:119], v[136:139], v[224:227], 0\n"
"v_cvt_pk_bf16_f32 v228, v152, v153\n"
"v_exp_f32_e64 v157, v157\n"
"v_mfma_f32_16x16x32_bf16 v[72:75], a[184:187], v[224:227], 0\n"
"v_exp_f32_e64 v158, v158\n"
"v_cvt_pk_bf16_f32 v229, v154, v155\n"
"v_exp_f32_e64 v159, v159\n"
"v_cvt_pk_bf16_f32 v230, v156, v157\n"
"v_cvt_pk_bf16_f32 v231, v158, v159\n"
"v_exp_f32_e64 v144, v144\n"
"v_mfma_f32_16x16x32_bf16 v[152:155], v[80:83], v[52:55], 0\n"
"s_waitcnt vmcnt(7)\n"
"ds_write_b128 v22, v[192:195] offset:32768\n"
"v_exp_f32 v145, v145\n"
"v_mfma_f32_16x16x32_bf16 v[156:159], v[84:87], v[52:55], v[30:33]\n"
"s_waitcnt vmcnt(6)\n"
"ds_write_b128 v22, v[196:199] offset:33792\n"
"v_exp_f32 v146, v146\n"
"v_mfma_f32_16x16x32_bf16 v[120:123], v[140:143], v[228:231], 0\n"
"v_exp_f32_e64 v147, v147\n"
"v_mfma_f32_16x16x32_bf16 v[124:127], v[136:139], v[228:231], 0\n"
"v_cvt_pk_bf16_f32 v224, v144, v145\n"
"v_cvt_pk_bf16_f32 v225, v146, v147\n"
"v_mfma_f32_16x16x32_bf16 v[76:79], a[184:187], v[228:231], 0\n"
"v_mov_b64 v[226:227], 0\n"
"v_exp_f32 v152, v152\n"
"v_mfma_f32_16x16x32_bf16 v[144:147], v[80:83], v[56:59], 0\n"
"s_waitcnt vmcnt(4)\n"
"ds_write_b128 v22, v[200:203] offset:34816\n"
"v_exp_f32 v153, v153\n"
"v_exp_f32_e64 v154, v154\n"
"v_mfma_f32_16x16x32_bf16 v[148:151], v[84:87], v[56:59], 0\n"
"s_waitcnt vmcnt(3)\n"
"ds_write_b128 v22, v[204:207] offset:35840\n"
"v_exp_f32 v155, v155\n"
"v_exp_f32_e64 v156, v156\n"
"v_mfma_f32_16x16x32_bf16 v[96:99], v[132:135], v[224:227], 0\n"
"v_cvt_pk_bf16_f32 v228, v152, v153\n"
"v_exp_f32_e64 v157, v157\n"
"v_mfma_f32_16x16x32_bf16 v[100:103], v[128:131], v[224:227], 0\n"
"v_exp_f32_e64 v158, v158\n"
"v_cvt_pk_bf16_f32 v229, v154, v155\n"
"v_mfma_f32_16x16x32_bf16 v[64:67], a[184:187], v[224:227], 0\n"
"v_exp_f32_e64 v159, v159\n"
"v_cvt_pk_bf16_f32 v230, v156, v157\n"
"v_cvt_pk_bf16_f32 v231, v158, v159\n"
"s_waitcnt lgkmcnt(0)\n"
"s_barrier\n"
"s_waitcnt vmcnt(0)\n"
"s_mov_b64 exec, 1\n"
"buffer_store_dword v23, off, s[16:19], s77 sc1\n"
"s_mov_b64 exec, -1\n"
"v_exp_f32 v144, v144\n"
"v_mfma_f32_16x16x32_bf16 v[152:155], v[80:83], v[60:63], 0\n"
"ds_read_b128 a[168:171], v22 offset:14336\n"
"v_exp_f32 v145, v145\n"
"v_exp_f32 v146, v146\n"
"v_mfma_f32_16x16x32_bf16 v[156:159], v[84:87], v[60:63], 0\n"
"ds_read_b128 a[172:175], v22 offset:15360\n"
"v_exp_f32 v147, v147\n"
"v_exp_f32 v148, v148\n"
"v_mfma_f32_16x16x32_bf16 v[104:107], v[132:135], v[228:231], 0\n"
"v_cvt_pk_bf16_f32 v224, v144, v145\n"
"v_exp_f32_e64 v149, v149\n"
"v_mfma_f32_16x16x32_bf16 v[108:111], v[128:131], v[228:231], 0\n"
"v_exp_f32_e64 v150, v150\n"
"v_cvt_pk_bf16_f32 v225, v146, v147\n"
"v_mfma_f32_16x16x32_bf16 v[68:71], a[184:187], v[228:231], 0\n"
"v_exp_f32_e64 v151, v151\n"
"v_cvt_pk_bf16_f32 v226, v148, v149\n"
"v_cvt_pk_bf16_f32 v227, v150, v151\n"
"v_exp_f32 v152, v152\n"
"s_waitcnt lgkmcnt(0)\n"
"v_mfma_f32_16x16x32_bf16 v[144:147], a[168:171], v[48:51], 0\n"
"v_exp_f32 v153, v153\n"
"v_exp_f32 v154, v154\n"
"v_mfma_f32_16x16x32_bf16 v[148:151], a[172:175], v[48:51], 0\n"
"v_exp_f32 v155, v155\n"
"v_exp_f32 v156, v156\n"
"v_mfma_f32_16x16x32_bf16 v[112:115], v[132:135], v[224:227], v[112:115]\n"
"v_cvt_pk_bf16_f32 v228, v152, v153\n"
"v_exp_f32_e64 v157, v157\n"
"v_mfma_f32_16x16x32_bf16 v[116:119], v[128:131], v[224:227], v[116:119]\n"
"v_exp_f32_e64 v158, v158\n"
"v_cvt_pk_bf16_f32 v229, v154, v155\n"
"v_mfma_f32_16x16x32_bf16 v[72:75], a[184:187], v[224:227], v[72:75]\n"
"v_exp_f32_e64 v159, v159\n"
"v_cvt_pk_bf16_f32 v230, v156, v157\n"
"v_cvt_pk_bf16_f32 v231, v158, v159\n"
"s_cmp_eq_u32 s91, 7\n"
"s_cbranch_scc1 .Lax7f\n"
"v_exp_f32_e64 v144, v144\n"
"v_mfma_f32_16x16x32_bf16 v[152:155], a[168:171], v[52:55], 0\n"
"v_exp_f32 v145, v145\n"
"v_exp_f32 v146, v146\n"
"v_mfma_f32_16x16x32_bf16 v[156:159], a[172:175], v[52:55], 0\n"
"v_exp_f32 v147, v147\n"
"v_exp_f32 v148, v148\n"
"v_mfma_f32_16x16x32_bf16 v[120:123], v[132:135], v[228:231], v[120:123]\n"
"ds_read_b128 a[176:179], v22 offset:47104\n"
"v_cvt_pk_bf16_f32 v224, v144, v145\n"
"v_exp_f32_e64 v149, v149\n"
"v_mfma_f32_16x16x32_bf16 v[124:127], v[128:131], v[228:231], v[124:127]\n"
"ds_read_b128 a[180:183], v22 offset:48128\n"
"v_exp_f32_e64 v150, v150\n"
"v_cvt_pk_bf16_f32 v225, v146, v147\n"
"v_mfma_f32_16x16x32_bf16 v[76:79], a[184:187], v[228:231], v[76:79]\n"
"v_exp_f32_e64 v151, v151\n"
"v_cvt_pk_bf16_f32 v226, v148, v149\n"
"v_cvt_pk_bf16_f32 v227, v150, v151\n"
"v_exp_f32_e64 v152, v152\n"
"v_mfma_f32_16x16x32_bf16 v[144:147], a[168:171], v[56:59], 0\n"
"v_exp_f32 v153, v153\n"
"v_exp_f32 v154, v154\n"
"v_mfma_f32_16x16x32_bf16 v[148:151], a[172:175], v[56:59], 0\n"
".Lpad9:\n"
"s_cbranch_execz .Lpad13\n"
"v_exp_f32 v155, v155\n"
"v_exp_f32_e64 v156, v156\n"
"s_waitcnt lgkmcnt(0)\n"
"v_mfma_f32_16x16x32_bf16 v[96:99], a[176:179], v[224:227], v[96:99]\n"
"v_cvt_pk_bf16_f32 v228, v152, v153\n"
"v_exp_f32_e64 v157, v157\n"
"v_mfma_f32_16x16x32_bf16 v[100:103], a[180:183], v[224:227], v[100:103]\n"
"v_exp_f32_e64 v158, v158\n"
"v_cvt_pk_bf16_f32 v229, v154, v155\n"
"v_mfma_f32_16x16x32_bf16 v[64:67], a[184:187], v[224:227], v[64:67]\n"
"v_exp_f32_e64 v159, v159\n"
"v_cvt_pk_bf16_f32 v230, v156, v157\n"
"v_cvt_pk_bf16_f32 v231, v158, v159\n"
"v_accvgpr_read_b32 v10, a245\n"
"v_accvgpr_read_b32 v11, a246\n"
"v_accvgpr_read_b32 v12, a247\n"
"v_accvgpr_read_b32 v13, a248\n"
"v_exp_f32_e64 v144, v144\n"
"v_mfma_f32_16x16x32_bf16 v[152:155], a[168:171], v[60:63], 0\n"
"ds_read_b128 a[168:171], v22 offset:12288\n"
"v_exp_f32 v145, v145\n"
"v_exp_f32 v146, v146\n"
"v_mfma_f32_16x16x32_bf16 v[156:159], a[172:175], v[60:63], 0\n"
"ds_read_b128 a[172:175], v22 offset:13312\n"
"v_exp_f32 v147, v147\n"
"v_exp_f32 v148, v148\n"
"v_mfma_f32_16x16x32_bf16 v[104:107], a[176:179], v[228:231], v[104:107]\n"
"s_add_u32 m0, s57, 122880\n"
"v_cvt_pk_bf16_f32 v224, v144, v145\n"
"v_exp_f32_e64 v149, v149\n"
"v_mfma_f32_16x16x32_bf16 v[108:111], a[180:183], v[228:231], v[108:111]\n"
"s_add_u32 s53, s70, s30\n"
"buffer_load_dwordx4 v10, s[20:23], s53 offen offset:0 lds\n"
"v_exp_f32 v150, v150\n"
"v_cvt_pk_bf16_f32 v225, v146, v147\n"
"v_mfma_f32_16x16x32_bf16 v[68:71], a[184:187], v[228:231], v[68:71]\n"
"s_add_u32 s53, s70, s31\n"
"buffer_load_dwordx4 v11, s[20:23], s53 offen offset:1024 lds\n"
"s_add_u32 s70, s70, 131072\n"
"v_exp_f32 v151, v151\n"
"v_cvt_pk_bf16_f32 v226, v148, v149\n"
"v_cvt_pk_bf16_f32 v227, v150, v151\n"
"v_exp_f32 v152, v152\n"
"s_waitcnt lgkmcnt(0)\n"
"v_mfma_f32_16x16x32_bf16 v[144:147], a[168:171], v[48:51], 0\n"
"s_add_u32 m0, s57, 131072\n"
"v_exp_f32 v153, v153\n"
"v_exp_f32 v154, v154\n"
"v_mfma_f32_16x16x32_bf16 v[148:151], a[172:175], v[48:51], 0\n"
"buffer_load_dwordx4 v10, s[20:23], s70 offen offset:0 lds\n"
"v_exp_f32 v155, v155\n"
"v_exp_f32 v156, v156\n"
"v_mfma_f32_16x16x32_bf16 v[112:115], a[176:179], v[224:227], v[112:115]\n"
"buffer_load_dwordx4 v11, s[20:23], s70 offen offset:1024 lds\n"
"v_cvt_pk_bf16_f32 v228, v152, v153\n"
"v_exp_f32_e64 v157, v157\n"
"v_mfma_f32_16x16x32_bf16 v[116:119], a[180:183], v[224:227], v[116:119]\n"
"s_add_u32 s70, s70, 131072\n"
"v_exp_f32_e64 v158, v158\n"
"v_cvt_pk_bf16_f32 v229, v154, v155\n"
"v_mfma_f32_16x16x32_bf16 v[72:75], a[184:187], v[224:227], v[72:75]\n"
"v_exp_f32_e64 v159, v159\n"
"v_cvt_pk_bf16_f32 v230, v156, v157\n"
"v_cvt_pk_bf16_f32 v231, v158, v159\n"
"v_exp_f32_e64 v144, v144\n"
"v_mfma_f32_16x16x32_bf16 v[152:155], a[168:171], v[52:55], 0\n"
"s_add_u32 m0, s57, 114688\n"
"v_exp_f32 v145, v145\n"
"v_exp_f32 v146, v146\n"
"v_mfma_f32_16x16x32_bf16 v[156:159], a[172:175], v[52:55], 0\n"
"buffer_load_dwordx4 v12, s[20:23], s71 offen offset:0 lds\n"
"v_exp_f32 v147, v147\n"
"v_exp_f32 v148, v148\n"
"v_mfma_f32_16x16x32_bf16 v[120:123], a[176:179], v[228:231], v[120:123]\n"
"ds_read_b128 a[176:179], v22 offset:45056\n"
"v_cvt_pk_bf16_f32 v224, v144, v145\n"
"v_exp_f32_e64 v149, v149\n"
"v_mfma_f32_16x16x32_bf16 v[124:127], a[180:183], v[228:231], v[124:127]\n"
"ds_read_b128 a[180:183], v22 offset:46080\n"
"v_exp_f32_e64 v150, v150\n"
"v_cvt_pk_bf16_f32 v225, v146, v147\n"
"v_mfma_f32_16x16x32_bf16 v[76:79], a[184:187], v[228:231], v[76:79]\n"
"buffer_load_dwordx4 v13, s[20:23], s71 offen offset:1024 lds\n"
"s_add_u32 s71, s71, 131072\n"
"v_exp_f32_e64 v151, v151\n"
"v_cvt_pk_bf16_f32 v226, v148, v149\n"
"v_cvt_pk_bf16_f32 v227, v150, v151\n"
"v_exp_f32_e64 v152, v152\n"
"v_mfma_f32_16x16x32_bf16 v[144:147], a[168:171], v[56:59], 0\n"
"s_add_u32 m0, s57, 139264\n"
"v_exp_f32 v153, v153\n"
"v_exp_f32 v154, v154\n"
"v_mfma_f32_16x16x32_bf16 v[148:151], a[172:175], v[56:59], 0\n"
"buffer_load_dwordx4 v10, s[20:23], s70 offen offset:0 lds\n"
"v_exp_f32 v155, v155\n"
"v_exp_f32_e64 v156, v156\n"
"s_waitcnt lgkmcnt(0)\n"
"v_mfma_f32_16x16x32_bf16 v[96:99], a[176:179], v[224:227], v[96:99]\n"
"buffer_load_dwordx4 v11, s[20:23], s70 offen offset:1024 lds\n"
"v_cvt_pk_bf16_f32 v228, v152, v153\n"
"v_exp_f32_e64 v157, v157\n"
"v_mfma_f32_16x16x32_bf16 v[100:103], a[180:183], v[224:227], v[100:103]\n"
"s_add_u32 s70, s70, 131072\n"
"v_exp_f32_e64 v158, v158\n"
"v_cvt_pk_bf16_f32 v229, v154, v155\n"
"v_mfma_f32_16x16x32_bf16 v[64:67], a[184:187], v[224:227], v[64:67]\n"
"v_exp_f32_e64 v159, v159\n"
"v_cvt_pk_bf16_f32 v230, v156, v157\n"
"v_cvt_pk_bf16_f32 v231, v158, v159\n"
"v_exp_f32_e64 v144, v144\n"
"v_mfma_f32_16x16x32_bf16 v[152:155], a[168:171], v[60:63], 0\n"
"ds_read_b128 a[168:171], v22 offset:10240\n"
"v_exp_f32 v145, v145\n"
"v_exp_f32 v146, v146\n"
"v_mfma_f32_16x16x32_bf16 v[156:159], a[172:175], v[60:63], 0\n"
"ds_read_b128 a[172:175], v22 offset:11264\n"
"v_exp_f32 v147, v147\n"
"v_exp_f32 v148, v148\n"
"v_mfma_f32_16x16x32_bf16 v[104:107], a[176:179], v[228:231], v[104:107]\n"
"s_add_u32 m0, s57, 147456\n"
"v_cvt_pk_bf16_f32 v224, v144, v145\n"
"v_exp_f32_e64 v149, v149\n"
"v_mfma_f32_16x16x32_bf16 v[108:111], a[180:183], v[228:231], v[108:111]\n"
"buffer_load_dwordx4 v10, s[20:23], s70 offen offset:0 lds\n"
"v_exp_f32_e64 v150, v150\n"
"v_cvt_pk_bf16_f32 v225, v146, v147\n"
"v_mfma_f32_16x16x32_bf16 v[68:71], a[184:187], v[228:231], v[68:71]\n"
"buffer_load_dwordx4 v11, s[20:23], s70 offen offset:1024 lds\n"
"s_add_u32 s70, s70, 131072\n"
"v_exp_f32_e64 v151, v151\n"
"v_cvt_pk_bf16_f32 v226, v148, v149\n"
"v_cvt_pk_bf16_f32 v227, v150, v151\n"
"v_exp_f32 v152, v152\n"
"s_waitcnt lgkmcnt(0)\n"
"v_mfma_f32_16x16x32_bf16 v[144:147], a[168:171], v[48:51], 0\n"
"s_add_u32 m0, s57, 90112\n"
"v_exp_f32 v153, v153\n"
"v_exp_f32 v154, v154\n"
"v_mfma_f32_16x16x32_bf16 v[148:151], a[172:175], v[48:51], 0\n"
"s_add_u32 s53, s70, s28\n"
"buffer_load_dwordx4 v10, s[20:23], s53 offen offset:0 lds\n"
"v_exp_f32 v155, v155\n"
"v_exp_f32_e64 v156, v156\n"
"v_mfma_f32_16x16x32_bf16 v[112:115], a[176:179], v[224:227], v[112:115]\n"
"s_add_u32 s53, s70, s29\n"
"buffer_load_dwordx4 v11, s[20:23], s53 offen offset:1024 lds\n"
"v_cvt_pk_bf16_f32 v228, v152, v153\n"
"v_exp_f32 v157, v157\n"
"v_mfma_f32_16x16x32_bf16 v[116:119], a[180:183], v[224:227], v[116:119]\n"
"s_add_u32 s70, s70, 131072\n"
"v_exp_f32_e64 v158, v158\n"
"v_cvt_pk_bf16_f32 v229, v154, v155\n"
"v_mfma_f32_16x16x32_bf16 v[72:75], a[184:187], v[224:227], v[72:75]\n"
"v_exp_f32_e64 v159, v159\n"
"v_cvt_pk_bf16_f32 v230, v156, v157\n"
"v_cvt_pk_bf16_f32 v231, v158, v159\n"
"s_cmp_eq_u32 s91, 5\n"
"s_cbranch_scc1 .Lax5f\n"
"v_exp_f32_e64 v144, v144\n"
"v_mfma_f32_16x16x32_bf16 v[152:155], a[168:171], v[52:55], 0\n"
"s_add_u32 m0, s57, 81920\n"
"v_exp_f32 v145, v145\n"
"v_exp_f32 v146, v146\n"
"v_mfma_f32_16x16x32_bf16 v[156:159], a[172:175], v[52:55], 0\n"
"buffer_load_dwordx4 v10, s[20:23], s70 offen offset:0 lds\n"
"v_exp_f32 v147, v147\n"
"v_exp_f32 v148, v148\n"
"v_mfma_f32_16x16x32_bf16 v[120:123], a[176:179], v[228:231], v[120:123]\n"
"ds_read_b128 a[176:179], v22 offset:43008\n"
"v_cvt_pk_bf16_f32 v224, v144, v145\n"
"v_exp_f32_e64 v149, v149\n"
"v_mfma_f32_16x16x32_bf16 v[124:127], a[180:183], v[228:231], v[124:127]\n"
"ds_read_b128 a[180:183], v22 offset:44032\n"
"v_exp_f32_e64 v150, v150\n"
"v_cvt_pk_bf16_f32 v225, v146, v147\n"
"v_mfma_f32_16x16x32_bf16 v[76:79], a[184:187], v[228:231], v[76:79]\n"
"buffer_load_dwordx4 v11, s[20:23], s70 offen offset:1024 lds\n"
"s_add_u32 s70, s70, 131072\n"
"v_exp_f32_e64 v151, v151\n"
"v_cvt_pk_bf16_f32 v226, v148, v149\n"
"v_cvt_pk_bf16_f32 v227, v150, v151\n"
"v_exp_f32_e64 v152, v152\n"
"v_mfma_f32_16x16x32_bf16 v[144:147], a[168:171], v[56:59], 0\n"
"s_add_u32 m0, s57, 73728\n"
"v_exp_f32 v153, v153\n"
"v_exp_f32 v154, v154\n"
"v_mfma_f32_16x16x32_bf16 v[148:151], a[172:175], v[56:59], 0\n"
"buffer_load_dwordx4 v10, s[20:23], s70 offen offset:0 lds\n"
"v_exp_f32 v155, v155\n"
"v_exp_f32_e64 v156, v156\n"
"s_waitcnt lgkmcnt(0)\n"
"v_mfma_f32_16x16x32_bf16 v[96:99], a[176:179], v[224:227], v[96:99]\n"
"buffer_load_dwordx4 v11, s[20:23], s70 offen offset:1024 lds\n"
"v_cvt_pk_bf16_f32 v228, v152, v153\n"
"v_exp_f32_e64 v157, v157\n"
"v_mfma_f32_16x16x32_bf16 v[100:103], a[180:183], v[224:227], v[100:103]\n"
"s_add_u32 s70, s70, 131072\n"
"v_exp_f32_e64 v158, v158\n"
"v_cvt_pk_bf16_f32 v229, v154, v155\n"
"v_mfma_f32_16x16x32_bf16 v[64:67], a[184:187], v[224:227], v[64:67]\n"
"v_exp_f32_e64 v159, v159\n"
"v_cvt_pk_bf16_f32 v230, v156, v157\n"
"v_cvt_pk_bf16_f32 v231, v158, v159\n"
"v_exp_f32_e64 v144, v144\n"
"v_mfma_f32_16x16x32_bf16 v[152:155], a[168:171], v[60:63], 0\n"
"ds_read_b128 a[168:171], v22 offset:8192\n"
"v_exp_f32 v145, v145\n"
"v_exp_f32 v146, v146\n"
"v_mfma_f32_16x16x32_bf16 v[156:159], a[172:175], v[60:63], 0\n"
"ds_read_b128 a[172:175], v22 offset:9216\n"
"v_exp_f32 v147, v147\n"
"v_exp_f32 v148, v148\n"
"v_mfma_f32_16x16x32_bf16 v[104:107], a[176:179], v[228:231], v[104:107]\n"
"s_add_u32 m0, s57, 65536\n"
"v_cvt_pk_bf16_f32 v224, v144, v145\n"
"v_exp_f32_e64 v149, v149\n"
"v_mfma_f32_16x16x32_bf16 v[108:111], a[180:183], v[228:231], v[108:111]\n"
"s_add_u32 s53, s70, s30\n"
"buffer_load_dwordx4 v10, s[20:23], s53 offen offset:0 lds\n"
"v_exp_f32 v150, v150\n"
"v_cvt_pk_bf16_f32 v225, v146, v147\n"
"v_mfma_f32_16x16x32_bf16 v[68:71], a[184:187], v[228:231], v[68:71]\n"
"s_add_u32 s53, s70, s31\n"
"buffer_load_dwordx4 v11, s[20:23], s53 offen offset:1024 lds\n"
"s_add_u32 s70, s70, 131072\n"
"v_exp_f32 v151, v151\n"
"v_cvt_pk_bf16_f32 v226, v148, v149\n"
"v_cvt_pk_bf16_f32 v227, v150, v151\n"
"v_exp_f32 v152, v152\n"
"s_waitcnt lgkmcnt(0)\n"
"v_mfma_f32_16x16x32_bf16 v[144:147], a[168:171], v[48:51], 0\n"
"v_exp_f32 v153, v153\n"
"v_exp_f32 v154, v154\n"
"v_mfma_f32_16x16x32_bf16 v[148:151], a[172:175], v[48:51], 0\n"
"v_exp_f32 v155, v155\n"
"v_exp_f32 v156, v156\n"
"v_mfma_f32_16x16x32_bf16 v[112:115], a[176:179], v[224:227], v[112:115]\n"
"v_cvt_pk_bf16_f32 v228, v152, v153\n"
"v_exp_f32_e64 v157, v157\n"
"v_mfma_f32_16x16x32_bf16 v[116:119], a[180:183], v[224:227], v[116:119]\n"
"v_exp_f32_e64 v158, v158\n"
"v_cvt_pk_bf16_f32 v229, v154, v155\n"
"v_mfma_f32_16x16x32_bf16 v[72:75], a[184:187], v[224:227], v[72:75]\n"
"v_exp_f32_e64 v159, v159\n"
"v_cvt_pk_bf16_f32 v230, v156, v157\n"
"v_cvt_pk_bf16_f32 v231, v158, v159\n"
"v_exp_f32_e64 v144, v144\n"
"v_mfma_f32_16x16x32_bf16 v[152:155], a[168:171], v[52:55], 0\n"
"v_exp_f32 v145, v145\n"
"v_exp_f32 v146, v146\n"
"v_mfma_f32_16x16x32_bf16 v[156:159], a[172:175], v[52:55], 0\n"
"v_exp_f32 v147, v147\n"
"v_exp_f32 v148, v148\n"
"v_mfma_f32_16x16x32_bf16 v[120:123], a[176:179], v[228:231], v[120:123]\n"
"ds_read_b128 a[176:179], v22 offset:40960\n"
"v_cvt_pk_bf16_f32 v224, v144, v145\n"
"v_exp_f32_e64 v149, v149\n"
"v_mfma_f32_16x16x32_bf16 v[124:127], a[180:183], v[228:231], v[124:127]\n"
"ds_read_b128 a[180:183], v22 offset:41984\n"
"v_exp_f32_e64 v150, v150\n"
"v_cvt_pk_bf16_f32 v225, v146, v147\n"
"v_mfma_f32_16x16x32_bf16 v[76:79], a[184:187], v[228:231], v[76:79]\n"
"v_exp_f32_e64 v151, v151\n"
"v_cvt_pk_bf16_f32 v226, v148, v149\n"
"v_cvt_pk_bf16_f32 v227, v150, v151\n"
"v_exp_f32_e64 v152, v152\n"
"v_mfma_f32_16x16x32_bf16 v[144:147], a[168:171], v[56:59], 0\n"
"v_exp_f32 v153, v153\n"
"v_exp_f32 v154, v154\n"
"v_mfma_f32_16x16x32_bf16 v[148:151], a[172:175], v[56:59], 0\n"
"v_exp_f32 v155, v155\n"
"v_exp_f32_e64 v156, v156\n"
"s_waitcnt lgkmcnt(0)\n"
"v_mfma_f32_16x16x32_bf16 v[96:99], a[176:179], v[224:227], v[96:99]\n"
"v_cvt_pk_bf16_f32 v228, v152, v153\n"
"v_exp_f32_e64 v157, v157\n"
"v_mfma_f32_16x16x32_bf16 v[100:103], a[180:183], v[224:227], v[100:103]\n"
"v_exp_f32_e64 v158, v158\n"
"v_cvt_pk_bf16_f32 v229, v154, v155\n"
"v_mfma_f32_16x16x32_bf16 v[64:67], a[184:187], v[224:227], v[64:67]\n"
"v_exp_f32_e64 v159, v159\n"
"v_cvt_pk_bf16_f32 v230, v156, v157\n"
"v_cvt_pk_bf16_f32 v231, v158, v159\n"
"v_exp_f32_e64 v144, v144\n"
"v_mfma_f32_16x16x32_bf16 v[152:155], a[168:171], v[60:63], 0\n"
"ds_read_b128 a[168:171], v22 offset:6144\n"
"v_exp_f32 v145, v145\n"
"v_exp_f32 v146, v146\n"
"v_mfma_f32_16x16x32_bf16 v[156:159], a[172:175], v[60:63], 0\n"
"ds_read_b128 a[172:175], v22 offset:7168\n"
"v_exp_f32 v147, v147\n"
"v_exp_f32 v148, v148\n"
"v_mfma_f32_16x16x32_bf16 v[104:107], a[176:179], v[228:231], v[104:107]\n"
"v_cvt_pk_bf16_f32 v224, v144, v145\n"
"v_exp_f32_e64 v149, v149\n"
"v_mfma_f32_16x16x32_bf16 v[108:111], a[180:183], v[228:231], v[108:111]\n"
"v_exp_f32_e64 v150, v150\n"
"v_cvt_pk_bf16_f32 v225, v146, v147\n"
"v_mfma_f32_16x16x32_bf16 v[68:71], a[184:187], v[228:231], v[68:71]\n"
"v_exp_f32_e64 v151, v151\n"
"v_cvt_pk_bf16_f32 v226, v148, v149\n"
"v_cvt_pk_bf16_f32 v227, v150, v151\n"
"v_exp_f32 v152, v152\n"
"s_waitcnt lgkmcnt(0)\n"
"v_mfma_f32_16x16x32_bf16 v[144:147], a[168:171], v[48:51], 0\n"
"v_exp_f32 v153, v153\n"
"v_exp_f32 v154, v154\n"
"v_mfma_f32_16x16x32_bf16 v[148:151], a[172:175], v[48:51], 0\n"
"v_exp_f32 v155, v155\n"
"v_exp_f32 v156, v156\n"
"v_mfma_f32_16x16x32_bf16 v[112:115], a[176:179], v[224:227], v[112:115]\n"
"v_cvt_pk_bf16_f32 v228, v152, v153\n"
"v_exp_f32_e64 v157, v157\n"
"v_mfma_f32_16x16x32_bf16 v[116:119], a[180:183], v[224:227], v[116:119]\n"
"v_exp_f32_e64 v158, v158\n"
"v_cvt_pk_bf16_f32 v229, v154, v155\n"
"v_mfma_f32_16x16x32_bf16 v[72:75], a[184:187], v[224:227], v[72:75]\n"
"v_exp_f32_e64 v159, v159\n"
"v_cvt_pk_bf16_f32 v230, v156, v157\n"
"v_cvt_pk_bf16_f32 v231, v158, v159\n"
"s_cmp_eq_u32 s91, 3\n"
"s_cbranch_scc1 .Lax3f\n"
"v_cmp_gt_u32 vcc, 8, v0\n"
"v_lshl_add_u32 v34, v0, 2, s88\n"
"v_mov_b32 v36, 2130706432\n"
"v_cndmask_b32 v34, v36, v34, vcc\n"
"buffer_load_dword v35, v34, s[16:19], 0 offen sc1\n"
"v_add_u32_e64 v14, s89, v1\n"
"v_xor_b32 v15, 16384, v14\n"
"v_xor_b32 v16, 1024, v14\n"
"v_xor_b32 v17, 17408, v14\n"
"v_xor_b32 v18, 2048, v14\n"
"v_xor_b32 v19, 18432, v14\n"
"v_xor_b32 v20, 3072, v14\n"
"v_xor_b32 v21, 19456, v14\n"
"v_exp_f32_e64 v144, v144\n"
"v_mfma_f32_16x16x32_bf16 v[152:155], a[168:171], v[52:55], 0\n"
"v_exp_f32 v145, v145\n"
"v_exp_f32 v146, v146\n"
"v_mfma_f32_16x16x32_bf16 v[156:159], a[172:175], v[52:55], 0\n"
"v_exp_f32 v147, v147\n"
"v_exp_f32 v148, v148\n"
"v_mfma_f32_16x16x32_bf16 v[120:123], a[176:179], v[228:231], v[120:123]\n"
"ds_read_b128 a[176:179], v22 offset:38912\n"
"v_cvt_pk_bf16_f32 v224, v144, v145\n"
"v_exp_f32_e64 v149, v149\n"
"v_mfma_f32_16x16x32_bf16 v[124:127], a[180:183], v[228:231], v[124:127]\n"
"ds_read_b128 a[180:183], v22 offset:39936\n"
"v_exp_f32_e64 v150, v150\n"
"v_cvt_pk_bf16_f32 v225, v146, v147\n"
"v_mfma_f32_16x16x32_bf16 v[76:79], a[184:187], v[228:231], v[76:79]\n"
"v_exp_f32_e64 v151, v151\n"
"v_cvt_pk_bf16_f32 v226, v148, v149\n"
"v_cvt_pk_bf16_f32 v227, v150, v151\n"
"v_exp_f32_e64 v152, v152\n"
"v_mfma_f32_16x16x32_bf16 v[144:147], a[168:171], v[56:59], 0\n"
"v_exp_f32 v153, v153\n"
"v_exp_f32 v154, v154\n"
"v_mfma_f32_16x16x32_bf16 v[148:151], a[172:175], v[56:59], 0\n"
"v_exp_f32 v155, v155\n"
"v_exp_f32_e64 v156, v156\n"
"s_waitcnt lgkmcnt(0)\n"
"v_mfma_f32_16x16x32_bf16 v[96:99], a[176:179], v[224:227], v[96:99]\n"
"v_cvt_pk_bf16_f32 v228, v152, v153\n"
"v_exp_f32_e64 v157, v157\n"
"v_mfma_f32_16x16x32_bf16 v[100:103], a[180:183], v[224:227], v[100:103]\n"
"v_exp_f32_e64 v158, v158\n"
"v_cvt_pk_bf16_f32 v229, v154, v155\n"
"v_mfma_f32_16x16x32_bf16 v[64:67], a[184:187], v[224:227], v[64:67]\n"
"v_exp_f32_e64 v159, v159\n"
"v_cvt_pk_bf16_f32 v230, v156, v157\n"
"v_cvt_pk_bf16_f32 v231, v158, v159\n"
"v_exp_f32_e64 v144, v144\n"
"v_mfma_f32_16x16x32_bf16 v[152:155], a[168:171], v[60:63], 0\n"
"ds_read_b128 a[168:171], v22 offset:4096\n"
"v_exp_f32 v145, v145\n"
"v_exp_f32 v146, v146\n"
"v_mfma_f32_16x16x32_bf16 v[156:159], a[172:175], v[60:63], 0\n"
"ds_read_b128 a[172:175], v22 offset:5120\n"
"v_exp_f32 v147, v147\n"
"v_exp_f32 v148, v148\n"
"v_mfma_f32_16x16x32_bf16 v[104:107], a[176:179], v[228:231], v[104:107]\n"
"v_cvt_pk_bf16_f32 v224, v144, v145\n"
"v_exp_f32_e64 v149, v149\n"
"v_mfma_f32_16x16x32_bf16 v[108:111], a[180:183], v[228:231], v[108:111]\n"
"v_exp_f32_e64 v150, v150\n"
"v_cvt_pk_bf16_f32 v225, v146, v147\n"
"v_mfma_f32_16x16x32_bf16 v[68:71], a[184:187], v[228:231], v[68:71]\n"
"v_exp_f32_e64 v151, v151\n"
"v_cvt_pk_bf16_f32 v226, v148, v149\n"
"v_cvt_pk_bf16_f32 v227, v150, v151\n"
"v_exp_f32 v152, v152\n"
"s_waitcnt lgkmcnt(0)\n"
"v_mfma_f32_16x16x32_bf16 v[144:147], a[168:171], v[48:51], 0\n"
"v_exp_f32 v153, v153\n"
"v_exp_f32 v154, v154\n"
"v_mfma_f32_16x16x32_bf16 v[148:151], a[172:175], v[48:51], 0\n"
"v_exp_f32 v155, v155\n"
"v_exp_f32 v156, v156\n"
"v_mfma_f32_16x16x32_bf16 v[112:115], a[176:179], v[224:227], v[112:115]\n"
"v_cvt_pk_bf16_f32 v228, v152, v153\n"
"v_exp_f32_e64 v157, v157\n"
"v_mfma_f32_16x16x32_bf16 v[116:119], a[180:183], v[224:227], v[116:119]\n"
"v_exp_f32_e64 v158, v158\n"
"v_cvt_pk_bf16_f32 v229, v154, v155\n"
"v_mfma_f32_16x16x32_bf16 v[72:75], a[184:187], v[224:227], v[72:75]\n"
"v_exp_f32_e64 v159, v159\n"
"v_cvt_pk_bf16_f32 v230, v156, v157\n"
"v_cvt_pk_bf16_f32 v231, v158, v159\n"
"s_waitcnt vmcnt(0)\n"
"v_cmp_ne_u32 vcc, 0, v35\n"
"s_mov_b32 s72, 0\n"
"s_nop 0\n"
"s_cbranch_vccz .Lfh_ok\n"
".Lfh_lp:\n"
"buffer_load_dword v35, v34, s[16:19], 0 offen sc1\n"
"s_add_u32 s72, s72, 1\n"
"s_waitcnt vmcnt(0)\n"
"v_cmp_ne_u32 vcc, 0, v35\n"
"s_cmp_lt_u32 s72, 262144\n"
"s_nop 0\n"
"s_cselect_b64 vcc, vcc, 0\n"
"s_cbranch_vccnz .Lfh_lp\n"
".Lfh_ok:\n"
"v_exp_f32 v144, v144\n"
"v_mfma_f32_16x16x32_bf16 v[152:155], a[168:171], v[52:55], 0\n"
"buffer_load_dwordx4 a[96:99], v14, s[16:19], s46 offen sc1\n"
"v_exp_f32 v145, v145\n"
"v_exp_f32 v146, v146\n"
"v_mfma_f32_16x16x32_bf16 v[156:159], a[172:175], v[52:55], 0\n"
"buffer_load_dwordx4 a[100:103], v15, s[16:19], s46 offen sc1\n"
"v_exp_f32 v147, v147\n"
"v_exp_f32 v148, v148\n"
"v_mfma_f32_16x16x32_bf16 v[120:123], a[176:179], v[228:231], v[120:123]\n"
"ds_read_b128 a[176:179], v22 offset:36864\n"
"v_cvt_pk_bf16_f32 v224, v144, v145\n"
"v_exp_f32_e64 v149, v149\n"
"v_mfma_f32_16x16x32_bf16 v[124:127], a[180:183], v[228:231], v[124:127]\n"
"ds_read_b128 a[180:183], v22 offset:37888\n"
"v_exp_f32_e64 v150, v150\n"
"v_cvt_pk_bf16_f32 v225, v146, v147\n"
"v_mfma_f32_16x16x32_bf16 v[76:79], a[184:187], v[228:231], v[76:79]\n"
"s_add_u32 s46, s46, 32768\n"
"v_exp_f32_e64 v151, v151\n"
"v_cvt_pk_bf16_f32 v226, v148, v149\n"
"v_cvt_pk_bf16_f32 v227, v150, v151\n"
"v_exp_f32_e64 v152, v152\n"
"v_mfma_f32_16x16x32_bf16 v[144:147], a[168:171], v[56:59], 0\n"
"buffer_load_dwordx4 a[112:115], v14, s[16:19], s46 offen sc1\n"
"v_exp_f32 v153, v153\n"
"v_exp_f32 v154, v154\n"
"v_mfma_f32_16x16x32_bf16 v[148:151], a[172:175], v[56:59], 0\n"
"buffer_load_dwordx4 a[116:119], v15, s[16:19], s46 offen sc1\n"
"v_exp_f32 v155, v155\n"
"v_exp_f32_e64 v156, v156\n"
"s_waitcnt lgkmcnt(0)\n"
"v_mfma_f32_16x16x32_bf16 v[96:99], a[176:179], v[224:227], v[96:99]\n"
"s_add_u32 s46, s46, 32768\n"
"v_cvt_pk_bf16_f32 v228, v152, v153\n"
"v_exp_f32_e64 v157, v157\n"
"v_mfma_f32_16x16x32_bf16 v[100:103], a[180:183], v[224:227], v[100:103]\n"
"v_exp_f32_e64 v158, v158\n"
"v_cvt_pk_bf16_f32 v229, v154, v155\n"
"v_mfma_f32_16x16x32_bf16 v[64:67], a[184:187], v[224:227], v[64:67]\n"
"v_exp_f32_e64 v159, v159\n"
"v_cvt_pk_bf16_f32 v230, v156, v157\n"
"v_cvt_pk_bf16_f32 v231, v158, v159\n"
"v_exp_f32_e64 v144, v144\n"
"v_mfma_f32_16x16x32_bf16 v[152:155], a[168:171], v[60:63], 0\n"
"ds_read_b128 a[168:171], v22 offset:2048\n"
"v_exp_f32 v145, v145\n"
"v_exp_f32 v146, v146\n"
"v_mfma_f32_16x16x32_bf16 v[156:159], a[172:175], v[60:63], 0\n"
"ds_read_b128 a[172:175], v22 offset:3072\n"
"v_exp_f32 v147, v147\n"
"v_exp_f32 v148, v148\n"
"v_mfma_f32_16x16x32_bf16 v[104:107], a[176:179], v[228:231], v[104:107]\n"
"buffer_load_dwordx4 a[0:3], v18, s[16:19], s47 offen sc1\n"
"v_cvt_pk_bf16_f32 v224, v144, v145\n"
"v_exp_f32_e64 v149, v149\n"
"v_mfma_f32_16x16x32_bf16 v[108:111], a[180:183], v[228:231], v[108:111]\n"
"buffer_load_dwordx4 a[4:7], v19, s[16:19], s47 offen sc1\n"
"v_exp_f32_e64 v150, v150\n"
"v_cvt_pk_bf16_f32 v225, v146, v147\n"
"v_mfma_f32_16x16x32_bf16 v[68:71], a[184:187], v[228:231], v[68:71]\n"
"v_exp_f32_e64 v151, v151\n"
"v_cvt_pk_bf16_f32 v226, v148, v149\n"
"v_cvt_pk_bf16_f32 v227, v150, v151\n"
"v_exp_f32 v152, v152\n"
"s_waitcnt lgkmcnt(0)\n"
"v_mfma_f32_16x16x32_bf16 v[144:147], a[168:171], v[48:51], 0\n"
"buffer_load_dwordx4 a[8:11], v20, s[16:19], s47 offen sc1\n"
"v_exp_f32 v153, v153\n"
"v_exp_f32 v154, v154\n"
"v_mfma_f32_16x16x32_bf16 v[148:151], a[172:175], v[48:51], 0\n"
"buffer_load_dwordx4 a[12:15], v21, s[16:19], s47 offen sc1\n"
"v_exp_f32 v155, v155\n"
"v_exp_f32 v156, v156\n"
"v_mfma_f32_16x16x32_bf16 v[112:115], a[176:179], v[224:227], v[112:115]\n"
"s_add_u32 s47, s47, 32768\n"
"v_cvt_pk_bf16_f32 v228, v152, v153\n"
"v_exp_f32_e64 v157, v157\n"
"v_mfma_f32_16x16x32_bf16 v[116:119], a[180:183], v[224:227], v[116:119]\n"
"v_exp_f32_e64 v158, v158\n"
"v_cvt_pk_bf16_f32 v229, v154, v155\n"
"v_mfma_f32_16x16x32_bf16 v[72:75], a[184:187], v[224:227], v[72:75]\n"
"v_exp_f32_e64 v159, v159\n"
"v_cvt_pk_bf16_f32 v230, v156, v157\n"
"v_cvt_pk_bf16_f32 v231, v158, v159\n"
"s_cmp_eq_u32 s91, 1\n"
"s_cbranch_scc1 .Lax1f\n"
"v_exp_f32_e64 v144, v144\n"
"v_mfma_f32_16x16x32_bf16 v[152:155], a[168:171], v[52:55], 0\n"
"buffer_load_dwordx4 a[104:107], v16, s[16:19], s54 offen sc1\n"
"v_exp_f32 v145, v145\n"
"v_exp_f32 v146, v146\n"
"v_mfma_f32_16x16x32_bf16 v[156:159], a[172:175], v[52:55], 0\n"
"buffer_load_dwordx4 a[108:111], v17, s[16:19], s54 offen sc1\n"
"v_exp_f32 v147, v147\n"
"v_exp_f32 v148, v148\n"
"v_mfma_f32_16x16x32_bf16 v[120:123], a[176:179], v[228:231], v[120:123]\n"
"ds_read_b128 a[176:179], v22 offset:34816\n"
"v_cvt_pk_bf16_f32 v224, v144, v145\n"
"v_exp_f32_e64 v149, v149\n"
"v_mfma_f32_16x16x32_bf16 v[124:127], a[180:183], v[228:231], v[124:127]\n"
"ds_read_b128 a[180:183], v22 offset:35840\n"
"v_exp_f32_e64 v150, v150\n"
"v_cvt_pk_bf16_f32 v225, v146, v147\n"
"v_mfma_f32_16x16x32_bf16 v[76:79], a[184:187], v[228:231], v[76:79]\n"
"s_add_u32 s54, s54, 32768\n"
"v_exp_f32_e64 v151, v151\n"
"v_cvt_pk_bf16_f32 v226, v148, v149\n"
"v_cvt_pk_bf16_f32 v227, v150, v151\n"
"v_exp_f32_e64 v152, v152\n"
"v_mfma_f32_16x16x32_bf16 v[144:147], a[168:171], v[56:59], v[26:29]\n"
"buffer_load_dwordx4 a[128:131], v14, s[16:19], s46 offen sc1\n"
"v_exp_f32 v153, v153\n"
"v_exp_f32 v154, v154\n"
"v_mfma_f32_16x16x32_bf16 v[148:151], a[172:175], v[56:59], 0\n"
"buffer_load_dwordx4 a[132:135], v15, s[16:19], s46 offen sc1\n"
"v_exp_f32 v155, v155\n"
"v_exp_f32_e64 v156, v156\n"
"s_waitcnt lgkmcnt(0)\n"
"v_mfma_f32_16x16x32_bf16 v[96:99], a[176:179], v[224:227], v[96:99]\n"
"s_add_u32 s46, s46, 32768\n"
"v_cvt_pk_bf16_f32 v228, v152, v153\n"
"v_exp_f32_e64 v157, v157\n"
"v_mfma_f32_16x16x32_bf16 v[100:103], a[180:183], v[224:227], v[100:103]\n"
"v_exp_f32_e64 v158, v158\n"
"v_cvt_pk_bf16_f32 v229, v154, v155\n"
"v_mfma_f32_16x16x32_bf16 v[64:67], a[184:187], v[224:227], v[64:67]\n"
"v_exp_f32_e64 v159, v159\n"
"v_cvt_pk_bf16_f32 v230, v156, v157\n"
"v_cvt_pk_bf16_f32 v231, v158, v159\n"
"v_exp_f32_e64 v144, v144\n"
"ds_read_b128 a[168:171], v22 offset:0\n"
"v_exp_f32 v145, v145\n"
"v_exp_f32 v146, v146\n"
"v_mfma_f32_16x16x32_bf16 v[156:159], a[172:175], v[60:63], v[26:29]\n"
"ds_read_b128 a[172:175], v22 offset:1024\n"
"v_exp_f32 v147, v147\n"
"v_exp_f32 v148, v148\n"
"v_mfma_f32_16x16x32_bf16 v[104:107], a[176:179], v[228:231], v[104:107]\n"
"buffer_load_dwordx4 a[16:19], v18, s[16:19], s47 offen sc1\n"
"v_cvt_pk_bf16_f32 v224, v144, v145\n"
"v_exp_f32_e64 v149, v149\n"
"v_mfma_f32_16x16x32_bf16 v[108:111], a[180:183], v[228:231], v[108:111]\n"
"buffer_load_dwordx4 a[20:23], v19, s[16:19], s47 offen sc1\n"
"v_exp_f32_e64 v150, v150\n"
"v_cvt_pk_bf16_f32 v225, v146, v147\n"
"v_mfma_f32_16x16x32_bf16 v[68:71], a[184:187], v[228:231], v[68:71]\n"
"v_exp_f32_e64 v151, v151\n"
"v_cvt_pk_bf16_f32 v226, v148, v149\n"
"v_cvt_pk_bf16_f32 v227, v150, v151\n"
"v_exp_f32 v156, v156\n"
"s_waitcnt lgkmcnt(0)\n"
"v_mfma_f32_16x16x32_bf16 v[144:147], a[168:171], v[48:51], v[26:29]\n"
"buffer_load_dwordx4 a[24:27], v20, s[16:19], s47 offen sc1\n"
"v_exp_f32_e64 v157, v157\n"
"v_mfma_f32_16x16x32_bf16 v[148:151], a[172:175], v[48:51], 0\n"
"buffer_load_dwordx4 a[28:31], v21, s[16:19], s47 offen sc1\n"
"v_exp_f32_e64 v158, v158\n"
"v_mfma_f32_16x16x32_bf16 v[112:115], a[176:179], v[224:227], v[112:115]\n"
"s_add_u32 s47, s47, 32768\n"
"v_exp_f32_e64 v159, v159\n"
"v_mfma_f32_16x16x32_bf16 v[116:119], a[180:183], v[224:227], v[116:119]\n"
"v_mov_b64_e64 v[228:229], 0\n"
".Lpad10:\n"
"s_cbranch_execz .Lpad14\n"
"v_cvt_pk_bf16_f32 v230, v156, v157\n"
"v_mfma_f32_16x16x32_bf16 v[72:75], a[184:187], v[224:227], v[72:75]\n"
"v_cvt_pk_bf16_f32 v231, v158, v159\n"
"v_exp_f32_e64 v144, v144\n"
"v_mfma_f32_16x16x32_bf16 v[156:159], a[172:175], v[52:55], v[26:29]\n"
"buffer_load_dwordx4 a[120:123], v16, s[16:19], s54 offen sc1\n"
"v_exp_f32 v145, v145\n"
"v_exp_f32 v146, v146\n"
"v_mfma_f32_16x16x32_bf16 v[120:123], a[176:179], v[228:231], v[120:123]\n"
"ds_read_b128 a[176:179], v22 offset:32768\n"
"v_exp_f32 v147, v147\n"
"v_exp_f32 v148, v148\n"
"v_mfma_f32_16x16x32_bf16 v[124:127], a[180:183], v[228:231], v[124:127]\n"
"ds_read_b128 a[180:183], v22 offset:33792\n"
"v_cvt_pk_bf16_f32 v224, v144, v145\n"
"v_exp_f32_e64 v149, v149\n"
"v_mfma_f32_16x16x32_bf16 v[76:79], a[184:187], v[228:231], v[76:79]\n"
"buffer_load_dwordx4 a[124:127], v17, s[16:19], s54 offen sc1\n"
"s_add_u32 s54, s54, 32768\n"
"v_exp_f32_e64 v150, v150\n"
"v_cvt_pk_bf16_f32 v225, v146, v147\n"
"v_exp_f32_e64 v151, v151\n"
"v_cvt_pk_bf16_f32 v226, v148, v149\n"
"v_cvt_pk_bf16_f32 v227, v150, v151\n"
"v_bfe_u32 v3, v0, 2, 3\n"
"v_lshrrev_b32 v4, 5, v0\n"
"v_lshlrev_b32 v4, 11, v4\n"
"v_lshl_add_u32 v4, v3, 7, v4\n"
"v_lshrrev_b32_e64 v3, 1, v3\n"
"v_lshl_add_u32 v4, v3, 5, v4\n"
"v_and_b32_e64 v3, 3, v0\n"
"v_lshl_add_u32 v4, v3, 3, v4\n"
"v_add_u32 v2, 98304, v4\n"
"v_add_u32 v6, 32768, v4\n"
"v_xor_b32 v3, 32, v2\n"
"v_xor_b32 v4, 64, v2\n"
"v_xor_b32 v5, 96, v2\n"
"v_exp_f32 v156, v156\n"
"s_waitcnt lgkmcnt(0)\n"
"v_mfma_f32_16x16x32_bf16 v[96:99], a[176:179], v[224:227], v[96:99]\n"
"v_exp_f32_e64 v157, v157\n"
"v_mfma_f32_16x16x32_bf16 v[100:103], a[180:183], v[224:227], v[100:103]\n"
"v_exp_f32_e64 v158, v158\n"
"v_mfma_f32_16x16x32_bf16 v[64:67], a[184:187], v[224:227], v[64:67]\n"
"v_exp_f32 v159, v159\n"
"v_mov_b64 v[228:229], 0\n"
"v_cvt_pk_bf16_f32 v230, v156, v157\n"
"v_cvt_pk_bf16_f32 v231, v158, v159\n"
"s_nop 1\n"
"v_mfma_f32_16x16x32_bf16 v[104:107], a[176:179], v[228:231], v[104:107]\n"
"v_mfma_f32_16x16x32_bf16 v[108:111], a[180:183], v[228:231], v[108:111]\n"
"v_mfma_f32_16x16x32_bf16 v[68:71], a[184:187], v[228:231], v[68:71]\n"
".Latd:\n"
"ds_read_b64_tr_b16 a[48:49], v2 offset:0\n"
"s_mov_b32 s65, 0x1f800000\n"
"ds_read_b64_tr_b16 a[50:51], v2 offset:1024\n"
"s_mov_b32 s66, 0x5f800000\n"
"ds_read_b64_tr_b16 a[52:53], v2 offset:4096\n"
"v_min3_f32 v25, v72, v76, v64\n"
"ds_read_b64_tr_b16 a[54:55], v2 offset:5120\n"
"v_max3_f32 v46, v72, v76, v64\n"
"ds_read_b64_tr_b16 a[56:57], v2 offset:57344\n"
"v_min_f32 v25, v25, v68\n"
"ds_read_b64_tr_b16 a[58:59], v2 offset:58368\n"
"v_max_f32_e64 v46, v46, v68\n"
"ds_read_b64_tr_b16 a[60:61], v2 offset:61440\n"
"v_cmp_gt_f32 vcc, s65, v25\n"
"ds_read_b64_tr_b16 a[62:63], v2 offset:62464\n"
"v_cmp_lt_f32 s[68:69], s66, v46\n"
"ds_read_b64_tr_b16 a[64:65], v3 offset:0\n"
"s_nop 0\n"
"s_or_b64 vcc, vcc, s[68:69]\n"
"ds_read_b64_tr_b16 a[66:67], v3 offset:1024\n"
"s_cbranch_vccnz .Lslow\n"
".Lnorm:\n"
"ds_read_b64_tr_b16 a[68:69], v3 offset:4096\n"
"v_rcp_f32_e64 v64, v64\n"
"ds_read_b64_tr_b16 a[70:71], v3 offset:5120\n"
"v_rcp_f32_e64 v68, v68\n"
"ds_read_b64_tr_b16 a[72:73], v3 offset:57344\n"
"v_rcp_f32 v72, v72\n"
"v_rcp_f32 v76, v76\n"
"buffer_load_dwordx4 a[32:35], v18, s[16:19], s47 offen sc1\n"
"buffer_load_dwordx4 a[36:39], v19, s[16:19], s47 offen sc1\n"
"s_waitcnt vmcnt(14) lgkmcnt(9)\n"
"v_mfma_f32_16x16x32_bf16 v[208:211], a[48:51], a[0:3], 0\n"
"ds_read_b64_tr_b16 a[74:75], v3 offset:58368\n"
"v_xor_b32 v7, 32, v6\n"
"v_mfma_f32_16x16x32_bf16 v[208:211], a[52:55], a[4:7], v[208:211]\n"
"buffer_load_dwordx4 a[40:43], v20, s[16:19], s47 offen sc1\n"
"v_xor_b32 v8, 64, v6\n"
"s_waitcnt vmcnt(13)\n"
"v_mfma_f32_16x16x32_bf16 v[240:243], a[48:51], a[8:11], 0\n"
"ds_read_b64_tr_b16 a[76:77], v3 offset:61440\n"
"v_xor_b32 v9, 96, v6\n"
"v_mfma_f32_16x16x32_bf16 v[240:243], a[52:55], a[12:15], v[240:243]\n"
"ds_read_b64_tr_b16 a[78:79], v3 offset:62464\n"
"v_mul_f32 v96, v96, v64\n"
"v_mfma_f32_16x16x32_bf16 v[144:147], a[48:51], a[96:99], 0\n"
"v_mul_f32 v97, v97, v64\n"
"v_mul_f32 v98, v98, v64\n"
"v_mfma_f32_16x16x32_bf16 v[144:147], a[52:55], a[100:103], v[144:147]\n"
"v_mul_f32 v99, v99, v64\n"
"v_mul_f32 v100, v100, v64\n"
"s_waitcnt vmcnt(11)\n"
"v_mfma_f32_16x16x32_bf16 v[176:179], a[48:51], a[104:107], 0\n"
"buffer_load_dwordx4 a[44:47], v21, s[16:19], s47 offen sc1\n"
"v_mul_f32_e64 v101, v101, v64\n"
"v_mfma_f32_16x16x32_bf16 v[176:179], a[52:55], a[108:111], v[176:179]\n"
"s_add_u32 s47, s47, 32768\n"
"s_waitcnt lgkmcnt(8)\n"
"v_mfma_f32_16x16x32_bf16 v[192:195], a[56:59], a[0:3], 0\n"
"ds_read_b64_tr_b16 a[80:81], v4 offset:0\n"
"v_mul_f32 v102, v102, v64\n"
"v_mfma_f32_16x16x32_bf16 v[192:195], a[60:63], a[4:7], v[192:195]\n"
"ds_read_b64_tr_b16 a[82:83], v4 offset:1024\n"
"v_mul_f32 v103, v103, v64\n"
"v_mfma_f32_16x16x32_bf16 v[224:227], a[56:59], a[8:11], 0\n"
"ds_read_b64_tr_b16 a[84:85], v4 offset:4096\n"
"v_cvt_pk_bf16_f32 v96, v96, v97\n"
"v_mfma_f32_16x16x32_bf16 v[224:227], a[60:63], a[12:15], v[224:227]\n"
"buffer_load_dwordx4 a[136:139], v16, s[16:19], s54 offen sc1\n"
"v_cvt_pk_bf16_f32 v97, v98, v99\n"
"v_mfma_f32_16x16x32_bf16 v[128:131], a[56:59], a[96:99], 0\n"
"ds_read_b64_tr_b16 a[86:87], v4 offset:5120\n"
"v_cvt_pk_bf16_f32 v98, v100, v101\n"
"v_mfma_f32_16x16x32_bf16 v[128:131], a[60:63], a[100:103], v[128:131]\n"
"v_cvt_pk_bf16_f32 v99, v102, v103\n"
"v_mul_f32 v104, v104, v68\n"
"v_mfma_f32_16x16x32_bf16 v[160:163], a[56:59], a[104:107], 0\n"
"ds_read_b64_tr_b16 a[88:89], v4 offset:57344\n"
"v_mul_f32 v105, v105, v68\n"
"v_mfma_f32_16x16x32_bf16 v[160:163], a[60:63], a[108:111], v[160:163]\n"
"ds_read_b64_tr_b16 a[90:91], v4 offset:58368\n"
"s_waitcnt lgkmcnt(10)\n"
"v_mfma_f32_16x16x32_bf16 v[212:215], a[64:67], a[0:3], 0\n"
"buffer_load_dwordx4 a[140:143], v17, s[16:19], s54 offen sc1\n"
"v_mul_f32 v106, v106, v68\n"
"v_mfma_f32_16x16x32_bf16 v[212:215], a[68:71], a[4:7], v[212:215]\n"
"s_add_u32 s54, s54, 32768\n"
"ds_read_b64_tr_b16 a[92:93], v4 offset:61440\n"
"v_mfma_f32_16x16x32_bf16 v[244:247], a[64:67], a[8:11], 0\n"
"ds_read_b64_tr_b16 a[94:95], v4 offset:62464\n"
"v_mul_f32 v107, v107, v68\n"
"v_mfma_f32_16x16x32_bf16 v[244:247], a[68:71], a[12:15], v[244:247]\n"
"buffer_store_dwordx4 v[96:99], v1, s[16:19], s73 offen offset:0 sc1\n"
"v_mul_f32 v108, v108, v68\n"
"v_mfma_f32_16x16x32_bf16 v[148:151], a[64:67], a[96:99], 0\n"
"v_mul_f32 v109, v109, v68\n"
"v_mul_f32 v110, v110, v68\n"
"v_mfma_f32_16x16x32_bf16 v[148:151], a[68:71], a[100:103], v[148:151]\n"
"buffer_load_dwordx4 a[144:147], v14, s[16:19], s46 offen sc1\n"
"v_mul_f32 v111, v111, v68\n"
"v_mfma_f32_16x16x32_bf16 v[180:183], a[64:67], a[104:107], 0\n"
"ds_read_b64_tr_b16 a[48:49], v5 offset:0\n"
"v_cvt_pk_bf16_f32 v104, v104, v105\n"
"v_mfma_f32_16x16x32_bf16 v[180:183], a[68:71], a[108:111], v[180:183]\n"
"ds_read_b64_tr_b16 a[50:51], v5 offset:1024\n"
"s_waitcnt lgkmcnt(10)\n"
"v_mfma_f32_16x16x32_bf16 v[196:199], a[72:75], a[0:3], 0\n"
"ds_read_b64_tr_b16 a[52:53], v5 offset:4096\n"
"v_cvt_pk_bf16_f32 v105, v106, v107\n"
"v_mfma_f32_16x16x32_bf16 v[196:199], a[76:79], a[4:7], v[196:199]\n"
"ds_read_b64_tr_b16 a[54:55], v5 offset:5120\n"
"v_cvt_pk_bf16_f32 v106, v108, v109\n"
"v_mfma_f32_16x16x32_bf16 v[228:231], a[72:75], a[8:11], 0\n"
"buffer_load_dwordx4 a[148:151], v15, s[16:19], s46 offen sc1\n"
"v_cvt_pk_bf16_f32 v107, v110, v111\n"
"v_mfma_f32_16x16x32_bf16 v[228:231], a[76:79], a[12:15], v[228:231]\n"
"s_add_u32 s46, s46, 32768\n"
"v_mul_f32 v112, v112, v72\n"
"v_mfma_f32_16x16x32_bf16 v[132:135], a[72:75], a[96:99], 0\n"
"buffer_store_dwordx4 v[104:107], v1, s[16:19], s73 offen offset:1024 sc1\n"
"v_mul_f32 v113, v113, v72\n"
"v_mfma_f32_16x16x32_bf16 v[132:135], a[76:79], a[100:103], v[132:135]\n"
"v_mul_f32 v114, v114, v72\n"
"v_mul_f32 v115, v115, v72\n"
"v_mfma_f32_16x16x32_bf16 v[164:167], a[72:75], a[104:107], 0\n"
"ds_read_b64_tr_b16 a[56:57], v5 offset:57344\n"
"v_mul_f32 v116, v116, v72\n"
"v_mfma_f32_16x16x32_bf16 v[164:167], a[76:79], a[108:111], v[164:167]\n"
"ds_read_b64_tr_b16 a[58:59], v5 offset:58368\n"
"s_waitcnt lgkmcnt(10)\n"
"v_mfma_f32_16x16x32_bf16 v[216:219], a[80:83], a[0:3], 0\n"
"buffer_load_dwordx4 a[152:155], v16, s[16:19], s54 offen sc1\n"
"v_mul_f32 v117, v117, v72\n"
"v_mfma_f32_16x16x32_bf16 v[216:219], a[84:87], a[4:7], v[216:219]\n"
"ds_read_b64_tr_b16 a[60:61], v5 offset:61440\n"
"v_mul_f32 v118, v118, v72\n"
"v_mfma_f32_16x16x32_bf16 v[248:251], a[80:83], a[8:11], 0\n"
"ds_read_b64_tr_b16 a[62:63], v5 offset:62464\n"
"v_mul_f32 v119, v119, v72\n"
"v_mfma_f32_16x16x32_bf16 v[248:251], a[84:87], a[12:15], v[248:251]\n"
"v_cvt_pk_bf16_f32 v112, v112, v113\n"
"v_cvt_pk_bf16_f32 v113, v114, v115\n"
"v_mfma_f32_16x16x32_bf16 v[152:155], a[80:83], a[96:99], 0\n"
"buffer_load_dwordx4 a[156:159], v17, s[16:19], s54 offen sc1\n"
"v_cvt_pk_bf16_f32 v114, v116, v117\n"
"v_mfma_f32_16x16x32_bf16 v[152:155], a[84:87], a[100:103], v[152:155]\n"
"s_add_u32 s54, s54, 32768\n"
"v_cvt_pk_bf16_f32 v115, v118, v119\n"
"v_mfma_f32_16x16x32_bf16 v[184:187], a[80:83], a[104:107], 0\n"
"buffer_store_dwordx4 v[112:115], v1, s[16:19], s73 offen offset:2048 sc1\n"
"v_mul_f32 v120, v120, v76\n"
"v_mfma_f32_16x16x32_bf16 v[184:187], a[84:87], a[108:111], v[184:187]\n"
"v_mul_f32 v121, v121, v76\n"
"s_waitcnt lgkmcnt(8)\n"
"v_mfma_f32_16x16x32_bf16 v[200:203], a[88:91], a[0:3], 0\n"
"v_mul_f32 v122, v122, v76\n"
"v_mul_f32 v123, v123, v76\n"
"v_mfma_f32_16x16x32_bf16 v[200:203], a[92:95], a[4:7], v[200:203]\n"
"buffer_load_dwordx4 a[160:163], v14, s[16:19], s46 offen sc1\n"
"v_mul_f32 v124, v124, v76\n"
"v_mfma_f32_16x16x32_bf16 v[232:235], a[88:91], a[8:11], 0\n"
"v_mul_f32 v125, v125, v76\n"
"v_mul_f32 v126, v126, v76\n"
"v_mfma_f32_16x16x32_bf16 v[232:235], a[92:95], a[12:15], v[232:235]\n"
"v_mul_f32 v127, v127, v76\n"
"v_cvt_pk_bf16_f32 v120, v120, v121\n"
"v_mfma_f32_16x16x32_bf16 v[136:139], a[88:91], a[96:99], 0\n"
"v_cvt_pk_bf16_f32 v121, v122, v123\n"
"v_cvt_pk_bf16_f32 v122, v124, v125\n"
"v_mfma_f32_16x16x32_bf16 v[136:139], a[92:95], a[100:103], v[136:139]\n"
"v_cvt_pk_bf16_f32 v123, v126, v127\n"
"v_mfma_f32_16x16x32_bf16 v[168:171], a[88:91], a[104:107], 0\n"
"buffer_load_dwordx4 a[164:167], v15, s[16:19], s46 offen sc1\n"
"v_mfma_f32_16x16x32_bf16 v[168:171], a[92:95], a[108:111], v[168:171]\n"
"s_waitcnt lgkmcnt(0)\n"
"s_barrier\n"
"v_mfma_f32_16x16x32_bf16 v[220:223], a[48:51], a[0:3], 0\n"
"s_add_u32 s46, s46, 32768\n"
"ds_read_b64_tr_b16 a[64:65], v2 offset:8192\n"
"v_mfma_f32_16x16x32_bf16 v[220:223], a[52:55], a[4:7], v[220:223]\n"
"ds_read_b64_tr_b16 a[66:67], v2 offset:9216\n"
"v_mfma_f32_16x16x32_bf16 v[252:255], a[48:51], a[8:11], 0\n"
"s_add_u32 m0, s57, 98304\n"
"ds_read_b64_tr_b16 a[68:69], v2 offset:12288\n"
"v_mfma_f32_16x16x32_bf16 v[252:255], a[52:55], a[12:15], v[252:255]\n"
"ds_read_b64_tr_b16 a[70:71], v2 offset:13312\n"
"v_mfma_f32_16x16x32_bf16 v[156:159], a[48:51], a[96:99], 0\n"
"buffer_load_dwordx4 v12, s[20:23], s71 offen offset:0 lds\n"
"v_mfma_f32_16x16x32_bf16 v[156:159], a[52:55], a[100:103], v[156:159]\n"
"ds_read_b64_tr_b16 a[72:73], v2 offset:24576\n"
"v_mfma_f32_16x16x32_bf16 v[188:191], a[48:51], a[104:107], 0\n"
"ds_read_b64_tr_b16 a[74:75], v2 offset:25600\n"
"v_mfma_f32_16x16x32_bf16 v[188:191], a[52:55], a[108:111], v[188:191]\n"
"ds_read_b64_tr_b16 a[76:77], v2 offset:28672\n"
"v_mfma_f32_16x16x32_bf16 v[204:207], a[56:59], a[0:3], 0\n"
"ds_read_b64_tr_b16 a[78:79], v2 offset:29696\n"
"v_mfma_f32_16x16x32_bf16 v[204:207], a[60:63], a[4:7], v[204:207]\n"
"buffer_load_dwordx4 v13, s[20:23], s71 offen offset:1024 lds\n"
"v_mfma_f32_16x16x32_bf16 v[236:239], a[56:59], a[8:11], 0\n"
"s_add_u32 s71, s71, 131072\n"
"ds_read_b64_tr_b16 a[80:81], v3 offset:8192\n"
"v_mfma_f32_16x16x32_bf16 v[236:239], a[60:63], a[12:15], v[236:239]\n"
"ds_read_b64_tr_b16 a[82:83], v3 offset:9216\n"
"v_mfma_f32_16x16x32_bf16 v[140:143], a[56:59], a[96:99], 0\n"
"ds_read_b64_tr_b16 a[84:85], v3 offset:12288\n"
"v_mfma_f32_16x16x32_bf16 v[140:143], a[60:63], a[100:103], v[140:143]\n"
"buffer_load_dwordx4 a[168:171], v16, s[16:19], s54 offen sc1\n"
"v_mfma_f32_16x16x32_bf16 v[172:175], a[56:59], a[104:107], 0\n"
"ds_read_b64_tr_b16 a[86:87], v3 offset:13312\n"
"v_mfma_f32_16x16x32_bf16 v[172:175], a[60:63], a[108:111], v[172:175]\n"
"ds_read_b64_tr_b16 a[88:89], v3 offset:24576\n"
"s_waitcnt vmcnt(22) lgkmcnt(9)\n"
"v_mfma_f32_16x16x32_bf16 v[208:211], a[64:67], a[16:19], v[208:211]\n"
"ds_read_b64_tr_b16 a[90:91], v3 offset:25600\n"
"v_mfma_f32_16x16x32_bf16 v[208:211], a[68:71], a[20:23], v[208:211]\n"
"ds_read_b64_tr_b16 a[92:93], v3 offset:28672\n"
"s_waitcnt vmcnt(20)\n"
"v_mfma_f32_16x16x32_bf16 v[240:243], a[64:67], a[24:27], v[240:243]\n"
"buffer_load_dwordx4 a[172:175], v17, s[16:19], s54 offen sc1\n"
"v_mfma_f32_16x16x32_bf16 v[240:243], a[68:71], a[28:31], v[240:243]\n"
"s_add_u32 s54, s54, 32768\n"
"ds_read_b64_tr_b16 a[94:95], v3 offset:29696\n"
"v_mfma_f32_16x16x32_bf16 v[144:147], a[64:67], a[112:115], v[144:147]\n"
"buffer_store_dwordx4 v[120:123], v1, s[16:19], s73 offen offset:3072 sc1\n"
"v_mfma_f32_16x16x32_bf16 v[144:147], a[68:71], a[116:119], v[144:147]\n"
"s_waitcnt vmcnt(20)\n"
"v_mfma_f32_16x16x32_bf16 v[176:179], a[64:67], a[120:123], v[176:179]\n"
"ds_read_b64_tr_b16 a[48:49], v4 offset:8192\n"
"v_mfma_f32_16x16x32_bf16 v[176:179], a[68:71], a[124:127], v[176:179]\n"
"ds_read_b64_tr_b16 a[50:51], v4 offset:9216\n"
"s_waitcnt lgkmcnt(10)\n"
"v_mfma_f32_16x16x32_bf16 v[192:195], a[72:75], a[16:19], v[192:195]\n"
"buffer_load_dwordx4 a[0:3], v18, s[16:19], s47 offen sc1\n"
"v_mfma_f32_16x16x32_bf16 v[192:195], a[76:79], a[20:23], v[192:195]\n"
"ds_read_b64_tr_b16 a[52:53], v4 offset:12288\n"
"v_mfma_f32_16x16x32_bf16 v[224:227], a[72:75], a[24:27], v[224:227]\n"
"ds_read_b64_tr_b16 a[54:55], v4 offset:13312\n"
"v_mfma_f32_16x16x32_bf16 v[224:227], a[76:79], a[28:31], v[224:227]\n"
"v_mfma_f32_16x16x32_bf16 v[128:131], a[72:75], a[112:115], v[128:131]\n"
"buffer_load_dwordx4 a[4:7], v19, s[16:19], s47 offen sc1\n"
"v_mfma_f32_16x16x32_bf16 v[128:131], a[76:79], a[116:119], v[128:131]\n"
"v_mfma_f32_16x16x32_bf16 v[160:163], a[72:75], a[120:123], v[160:163]\n"
"ds_read_b64_tr_b16 a[56:57], v4 offset:24576\n"
"v_mfma_f32_16x16x32_bf16 v[160:163], a[76:79], a[124:127], v[160:163]\n"
"ds_read_b64_tr_b16 a[58:59], v4 offset:25600\n"
"s_waitcnt lgkmcnt(10)\n"
"v_mfma_f32_16x16x32_bf16 v[212:215], a[80:83], a[16:19], v[212:215]\n"
"ds_read_b64_tr_b16 a[60:61], v4 offset:28672\n"
"v_mfma_f32_16x16x32_bf16 v[212:215], a[84:87], a[20:23], v[212:215]\n"
"buffer_load_dwordx4 a[8:11], v20, s[16:19], s47 offen sc1\n"
"v_mfma_f32_16x16x32_bf16 v[244:247], a[80:83], a[24:27], v[244:247]\n"
"ds_read_b64_tr_b16 a[62:63], v4 offset:29696\n"
"v_mfma_f32_16x16x32_bf16 v[244:247], a[84:87], a[28:31], v[244:247]\n"
"v_mfma_f32_16x16x32_bf16 v[148:151], a[80:83], a[112:115], v[148:151]\n"
"v_mfma_f32_16x16x32_bf16 v[148:151], a[84:87], a[116:119], v[148:151]\n"
"v_mfma_f32_16x16x32_bf16 v[180:183], a[80:83], a[120:123], v[180:183]\n"
"buffer_load_dwordx4 a[12:15], v21, s[16:19], s47 offen sc1\n"
"v_mfma_f32_16x16x32_bf16 v[180:183], a[84:87], a[124:127], v[180:183]\n"
"s_add_u32 s47, s47, 32768\n"
"s_waitcnt lgkmcnt(8)\n"
"v_mfma_f32_16x16x32_bf16 v[196:199], a[88:91], a[16:19], v[196:199]\n"
"ds_read_b64_tr_b16 a[64:65], v5 offset:8192\n"
"v_mfma_f32_16x16x32_bf16 v[196:199], a[92:95], a[20:23], v[196:199]\n"
"ds_read_b64_tr_b16 a[66:67], v5 offset:9216\n"
"v_mfma_f32_16x16x32_bf16 v[228:231], a[88:91], a[24:27], v[228:231]\n"
"ds_read_b64_tr_b16 a[68:69], v5 offset:12288\n"
"v_mfma_f32_16x16x32_bf16 v[228:231], a[92:95], a[28:31], v[228:231]\n"
"buffer_load_dwordx4 a[176:179], v14, s[16:19], s46 offen sc1\n"
"v_mfma_f32_16x16x32_bf16 v[132:135], a[88:91], a[112:115], v[132:135]\n"
"ds_read_b64_tr_b16 a[70:71], v5 offset:13312\n"
"v_mfma_f32_16x16x32_bf16 v[132:135], a[92:95], a[116:119], v[132:135]\n"
"v_mfma_f32_16x16x32_bf16 v[164:167], a[88:91], a[120:123], v[164:167]\n"
"ds_read_b64_tr_b16 a[72:73], v5 offset:24576\n"
"v_mfma_f32_16x16x32_bf16 v[164:167], a[92:95], a[124:127], v[164:167]\n"
"ds_read_b64_tr_b16 a[74:75], v5 offset:25600\n"
"s_waitcnt lgkmcnt(10)\n"
"v_mfma_f32_16x16x32_bf16 v[216:219], a[48:51], a[16:19], v[216:219]\n"
"buffer_load_dwordx4 a[180:183], v15, s[16:19], s46 offen sc1\n"
"v_mfma_f32_16x16x32_bf16 v[216:219], a[52:55], a[20:23], v[216:219]\n"
"s_add_u32 s46, s46, 32768\n"
"ds_read_b64_tr_b16 a[76:77], v5 offset:28672\n"
"v_mfma_f32_16x16x32_bf16 v[248:251], a[48:51], a[24:27], v[248:251]\n"
"ds_read_b64_tr_b16 a[78:79], v5 offset:29696\n"
"v_mfma_f32_16x16x32_bf16 v[248:251], a[52:55], a[28:31], v[248:251]\n"
"v_mfma_f32_16x16x32_bf16 v[152:155], a[48:51], a[112:115], v[152:155]\n"
"buffer_load_dwordx4 a[184:187], v16, s[16:19], s54 offen sc1\n"
"v_mfma_f32_16x16x32_bf16 v[152:155], a[52:55], a[116:119], v[152:155]\n"
"v_mfma_f32_16x16x32_bf16 v[184:187], a[48:51], a[120:123], v[184:187]\n"
"v_mfma_f32_16x16x32_bf16 v[184:187], a[52:55], a[124:127], v[184:187]\n"
"s_waitcnt lgkmcnt(8)\n"
"v_mfma_f32_16x16x32_bf16 v[200:203], a[56:59], a[16:19], v[200:203]\n"
"v_mfma_f32_16x16x32_bf16 v[200:203], a[60:63], a[20:23], v[200:203]\n"
"buffer_load_dwordx4 a[188:191], v17, s[16:19], s54 offen sc1\n"
"v_mfma_f32_16x16x32_bf16 v[232:235], a[56:59], a[24:27], v[232:235]\n"
"s_add_u32 s54, s54, 32768\n"
"v_mfma_f32_16x16x32_bf16 v[232:235], a[60:63], a[28:31], v[232:235]\n"
"v_mfma_f32_16x16x32_bf16 v[136:139], a[56:59], a[112:115], v[136:139]\n"
"v_mfma_f32_16x16x32_bf16 v[136:139], a[60:63], a[116:119], v[136:139]\n"
"v_mfma_f32_16x16x32_bf16 v[168:171], a[56:59], a[120:123], v[168:171]\n"
"buffer_load_dwordx4 a[192:195], v14, s[16:19], s46 offen sc1\n"
"v_mfma_f32_16x16x32_bf16 v[168:171], a[60:63], a[124:127], v[168:171]\n"
"s_waitcnt lgkmcnt(0)\n"
"s_barrier\n"
"v_mfma_f32_16x16x32_bf16 v[220:223], a[64:67], a[16:19], v[220:223]\n"
"ds_read_b64_tr_b16 a[80:81], v2 offset:16384\n"
"v_mfma_f32_16x16x32_bf16 v[220:223], a[68:71], a[20:23], v[220:223]\n"
"ds_read_b64_tr_b16 a[82:83], v2 offset:17408\n"
"v_mfma_f32_16x16x32_bf16 v[252:255], a[64:67], a[24:27], v[252:255]\n"
"ds_read_b64_tr_b16 a[84:85], v2 offset:20480\n"
"v_mfma_f32_16x16x32_bf16 v[252:255], a[68:71], a[28:31], v[252:255]\n"
"buffer_load_dwordx4 a[196:199], v15, s[16:19], s46 offen sc1\n"
"v_mfma_f32_16x16x32_bf16 v[156:159], a[64:67], a[112:115], v[156:159]\n"
"s_add_u32 s46, s46, 32768\n"
"ds_read_b64_tr_b16 a[86:87], v2 offset:21504\n"
"v_mfma_f32_16x16x32_bf16 v[156:159], a[68:71], a[116:119], v[156:159]\n"
"ds_read_b64_tr_b16 a[88:89], v2 offset:32768\n"
"v_mfma_f32_16x16x32_bf16 v[188:191], a[64:67], a[120:123], v[188:191]\n"
"ds_read_b64_tr_b16 a[90:91], v2 offset:33792\n"
"v_mfma_f32_16x16x32_bf16 v[188:191], a[68:71], a[124:127], v[188:191]\n"
"s_add_u32 m0, s57, 106496\n"
"v_mfma_f32_16x16x32_bf16 v[204:207], a[72:75], a[16:19], v[204:207]\n"
"ds_read_b64_tr_b16 a[92:93], v2 offset:36864\n"
"v_mfma_f32_16x16x32_bf16 v[204:207], a[76:79], a[20:23], v[204:207]\n"
"buffer_load_dwordx4 v12, s[20:23], s71 offen offset:0 lds\n"
"v_mfma_f32_16x16x32_bf16 v[236:239], a[72:75], a[24:27], v[236:239]\n"
"ds_read_b64_tr_b16 a[94:95], v2 offset:37888\n"
"v_mfma_f32_16x16x32_bf16 v[236:239], a[76:79], a[28:31], v[236:239]\n"
"ds_read_b64_tr_b16 a[48:49], v3 offset:16384\n"
"v_mfma_f32_16x16x32_bf16 v[140:143], a[72:75], a[112:115], v[140:143]\n"
"ds_read_b64_tr_b16 a[50:51], v3 offset:17408\n"
"v_mfma_f32_16x16x32_bf16 v[140:143], a[76:79], a[116:119], v[140:143]\n"
"ds_read_b64_tr_b16 a[52:53], v3 offset:20480\n"
"v_mfma_f32_16x16x32_bf16 v[172:175], a[72:75], a[120:123], v[172:175]\n"
"buffer_load_dwordx4 v13, s[20:23], s71 offen offset:1024 lds\n"
"v_mfma_f32_16x16x32_bf16 v[172:175], a[76:79], a[124:127], v[172:175]\n"
"s_add_u32 s71, s71, 131072\n"
"s_waitcnt vmcnt(30) lgkmcnt(7)\n"
"v_mfma_f32_16x16x32_bf16 v[208:211], a[80:83], a[32:35], v[208:211]\n"
"ds_read_b64_tr_b16 a[54:55], v3 offset:21504\n"
"v_mfma_f32_16x16x32_bf16 v[208:211], a[84:87], a[36:39], v[208:211]\n"
"ds_read_b64_tr_b16 a[56:57], v3 offset:32768\n"
"s_waitcnt vmcnt(28)\n"
"v_mfma_f32_16x16x32_bf16 v[240:243], a[80:83], a[40:43], v[240:243]\n"
"buffer_load_dwordx4 a[16:19], v18, s[16:19], s47 offen sc1\n"
"v_mfma_f32_16x16x32_bf16 v[240:243], a[84:87], a[44:47], v[240:243]\n"
"ds_read_b64_tr_b16 a[58:59], v3 offset:33792\n"
"v_mfma_f32_16x16x32_bf16 v[144:147], a[80:83], a[128:131], v[144:147]\n"
"ds_read_b64_tr_b16 a[60:61], v3 offset:36864\n"
"v_mfma_f32_16x16x32_bf16 v[144:147], a[84:87], a[132:135], v[144:147]\n"
"ds_read_b64_tr_b16 a[62:63], v3 offset:37888\n"
"s_waitcnt vmcnt(27)\n"
"v_mfma_f32_16x16x32_bf16 v[176:179], a[80:83], a[136:139], v[176:179]\n"
"ds_read_b64_tr_b16 a[64:65], v4 offset:16384\n"
"v_mfma_f32_16x16x32_bf16 v[176:179], a[84:87], a[140:143], v[176:179]\n"
"ds_read_b64_tr_b16 a[66:67], v4 offset:17408\n"
"s_waitcnt lgkmcnt(10)\n"
"v_mfma_f32_16x16x32_bf16 v[192:195], a[88:91], a[32:35], v[192:195]\n"
"buffer_load_dwordx4 a[20:23], v19, s[16:19], s47 offen sc1\n"
"v_mfma_f32_16x16x32_bf16 v[192:195], a[92:95], a[36:39], v[192:195]\n"
"ds_read_b64_tr_b16 a[68:69], v4 offset:20480\n"
"s_waitcnt vmcnt(14)\n"
"v_mfma_f32_16x16x32_bf16 v[224:227], a[88:91], a[40:43], v[224:227]\n"
"ds_read_b64_tr_b16 a[70:71], v4 offset:21504\n"
"v_mfma_f32_16x16x32_bf16 v[224:227], a[92:95], a[44:47], v[224:227]\n"
"s_mov_b64 exec, 1\n"
"buffer_store_dword v23, off, s[16:19], s78 sc1\n"
"s_mov_b64 exec, -1\n"
"v_mfma_f32_16x16x32_bf16 v[128:131], a[88:91], a[128:131], v[128:131]\n"
"buffer_load_dwordx4 a[24:27], v20, s[16:19], s47 offen sc1\n"
"v_mfma_f32_16x16x32_bf16 v[128:131], a[92:95], a[132:135], v[128:131]\n"
"s_lshl2_add_u32 s64, s96, s40\n"
"s_mul_i32 s60, s64, 12288\n"
"s_add_u32 s60, s60, 28311552\n"
"v_mfma_f32_16x16x32_bf16 v[160:163], a[88:91], a[136:139], v[160:163]\n"
"ds_read_b64_tr_b16 a[72:73], v4 offset:32768\n"
"s_lshl_b32 s64, s64, 4\n"
"v_mfma_f32_16x16x32_bf16 v[160:163], a[92:95], a[140:143], v[160:163]\n"
"ds_read_b64_tr_b16 a[74:75], v4 offset:33792\n"
"s_waitcnt lgkmcnt(10)\n"
"v_mfma_f32_16x16x32_bf16 v[212:215], a[48:51], a[32:35], v[212:215]\n"
"ds_read_b64_tr_b16 a[76:77], v4 offset:36864\n"
"s_add_u32 s64, s64, 241664\n"
"v_mfma_f32_16x16x32_bf16 v[212:215], a[52:55], a[36:39], v[212:215]\n"
"buffer_load_dwordx4 a[28:31], v21, s[16:19], s47 offen sc1\n"
"v_mov_b32 v38, 2130706432\n"
"v_mfma_f32_16x16x32_bf16 v[244:247], a[48:51], a[40:43], v[244:247]\n"
"s_add_u32 s47, s47, 32768\n"
"ds_read_b64_tr_b16 a[78:79], v4 offset:37888\n"
"v_mfma_f32_16x16x32_bf16 v[244:247], a[52:55], a[44:47], v[244:247]\n"
"v_lshl_add_u32 v39, v0, 2, s64\n"
"v_cmp_gt_u32 vcc, 3, v0\n"
"v_mfma_f32_16x16x32_bf16 v[148:151], a[48:51], a[128:131], v[148:151]\n"
"s_mul_i32 s61, s79, 12288\n"
"s_add_u32 s61, s61, 28311552\n"
"s_mul_i32 s62, s80, 12288\n"
"v_mfma_f32_16x16x32_bf16 v[148:151], a[52:55], a[132:135], v[148:151]\n"
"s_add_u32 s62, s62, 28315648\n"
"s_mul_i32 s63, s81, 12288\n"
"s_add_u32 s63, s63, 28319744\n"
"v_mfma_f32_16x16x32_bf16 v[180:183], a[48:51], a[136:139], v[180:183]\n"
"buffer_load_dwordx4 a[200:203], v16, s[16:19], s54 offen sc1\n"
"v_cndmask_b32 v39, v38, v39, vcc\n"
"v_mfma_f32_16x16x32_bf16 v[180:183], a[52:55], a[140:143], v[180:183]\n"
"ds_read_b64_tr_b16 a[80:81], v5 offset:16384\n"
"s_waitcnt lgkmcnt(9)\n"
"v_mfma_f32_16x16x32_bf16 v[196:199], a[56:59], a[32:35], v[196:199]\n"
"ds_read_b64_tr_b16 a[82:83], v5 offset:17408\n"
"s_lshl_b32 s64, s79, 4\n"
"v_mfma_f32_16x16x32_bf16 v[196:199], a[60:63], a[36:39], v[196:199]\n"
"ds_read_b64_tr_b16 a[84:85], v5 offset:20480\n"
"s_add_u32 s64, s64, 241664\n"
"v_mfma_f32_16x16x32_bf16 v[228:231], a[56:59], a[40:43], v[228:231]\n"
"ds_read_b64_tr_b16 a[86:87], v5 offset:21504\n"
"v_writelane_b32 v38, s64, 1\n"
"v_mfma_f32_16x16x32_bf16 v[228:231], a[60:63], a[44:47], v[228:231]\n"
"buffer_load_dwordx4 a[204:207], v17, s[16:19], s54 offen sc1\n"
"v_mfma_f32_16x16x32_bf16 v[132:135], a[56:59], a[128:131], v[132:135]\n"
"s_add_u32 s54, s54, 32768\n"
"s_lshl_b32 s64, s80, 4\n"
"s_add_u32 s64, s64, 241668\n"
"v_mfma_f32_16x16x32_bf16 v[132:135], a[60:63], a[132:135], v[132:135]\n"
"v_writelane_b32 v38, s64, 2\n"
"s_lshl_b32 s64, s81, 4\n"
"v_mfma_f32_16x16x32_bf16 v[164:167], a[56:59], a[136:139], v[164:167]\n"
"ds_read_b64_tr_b16 a[88:89], v5 offset:32768\n"
"s_add_u32 s64, s64, 241672\n"
"v_mfma_f32_16x16x32_bf16 v[164:167], a[60:63], a[140:143], v[164:167]\n"
"ds_read_b64_tr_b16 a[90:91], v5 offset:33792\n"
"s_waitcnt lgkmcnt(10)\n"
"v_mfma_f32_16x16x32_bf16 v[216:219], a[64:67], a[32:35], v[216:219]\n"
"buffer_load_dwordx4 a[208:211], v14, s[16:19], s46 offen sc1\n"
"v_writelane_b32 v38, s64, 3\n"
"v_mfma_f32_16x16x32_bf16 v[216:219], a[68:71], a[36:39], v[216:219]\n"
"ds_read_b64_tr_b16 a[92:93], v5 offset:36864\n"
"s_lshl2_add_u32 s64, s44, s40\n"
"v_mfma_f32_16x16x32_bf16 v[248:251], a[64:67], a[40:43], v[248:251]\n"
"ds_read_b64_tr_b16 a[94:95], v5 offset:37888\n"
"s_lshl2_add_u32 s64, s64, s90\n"
"v_mfma_f32_16x16x32_bf16 v[248:251], a[68:71], a[44:47], v[248:251]\n"
"s_lshl_b32 s64, s64, 15\n"
"s_lshr_b32 s65, s45, 1\n"
"s_lshl1_add_u32 s65, s41, s65\n"
"v_mfma_f32_16x16x32_bf16 v[152:155], a[64:67], a[128:131], v[152:155]\n"
"s_lshl_b32 s65, s65, 7\n"
"s_add_u32 s64, s64, s65\n"
"v_mfma_f32_16x16x32_bf16 v[152:155], a[68:71], a[132:135], v[152:155]\n"
"buffer_load_dwordx4 a[212:215], v15, s[16:19], s46 offen sc1\n"
"v_lshrrev_b32 v36, 3, v0\n"
"v_mfma_f32_16x16x32_bf16 v[184:187], a[64:67], a[136:139], v[184:187]\n"
"s_add_u32 s46, s46, 32768\n"
"v_and_b32 v37, 0x70, v1\n"
"v_mfma_f32_16x16x32_bf16 v[184:187], a[68:71], a[140:143], v[184:187]\n"
"v_lshl_add_u32 v26, v36, 11, v37\n"
"s_waitcnt lgkmcnt(8)\n"
"v_mfma_f32_16x16x32_bf16 v[200:203], a[72:75], a[32:35], v[200:203]\n"
"v_add_u32 v26, s64, v26\n"
"v_xor_b32 v27, 16384, v26\n"
"v_mfma_f32_16x16x32_bf16 v[200:203], a[76:79], a[36:39], v[200:203]\n"
"v_xor_b32 v28, 32768, v26\n"
"v_xor_b32 v29, 49152, v26\n"
"v_mfma_f32_16x16x32_bf16 v[232:235], a[72:75], a[40:43], v[232:235]\n"
"buffer_load_dwordx4 a[224:227], v14, s[16:19], s46 offen sc1\n"
"v_mfma_f32_16x16x32_bf16 v[232:235], a[76:79], a[44:47], v[232:235]\n"
"s_lshl_b32 s67, s40, 13\n"
"v_and_b32 v41, 15, v0\n"
"v_mfma_f32_16x16x32_bf16 v[136:139], a[72:75], a[128:131], v[136:139]\n"
"v_lshlrev_b32 v41, 8, v41\n"
"v_lshrrev_b32 v42, 4, v0\n"
"v_mfma_f32_16x16x32_bf16 v[136:139], a[76:79], a[132:135], v[136:139]\n"
"v_and_b32 v43, 7, v0\n"
"v_xor_b32 v42, v42, v43\n"
"v_mfma_f32_16x16x32_bf16 v[168:171], a[72:75], a[136:139], v[168:171]\n"
"v_lshl_add_u32 v41, v42, 4, v41\n"
"v_add_u32 v41, s67, v41\n"
"v_mfma_f32_16x16x32_bf16 v[168:171], a[76:79], a[140:143], v[168:171]\n"
"s_waitcnt lgkmcnt(0)\n"
"s_barrier\n"
"v_mfma_f32_16x16x32_bf16 v[220:223], a[80:83], a[32:35], v[220:223]\n"
"buffer_load_dwordx4 a[228:231], v15, s[16:19], s46 offen sc1\n"
"v_xor_b32 v45, 64, v41\n"
"v_mfma_f32_16x16x32_bf16 v[220:223], a[84:87], a[36:39], v[220:223]\n"
"s_add_u32 s46, s46, 32768\n"
"ds_read_b64_tr_b16 a[48:49], v2 offset:0\n"
"v_mfma_f32_16x16x32_bf16 v[252:255], a[80:83], a[40:43], v[252:255]\n"
"ds_read_b64_tr_b16 a[50:51], v2 offset:1024\n"
"v_lshlrev_b32 v42, 1, v43\n"
"v_mfma_f32_16x16x32_bf16 v[252:255], a[84:87], a[44:47], v[252:255]\n"
"s_add_u32 m0, s57, 114688\n"
"s_add_u32 s53, s71, s28\n"
"v_mfma_f32_16x16x32_bf16 v[156:159], a[80:83], a[128:131], v[156:159]\n"
"ds_read_b64_tr_b16 a[52:53], v2 offset:4096\n"
"v_xor_b32 v42, v42, v36\n"
"v_mfma_f32_16x16x32_bf16 v[156:159], a[84:87], a[132:135], v[156:159]\n"
"buffer_load_dwordx4 v12, s[20:23], s53 offen offset:0 lds\n"
"v_lshlrev_b32 v42, 4, v42\n"
"v_mfma_f32_16x16x32_bf16 v[188:191], a[80:83], a[136:139], v[188:191]\n"
"s_add_u32 s53, s71, s29\n"
"ds_read_b64_tr_b16 a[54:55], v2 offset:5120\n"
"v_mfma_f32_16x16x32_bf16 v[188:191], a[84:87], a[140:143], v[188:191]\n"
"ds_read_b64_tr_b16 a[56:57], v2 offset:40960\n"
"v_mfma_f32_16x16x32_bf16 v[204:207], a[88:91], a[32:35], v[204:207]\n"
"ds_read_b64_tr_b16 a[58:59], v2 offset:41984\n"
"v_lshl_add_u32 v42, v36, 8, v42\n"
"v_mfma_f32_16x16x32_bf16 v[204:207], a[92:95], a[36:39], v[204:207]\n"
"ds_read_b64_tr_b16 a[60:61], v2 offset:45056\n"
"v_add_u32 v42, s67, v42\n"
"v_mfma_f32_16x16x32_bf16 v[236:239], a[88:91], a[40:43], v[236:239]\n"
"buffer_load_dwordx4 v13, s[20:23], s53 offen offset:1024 lds\n"
"v_xor_b32 v43, 16, v42\n"
"v_mfma_f32_16x16x32_bf16 v[236:239], a[92:95], a[44:47], v[236:239]\n"
"s_add_u32 s71, s71, 131072\n"
"ds_read_b64_tr_b16 a[62:63], v2 offset:46080\n"
"v_mfma_f32_16x16x32_bf16 v[140:143], a[88:91], a[128:131], v[140:143]\n"
"ds_read_b64_tr_b16 a[64:65], v3 offset:0\n"
"s_mov_b32 s82, 0x3f80\n"
"v_mfma_f32_16x16x32_bf16 v[140:143], a[92:95], a[132:135], v[140:143]\n"
"ds_read_b64_tr_b16 a[66:67], v3 offset:1024\n"
"s_mov_b32 s83, 0x3f800000\n"
"v_mfma_f32_16x16x32_bf16 v[172:175], a[88:91], a[136:139], v[172:175]\n"
".Lpad11:\n"
"s_cbranch_execz .Lpad15\n"
"buffer_load_dwordx4 a[216:219], v16, s[16:19], s54 offen sc1\n"
"v_add_u32 v34, 4096, v34\n"
"v_mfma_f32_16x16x32_bf16 v[172:175], a[92:95], a[140:143], v[172:175]\n"
"ds_read_b64_tr_b16 a[68:69], v3 offset:4096\n"
"s_waitcnt vmcnt(24) lgkmcnt(7)\n"
"v_mfma_f32_16x16x32_bf16 v[208:211], a[48:51], a[0:3], v[208:211]\n"
"ds_read_b64_tr_b16 a[70:71], v3 offset:5120\n"
"v_mfma_f32_16x16x32_bf16 v[208:211], a[52:55], a[4:7], v[208:211]\n"
"ds_read_b64_tr_b16 a[72:73], v3 offset:40960\n"
"s_waitcnt vmcnt(22)\n"
"v_mfma_f32_16x16x32_bf16 v[240:243], a[48:51], a[8:11], v[240:243]\n"
"ds_read_b64_tr_b16 a[74:75], v3 offset:41984\n"
"v_mfma_f32_16x16x32_bf16 v[240:243], a[52:55], a[12:15], v[240:243]\n"
"buffer_load_dwordx4 a[220:223], v17, s[16:19], s54 offen sc1\n"
"v_mfma_f32_16x16x32_bf16 v[144:147], a[48:51], a[144:147], v[144:147]\n"
"s_add_u32 s54, s54, 32768\n"
"ds_read_b64_tr_b16 a[76:77], v3 offset:45056\n"
"v_mfma_f32_16x16x32_bf16 v[144:147], a[52:55], a[148:151], v[144:147]\n"
"ds_read_b64_tr_b16 a[78:79], v3 offset:46080\n"
"v_mfma_f32_16x16x32_bf16 v[176:179], a[48:51], a[152:155], v[176:179]\n"
"ds_read_b64_tr_b16 a[80:81], v4 offset:0\n"
"v_mfma_f32_16x16x32_bf16 v[176:179], a[52:55], a[156:159], v[176:179]\n"
"ds_read_b64_tr_b16 a[82:83], v4 offset:1024\n"
"s_waitcnt lgkmcnt(10)\n"
"v_mfma_f32_16x16x32_bf16 v[192:195], a[56:59], a[0:3], v[192:195]\n"
"buffer_load_dwordx4 a[32:35], v18, s[16:19], s47 offen sc1\n"
"v_mfma_f32_16x16x32_bf16 v[192:195], a[60:63], a[4:7], v[192:195]\n"
"ds_read_b64_tr_b16 a[84:85], v4 offset:4096\n"
"v_mfma_f32_16x16x32_bf16 v[224:227], a[56:59], a[8:11], v[224:227]\n"
"ds_read_b64_tr_b16 a[86:87], v4 offset:5120\n"
"v_mfma_f32_16x16x32_bf16 v[224:227], a[60:63], a[12:15], v[224:227]\n"
"v_mfma_f32_16x16x32_bf16 v[128:131], a[56:59], a[144:147], v[128:131]\n"
"v_mfma_f32_16x16x32_bf16 v[128:131], a[60:63], a[148:151], v[128:131]\n"
"buffer_load_dwordx4 a[36:39], v19, s[16:19], s47 offen sc1\n"
"v_mfma_f32_16x16x32_bf16 v[160:163], a[56:59], a[152:155], v[160:163]\n"
"ds_read_b64_tr_b16 a[88:89], v4 offset:40960\n"
"v_mfma_f32_16x16x32_bf16 v[160:163], a[60:63], a[156:159], v[160:163]\n"
"ds_read_b64_tr_b16 a[90:91], v4 offset:41984\n"
"s_waitcnt lgkmcnt(10)\n"
"v_mfma_f32_16x16x32_bf16 v[212:215], a[64:67], a[0:3], v[212:215]\n"
"ds_read_b64_tr_b16 a[92:93], v4 offset:45056\n"
"v_mfma_f32_16x16x32_bf16 v[212:215], a[68:71], a[4:7], v[212:215]\n"
"buffer_load_dwordx4 a[40:43], v20, s[16:19], s47 offen sc1\n"
"v_mfma_f32_16x16x32_bf16 v[244:247], a[64:67], a[8:11], v[244:247]\n"
"ds_read_b64_tr_b16 a[94:95], v4 offset:46080\n"
"v_mfma_f32_16x16x32_bf16 v[244:247], a[68:71], a[12:15], v[244:247]\n"
"v_mfma_f32_16x16x32_bf16 v[148:151], a[64:67], a[144:147], v[148:151]\n"
"v_mfma_f32_16x16x32_bf16 v[148:151], a[68:71], a[148:151], v[148:151]\n"
"v_mfma_f32_16x16x32_bf16 v[180:183], a[64:67], a[152:155], v[180:183]\n"
"buffer_load_dwordx4 a[44:47], v21, s[16:19], s47 offen sc1\n"
"v_mfma_f32_16x16x32_bf16 v[180:183], a[68:71], a[156:159], v[180:183]\n"
"s_add_u32 s47, s47, 32768\n"
"s_waitcnt lgkmcnt(8)\n"
"v_mfma_f32_16x16x32_bf16 v[196:199], a[72:75], a[0:3], v[196:199]\n"
"ds_read_b64_tr_b16 a[48:49], v5 offset:0\n"
"v_mfma_f32_16x16x32_bf16 v[196:199], a[76:79], a[4:7], v[196:199]\n"
"ds_read_b64_tr_b16 a[50:51], v5 offset:1024\n"
"v_mfma_f32_16x16x32_bf16 v[228:231], a[72:75], a[8:11], v[228:231]\n"
"ds_read_b64_tr_b16 a[52:53], v5 offset:4096\n"
"v_mfma_f32_16x16x32_bf16 v[228:231], a[76:79], a[12:15], v[228:231]\n"
"buffer_load_dwordx4 v[48:51], v14, s[16:19], s46 offen sc1\n"
"v_mfma_f32_16x16x32_bf16 v[132:135], a[72:75], a[144:147], v[132:135]\n"
"ds_read_b64_tr_b16 a[54:55], v5 offset:5120\n"
"v_mfma_f32_16x16x32_bf16 v[132:135], a[76:79], a[148:151], v[132:135]\n"
"v_mfma_f32_16x16x32_bf16 v[164:167], a[72:75], a[152:155], v[164:167]\n"
"ds_read_b64_tr_b16 a[56:57], v5 offset:40960\n"
"v_mfma_f32_16x16x32_bf16 v[164:167], a[76:79], a[156:159], v[164:167]\n"
"ds_read_b64_tr_b16 a[58:59], v5 offset:41984\n"
"s_waitcnt lgkmcnt(10)\n"
"v_mfma_f32_16x16x32_bf16 v[216:219], a[80:83], a[0:3], v[216:219]\n"
"buffer_load_dwordx4 v[52:55], v15, s[16:19], s46 offen sc1\n"
"v_mfma_f32_16x16x32_bf16 v[216:219], a[84:87], a[4:7], v[216:219]\n"
"s_add_u32 s46, s46, 32768\n"
"ds_read_b64_tr_b16 a[60:61], v5 offset:45056\n"
"v_mfma_f32_16x16x32_bf16 v[248:251], a[80:83], a[8:11], v[248:251]\n"
"ds_read_b64_tr_b16 a[62:63], v5 offset:46080\n"
"v_mfma_f32_16x16x32_bf16 v[248:251], a[84:87], a[12:15], v[248:251]\n"
"v_mfma_f32_16x16x32_bf16 v[152:155], a[80:83], a[144:147], v[152:155]\n"
"s_add_u32 m0, s57, 57344\n"
"v_mfma_f32_16x16x32_bf16 v[152:155], a[84:87], a[148:151], v[152:155]\n"
"v_mfma_f32_16x16x32_bf16 v[184:187], a[80:83], a[152:155], v[184:187]\n"
"buffer_load_dwordx4 v10, s[20:23], s70 offen offset:0 lds\n"
"v_mfma_f32_16x16x32_bf16 v[184:187], a[84:87], a[156:159], v[184:187]\n"
"s_waitcnt lgkmcnt(8)\n"
"v_mfma_f32_16x16x32_bf16 v[200:203], a[88:91], a[0:3], v[200:203]\n"
"v_mfma_f32_16x16x32_bf16 v[200:203], a[92:95], a[4:7], v[200:203]\n"
"v_mfma_f32_16x16x32_bf16 v[232:235], a[88:91], a[8:11], v[232:235]\n"
"v_mfma_f32_16x16x32_bf16 v[232:235], a[92:95], a[12:15], v[232:235]\n"
"buffer_load_dwordx4 v11, s[20:23], s70 offen offset:1024 lds\n"
"v_mfma_f32_16x16x32_bf16 v[136:139], a[88:91], a[144:147], v[136:139]\n"
"s_add_u32 s70, s70, 131072\n"
"v_mfma_f32_16x16x32_bf16 v[136:139], a[92:95], a[148:151], v[136:139]\n"
"v_mfma_f32_16x16x32_bf16 v[168:171], a[88:91], a[152:155], v[168:171]\n"
"v_mfma_f32_16x16x32_bf16 v[168:171], a[92:95], a[156:159], v[168:171]\n"
"s_waitcnt vmcnt(23) lgkmcnt(0)\n"
"s_barrier\n"
"v_mfma_f32_16x16x32_bf16 v[220:223], a[48:51], a[0:3], v[220:223]\n"
"buffer_load_dwordx4 a[232:235], v16, s[16:19], s54 offen sc1\n"
"v_mfma_f32_16x16x32_bf16 v[220:223], a[52:55], a[4:7], v[220:223]\n"
"ds_read_b64_tr_b16 a[64:65], v2 offset:8192\n"
"v_mfma_f32_16x16x32_bf16 v[252:255], a[48:51], a[8:11], v[252:255]\n"
"ds_read_b64_tr_b16 a[66:67], v2 offset:9216\n"
"v_mfma_f32_16x16x32_bf16 v[252:255], a[52:55], a[12:15], v[252:255]\n"
"ds_read_b64_tr_b16 a[68:69], v2 offset:12288\n"
"v_mfma_f32_16x16x32_bf16 v[156:159], a[48:51], a[144:147], v[156:159]\n"
"buffer_load_dwordx4 a[236:239], v17, s[16:19], s54 offen sc1\n"
"v_mfma_f32_16x16x32_bf16 v[156:159], a[52:55], a[148:151], v[156:159]\n"
"s_add_u32 s54, s54, 32768\n"
"ds_read_b64_tr_b16 a[70:71], v2 offset:13312\n"
"v_mfma_f32_16x16x32_bf16 v[188:191], a[48:51], a[152:155], v[188:191]\n"
"ds_read_b64_tr_b16 a[72:73], v2 offset:49152\n"
"v_mfma_f32_16x16x32_bf16 v[188:191], a[52:55], a[156:159], v[188:191]\n"
"ds_read_b64_tr_b16 a[74:75], v2 offset:50176\n"
"v_mfma_f32_16x16x32_bf16 v[204:207], a[56:59], a[0:3], v[204:207]\n"
"s_add_u32 m0, s57, 98304\n"
"ds_read_b64_tr_b16 a[76:77], v2 offset:53248\n"
"v_mfma_f32_16x16x32_bf16 v[204:207], a[60:63], a[4:7], v[204:207]\n"
"ds_read_b64_tr_b16 a[78:79], v2 offset:54272\n"
"v_mfma_f32_16x16x32_bf16 v[236:239], a[56:59], a[8:11], v[236:239]\n"
"buffer_load_dwordx4 v12, s[20:23], s71 offen offset:0 lds\n"
"v_mfma_f32_16x16x32_bf16 v[236:239], a[60:63], a[12:15], v[236:239]\n"
"ds_read_b64_tr_b16 a[80:81], v3 offset:8192\n"
"v_mfma_f32_16x16x32_bf16 v[140:143], a[56:59], a[144:147], v[140:143]\n"
"ds_read_b64_tr_b16 a[82:83], v3 offset:9216\n"
"v_mfma_f32_16x16x32_bf16 v[140:143], a[60:63], a[148:151], v[140:143]\n"
"ds_read_b64_tr_b16 a[84:85], v3 offset:12288\n"
"v_mfma_f32_16x16x32_bf16 v[172:175], a[56:59], a[152:155], v[172:175]\n"
"ds_read_b64_tr_b16 a[86:87], v3 offset:13312\n"
"v_mfma_f32_16x16x32_bf16 v[172:175], a[60:63], a[156:159], v[172:175]\n"
"ds_read_b64_tr_b16 a[88:89], v3 offset:49152\n"
"s_waitcnt vmcnt(24) lgkmcnt(9)\n"
"v_mfma_f32_16x16x32_bf16 v[208:211], a[64:67], a[16:19], v[208:211]\n"
"buffer_load_dwordx4 v13, s[20:23], s71 offen offset:1024 lds\n"
"v_mfma_f32_16x16x32_bf16 v[208:211], a[68:71], a[20:23], v[208:211]\n"
"s_add_u32 s71, s71, 131072\n"
"ds_read_b64_tr_b16 a[90:91], v3 offset:50176\n"
"s_waitcnt vmcnt(22)\n"
"v_mfma_f32_16x16x32_bf16 v[240:243], a[64:67], a[24:27], v[240:243]\n"
"ds_read_b64_tr_b16 a[92:93], v3 offset:53248\n"
"v_mfma_f32_16x16x32_bf16 v[240:243], a[68:71], a[28:31], v[240:243]\n"
"buffer_load_dwordx4 a[0:3], v18, s[16:19], s47 offen sc1\n"
"v_mfma_f32_16x16x32_bf16 v[144:147], a[64:67], a[160:163], v[144:147]\n"
"ds_read_b64_tr_b16 a[94:95], v3 offset:54272\n"
"v_mfma_f32_16x16x32_bf16 v[144:147], a[68:71], a[164:167], v[144:147]\n"
"v_mfma_f32_16x16x32_bf16 v[176:179], a[64:67], a[168:171], v[176:179]\n"
"ds_read_b64_tr_b16 a[48:49], v4 offset:8192\n"
"v_mfma_f32_16x16x32_bf16 v[176:179], a[68:71], a[172:175], v[176:179]\n"
"ds_read_b64_tr_b16 a[50:51], v4 offset:9216\n"
"s_waitcnt lgkmcnt(10)\n"
"v_mfma_f32_16x16x32_bf16 v[192:195], a[72:75], a[16:19], v[192:195]\n"
"buffer_load_dwordx4 a[4:7], v19, s[16:19], s47 offen sc1\n"
"v_mfma_f32_16x16x32_bf16 v[192:195], a[76:79], a[20:23], v[192:195]\n"
"ds_read_b64_tr_b16 a[52:53], v4 offset:12288\n"
"v_mfma_f32_16x16x32_bf16 v[224:227], a[72:75], a[24:27], v[224:227]\n"
"ds_read_b64_tr_b16 a[54:55], v4 offset:13312\n"
"v_mfma_f32_16x16x32_bf16 v[224:227], a[76:79], a[28:31], v[224:227]\n"
"v_mfma_f32_16x16x32_bf16 v[128:131], a[72:75], a[160:163], v[128:131]\n"
"v_mfma_f32_16x16x32_bf16 v[128:131], a[76:79], a[164:167], v[128:131]\n"
"buffer_load_dwordx4 a[8:11], v20, s[16:19], s47 offen sc1\n"
"v_mfma_f32_16x16x32_bf16 v[160:163], a[72:75], a[168:171], v[160:163]\n"
"ds_read_b64_tr_b16 a[56:57], v4 offset:49152\n"
"v_mfma_f32_16x16x32_bf16 v[160:163], a[76:79], a[172:175], v[160:163]\n"
"ds_read_b64_tr_b16 a[58:59], v4 offset:50176\n"
"s_waitcnt lgkmcnt(10)\n"
"v_mfma_f32_16x16x32_bf16 v[212:215], a[80:83], a[16:19], v[212:215]\n"
"ds_read_b64_tr_b16 a[60:61], v4 offset:53248\n"
"v_mfma_f32_16x16x32_bf16 v[212:215], a[84:87], a[20:23], v[212:215]\n"
"ds_read_b64_tr_b16 a[62:63], v4 offset:54272\n"
"v_mfma_f32_16x16x32_bf16 v[244:247], a[80:83], a[24:27], v[244:247]\n"
"buffer_load_dwordx4 a[12:15], v21, s[16:19], s47 offen sc1\n"
"v_mfma_f32_16x16x32_bf16 v[244:247], a[84:87], a[28:31], v[244:247]\n"
"s_add_u32 s47, s47, 32768\n"
"v_mfma_f32_16x16x32_bf16 v[148:151], a[80:83], a[160:163], v[148:151]\n"
"v_mfma_f32_16x16x32_bf16 v[148:151], a[84:87], a[164:167], v[148:151]\n"
"v_mfma_f32_16x16x32_bf16 v[180:183], a[80:83], a[168:171], v[180:183]\n"
"ds_read_b64_tr_b16 a[64:65], v5 offset:8192\n"
"v_mfma_f32_16x16x32_bf16 v[180:183], a[84:87], a[172:175], v[180:183]\n"
"ds_read_b64_tr_b16 a[66:67], v5 offset:9216\n"
"s_waitcnt lgkmcnt(10)\n"
"v_mfma_f32_16x16x32_bf16 v[196:199], a[88:91], a[16:19], v[196:199]\n"
"buffer_load_dwordx4 v[56:59], v16, s[16:19], s54 offen sc1\n"
"v_mfma_f32_16x16x32_bf16 v[196:199], a[92:95], a[20:23], v[196:199]\n"
"ds_read_b64_tr_b16 a[68:69], v5 offset:12288\n"
"v_mfma_f32_16x16x32_bf16 v[228:231], a[88:91], a[24:27], v[228:231]\n"
"ds_read_b64_tr_b16 a[70:71], v5 offset:13312\n"
"v_mfma_f32_16x16x32_bf16 v[228:231], a[92:95], a[28:31], v[228:231]\n"
"v_mfma_f32_16x16x32_bf16 v[132:135], a[88:91], a[160:163], v[132:135]\n"
"buffer_load_dwordx4 v[60:63], v17, s[16:19], s54 offen sc1\n"
"v_mfma_f32_16x16x32_bf16 v[132:135], a[92:95], a[164:167], v[132:135]\n"
"s_add_u32 s54, s54, 32768\n"
"v_mfma_f32_16x16x32_bf16 v[164:167], a[88:91], a[168:171], v[164:167]\n"
"ds_read_b64_tr_b16 a[72:73], v5 offset:49152\n"
"v_mfma_f32_16x16x32_bf16 v[164:167], a[92:95], a[172:175], v[164:167]\n"
"ds_read_b64_tr_b16 a[74:75], v5 offset:50176\n"
"s_waitcnt lgkmcnt(10)\n"
"v_mfma_f32_16x16x32_bf16 v[216:219], a[48:51], a[16:19], v[216:219]\n"
"ds_read_b64_tr_b16 a[76:77], v5 offset:53248\n"
"v_mfma_f32_16x16x32_bf16 v[216:219], a[52:55], a[20:23], v[216:219]\n"
"buffer_load_dwordx4 v[64:67], v14, s[16:19], s46 offen sc1\n"
"v_mfma_f32_16x16x32_bf16 v[248:251], a[48:51], a[24:27], v[248:251]\n"
"ds_read_b64_tr_b16 a[78:79], v5 offset:54272\n"
"v_mfma_f32_16x16x32_bf16 v[248:251], a[52:55], a[28:31], v[248:251]\n"
"v_mfma_f32_16x16x32_bf16 v[152:155], a[48:51], a[160:163], v[152:155]\n"
"v_mfma_f32_16x16x32_bf16 v[152:155], a[52:55], a[164:167], v[152:155]\n"
"v_mfma_f32_16x16x32_bf16 v[184:187], a[48:51], a[168:171], v[184:187]\n"
"buffer_load_dwordx4 v[68:71], v15, s[16:19], s46 offen sc1\n"
"v_mfma_f32_16x16x32_bf16 v[184:187], a[52:55], a[172:175], v[184:187]\n"
"s_add_u32 s46, s46, 32768\n"
"s_waitcnt lgkmcnt(8)\n"
"v_mfma_f32_16x16x32_bf16 v[200:203], a[56:59], a[16:19], v[200:203]\n"
"v_mfma_f32_16x16x32_bf16 v[200:203], a[60:63], a[20:23], v[200:203]\n"
"v_mfma_f32_16x16x32_bf16 v[232:235], a[56:59], a[24:27], v[232:235]\n"
"s_add_u32 m0, s57, 49152\n"
"v_mfma_f32_16x16x32_bf16 v[232:235], a[60:63], a[28:31], v[232:235]\n"
"v_mfma_f32_16x16x32_bf16 v[136:139], a[56:59], a[160:163], v[136:139]\n"
"buffer_load_dwordx4 v10, s[20:23], s70 offen offset:0 lds\n"
"v_mfma_f32_16x16x32_bf16 v[136:139], a[60:63], a[164:167], v[136:139]\n"
"v_mfma_f32_16x16x32_bf16 v[168:171], a[56:59], a[168:171], v[168:171]\n"
"v_mfma_f32_16x16x32_bf16 v[168:171], a[60:63], a[172:175], v[168:171]\n"
"s_waitcnt vmcnt(23) lgkmcnt(0)\n"
"s_barrier\n"
"v_mfma_f32_16x16x32_bf16 v[220:223], a[64:67], a[16:19], v[220:223]\n"
"ds_read_b64_tr_b16 a[80:81], v2 offset:16384\n"
"v_mfma_f32_16x16x32_bf16 v[220:223], a[68:71], a[20:23], v[220:223]\n"
"buffer_load_dwordx4 v11, s[20:23], s70 offen offset:1024 lds\n"
"v_mfma_f32_16x16x32_bf16 v[252:255], a[64:67], a[24:27], v[252:255]\n"
"s_add_u32 s70, s70, 131072\n"
"ds_read_b64_tr_b16 a[82:83], v2 offset:17408\n"
"v_mfma_f32_16x16x32_bf16 v[252:255], a[68:71], a[28:31], v[252:255]\n"
"ds_read_b64_tr_b16 a[84:85], v2 offset:20480\n"
"v_mfma_f32_16x16x32_bf16 v[156:159], a[64:67], a[160:163], v[156:159]\n"
"s_add_u32 m0, s57, 106496\n"
"ds_read_b64_tr_b16 a[86:87], v2 offset:21504\n"
"v_mfma_f32_16x16x32_bf16 v[156:159], a[68:71], a[164:167], v[156:159]\n"
"ds_read_b64_tr_b16 a[88:89], v6 offset:57344\n"
"v_mfma_f32_16x16x32_bf16 v[188:191], a[64:67], a[168:171], v[188:191]\n"
"buffer_load_dwordx4 v12, s[20:23], s71 offen offset:0 lds\n"
"v_mfma_f32_16x16x32_bf16 v[188:191], a[68:71], a[172:175], v[188:191]\n"
"ds_read_b64_tr_b16 a[90:91], v6 offset:58368\n"
"v_mfma_f32_16x16x32_bf16 v[204:207], a[72:75], a[16:19], v[204:207]\n"
"ds_read_b64_tr_b16 a[92:93], v6 offset:61440\n"
"v_mfma_f32_16x16x32_bf16 v[204:207], a[76:79], a[20:23], v[204:207]\n"
"ds_read_b64_tr_b16 a[94:95], v6 offset:62464\n"
"v_mfma_f32_16x16x32_bf16 v[236:239], a[72:75], a[24:27], v[236:239]\n"
"ds_read_b64_tr_b16 a[48:49], v3 offset:16384\n"
"v_mfma_f32_16x16x32_bf16 v[236:239], a[76:79], a[28:31], v[236:239]\n"
"buffer_load_dwordx4 v13, s[20:23], s71 offen offset:1024 lds\n"
"v_mfma_f32_16x16x32_bf16 v[140:143], a[72:75], a[160:163], v[140:143]\n"
"s_add_u32 s71, s71, 131072\n"
"ds_read_b64_tr_b16 a[50:51], v3 offset:17408\n"
"v_mfma_f32_16x16x32_bf16 v[140:143], a[76:79], a[164:167], v[140:143]\n"
"ds_read_b64_tr_b16 a[52:53], v3 offset:20480\n"
"v_mfma_f32_16x16x32_bf16 v[172:175], a[72:75], a[168:171], v[172:175]\n"
"buffer_load_dwordx4 v[80:83], v14, s[16:19], s46 offen sc1\n"
"v_mfma_f32_16x16x32_bf16 v[172:175], a[76:79], a[172:175], v[172:175]\n"
"ds_read_b64_tr_b16 a[54:55], v3 offset:21504\n"
"s_waitcnt vmcnt(23) lgkmcnt(8)\n"
"v_mfma_f32_16x16x32_bf16 v[208:211], a[80:83], a[32:35], v[208:211]\n"
"ds_read_b64_tr_b16 a[56:57], v7 offset:57344\n"
"v_mfma_f32_16x16x32_bf16 v[208:211], a[84:87], a[36:39], v[208:211]\n"
"ds_read_b64_tr_b16 a[58:59], v7 offset:58368\n"
"s_waitcnt vmcnt(21)\n"
"v_mfma_f32_16x16x32_bf16 v[240:243], a[80:83], a[40:43], v[240:243]\n"
"ds_read_b64_tr_b16 a[60:61], v7 offset:61440\n"
"v_mfma_f32_16x16x32_bf16 v[240:243], a[84:87], a[44:47], v[240:243]\n"
"buffer_load_dwordx4 v[84:87], v15, s[16:19], s46 offen sc1\n"
"v_mfma_f32_16x16x32_bf16 v[144:147], a[80:83], a[176:179], v[144:147]\n"
"s_add_u32 s46, s46, 32768\n"
"ds_read_b64_tr_b16 a[62:63], v7 offset:62464\n"
"v_mfma_f32_16x16x32_bf16 v[144:147], a[84:87], a[180:183], v[144:147]\n"
"v_mfma_f32_16x16x32_bf16 v[176:179], a[80:83], a[184:187], v[176:179]\n"
"ds_read_b64_tr_b16 a[64:65], v4 offset:16384\n"
"v_mfma_f32_16x16x32_bf16 v[176:179], a[84:87], a[188:191], v[176:179]\n"
"ds_read_b64_tr_b16 a[66:67], v4 offset:17408\n"
"s_waitcnt lgkmcnt(10)\n"
"v_mfma_f32_16x16x32_bf16 v[192:195], a[88:91], a[32:35], v[192:195]\n"
"buffer_load_dwordx4 a[16:19], v18, s[16:19], s47 offen sc1\n"
"v_mfma_f32_16x16x32_bf16 v[192:195], a[92:95], a[36:39], v[192:195]\n"
"ds_read_b64_tr_b16 a[68:69], v4 offset:20480\n"
"v_mfma_f32_16x16x32_bf16 v[224:227], a[88:91], a[40:43], v[224:227]\n"
"ds_read_b64_tr_b16 a[70:71], v4 offset:21504\n"
"v_mfma_f32_16x16x32_bf16 v[224:227], a[92:95], a[44:47], v[224:227]\n"
"v_mfma_f32_16x16x32_bf16 v[128:131], a[88:91], a[176:179], v[128:131]\n"
"v_mfma_f32_16x16x32_bf16 v[128:131], a[92:95], a[180:183], v[128:131]\n"
"buffer_load_dwordx4 a[20:23], v19, s[16:19], s47 offen sc1\n"
"v_mfma_f32_16x16x32_bf16 v[160:163], a[88:91], a[184:187], v[160:163]\n"
"ds_read_b64_tr_b16 a[72:73], v8 offset:57344\n"
"v_mfma_f32_16x16x32_bf16 v[160:163], a[92:95], a[188:191], v[160:163]\n"
"ds_read_b64_tr_b16 a[74:75], v8 offset:58368\n"
"s_waitcnt lgkmcnt(10)\n"
"v_mfma_f32_16x16x32_bf16 v[212:215], a[48:51], a[32:35], v[212:215]\n"
"ds_read_b64_tr_b16 a[76:77], v8 offset:61440\n"
"v_mfma_f32_16x16x32_bf16 v[212:215], a[52:55], a[36:39], v[212:215]\n"
"ds_read_b64_tr_b16 a[78:79], v8 offset:62464\n"
"v_mfma_f32_16x16x32_bf16 v[244:247], a[48:51], a[40:43], v[244:247]\n"
"buffer_load_dwordx4 a[24:27], v20, s[16:19], s47 offen sc1\n"
"v_mfma_f32_16x16x32_bf16 v[244:247], a[52:55], a[44:47], v[244:247]\n"
"v_mfma_f32_16x16x32_bf16 v[148:151], a[48:51], a[176:179], v[148:151]\n"
"v_mfma_f32_16x16x32_bf16 v[148:151], a[52:55], a[180:183], v[148:151]\n"
"v_mfma_f32_16x16x32_bf16 v[180:183], a[48:51], a[184:187], v[180:183]\n"
"ds_read_b64_tr_b16 a[80:81], v5 offset:16384\n"
"v_mfma_f32_16x16x32_bf16 v[180:183], a[52:55], a[188:191], v[180:183]\n"
"ds_read_b64_tr_b16 a[82:83], v5 offset:17408\n"
"s_waitcnt lgkmcnt(10)\n"
"v_mfma_f32_16x16x32_bf16 v[196:199], a[56:59], a[32:35], v[196:199]\n"
"buffer_load_dwordx4 a[28:31], v21, s[16:19], s47 offen sc1\n"
"v_mfma_f32_16x16x32_bf16 v[196:199], a[60:63], a[36:39], v[196:199]\n"
"s_add_u32 s47, s47, 32768\n"
"ds_read_b64_tr_b16 a[84:85], v5 offset:20480\n"
"v_mfma_f32_16x16x32_bf16 v[228:231], a[56:59], a[40:43], v[228:231]\n"
"ds_read_b64_tr_b16 a[86:87], v5 offset:21504\n"
"v_mfma_f32_16x16x32_bf16 v[228:231], a[60:63], a[44:47], v[228:231]\n"
"v_mfma_f32_16x16x32_bf16 v[132:135], a[56:59], a[176:179], v[132:135]\n"
"buffer_load_dwordx4 v[72:75], v16, s[16:19], s54 offen sc1\n"
"v_mfma_f32_16x16x32_bf16 v[132:135], a[60:63], a[180:183], v[132:135]\n"
"v_mfma_f32_16x16x32_bf16 v[164:167], a[56:59], a[184:187], v[164:167]\n"
"ds_read_b64_tr_b16 a[88:89], v9 offset:57344\n"
"v_mfma_f32_16x16x32_bf16 v[164:167], a[60:63], a[188:191], v[164:167]\n"
"ds_read_b64_tr_b16 a[90:91], v9 offset:58368\n"
"s_waitcnt lgkmcnt(10)\n"
"v_mfma_f32_16x16x32_bf16 v[216:219], a[64:67], a[32:35], v[216:219]\n"
"ds_read_b64_tr_b16 a[92:93], v9 offset:61440\n"
"v_mfma_f32_16x16x32_bf16 v[216:219], a[68:71], a[36:39], v[216:219]\n"
"buffer_load_dwordx4 v[76:79], v17, s[16:19], s54 offen sc1\n"
"v_mfma_f32_16x16x32_bf16 v[248:251], a[64:67], a[40:43], v[248:251]\n"
"s_add_u32 s54, s54, 32768\n"
"ds_read_b64_tr_b16 a[94:95], v9 offset:62464\n"
"v_mfma_f32_16x16x32_bf16 v[248:251], a[68:71], a[44:47], v[248:251]\n"
"v_mfma_f32_16x16x32_bf16 v[152:155], a[64:67], a[176:179], v[152:155]\n"
"v_mfma_f32_16x16x32_bf16 v[152:155], a[68:71], a[180:183], v[152:155]\n"
"s_add_u32 m0, s57, 40960\n"
"v_mfma_f32_16x16x32_bf16 v[184:187], a[64:67], a[184:187], v[184:187]\n"
"v_mfma_f32_16x16x32_bf16 v[184:187], a[68:71], a[188:191], v[184:187]\n"
"s_waitcnt lgkmcnt(8)\n"
"v_mfma_f32_16x16x32_bf16 v[200:203], a[72:75], a[32:35], v[200:203]\n"
"buffer_load_dwordx4 v10, s[20:23], s70 offen offset:0 lds\n"
"v_mfma_f32_16x16x32_bf16 v[200:203], a[76:79], a[36:39], v[200:203]\n"
"v_mfma_f32_16x16x32_bf16 v[232:235], a[72:75], a[40:43], v[232:235]\n"
"v_mfma_f32_16x16x32_bf16 v[232:235], a[76:79], a[44:47], v[232:235]\n"
"v_mfma_f32_16x16x32_bf16 v[136:139], a[72:75], a[176:179], v[136:139]\n"
"buffer_load_dwordx4 v11, s[20:23], s70 offen offset:1024 lds\n"
"v_mfma_f32_16x16x32_bf16 v[136:139], a[76:79], a[180:183], v[136:139]\n"
"s_add_u32 s70, s70, 131072\n"
"v_mfma_f32_16x16x32_bf16 v[168:171], a[72:75], a[184:187], v[168:171]\n"
"v_mfma_f32_16x16x32_bf16 v[168:171], a[76:79], a[188:191], v[168:171]\n"
"s_waitcnt vmcnt(22) lgkmcnt(0)\n"
"s_barrier\n"
"v_mfma_f32_16x16x32_bf16 v[220:223], a[80:83], a[32:35], v[220:223]\n"
"s_add_u32 m0, s57, 114688\n"
"s_add_u32 s53, s71, s30\n"
"v_mfma_f32_16x16x32_bf16 v[220:223], a[84:87], a[36:39], v[220:223]\n"
"buffer_load_dwordx4 v12, s[20:23], s53 offen offset:0 lds\n"
"v_mfma_f32_16x16x32_bf16 v[252:255], a[80:83], a[40:43], v[252:255]\n"
"s_add_u32 s53, s71, s31\n"
"ds_read_b64_tr_b16 a[48:49], v2 offset:0\n"
"v_mfma_f32_16x16x32_bf16 v[252:255], a[84:87], a[44:47], v[252:255]\n"
"ds_read_b64_tr_b16 a[50:51], v2 offset:1024\n"
"v_mfma_f32_16x16x32_bf16 v[156:159], a[80:83], a[176:179], v[156:159]\n"
"ds_read_b64_tr_b16 a[52:53], v2 offset:4096\n"
"v_mfma_f32_16x16x32_bf16 v[156:159], a[84:87], a[180:183], v[156:159]\n"
"ds_read_b64_tr_b16 a[54:55], v2 offset:5120\n"
"v_mfma_f32_16x16x32_bf16 v[188:191], a[80:83], a[184:187], v[188:191]\n"
"buffer_load_dwordx4 v13, s[20:23], s53 offen offset:1024 lds\n"
"v_mfma_f32_16x16x32_bf16 v[188:191], a[84:87], a[188:191], v[188:191]\n"
"s_add_u32 s71, s71, 131072\n"
"v_mfma_f32_16x16x32_bf16 v[204:207], a[88:91], a[32:35], v[204:207]\n"
"ds_read_b64_tr_b16 a[56:57], v6 offset:49152\n"
"v_mfma_f32_16x16x32_bf16 v[204:207], a[92:95], a[36:39], v[204:207]\n"
"ds_read_b64_tr_b16 a[58:59], v6 offset:50176\n"
"v_mfma_f32_16x16x32_bf16 v[236:239], a[88:91], a[40:43], v[236:239]\n"
"buffer_load_dwordx4 v[88:91], v16, s[16:19], s54 offen sc1\n"
"v_mfma_f32_16x16x32_bf16 v[236:239], a[92:95], a[44:47], v[236:239]\n"
"ds_read_b64_tr_b16 a[60:61], v6 offset:53248\n"
"v_mfma_f32_16x16x32_bf16 v[140:143], a[88:91], a[176:179], v[140:143]\n"
"ds_read_b64_tr_b16 a[62:63], v6 offset:54272\n"
"v_mfma_f32_16x16x32_bf16 v[140:143], a[92:95], a[180:183], v[140:143]\n"
"ds_read_b64_tr_b16 a[64:65], v3 offset:0\n"
"v_mfma_f32_16x16x32_bf16 v[172:175], a[88:91], a[184:187], v[172:175]\n"
"ds_read_b64_tr_b16 a[66:67], v3 offset:1024\n"
"v_mfma_f32_16x16x32_bf16 v[172:175], a[92:95], a[188:191], v[172:175]\n"
"ds_read_b64_tr_b16 a[68:69], v3 offset:4096\n"
"s_waitcnt vmcnt(23) lgkmcnt(7)\n"
"v_mfma_f32_16x16x32_bf16 v[208:211], a[48:51], a[0:3], v[208:211]\n"
"buffer_load_dwordx4 v[92:95], v17, s[16:19], s54 offen sc1\n"
"v_mfma_f32_16x16x32_bf16 v[208:211], a[52:55], a[4:7], v[208:211]\n"
"s_add_u32 s54, s54, 32768\n"
"ds_read_b64_tr_b16 a[70:71], v3 offset:5120\n"
"s_waitcnt vmcnt(22)\n"
"v_mfma_f32_16x16x32_bf16 v[240:243], a[48:51], a[8:11], v[240:243]\n"
"ds_read_b64_tr_b16 a[72:73], v7 offset:49152\n"
"v_mfma_f32_16x16x32_bf16 v[240:243], a[52:55], a[12:15], v[240:243]\n"
"ds_read_b64_tr_b16 a[74:75], v7 offset:50176\n"
"v_mfma_f32_16x16x32_bf16 v[144:147], a[48:51], a[192:195], v[144:147]\n"
"buffer_load_dwordx4 a[32:35], v18, s[16:19], s47 offen sc1\n"
"v_mfma_f32_16x16x32_bf16 v[144:147], a[52:55], a[196:199], v[144:147]\n"
"ds_read_b64_tr_b16 a[76:77], v7 offset:53248\n"
"v_mfma_f32_16x16x32_bf16 v[176:179], a[48:51], a[200:203], v[176:179]\n"
"ds_read_b64_tr_b16 a[78:79], v7 offset:54272\n"
"v_mfma_f32_16x16x32_bf16 v[176:179], a[52:55], a[204:207], v[176:179]\n"
"ds_read_b64_tr_b16 a[80:81], v4 offset:0\n"
"s_waitcnt lgkmcnt(9)\n"
"v_mfma_f32_16x16x32_bf16 v[192:195], a[56:59], a[0:3], v[192:195]\n"
"ds_read_b64_tr_b16 a[82:83], v4 offset:1024\n"
"v_mfma_f32_16x16x32_bf16 v[192:195], a[60:63], a[4:7], v[192:195]\n"
"buffer_load_dwordx4 a[36:39], v19, s[16:19], s47 offen sc1\n"
"v_mfma_f32_16x16x32_bf16 v[224:227], a[56:59], a[8:11], v[224:227]\n"
"ds_read_b64_tr_b16 a[84:85], v4 offset:4096\n"
"v_mfma_f32_16x16x32_bf16 v[224:227], a[60:63], a[12:15], v[224:227]\n"
"ds_read_b64_tr_b16 a[86:87], v4 offset:5120\n"
"s_waitcnt lgkmcnt(8)\n"
"v_mfma_f32_16x16x32_bf16 v[212:215], a[64:67], a[0:3], v[212:215]\n"
"ds_read_b64_tr_b16 a[88:89], v8 offset:49152\n"
"v_mfma_f32_16x16x32_bf16 v[212:215], a[68:71], a[4:7], v[212:215]\n"
"ds_read_b64_tr_b16 a[90:91], v8 offset:50176\n"
"v_mfma_f32_16x16x32_bf16 v[244:247], a[64:67], a[8:11], v[244:247]\n"
"buffer_load_dwordx4 a[40:43], v20, s[16:19], s47 offen sc1\n"
"v_mfma_f32_16x16x32_bf16 v[244:247], a[68:71], a[12:15], v[244:247]\n"
"ds_read_b64_tr_b16 a[92:93], v8 offset:53248\n"
"v_mfma_f32_16x16x32_bf16 v[148:151], a[64:67], a[192:195], v[148:151]\n"
"ds_read_b64_tr_b16 a[94:95], v8 offset:54272\n"
"v_mfma_f32_16x16x32_bf16 v[148:151], a[68:71], a[196:199], v[148:151]\n"
"ds_read_b64_tr_b16 a[48:49], v5 offset:0\n"
"v_mfma_f32_16x16x32_bf16 v[180:183], a[64:67], a[200:203], v[180:183]\n"
"ds_read_b64_tr_b16 a[50:51], v5 offset:1024\n"
"v_mfma_f32_16x16x32_bf16 v[180:183], a[68:71], a[204:207], v[180:183]\n"
"ds_read_b64_tr_b16 a[52:53], v5 offset:4096\n"
"s_waitcnt lgkmcnt(11)\n"
"v_mfma_f32_16x16x32_bf16 v[196:199], a[72:75], a[0:3], v[196:199]\n"
"buffer_load_dwordx4 a[44:47], v21, s[16:19], s47 offen sc1\n"
"v_mfma_f32_16x16x32_bf16 v[196:199], a[76:79], a[4:7], v[196:199]\n"
"s_add_u32 s47, s47, 32768\n"
"ds_read_b64_tr_b16 a[54:55], v5 offset:5120\n"
"v_mfma_f32_16x16x32_bf16 v[228:231], a[72:75], a[8:11], v[228:231]\n"
"ds_read_b64_tr_b16 a[56:57], v9 offset:49152\n"
"v_mfma_f32_16x16x32_bf16 v[228:231], a[76:79], a[12:15], v[228:231]\n"
"ds_read_b64_tr_b16 a[58:59], v9 offset:50176\n"
"s_waitcnt lgkmcnt(10)\n"
"v_mfma_f32_16x16x32_bf16 v[216:219], a[80:83], a[0:3], v[216:219]\n"
"buffer_load_dwordx4 a[96:99], v14, s[16:19], s46 offen sc1\n"
"v_mfma_f32_16x16x32_bf16 v[216:219], a[84:87], a[4:7], v[216:219]\n"
"ds_read_b64_tr_b16 a[60:61], v9 offset:53248\n"
"v_mfma_f32_16x16x32_bf16 v[248:251], a[80:83], a[8:11], v[248:251]\n"
"ds_read_b64_tr_b16 a[62:63], v9 offset:54272\n"
"v_mfma_f32_16x16x32_bf16 v[248:251], a[84:87], a[12:15], v[248:251]\n"
"v_mfma_f32_16x16x32_bf16 v[152:155], a[80:83], a[192:195], v[152:155]\n"
"v_mfma_f32_16x16x32_bf16 v[152:155], a[84:87], a[196:199], v[152:155]\n"
"buffer_load_dwordx4 a[100:103], v15, s[16:19], s46 offen sc1\n"
"v_mfma_f32_16x16x32_bf16 v[184:187], a[80:83], a[200:203], v[184:187]\n"
"s_add_u32 s46, s46, 32768\n"
"v_mfma_f32_16x16x32_bf16 v[184:187], a[84:87], a[204:207], v[184:187]\n"
"s_waitcnt vmcnt(20) lgkmcnt(0)\n"
"s_barrier\n"
"v_mfma_f32_16x16x32_bf16 v[200:203], a[88:91], a[0:3], v[200:203]\n"
"ds_read_b64_tr_b16 a[64:65], v2 offset:8192\n"
"v_mfma_f32_16x16x32_bf16 v[200:203], a[92:95], a[4:7], v[200:203]\n"
"s_add_u32 m0, s57, 98304\n"
"ds_read_b64_tr_b16 a[66:67], v2 offset:9216\n"
"v_mfma_f32_16x16x32_bf16 v[232:235], a[88:91], a[8:11], v[232:235]\n"
"ds_read_b64_tr_b16 a[68:69], v2 offset:12288\n"
"v_mfma_f32_16x16x32_bf16 v[232:235], a[92:95], a[12:15], v[232:235]\n"
"ds_read_b64_tr_b16 a[70:71], v2 offset:13312\n"
"v_mfma_f32_16x16x32_bf16 v[220:223], a[48:51], a[0:3], v[220:223]\n"
"buffer_load_dwordx4 v12, s[20:23], s71 offen offset:0 lds\n"
"v_mfma_f32_16x16x32_bf16 v[220:223], a[52:55], a[4:7], v[220:223]\n"
"ds_read_b64_tr_b16 a[72:73], v6 offset:40960\n"
"v_mfma_f32_16x16x32_bf16 v[252:255], a[48:51], a[8:11], v[252:255]\n"
"ds_read_b64_tr_b16 a[74:75], v6 offset:41984\n"
"v_mfma_f32_16x16x32_bf16 v[252:255], a[52:55], a[12:15], v[252:255]\n"
"ds_read_b64_tr_b16 a[76:77], v6 offset:45056\n"
"v_mfma_f32_16x16x32_bf16 v[156:159], a[48:51], a[192:195], v[156:159]\n"
"buffer_load_dwordx4 v13, s[20:23], s71 offen offset:1024 lds\n"
"v_mfma_f32_16x16x32_bf16 v[156:159], a[52:55], a[196:199], v[156:159]\n"
"s_add_u32 s71, s71, 131072\n"
"ds_read_b64_tr_b16 a[78:79], v6 offset:46080\n"
"v_mfma_f32_16x16x32_bf16 v[188:191], a[48:51], a[200:203], v[188:191]\n"
"s_add_u32 m0, s57, 155648\n"
"s_add_u32 s53, s70, s28\n"
"v_mfma_f32_16x16x32_bf16 v[188:191], a[52:55], a[204:207], v[188:191]\n"
"ds_read_b64_tr_b16 a[80:81], v3 offset:8192\n"
"v_mfma_f32_16x16x32_bf16 v[204:207], a[56:59], a[0:3], v[204:207]\n"
"buffer_load_dwordx4 v10, s[20:23], s53 offen offset:0 lds\n"
"v_mfma_f32_16x16x32_bf16 v[204:207], a[60:63], a[4:7], v[204:207]\n"
"s_add_u32 s53, s70, s29\n"
"ds_read_b64_tr_b16 a[82:83], v3 offset:9216\n"
"v_mfma_f32_16x16x32_bf16 v[236:239], a[56:59], a[8:11], v[236:239]\n"
"ds_read_b64_tr_b16 a[84:85], v3 offset:12288\n"
"v_mfma_f32_16x16x32_bf16 v[236:239], a[60:63], a[12:15], v[236:239]\n"
"ds_read_b64_tr_b16 a[86:87], v3 offset:13312\n"
"s_waitcnt vmcnt(19) lgkmcnt(8)\n"
"v_mfma_f32_16x16x32_bf16 v[208:211], a[64:67], a[16:19], v[208:211]\n"
"ds_read_b64_tr_b16 a[88:89], v7 offset:40960\n"
"v_mfma_f32_16x16x32_bf16 v[208:211], a[68:71], a[20:23], v[208:211]\n"
"buffer_load_dwordx4 v11, s[20:23], s53 offen offset:1024 lds\n"
"s_waitcnt vmcnt(18)\n"
"v_mfma_f32_16x16x32_bf16 v[240:243], a[64:67], a[24:27], v[240:243]\n"
"s_add_u32 s70, s70, 131072\n"
"ds_read_b64_tr_b16 a[90:91], v7 offset:41984\n"
"v_mfma_f32_16x16x32_bf16 v[240:243], a[68:71], a[28:31], v[240:243]\n"
"ds_read_b64_tr_b16 a[92:93], v7 offset:45056\n"
"v_mfma_f32_16x16x32_bf16 v[144:147], a[64:67], a[208:211], v[144:147]\n"
"ds_read_b64_tr_b16 a[94:95], v7 offset:46080\n"
"v_mfma_f32_16x16x32_bf16 v[144:147], a[68:71], a[212:215], v[144:147]\n"
"buffer_load_dwordx4 a[0:3], v18, s[16:19], s47 offen sc1\n"
"v_mfma_f32_16x16x32_bf16 v[176:179], a[64:67], a[216:219], v[176:179]\n"
"ds_read_b64_tr_b16 a[48:49], v4 offset:8192\n"
"v_mfma_f32_16x16x32_bf16 v[176:179], a[68:71], a[220:223], v[176:179]\n"
"ds_read_b64_tr_b16 a[50:51], v4 offset:9216\n"
"s_waitcnt lgkmcnt(10)\n"
"v_mfma_f32_16x16x32_bf16 v[192:195], a[72:75], a[16:19], v[192:195]\n"
"ds_read_b64_tr_b16 a[52:53], v4 offset:12288\n"
"v_mfma_f32_16x16x32_bf16 v[192:195], a[76:79], a[20:23], v[192:195]\n"
"ds_read_b64_tr_b16 a[54:55], v4 offset:13312\n"
"v_mfma_f32_16x16x32_bf16 v[224:227], a[72:75], a[24:27], v[224:227]\n"
"buffer_load_dwordx4 a[4:7], v19, s[16:19], s47 offen sc1\n"
"v_mfma_f32_16x16x32_bf16 v[224:227], a[76:79], a[28:31], v[224:227]\n"
"ds_read_b64_tr_b16 a[56:57], v8 offset:40960\n"
"s_waitcnt lgkmcnt(9)\n"
"v_mfma_f32_16x16x32_bf16 v[212:215], a[80:83], a[16:19], v[212:215]\n"
"ds_read_b64_tr_b16 a[58:59], v8 offset:41984\n"
"v_mfma_f32_16x16x32_bf16 v[212:215], a[84:87], a[20:23], v[212:215]\n"
"ds_read_b64_tr_b16 a[60:61], v8 offset:45056\n"
"v_mfma_f32_16x16x32_bf16 v[244:247], a[80:83], a[24:27], v[244:247]\n"
"ds_read_b64_tr_b16 a[62:63], v8 offset:46080\n"
"v_mfma_f32_16x16x32_bf16 v[244:247], a[84:87], a[28:31], v[244:247]\n"
"buffer_load_dwordx4 a[8:11], v20, s[16:19], s47 offen sc1\n"
"v_mfma_f32_16x16x32_bf16 v[148:151], a[80:83], a[208:211], v[148:151]\n"
"ds_read_b64_tr_b16 a[64:65], v5 offset:8192\n"
"v_mfma_f32_16x16x32_bf16 v[148:151], a[84:87], a[212:215], v[148:151]\n"
"ds_read_b64_tr_b16 a[66:67], v5 offset:9216\n"
"v_mfma_f32_16x16x32_bf16 v[180:183], a[80:83], a[216:219], v[180:183]\n"
"ds_read_b64_tr_b16 a[68:69], v5 offset:12288\n"
"v_mfma_f32_16x16x32_bf16 v[180:183], a[84:87], a[220:223], v[180:183]\n"
"ds_read_b64_tr_b16 a[70:71], v5 offset:13312\n"
".Lpad12:\n"
"s_cbranch_execz .Lwr1\n"
"s_waitcnt lgkmcnt(12)\n"
"v_mfma_f32_16x16x32_bf16 v[196:199], a[88:91], a[16:19], v[196:199]\n"
"buffer_load_dwordx4 a[12:15], v21, s[16:19], s47 offen sc1\n"
"v_mfma_f32_16x16x32_bf16 v[196:199], a[92:95], a[20:23], v[196:199]\n"
"s_add_u32 s47, s47, 32768\n"
"ds_read_b64_tr_b16 a[72:73], v9 offset:40960\n"
"v_mfma_f32_16x16x32_bf16 v[228:231], a[88:91], a[24:27], v[228:231]\n"
"ds_read_b64_tr_b16 a[74:75], v9 offset:41984\n"
"v_mfma_f32_16x16x32_bf16 v[228:231], a[92:95], a[28:31], v[228:231]\n"
"ds_read_b64_tr_b16 a[76:77], v9 offset:45056\n"
"s_waitcnt lgkmcnt(11)\n"
"v_mfma_f32_16x16x32_bf16 v[216:219], a[48:51], a[16:19], v[216:219]\n"
"ds_read_b64_tr_b16 a[78:79], v9 offset:46080\n"
"v_mfma_f32_16x16x32_bf16 v[216:219], a[52:55], a[20:23], v[216:219]\n"
"buffer_load_dwordx4 a[104:107], v16, s[16:19], s54 offen sc1\n"
"v_mfma_f32_16x16x32_bf16 v[248:251], a[48:51], a[24:27], v[248:251]\n"
"buffer_load_dword v35, v34, s[16:19], 0 offen sc1\n"
"v_lshlrev_b32 v36, 7, v0\n"
"v_mfma_f32_16x16x32_bf16 v[248:251], a[52:55], a[28:31], v[248:251]\n"
"s_mov_b64 exec, 31\n"
"buffer_load_dword v44, v36, s[0:3], s97 offen\n"
"s_mov_b64 exec, -1\n"
"v_mfma_f32_16x16x32_bf16 v[152:155], a[48:51], a[208:211], v[152:155]\n"
"v_mfma_f32_16x16x32_bf16 v[152:155], a[52:55], a[212:215], v[152:155]\n"
"v_mfma_f32_16x16x32_bf16 v[184:187], a[48:51], a[216:219], v[184:187]\n"
"buffer_load_dwordx4 a[108:111], v17, s[16:19], s54 offen sc1\n"
"v_mfma_f32_16x16x32_bf16 v[184:187], a[52:55], a[220:223], v[184:187]\n"
"s_waitcnt vmcnt(20) lgkmcnt(0)\n"
"s_barrier\n"
"v_mfma_f32_16x16x32_bf16 v[200:203], a[56:59], a[16:19], v[200:203]\n"
"s_add_u32 s54, s54, 32768\n"
"ds_read_b64_tr_b16 a[80:81], v2 offset:16384\n"
"v_mfma_f32_16x16x32_bf16 v[200:203], a[60:63], a[20:23], v[200:203]\n"
"ds_read_b64_tr_b16 a[82:83], v2 offset:17408\n"
"v_mfma_f32_16x16x32_bf16 v[232:235], a[56:59], a[24:27], v[232:235]\n"
"s_add_u32 m0, s57, 106496\n"
"ds_read_b64_tr_b16 a[84:85], v2 offset:20480\n"
"v_mfma_f32_16x16x32_bf16 v[232:235], a[60:63], a[28:31], v[232:235]\n"
"ds_read_b64_tr_b16 a[86:87], v2 offset:21504\n"
"v_mfma_f32_16x16x32_bf16 v[220:223], a[64:67], a[16:19], v[220:223]\n"
"buffer_load_dwordx4 v12, s[20:23], s71 offen offset:0 lds\n"
"v_mfma_f32_16x16x32_bf16 v[220:223], a[68:71], a[20:23], v[220:223]\n"
"ds_read_b64_tr_b16 a[88:89], v6 offset:32768\n"
"v_mfma_f32_16x16x32_bf16 v[252:255], a[64:67], a[24:27], v[252:255]\n"
"ds_read_b64_tr_b16 a[90:91], v6 offset:33792\n"
"v_mfma_f32_16x16x32_bf16 v[252:255], a[68:71], a[28:31], v[252:255]\n"
"ds_read_b64_tr_b16 a[92:93], v6 offset:36864\n"
"v_mfma_f32_16x16x32_bf16 v[156:159], a[64:67], a[208:211], v[156:159]\n"
"ds_read_b64_tr_b16 a[94:95], v6 offset:37888\n"
"v_mfma_f32_16x16x32_bf16 v[156:159], a[68:71], a[212:215], v[156:159]\n"
"buffer_load_dwordx4 v13, s[20:23], s71 offen offset:1024 lds\n"
"v_mfma_f32_16x16x32_bf16 v[188:191], a[64:67], a[216:219], v[188:191]\n"
"s_add_u32 s71, s71, 131072\n"
"ds_read_b64_tr_b16 a[48:49], v3 offset:16384\n"
"v_mfma_f32_16x16x32_bf16 v[188:191], a[68:71], a[220:223], v[188:191]\n"
"ds_read_b64_tr_b16 a[50:51], v3 offset:17408\n"
"v_mfma_f32_16x16x32_bf16 v[204:207], a[72:75], a[16:19], v[204:207]\n"
"ds_read_b64_tr_b16 a[52:53], v3 offset:20480\n"
"v_mfma_f32_16x16x32_bf16 v[204:207], a[76:79], a[20:23], v[204:207]\n"
"buffer_load_dwordx4 a[112:115], v14, s[16:19], s46 offen sc1\n"
"v_mfma_f32_16x16x32_bf16 v[236:239], a[72:75], a[24:27], v[236:239]\n"
"ds_read_b64_tr_b16 a[54:55], v3 offset:21504\n"
"v_mfma_f32_16x16x32_bf16 v[236:239], a[76:79], a[28:31], v[236:239]\n"
"ds_read_b64_tr_b16 a[56:57], v7 offset:32768\n"
"s_waitcnt vmcnt(19) lgkmcnt(9)\n"
"v_mfma_f32_16x16x32_bf16 v[208:211], a[80:83], a[32:35], v[208:211]\n"
"ds_read_b64_tr_b16 a[58:59], v7 offset:33792\n"
"v_mfma_f32_16x16x32_bf16 v[208:211], a[84:87], a[36:39], v[208:211]\n"
"ds_read_b64_tr_b16 a[60:61], v7 offset:36864\n"
"s_waitcnt vmcnt(17)\n"
"v_mfma_f32_16x16x32_bf16 v[240:243], a[80:83], a[40:43], v[240:243]\n"
"buffer_load_dwordx4 a[116:119], v15, s[16:19], s46 offen sc1\n"
"v_mfma_f32_16x16x32_bf16 v[240:243], a[84:87], a[44:47], v[240:243]\n"
"s_add_u32 s46, s46, 32768\n"
"ds_read_b64_tr_b16 a[62:63], v7 offset:37888\n"
"v_mfma_f32_16x16x32_bf16 v[144:147], a[80:83], a[224:227], v[144:147]\n"
"ds_read_b64_tr_b16 a[64:65], v4 offset:16384\n"
"v_mfma_f32_16x16x32_bf16 v[144:147], a[84:87], a[228:231], v[144:147]\n"
"ds_read_b64_tr_b16 a[66:67], v4 offset:17408\n"
"v_mfma_f32_16x16x32_bf16 v[176:179], a[80:83], a[232:235], v[176:179]\n"
"ds_read_b64_tr_b16 a[68:69], v4 offset:20480\n"
"v_mfma_f32_16x16x32_bf16 v[176:179], a[84:87], a[236:239], v[176:179]\n"
"ds_read_b64_tr_b16 a[70:71], v4 offset:21504\n"
"s_waitcnt lgkmcnt(12)\n"
"v_mfma_f32_16x16x32_bf16 v[192:195], a[88:91], a[32:35], v[192:195]\n"
"buffer_load_dwordx4 a[16:19], v18, s[16:19], s47 offen sc1\n"
"v_mfma_f32_16x16x32_bf16 v[192:195], a[92:95], a[36:39], v[192:195]\n"
"ds_read_b64_tr_b16 a[72:73], v8 offset:32768\n"
"v_mfma_f32_16x16x32_bf16 v[224:227], a[88:91], a[40:43], v[224:227]\n"
"ds_read_b64_tr_b16 a[74:75], v8 offset:33792\n"
"v_mfma_f32_16x16x32_bf16 v[224:227], a[92:95], a[44:47], v[224:227]\n"
"ds_read_b64_tr_b16 a[76:77], v8 offset:36864\n"
"s_waitcnt lgkmcnt(11)\n"
"v_mfma_f32_16x16x32_bf16 v[212:215], a[48:51], a[32:35], v[212:215]\n"
"buffer_load_dwordx4 a[20:23], v19, s[16:19], s47 offen sc1\n"
"v_mfma_f32_16x16x32_bf16 v[212:215], a[52:55], a[36:39], v[212:215]\n"
"ds_read_b64_tr_b16 a[78:79], v8 offset:37888\n"
"v_mfma_f32_16x16x32_bf16 v[244:247], a[48:51], a[40:43], v[244:247]\n"
"ds_read_b64_tr_b16 a[80:81], v5 offset:16384\n"
"v_mfma_f32_16x16x32_bf16 v[244:247], a[52:55], a[44:47], v[244:247]\n"
"ds_read_b64_tr_b16 a[82:83], v5 offset:17408\n"
"v_mfma_f32_16x16x32_bf16 v[148:151], a[48:51], a[224:227], v[148:151]\n"
"ds_read_b64_tr_b16 a[84:85], v5 offset:20480\n"
"v_mfma_f32_16x16x32_bf16 v[148:151], a[52:55], a[228:231], v[148:151]\n"
"buffer_load_dwordx4 a[24:27], v20, s[16:19], s47 offen sc1\n"
"v_mfma_f32_16x16x32_bf16 v[180:183], a[48:51], a[232:235], v[180:183]\n"
"ds_read_b64_tr_b16 a[86:87], v5 offset:21504\n"
"v_mfma_f32_16x16x32_bf16 v[180:183], a[52:55], a[236:239], v[180:183]\n"
"ds_read_b64_tr_b16 a[88:89], v9 offset:32768\n"
"s_waitcnt lgkmcnt(13)\n"
"v_mfma_f32_16x16x32_bf16 v[196:199], a[56:59], a[32:35], v[196:199]\n"
"ds_read_b64_tr_b16 a[90:91], v9 offset:33792\n"
"v_mfma_f32_16x16x32_bf16 v[196:199], a[60:63], a[36:39], v[196:199]\n"
"ds_read_b64_tr_b16 a[92:93], v9 offset:36864\n"
"v_mfma_f32_16x16x32_bf16 v[228:231], a[56:59], a[40:43], v[228:231]\n"
"buffer_load_dwordx4 a[28:31], v21, s[16:19], s47 offen sc1\n"
"v_mfma_f32_16x16x32_bf16 v[228:231], a[60:63], a[44:47], v[228:231]\n"
"s_add_u32 s47, s47, 32768\n"
"s_waitcnt lgkmcnt(11)\n"
"v_mfma_f32_16x16x32_bf16 v[216:219], a[64:67], a[32:35], v[216:219]\n"
"ds_read_b64_tr_b16 a[94:95], v9 offset:37888\n"
"v_mfma_f32_16x16x32_bf16 v[216:219], a[68:71], a[36:39], v[216:219]\n"
"v_mfma_f32_16x16x32_bf16 v[248:251], a[64:67], a[40:43], v[248:251]\n"
"s_add_u32 m0, s57, 122880\n"
"v_mfma_f32_16x16x32_bf16 v[248:251], a[68:71], a[44:47], v[248:251]\n"
"v_mfma_f32_16x16x32_bf16 v[152:155], a[64:67], a[224:227], v[152:155]\n"
"buffer_load_dwordx4 v10, s[20:23], s70 offen offset:0 lds\n"
"v_mfma_f32_16x16x32_bf16 v[152:155], a[68:71], a[228:231], v[152:155]\n"
"v_mfma_f32_16x16x32_bf16 v[184:187], a[64:67], a[232:235], v[184:187]\n"
"v_mfma_f32_16x16x32_bf16 v[184:187], a[68:71], a[236:239], v[184:187]\n"
"s_waitcnt vmcnt(19) lgkmcnt(0)\n"
"s_barrier\n"
"v_mfma_f32_16x16x32_bf16 v[200:203], a[72:75], a[32:35], v[200:203]\n"
"ds_read_b64_tr_b16 a[48:49], v2 offset:0\n"
"v_mfma_f32_16x16x32_bf16 v[200:203], a[76:79], a[36:39], v[200:203]\n"
"buffer_load_dwordx4 v11, s[20:23], s70 offen offset:1024 lds\n"
"v_mfma_f32_16x16x32_bf16 v[232:235], a[72:75], a[40:43], v[232:235]\n"
"s_mov_b32 s70, s55\n"
"ds_read_b64_tr_b16 a[50:51], v2 offset:1024\n"
"v_mfma_f32_16x16x32_bf16 v[232:235], a[76:79], a[44:47], v[232:235]\n"
"ds_read_b64_tr_b16 a[52:53], v2 offset:4096\n"
"v_mfma_f32_16x16x32_bf16 v[220:223], a[80:83], a[32:35], v[220:223]\n"
"s_add_u32 m0, s57, 114688\n"
"ds_read_b64_tr_b16 a[54:55], v2 offset:5120\n"
"v_mfma_f32_16x16x32_bf16 v[220:223], a[84:87], a[36:39], v[220:223]\n"
"ds_read_b64_tr_b16 a[56:57], v6 offset:24576\n"
"s_waitcnt vmcnt(12)\n"
"v_mfma_f32_16x16x32_bf16 v[252:255], a[80:83], a[40:43], v[252:255]\n"
"buffer_load_dwordx4 v12, s[20:23], s71 offen offset:0 lds\n"
"v_cmp_ne_u32 vcc, 0, v35\n"
"v_mfma_f32_16x16x32_bf16 v[252:255], a[84:87], a[44:47], v[252:255]\n"
"ds_read_b64_tr_b16 a[58:59], v6 offset:25600\n"
"s_cbranch_vccnz .Lfo_sl\n"
".Lfo_ok:\n"
"v_mfma_f32_16x16x32_bf16 v[156:159], a[80:83], a[224:227], v[156:159]\n"
"ds_read_b64_tr_b16 a[60:61], v6 offset:28672\n"
"v_mfma_f32_16x16x32_bf16 v[156:159], a[84:87], a[228:231], v[156:159]\n"
"ds_read_b64_tr_b16 a[62:63], v6 offset:29696\n"
"v_mfma_f32_16x16x32_bf16 v[188:191], a[80:83], a[232:235], v[188:191]\n"
"ds_read_b64_tr_b16 a[64:65], v3 offset:0\n"
"v_mfma_f32_16x16x32_bf16 v[188:191], a[84:87], a[236:239], v[188:191]\n"
"ds_read_b64_tr_b16 a[66:67], v3 offset:1024\n"
"v_mfma_f32_16x16x32_bf16 v[204:207], a[88:91], a[32:35], v[204:207]\n"
"buffer_load_dwordx4 v13, s[20:23], s71 offen offset:1024 lds\n"
"v_mfma_f32_16x16x32_bf16 v[204:207], a[92:95], a[36:39], v[204:207]\n"
"s_add_u32 s71, s71, 131072\n"
"ds_read_b64_tr_b16 a[68:69], v3 offset:4096\n"
"v_mfma_f32_16x16x32_bf16 v[236:239], a[88:91], a[40:43], v[236:239]\n"
"ds_read_b64_tr_b16 a[70:71], v3 offset:5120\n"
"v_mfma_f32_16x16x32_bf16 v[236:239], a[92:95], a[44:47], v[236:239]\n"
"ds_read_b64_tr_b16 a[72:73], v7 offset:24576\n"
"s_waitcnt lgkmcnt(9)\n"
"v_mfma_f32_16x16x32_bf16 v[208:211], a[48:51], a[0:3], v[208:211]\n"
"buffer_load_dwordx4 a[120:123], v16, s[16:19], s54 offen sc1\n"
"v_mfma_f32_16x16x32_bf16 v[208:211], a[52:55], a[4:7], v[208:211]\n"
"ds_read_b64_tr_b16 a[74:75], v7 offset:25600\n"
"v_mfma_f32_16x16x32_bf16 v[240:243], a[48:51], a[8:11], v[240:243]\n"
"ds_read_b64_tr_b16 a[76:77], v7 offset:28672\n"
"v_mfma_f32_16x16x32_bf16 v[240:243], a[52:55], a[12:15], v[240:243]\n"
"buffer_load_dwordx4 a[124:127], v17, s[16:19], s54 offen sc1\n"
"v_mfma_f32_16x16x32_bf16 v[144:147], a[48:51], v[48:51], v[144:147]\n"
"s_add_u32 s54, s54, 32768\n"
"ds_read_b64_tr_b16 a[78:79], v7 offset:29696\n"
"v_mfma_f32_16x16x32_bf16 v[144:147], a[52:55], v[52:55], v[144:147]\n"
"ds_read_b64_tr_b16 a[80:81], v4 offset:0\n"
"v_mfma_f32_16x16x32_bf16 v[176:179], a[48:51], v[56:59], v[176:179]\n"
"ds_read_b64_tr_b16 a[82:83], v4 offset:1024\n"
"v_mfma_f32_16x16x32_bf16 v[176:179], a[52:55], v[60:63], v[176:179]\n"
"ds_read_b64_tr_b16 a[84:85], v4 offset:4096\n"
"s_waitcnt lgkmcnt(11)\n"
"v_mfma_f32_16x16x32_bf16 v[192:195], a[56:59], a[0:3], v[192:195]\n"
"buffer_load_dwordx4 a[32:35], v18, s[16:19], s47 offen sc1\n"
"v_mfma_f32_16x16x32_bf16 v[192:195], a[60:63], a[4:7], v[192:195]\n"
"ds_read_b64_tr_b16 a[86:87], v4 offset:5120\n"
"v_mfma_f32_16x16x32_bf16 v[224:227], a[56:59], a[8:11], v[224:227]\n"
"ds_read_b64_tr_b16 a[88:89], v8 offset:24576\n"
"v_mfma_f32_16x16x32_bf16 v[224:227], a[60:63], a[12:15], v[224:227]\n"
"ds_read_b64_tr_b16 a[90:91], v8 offset:25600\n"
"s_waitcnt lgkmcnt(10)\n"
"v_mfma_f32_16x16x32_bf16 v[212:215], a[64:67], a[0:3], v[212:215]\n"
"ds_read_b64_tr_b16 a[92:93], v8 offset:28672\n"
"v_mfma_f32_16x16x32_bf16 v[212:215], a[68:71], a[4:7], v[212:215]\n"
"buffer_load_dwordx4 a[36:39], v19, s[16:19], s47 offen sc1\n"
"v_mfma_f32_16x16x32_bf16 v[244:247], a[64:67], a[8:11], v[244:247]\n"
"ds_read_b64_tr_b16 a[94:95], v8 offset:29696\n"
"v_mfma_f32_16x16x32_bf16 v[244:247], a[68:71], a[12:15], v[244:247]\n"
"ds_read_b64_tr_b16 a[48:49], v5 offset:0\n"
"v_mfma_f32_16x16x32_bf16 v[148:151], a[64:67], v[48:51], v[148:151]\n"
"ds_read_b64_tr_b16 a[50:51], v5 offset:1024\n"
"v_mfma_f32_16x16x32_bf16 v[148:151], a[68:71], v[52:55], v[148:151]\n"
"ds_read_b64_tr_b16 a[52:53], v5 offset:4096\n"
"v_mfma_f32_16x16x32_bf16 v[180:183], a[64:67], v[56:59], v[180:183]\n"
"buffer_load_dwordx4 a[40:43], v20, s[16:19], s47 offen sc1\n"
"v_mfma_f32_16x16x32_bf16 v[180:183], a[68:71], v[60:63], v[180:183]\n"
"ds_read_b64_tr_b16 a[54:55], v5 offset:5120\n"
"s_waitcnt lgkmcnt(12)\n"
"v_mfma_f32_16x16x32_bf16 v[196:199], a[72:75], a[0:3], v[196:199]\n"
"ds_read_b64_tr_b16 a[56:57], v9 offset:24576\n"
"v_mfma_f32_16x16x32_bf16 v[196:199], a[76:79], a[4:7], v[196:199]\n"
"ds_read_b64_tr_b16 a[58:59], v9 offset:25600\n"
"v_mfma_f32_16x16x32_bf16 v[228:231], a[72:75], a[8:11], v[228:231]\n"
"ds_read_b64_tr_b16 a[60:61], v9 offset:28672\n"
"v_mfma_f32_16x16x32_bf16 v[228:231], a[76:79], a[12:15], v[228:231]\n"
"ds_read_b64_tr_b16 a[62:63], v9 offset:29696\n"
"s_waitcnt lgkmcnt(12)\n"
"v_mfma_f32_16x16x32_bf16 v[216:219], a[80:83], a[0:3], v[216:219]\n"
"buffer_load_dwordx4 a[44:47], v21, s[16:19], s47 offen sc1\n"
"v_mfma_f32_16x16x32_bf16 v[216:219], a[84:87], a[4:7], v[216:219]\n"
"s_add_u32 s47, s47, 32768\n"
"v_mfma_f32_16x16x32_bf16 v[248:251], a[80:83], a[8:11], v[248:251]\n"
"v_mfma_f32_16x16x32_bf16 v[248:251], a[84:87], a[12:15], v[248:251]\n"
"v_mfma_f32_16x16x32_bf16 v[152:155], a[80:83], v[48:51], v[152:155]\n"
"buffer_load_dwordx4 a[128:131], v14, s[16:19], s46 offen sc1\n"
"v_mfma_f32_16x16x32_bf16 v[152:155], a[84:87], v[52:55], v[152:155]\n"
"v_mfma_f32_16x16x32_bf16 v[184:187], a[80:83], v[56:59], v[184:187]\n"
"v_mfma_f32_16x16x32_bf16 v[184:187], a[84:87], v[60:63], v[184:187]\n"
"s_waitcnt vmcnt(17) lgkmcnt(0)\n"
"s_barrier\n"
"v_mfma_f32_16x16x32_bf16 v[200:203], a[88:91], a[0:3], v[200:203]\n"
"ds_read_b64_tr_b16 a[64:65], v2 offset:8192\n"
"v_mfma_f32_16x16x32_bf16 v[200:203], a[92:95], a[4:7], v[200:203]\n"
"buffer_load_dwordx4 a[132:135], v15, s[16:19], s46 offen sc1\n"
"v_mfma_f32_16x16x32_bf16 v[232:235], a[88:91], a[8:11], v[232:235]\n"
"s_add_u32 s46, s46, 32768\n"
"ds_read_b64_tr_b16 a[66:67], v2 offset:9216\n"
"v_mfma_f32_16x16x32_bf16 v[232:235], a[92:95], a[12:15], v[232:235]\n"
"ds_read_b64_tr_b16 a[68:69], v2 offset:12288\n"
"v_mfma_f32_16x16x32_bf16 v[220:223], a[48:51], a[0:3], v[220:223]\n"
"ds_read_b64_tr_b16 a[70:71], v2 offset:13312\n"
"v_mfma_f32_16x16x32_bf16 v[220:223], a[52:55], a[4:7], v[220:223]\n"
"s_add_u32 m0, s57, 98304\n"
"s_add_u32 s53, s71, s28\n"
"v_mfma_f32_16x16x32_bf16 v[252:255], a[48:51], a[8:11], v[252:255]\n"
"ds_read_b64_tr_b16 a[72:73], v6 offset:16384\n"
"v_mfma_f32_16x16x32_bf16 v[252:255], a[52:55], a[12:15], v[252:255]\n"
"buffer_load_dwordx4 v12, s[20:23], s53 offen offset:0 lds\n"
"v_mfma_f32_16x16x32_bf16 v[156:159], a[48:51], v[48:51], v[156:159]\n"
"s_add_u32 s53, s71, s29\n"
"ds_read_b64_tr_b16 a[74:75], v6 offset:17408\n"
"v_mfma_f32_16x16x32_bf16 v[156:159], a[52:55], v[52:55], v[156:159]\n"
"ds_read_b64_tr_b16 a[76:77], v6 offset:20480\n"
"v_mfma_f32_16x16x32_bf16 v[188:191], a[48:51], v[56:59], v[188:191]\n"
"ds_read_b64_tr_b16 a[78:79], v6 offset:21504\n"
"v_mfma_f32_16x16x32_bf16 v[188:191], a[52:55], v[60:63], v[188:191]\n"
"ds_read_b64_tr_b16 a[80:81], v3 offset:8192\n"
"v_mfma_f32_16x16x32_bf16 v[204:207], a[56:59], a[0:3], v[204:207]\n"
"buffer_load_dwordx4 v13, s[20:23], s53 offen offset:1024 lds\n"
"v_mfma_f32_16x16x32_bf16 v[204:207], a[60:63], a[4:7], v[204:207]\n"
"s_add_u32 s71, s71, 131072\n"
"ds_read_b64_tr_b16 a[82:83], v3 offset:9216\n"
"v_mfma_f32_16x16x32_bf16 v[236:239], a[56:59], a[8:11], v[236:239]\n"
"ds_read_b64_tr_b16 a[84:85], v3 offset:12288\n"
"v_mfma_f32_16x16x32_bf16 v[236:239], a[60:63], a[12:15], v[236:239]\n"
"ds_read_b64_tr_b16 a[86:87], v3 offset:13312\n"
"s_waitcnt vmcnt(16) lgkmcnt(8)\n"
"v_mfma_f32_16x16x32_bf16 v[208:211], a[64:67], a[16:19], v[208:211]\n"
"buffer_load_dwordx4 a[0:3], v18, s[16:19], s47 offen sc1\n"
"v_mfma_f32_16x16x32_bf16 v[208:211], a[68:71], a[20:23], v[208:211]\n"
"ds_read_b64_tr_b16 a[88:89], v7 offset:16384\n"
"s_waitcnt vmcnt(15)\n"
"v_mfma_f32_16x16x32_bf16 v[240:243], a[64:67], a[24:27], v[240:243]\n"
"ds_read_b64_tr_b16 a[90:91], v7 offset:17408\n"
"v_mfma_f32_16x16x32_bf16 v[240:243], a[68:71], a[28:31], v[240:243]\n"
"ds_read_b64_tr_b16 a[92:93], v7 offset:20480\n"
"v_mfma_f32_16x16x32_bf16 v[144:147], a[64:67], v[64:67], v[144:147]\n"
"ds_read_b64_tr_b16 a[94:95], v7 offset:21504\n"
"v_mfma_f32_16x16x32_bf16 v[144:147], a[68:71], v[68:71], v[144:147]\n"
"buffer_load_dwordx4 a[4:7], v19, s[16:19], s47 offen sc1\n"
"v_mfma_f32_16x16x32_bf16 v[176:179], a[64:67], v[72:75], v[176:179]\n"
"ds_read_b64_tr_b16 a[48:49], v4 offset:8192\n"
"v_mfma_f32_16x16x32_bf16 v[176:179], a[68:71], v[76:79], v[176:179]\n"
"ds_read_b64_tr_b16 a[50:51], v4 offset:9216\n"
"s_waitcnt lgkmcnt(10)\n"
"v_mfma_f32_16x16x32_bf16 v[192:195], a[72:75], a[16:19], v[192:195]\n"
"ds_read_b64_tr_b16 a[52:53], v4 offset:12288\n"
"v_mfma_f32_16x16x32_bf16 v[192:195], a[76:79], a[20:23], v[192:195]\n"
"ds_read_b64_tr_b16 a[54:55], v4 offset:13312\n"
"v_mfma_f32_16x16x32_bf16 v[224:227], a[72:75], a[24:27], v[224:227]\n"
"buffer_load_dwordx4 a[8:11], v20, s[16:19], s47 offen sc1\n"
"v_mfma_f32_16x16x32_bf16 v[224:227], a[76:79], a[28:31], v[224:227]\n"
"ds_read_b64_tr_b16 a[56:57], v8 offset:16384\n"
"s_waitcnt lgkmcnt(9)\n"
"v_mfma_f32_16x16x32_bf16 v[212:215], a[80:83], a[16:19], v[212:215]\n"
"ds_read_b64_tr_b16 a[58:59], v8 offset:17408\n"
"v_mfma_f32_16x16x32_bf16 v[212:215], a[84:87], a[20:23], v[212:215]\n"
"ds_read_b64_tr_b16 a[60:61], v8 offset:20480\n"
"v_mfma_f32_16x16x32_bf16 v[244:247], a[80:83], a[24:27], v[244:247]\n"
"ds_read_b64_tr_b16 a[62:63], v8 offset:21504\n"
"v_mfma_f32_16x16x32_bf16 v[244:247], a[84:87], a[28:31], v[244:247]\n"
"buffer_load_dwordx4 a[12:15], v21, s[16:19], s47 offen sc1\n"
"v_mfma_f32_16x16x32_bf16 v[148:151], a[80:83], v[64:67], v[148:151]\n"
"s_add_u32 s47, s47, 32768\n"
"ds_read_b64_tr_b16 a[64:65], v5 offset:8192\n"
"v_mfma_f32_16x16x32_bf16 v[148:151], a[84:87], v[68:71], v[148:151]\n"
"ds_read_b64_tr_b16 a[66:67], v5 offset:9216\n"
"v_mfma_f32_16x16x32_bf16 v[180:183], a[80:83], v[72:75], v[180:183]\n"
"ds_read_b64_tr_b16 a[68:69], v5 offset:12288\n"
"v_mfma_f32_16x16x32_bf16 v[180:183], a[84:87], v[76:79], v[180:183]\n"
"s_add_u32 m0, s57, 131072\n"
"s_waitcnt lgkmcnt(11)\n"
"v_mfma_f32_16x16x32_bf16 v[196:199], a[88:91], a[16:19], v[196:199]\n"
"ds_read_b64_tr_b16 a[70:71], v5 offset:13312\n"
"v_mfma_f32_16x16x32_bf16 v[196:199], a[92:95], a[20:23], v[196:199]\n"
"buffer_load_dwordx4 v10, s[24:27], s70 offen offset:0 lds\n"
"v_mfma_f32_16x16x32_bf16 v[228:231], a[88:91], a[24:27], v[228:231]\n"
"ds_read_b64_tr_b16 a[72:73], v9 offset:16384\n"
"v_mfma_f32_16x16x32_bf16 v[228:231], a[92:95], a[28:31], v[228:231]\n"
"ds_read_b64_tr_b16 a[74:75], v9 offset:17408\n"
"s_waitcnt lgkmcnt(10)\n"
"v_mfma_f32_16x16x32_bf16 v[216:219], a[48:51], a[16:19], v[216:219]\n"
"ds_read_b64_tr_b16 a[76:77], v9 offset:20480\n"
"v_mfma_f32_16x16x32_bf16 v[216:219], a[52:55], a[20:23], v[216:219]\n"
"ds_read_b64_tr_b16 a[78:79], v9 offset:21504\n"
"v_mfma_f32_16x16x32_bf16 v[248:251], a[48:51], a[24:27], v[248:251]\n"
"buffer_load_dwordx4 v11, s[24:27], s70 offen offset:1024 lds\n"
"v_mfma_f32_16x16x32_bf16 v[248:251], a[52:55], a[28:31], v[248:251]\n"
"s_add_u32 s70, s70, 131072\n"
"v_mfma_f32_16x16x32_bf16 v[152:155], a[48:51], v[64:67], v[152:155]\n"
"buffer_load_dwordx4 v[112:115], v26, s[4:7], 0 offen\n"
"v_mfma_f32_16x16x32_bf16 v[152:155], a[52:55], v[68:71], v[152:155]\n"
"v_mfma_f32_16x16x32_bf16 v[184:187], a[48:51], v[72:75], v[184:187]\n"
"buffer_load_dwordx4 a[136:139], v16, s[16:19], s54 offen sc1\n"
"v_mfma_f32_16x16x32_bf16 v[184:187], a[52:55], v[76:79], v[184:187]\n"
"s_waitcnt vmcnt(18) lgkmcnt(0)\n"
"s_barrier\n"
"v_mfma_f32_16x16x32_bf16 v[200:203], a[56:59], a[16:19], v[200:203]\n"
"ds_read_b64_tr_b16 a[80:81], v2 offset:16384\n"
"v_mfma_f32_16x16x32_bf16 v[200:203], a[60:63], a[20:23], v[200:203]\n"
"ds_read_b64_tr_b16 a[82:83], v2 offset:17408\n"
"v_mfma_f32_16x16x32_bf16 v[232:235], a[56:59], a[24:27], v[232:235]\n"
"ds_read_b64_tr_b16 a[84:85], v2 offset:20480\n"
"v_mfma_f32_16x16x32_bf16 v[232:235], a[60:63], a[28:31], v[232:235]\n"
"ds_read_b64_tr_b16 a[86:87], v2 offset:21504\n"
"v_mfma_f32_16x16x32_bf16 v[220:223], a[64:67], a[16:19], v[220:223]\n"
"buffer_load_dwordx4 a[140:143], v17, s[16:19], s54 offen sc1\n"
"v_mfma_f32_16x16x32_bf16 v[220:223], a[68:71], a[20:23], v[220:223]\n"
"s_add_u32 s54, s54, 32768\n"
"ds_read_b64_tr_b16 a[88:89], v6 offset:8192\n"
"v_mfma_f32_16x16x32_bf16 v[252:255], a[64:67], a[24:27], v[252:255]\n"
"ds_read_b64_tr_b16 a[90:91], v6 offset:9216\n"
"v_mfma_f32_16x16x32_bf16 v[252:255], a[68:71], a[28:31], v[252:255]\n"
"s_add_u32 m0, s57, 106496\n"
"ds_read_b64_tr_b16 a[92:93], v6 offset:12288\n"
"v_mfma_f32_16x16x32_bf16 v[156:159], a[64:67], v[64:67], v[156:159]\n"
"ds_read_b64_tr_b16 a[94:95], v6 offset:13312\n"
"v_mfma_f32_16x16x32_bf16 v[156:159], a[68:71], v[68:71], v[156:159]\n"
"buffer_load_dwordx4 v12, s[20:23], s71 offen offset:0 lds\n"
"v_mfma_f32_16x16x32_bf16 v[188:191], a[64:67], v[72:75], v[188:191]\n"
"ds_read_b64_tr_b16 a[48:49], v3 offset:16384\n"
"v_mfma_f32_16x16x32_bf16 v[188:191], a[68:71], v[76:79], v[188:191]\n"
"ds_read_b64_tr_b16 a[50:51], v3 offset:17408\n"
"v_mfma_f32_16x16x32_bf16 v[204:207], a[72:75], a[16:19], v[204:207]\n"
"ds_read_b64_tr_b16 a[52:53], v3 offset:20480\n"
"v_mfma_f32_16x16x32_bf16 v[204:207], a[76:79], a[20:23], v[204:207]\n"
"ds_read_b64_tr_b16 a[54:55], v3 offset:21504\n"
"v_mfma_f32_16x16x32_bf16 v[236:239], a[72:75], a[24:27], v[236:239]\n"
"buffer_load_dwordx4 v13, s[20:23], s71 offen offset:1024 lds\n"
"v_mfma_f32_16x16x32_bf16 v[236:239], a[76:79], a[28:31], v[236:239]\n"
"s_mov_b32 s71, s55\n"
"s_waitcnt vmcnt(17) lgkmcnt(8)\n"
"v_mfma_f32_16x16x32_bf16 v[208:211], a[80:83], a[32:35], v[208:211]\n"
"ds_read_b64_tr_b16 a[56:57], v7 offset:8192\n"
"v_mfma_f32_16x16x32_bf16 v[208:211], a[84:87], a[36:39], v[208:211]\n"
"ds_read_b64_tr_b16 a[58:59], v7 offset:9216\n"
"s_waitcnt vmcnt(15)\n"
"v_mfma_f32_16x16x32_bf16 v[240:243], a[80:83], a[40:43], v[240:243]\n"
"buffer_load_dwordx4 a[16:19], v18, s[16:19], s47 offen sc1\n"
"v_mfma_f32_16x16x32_bf16 v[240:243], a[84:87], a[44:47], v[240:243]\n"
"ds_read_b64_tr_b16 a[60:61], v7 offset:12288\n"
"v_mfma_f32_16x16x32_bf16 v[144:147], a[80:83], v[80:83], v[144:147]\n"
"ds_read_b64_tr_b16 a[62:63], v7 offset:13312\n"
"v_mfma_f32_16x16x32_bf16 v[144:147], a[84:87], v[84:87], v[144:147]\n"
"ds_read_b64_tr_b16 a[64:65], v4 offset:16384\n"
"v_mfma_f32_16x16x32_bf16 v[176:179], a[80:83], v[88:91], v[176:179]\n"
"ds_read_b64_tr_b16 a[66:67], v4 offset:17408\n"
"v_mfma_f32_16x16x32_bf16 v[176:179], a[84:87], v[92:95], v[176:179]\n"
"ds_read_b64_tr_b16 a[68:69], v4 offset:20480\n"
"s_waitcnt lgkmcnt(11)\n"
"v_mfma_f32_16x16x32_bf16 v[192:195], a[88:91], a[32:35], v[192:195]\n"
"buffer_load_dwordx4 a[20:23], v19, s[16:19], s47 offen sc1\n"
"v_mfma_f32_16x16x32_bf16 v[192:195], a[92:95], a[36:39], v[192:195]\n"
"ds_read_b64_tr_b16 a[70:71], v4 offset:21504\n"
"v_mfma_f32_16x16x32_bf16 v[224:227], a[88:91], a[40:43], v[224:227]\n"
"ds_read_b64_tr_b16 a[72:73], v8 offset:8192\n"
"v_mfma_f32_16x16x32_bf16 v[224:227], a[92:95], a[44:47], v[224:227]\n"
"ds_read_b64_tr_b16 a[74:75], v8 offset:9216\n"
"s_waitcnt lgkmcnt(10)\n"
"v_mfma_f32_16x16x32_bf16 v[212:215], a[48:51], a[32:35], v[212:215]\n"
"buffer_load_dwordx4 a[24:27], v20, s[16:19], s47 offen sc1\n"
"v_mfma_f32_16x16x32_bf16 v[212:215], a[52:55], a[36:39], v[212:215]\n"
"ds_read_b64_tr_b16 a[76:77], v8 offset:12288\n"
"v_mfma_f32_16x16x32_bf16 v[244:247], a[48:51], a[40:43], v[244:247]\n"
"ds_read_b64_tr_b16 a[78:79], v8 offset:13312\n"
"v_mfma_f32_16x16x32_bf16 v[244:247], a[52:55], a[44:47], v[244:247]\n"
"ds_read_b64_tr_b16 a[80:81], v5 offset:16384\n"
"v_mfma_f32_16x16x32_bf16 v[148:151], a[48:51], v[80:83], v[148:151]\n"
"buffer_load_dwordx4 a[28:31], v21, s[16:19], s47 offen sc1\n"
"v_mfma_f32_16x16x32_bf16 v[148:151], a[52:55], v[84:87], v[148:151]\n"
"s_add_u32 s47, s47, 32768\n"
"ds_read_b64_tr_b16 a[82:83], v5 offset:17408\n"
"v_mfma_f32_16x16x32_bf16 v[180:183], a[48:51], v[88:91], v[180:183]\n"
"ds_read_b64_tr_b16 a[84:85], v5 offset:20480\n"
"v_mfma_f32_16x16x32_bf16 v[180:183], a[52:55], v[92:95], v[180:183]\n"
"ds_read_b64_tr_b16 a[86:87], v5 offset:21504\n"
"s_waitcnt lgkmcnt(12)\n"
"v_mfma_f32_16x16x32_bf16 v[196:199], a[56:59], a[32:35], v[196:199]\n"
"ds_read_b64_tr_b16 a[88:89], v9 offset:8192\n"
"v_mfma_f32_16x16x32_bf16 v[196:199], a[60:63], a[36:39], v[196:199]\n"
"buffer_load_dwordx4 a[144:147], v14, s[16:19], s46 offen sc1\n"
"v_mfma_f32_16x16x32_bf16 v[228:231], a[56:59], a[40:43], v[228:231]\n"
"ds_read_b64_tr_b16 a[90:91], v9 offset:9216\n"
"v_mfma_f32_16x16x32_bf16 v[228:231], a[60:63], a[44:47], v[228:231]\n"
"ds_read_b64_tr_b16 a[92:93], v9 offset:12288\n"
"s_waitcnt lgkmcnt(11)\n"
"v_mfma_f32_16x16x32_bf16 v[216:219], a[64:67], a[32:35], v[216:219]\n"
"ds_read_b64_tr_b16 a[94:95], v9 offset:13312\n"
"v_mfma_f32_16x16x32_bf16 v[216:219], a[68:71], a[36:39], v[216:219]\n"
"buffer_load_dwordx4 v[116:119], v27, s[4:7], 0 offen\n"
"v_mfma_f32_16x16x32_bf16 v[248:251], a[64:67], a[40:43], v[248:251]\n"
"buffer_load_dwordx4 a[148:151], v15, s[16:19], s46 offen sc1\n"
"v_mfma_f32_16x16x32_bf16 v[248:251], a[68:71], a[44:47], v[248:251]\n"
"s_add_u32 s46, s46, 32768\n"
"v_mfma_f32_16x16x32_bf16 v[152:155], a[64:67], v[80:83], v[152:155]\n"
"buffer_load_dwordx4 v[120:123], v28, s[4:7], 0 offen\n"
"v_mfma_f32_16x16x32_bf16 v[152:155], a[68:71], v[84:87], v[152:155]\n"
"v_mfma_f32_16x16x32_bf16 v[184:187], a[64:67], v[88:91], v[184:187]\n"
"s_add_u32 m0, s57, 139264\n"
"v_mfma_f32_16x16x32_bf16 v[184:187], a[68:71], v[92:95], v[184:187]\n"
"s_waitcnt vmcnt(19) lgkmcnt(0)\n"
"s_barrier\n"
"v_mfma_f32_16x16x32_bf16 v[200:203], a[72:75], a[32:35], v[200:203]\n"
"buffer_load_dwordx4 v10, s[24:27], s70 offen offset:0 lds\n"
"v_mfma_f32_16x16x32_bf16 v[200:203], a[76:79], a[36:39], v[200:203]\n"
"ds_read_b64_tr_b16 a[48:49], v2 offset:0\n"
"v_mfma_f32_16x16x32_bf16 v[232:235], a[72:75], a[40:43], v[232:235]\n"
"ds_read_b64_tr_b16 a[50:51], v2 offset:1024\n"
"v_mfma_f32_16x16x32_bf16 v[232:235], a[76:79], a[44:47], v[232:235]\n"
"ds_read_b64_tr_b16 a[52:53], v2 offset:4096\n"
"v_mfma_f32_16x16x32_bf16 v[220:223], a[80:83], a[32:35], v[220:223]\n"
"ds_read_b64_tr_b16 a[54:55], v2 offset:5120\n"
"v_mfma_f32_16x16x32_bf16 v[220:223], a[84:87], a[36:39], v[220:223]\n"
"buffer_load_dwordx4 v11, s[24:27], s70 offen offset:1024 lds\n"
"v_mfma_f32_16x16x32_bf16 v[252:255], a[80:83], a[40:43], v[252:255]\n"
"s_add_u32 s70, s70, 131072\n"
"ds_read_b64_tr_b16 a[56:57], v2 offset:57344\n"
"v_mfma_f32_16x16x32_bf16 v[252:255], a[84:87], a[44:47], v[252:255]\n"
"ds_read_b64_tr_b16 a[58:59], v2 offset:58368\n"
"v_mfma_f32_16x16x32_bf16 v[156:159], a[80:83], v[80:83], v[156:159]\n"
"s_add_u32 m0, s57, 114688\n"
"ds_read_b64_tr_b16 a[60:61], v2 offset:61440\n"
"v_mfma_f32_16x16x32_bf16 v[156:159], a[84:87], v[84:87], v[156:159]\n"
"ds_read_b64_tr_b16 a[62:63], v2 offset:62464\n"
"v_mfma_f32_16x16x32_bf16 v[188:191], a[80:83], v[88:91], v[188:191]\n"
"buffer_load_dwordx4 v12, s[24:27], s71 offen offset:0 lds\n"
"v_mfma_f32_16x16x32_bf16 v[188:191], a[84:87], v[92:95], v[188:191]\n"
"ds_read_b64_tr_b16 a[64:65], v3 offset:0\n"
"v_mfma_f32_16x16x32_bf16 v[204:207], a[88:91], a[32:35], v[204:207]\n"
"ds_read_b64_tr_b16 a[66:67], v3 offset:1024\n"
"v_mfma_f32_16x16x32_bf16 v[204:207], a[92:95], a[36:39], v[204:207]\n"
"ds_read_b64_tr_b16 a[68:69], v3 offset:4096\n"
"v_mfma_f32_16x16x32_bf16 v[236:239], a[88:91], a[40:43], v[236:239]\n"
"ds_read_b64_tr_b16 a[70:71], v3 offset:5120\n"
"v_mfma_f32_16x16x32_bf16 v[236:239], a[92:95], a[44:47], v[236:239]\n"
"ds_read_b64_tr_b16 a[72:73], v3 offset:57344\n"
"s_waitcnt vmcnt(20) lgkmcnt(9)\n"
"v_mfma_f32_16x16x32_bf16 v[208:211], a[48:51], a[0:3], v[208:211]\n"
"buffer_load_dwordx4 v13, s[24:27], s71 offen offset:1024 lds\n"
"v_mfma_f32_16x16x32_bf16 v[208:211], a[52:55], a[4:7], v[208:211]\n"
"s_add_u32 s71, s71, 131072\n"
"ds_read_b64_tr_b16 a[74:75], v3 offset:58368\n"
"s_waitcnt vmcnt(19)\n"
"v_mfma_f32_16x16x32_bf16 v[240:243], a[48:51], a[8:11], v[240:243]\n"
"ds_read_b64_tr_b16 a[76:77], v3 offset:61440\n"
"v_mfma_f32_16x16x32_bf16 v[240:243], a[52:55], a[12:15], v[240:243]\n"
"buffer_load_dwordx4 a[32:35], v18, s[16:19], s47 offen sc1\n"
"v_mfma_f32_16x16x32_bf16 v[144:147], a[48:51], a[96:99], v[144:147]\n"
"ds_read_b64_tr_b16 a[78:79], v3 offset:62464\n"
"v_mfma_f32_16x16x32_bf16 v[144:147], a[52:55], a[100:103], v[144:147]\n"
"ds_read_b64_tr_b16 a[80:81], v4 offset:0\n"
"v_mfma_f32_16x16x32_bf16 v[176:179], a[48:51], a[104:107], v[176:179]\n"
"ds_read_b64_tr_b16 a[82:83], v4 offset:1024\n"
"v_mfma_f32_16x16x32_bf16 v[176:179], a[52:55], a[108:111], v[176:179]\n"
"ds_read_b64_tr_b16 a[84:85], v4 offset:4096\n"
"s_waitcnt lgkmcnt(11)\n"
"v_mfma_f32_16x16x32_bf16 v[192:195], a[56:59], a[0:3], v[192:195]\n"
"buffer_load_dwordx4 a[36:39], v19, s[16:19], s47 offen sc1\n"
"v_mfma_f32_16x16x32_bf16 v[192:195], a[60:63], a[4:7], v[192:195]\n"
"ds_read_b64_tr_b16 a[86:87], v4 offset:5120\n"
"v_mfma_f32_16x16x32_bf16 v[224:227], a[56:59], a[8:11], v[224:227]\n"
"ds_read_b64_tr_b16 a[88:89], v4 offset:57344\n"
"v_mfma_f32_16x16x32_bf16 v[224:227], a[60:63], a[12:15], v[224:227]\n"
"ds_read_b64_tr_b16 a[90:91], v4 offset:58368\n"
"s_waitcnt lgkmcnt(10)\n"
"v_mfma_f32_16x16x32_bf16 v[212:215], a[64:67], a[0:3], v[212:215]\n"
"ds_read_b64_tr_b16 a[92:93], v4 offset:61440\n"
"v_mfma_f32_16x16x32_bf16 v[212:215], a[68:71], a[4:7], v[212:215]\n"
"buffer_load_dwordx4 a[40:43], v20, s[16:19], s47 offen sc1\n"
"v_mfma_f32_16x16x32_bf16 v[244:247], a[64:67], a[8:11], v[244:247]\n"
"ds_read_b64_tr_b16 a[94:95], v4 offset:62464\n"
"v_mfma_f32_16x16x32_bf16 v[244:247], a[68:71], a[12:15], v[244:247]\n"
"ds_read_b64_tr_b16 a[48:49], v5 offset:0\n"
"v_mfma_f32_16x16x32_bf16 v[148:151], a[64:67], a[96:99], v[148:151]\n"
"ds_read_b64_tr_b16 a[50:51], v5 offset:1024\n"
"v_mfma_f32_16x16x32_bf16 v[148:151], a[68:71], a[100:103], v[148:151]\n"
"ds_read_b64_tr_b16 a[52:53], v5 offset:4096\n"
"v_mfma_f32_16x16x32_bf16 v[180:183], a[64:67], a[104:107], v[180:183]\n"
"buffer_load_dwordx4 a[44:47], v21, s[16:19], s47 offen sc1\n"
"v_mfma_f32_16x16x32_bf16 v[180:183], a[68:71], a[108:111], v[180:183]\n"
"s_add_u32 s47, s47, 32768\n"
"s_waitcnt lgkmcnt(11)\n"
"v_mfma_f32_16x16x32_bf16 v[196:199], a[72:75], a[0:3], v[196:199]\n"
"ds_read_b64_tr_b16 a[54:55], v5 offset:5120\n"
"v_mfma_f32_16x16x32_bf16 v[196:199], a[76:79], a[4:7], v[196:199]\n"
"ds_read_b64_tr_b16 a[56:57], v5 offset:57344\n"
"v_mfma_f32_16x16x32_bf16 v[228:231], a[72:75], a[8:11], v[228:231]\n"
"ds_read_b64_tr_b16 a[58:59], v5 offset:58368\n"
"v_mfma_f32_16x16x32_bf16 v[228:231], a[76:79], a[12:15], v[228:231]\n"
"ds_read_b64_tr_b16 a[60:61], v5 offset:61440\n"
".Lpad13:\n"
"s_cbranch_execz .Lwr1\n"
"s_waitcnt lgkmcnt(11)\n"
"v_mfma_f32_16x16x32_bf16 v[216:219], a[80:83], a[0:3], v[216:219]\n"
"buffer_load_dwordx4 a[152:155], v16, s[16:19], s54 offen sc1\n"
"v_mfma_f32_16x16x32_bf16 v[216:219], a[84:87], a[4:7], v[216:219]\n"
"ds_read_b64_tr_b16 a[62:63], v5 offset:62464\n"
"v_mfma_f32_16x16x32_bf16 v[248:251], a[80:83], a[8:11], v[248:251]\n"
"buffer_load_dwordx4 v[124:127], v29, s[4:7], 0 offen\n"
"v_mfma_f32_16x16x32_bf16 v[248:251], a[84:87], a[12:15], v[248:251]\n"
"v_mfma_f32_16x16x32_bf16 v[152:155], a[80:83], a[96:99], v[152:155]\n"
"buffer_load_dwordx4 a[156:159], v17, s[16:19], s54 offen sc1\n"
"v_mfma_f32_16x16x32_bf16 v[152:155], a[84:87], a[100:103], v[152:155]\n"
"s_add_u32 s54, s54, 32768\n"
"v_mfma_f32_16x16x32_bf16 v[184:187], a[80:83], a[104:107], v[184:187]\n"
"buffer_load_dwordx4 v[30:33], v37, s[32:35], s65 offen\n"
"v_mfma_f32_16x16x32_bf16 v[184:187], a[84:87], a[108:111], v[184:187]\n"
"s_waitcnt vmcnt(20) lgkmcnt(0)\n"
"s_barrier\n"
"v_mfma_f32_16x16x32_bf16 v[200:203], a[88:91], a[0:3], v[200:203]\n"
"s_add_u32 m0, s57, 98304\n"
"ds_read_b64_tr_b16 a[64:65], v2 offset:8192\n"
"v_mfma_f32_16x16x32_bf16 v[200:203], a[92:95], a[4:7], v[200:203]\n"
"ds_read_b64_tr_b16 a[66:67], v2 offset:9216\n"
"s_add_u32 s65, s60, 4096\n"
"v_mfma_f32_16x16x32_bf16 v[232:235], a[88:91], a[8:11], v[232:235]\n"
"buffer_load_dwordx4 v12, s[24:27], s71 offen offset:0 lds\n"
"v_mfma_f32_16x16x32_bf16 v[232:235], a[92:95], a[12:15], v[232:235]\n"
"ds_read_b64_tr_b16 a[68:69], v2 offset:12288\n"
"v_mfma_f32_16x16x32_bf16 v[220:223], a[48:51], a[0:3], v[220:223]\n"
"ds_read_b64_tr_b16 a[70:71], v2 offset:13312\n"
"s_add_u32 s66, s60, 8192\n"
"v_mfma_f32_16x16x32_bf16 v[220:223], a[52:55], a[4:7], v[220:223]\n"
"ds_read_b64_tr_b16 a[72:73], v2 offset:24576\n"
"v_mfma_f32_16x16x32_bf16 v[252:255], a[48:51], a[8:11], v[252:255]\n"
"ds_read_b64_tr_b16 a[74:75], v2 offset:25600\n"
"v_mfma_f32_16x16x32_bf16 v[252:255], a[52:55], a[12:15], v[252:255]\n"
"buffer_load_dwordx4 v13, s[24:27], s71 offen offset:1024 lds\n"
"v_mfma_f32_16x16x32_bf16 v[156:159], a[48:51], a[96:99], v[156:159]\n"
"s_add_u32 s71, s71, 131072\n"
"ds_read_b64_tr_b16 a[76:77], v2 offset:28672\n"
"v_mfma_f32_16x16x32_bf16 v[156:159], a[52:55], a[100:103], v[156:159]\n"
"ds_read_b64_tr_b16 a[78:79], v2 offset:29696\n"
"v_mfma_f32_16x16x32_bf16 v[188:191], a[48:51], a[104:107], v[188:191]\n"
"ds_read_b64_tr_b16 a[80:81], v3 offset:8192\n"
"v_mfma_f32_16x16x32_bf16 v[188:191], a[52:55], a[108:111], v[188:191]\n"
"ds_read_b64_tr_b16 a[82:83], v3 offset:9216\n"
"v_mfma_f32_16x16x32_bf16 v[204:207], a[56:59], a[0:3], v[204:207]\n"
"buffer_load_dwordx4 a[160:163], v14, s[16:19], s46 offen sc1\n"
"v_mfma_f32_16x16x32_bf16 v[204:207], a[60:63], a[4:7], v[204:207]\n"
"ds_read_b64_tr_b16 a[84:85], v3 offset:12288\n"
"v_mfma_f32_16x16x32_bf16 v[236:239], a[56:59], a[8:11], v[236:239]\n"
"ds_read_b64_tr_b16 a[86:87], v3 offset:13312\n"
"v_mfma_f32_16x16x32_bf16 v[236:239], a[60:63], a[12:15], v[236:239]\n"
"ds_read_b64_tr_b16 a[88:89], v3 offset:24576\n"
"s_waitcnt vmcnt(21) lgkmcnt(9)\n"
"v_mfma_f32_16x16x32_bf16 v[208:211], a[64:67], a[16:19], v[208:211]\n"
"buffer_load_dwordx4 a[164:167], v15, s[16:19], s46 offen sc1\n"
"v_mfma_f32_16x16x32_bf16 v[208:211], a[68:71], a[20:23], v[208:211]\n"
"s_add_u32 s46, s46, 32768\n"
"ds_read_b64_tr_b16 a[90:91], v3 offset:25600\n"
"s_waitcnt vmcnt(20)\n"
"v_mfma_f32_16x16x32_bf16 v[240:243], a[64:67], a[24:27], v[240:243]\n"
"ds_read_b64_tr_b16 a[92:93], v3 offset:28672\n"
"v_mfma_f32_16x16x32_bf16 v[240:243], a[68:71], a[28:31], v[240:243]\n"
"ds_read_b64_tr_b16 a[94:95], v3 offset:29696\n"
"v_mfma_f32_16x16x32_bf16 v[144:147], a[64:67], a[112:115], v[144:147]\n"
"ds_read_b64_tr_b16 a[48:49], v4 offset:8192\n"
"v_mfma_f32_16x16x32_bf16 v[144:147], a[68:71], a[116:119], v[144:147]\n"
"buffer_load_dwordx4 a[0:3], v18, s[16:19], s47 offen sc1\n"
"v_mfma_f32_16x16x32_bf16 v[176:179], a[64:67], a[120:123], v[176:179]\n"
"ds_read_b64_tr_b16 a[50:51], v4 offset:9216\n"
"v_mfma_f32_16x16x32_bf16 v[176:179], a[68:71], a[124:127], v[176:179]\n"
"ds_read_b64_tr_b16 a[52:53], v4 offset:12288\n"
"s_waitcnt lgkmcnt(11)\n"
"v_mfma_f32_16x16x32_bf16 v[192:195], a[72:75], a[16:19], v[192:195]\n"
"ds_read_b64_tr_b16 a[54:55], v4 offset:13312\n"
"v_mfma_f32_16x16x32_bf16 v[192:195], a[76:79], a[20:23], v[192:195]\n"
"ds_read_b64_tr_b16 a[56:57], v4 offset:24576\n"
"v_mfma_f32_16x16x32_bf16 v[224:227], a[72:75], a[24:27], v[224:227]\n"
"buffer_load_dwordx4 a[4:7], v19, s[16:19], s47 offen sc1\n"
"v_mfma_f32_16x16x32_bf16 v[224:227], a[76:79], a[28:31], v[224:227]\n"
"ds_read_b64_tr_b16 a[58:59], v4 offset:25600\n"
"s_waitcnt lgkmcnt(10)\n"
"v_mfma_f32_16x16x32_bf16 v[212:215], a[80:83], a[16:19], v[212:215]\n"
"ds_read_b64_tr_b16 a[60:61], v4 offset:28672\n"
"v_mfma_f32_16x16x32_bf16 v[212:215], a[84:87], a[20:23], v[212:215]\n"
"ds_read_b64_tr_b16 a[62:63], v4 offset:29696\n"
"v_mfma_f32_16x16x32_bf16 v[244:247], a[80:83], a[24:27], v[244:247]\n"
"ds_read_b64_tr_b16 a[64:65], v5 offset:8192\n"
"v_mfma_f32_16x16x32_bf16 v[244:247], a[84:87], a[28:31], v[244:247]\n"
"buffer_load_dwordx4 a[8:11], v20, s[16:19], s47 offen sc1\n"
"v_mfma_f32_16x16x32_bf16 v[148:151], a[80:83], a[112:115], v[148:151]\n"
"ds_read_b64_tr_b16 a[66:67], v5 offset:9216\n"
"v_mfma_f32_16x16x32_bf16 v[148:151], a[84:87], a[116:119], v[148:151]\n"
"ds_read_b64_tr_b16 a[68:69], v5 offset:12288\n"
"v_mfma_f32_16x16x32_bf16 v[180:183], a[80:83], a[120:123], v[180:183]\n"
"ds_read_b64_tr_b16 a[70:71], v5 offset:13312\n"
"v_mfma_f32_16x16x32_bf16 v[180:183], a[84:87], a[124:127], v[180:183]\n"
"ds_read_b64_tr_b16 a[72:73], v5 offset:24576\n"
"s_waitcnt lgkmcnt(13)\n"
"v_mfma_f32_16x16x32_bf16 v[196:199], a[88:91], a[16:19], v[196:199]\n"
"buffer_load_dwordx4 a[12:15], v21, s[16:19], s47 offen sc1\n"
"v_mfma_f32_16x16x32_bf16 v[196:199], a[92:95], a[20:23], v[196:199]\n"
"s_add_u32 s47, s47, 32768\n"
"ds_read_b64_tr_b16 a[74:75], v5 offset:25600\n"
"v_mfma_f32_16x16x32_bf16 v[228:231], a[88:91], a[24:27], v[228:231]\n"
"ds_read_b64_tr_b16 a[76:77], v5 offset:28672\n"
"v_mfma_f32_16x16x32_bf16 v[228:231], a[92:95], a[28:31], v[228:231]\n"
"ds_read_b64_tr_b16 a[78:79], v5 offset:29696\n"
"s_waitcnt lgkmcnt(12)\n"
"v_mfma_f32_16x16x32_bf16 v[216:219], a[48:51], a[16:19], v[216:219]\n"
"v_mfma_f32_16x16x32_bf16 v[216:219], a[52:55], a[20:23], v[216:219]\n"
"buffer_load_dwordx4 a[176:179], v14, s[16:19], s46 offen sc1\n"
"v_mfma_f32_16x16x32_bf16 v[248:251], a[48:51], a[24:27], v[248:251]\n"
"v_mfma_f32_16x16x32_bf16 v[248:251], a[52:55], a[28:31], v[248:251]\n"
"v_mfma_f32_16x16x32_bf16 v[152:155], a[48:51], a[112:115], v[152:155]\n"
"v_mfma_f32_16x16x32_bf16 v[152:155], a[52:55], a[116:119], v[152:155]\n"
"buffer_load_dwordx4 a[180:183], v15, s[16:19], s46 offen sc1\n"
"v_mfma_f32_16x16x32_bf16 v[184:187], a[48:51], a[120:123], v[184:187]\n"
"s_add_u32 s46, s46, 32768\n"
"v_mfma_f32_16x16x32_bf16 v[184:187], a[52:55], a[124:127], v[184:187]\n"
"s_waitcnt vmcnt(18) lgkmcnt(0)\n"
"s_barrier\n"
"v_mfma_f32_16x16x32_bf16 v[200:203], a[56:59], a[16:19], v[200:203]\n"
"ds_read_b64_tr_b16 a[80:81], v2 offset:16384\n"
"v_mfma_f32_16x16x32_bf16 v[200:203], a[60:63], a[20:23], v[200:203]\n"
"s_add_u32 m0, s57, 106496\n"
"ds_read_b64_tr_b16 a[82:83], v2 offset:17408\n"
"v_mfma_f32_16x16x32_bf16 v[232:235], a[56:59], a[24:27], v[232:235]\n"
"ds_read_b64_tr_b16 a[84:85], v2 offset:20480\n"
"v_mfma_f32_16x16x32_bf16 v[232:235], a[60:63], a[28:31], v[232:235]\n"
"ds_read_b64_tr_b16 a[86:87], v2 offset:21504\n"
"v_mfma_f32_16x16x32_bf16 v[220:223], a[64:67], a[16:19], v[220:223]\n"
"buffer_load_dwordx4 v12, s[24:27], s71 offen offset:0 lds\n"
"v_mfma_f32_16x16x32_bf16 v[220:223], a[68:71], a[20:23], v[220:223]\n"
"ds_read_b64_tr_b16 a[88:89], v2 offset:32768\n"
"v_mfma_f32_16x16x32_bf16 v[252:255], a[64:67], a[24:27], v[252:255]\n"
"ds_read_b64_tr_b16 a[90:91], v2 offset:33792\n"
"v_mfma_f32_16x16x32_bf16 v[252:255], a[68:71], a[28:31], v[252:255]\n"
"ds_read_b64_tr_b16 a[92:93], v2 offset:36864\n"
"v_mfma_f32_16x16x32_bf16 v[156:159], a[64:67], a[112:115], v[156:159]\n"
"buffer_load_dwordx4 v13, s[24:27], s71 offen offset:1024 lds\n"
"v_mfma_f32_16x16x32_bf16 v[156:159], a[68:71], a[116:119], v[156:159]\n"
"s_add_u32 s71, s71, 131072\n"
"ds_read_b64_tr_b16 a[94:95], v2 offset:37888\n"
"v_mfma_f32_16x16x32_bf16 v[188:191], a[64:67], a[120:123], v[188:191]\n"
"ds_read_b64_tr_b16 a[48:49], v3 offset:16384\n"
"v_mfma_f32_16x16x32_bf16 v[188:191], a[68:71], a[124:127], v[188:191]\n"
"s_add_u32 m0, s57, 147456\n"
"v_mfma_f32_16x16x32_bf16 v[204:207], a[72:75], a[16:19], v[204:207]\n"
"ds_read_b64_tr_b16 a[50:51], v3 offset:17408\n"
"v_mfma_f32_16x16x32_bf16 v[204:207], a[76:79], a[20:23], v[204:207]\n"
"buffer_load_dwordx4 v10, s[24:27], s70 offen offset:0 lds\n"
"v_mfma_f32_16x16x32_bf16 v[236:239], a[72:75], a[24:27], v[236:239]\n"
"ds_read_b64_tr_b16 a[52:53], v3 offset:20480\n"
"v_mfma_f32_16x16x32_bf16 v[236:239], a[76:79], a[28:31], v[236:239]\n"
"ds_read_b64_tr_b16 a[54:55], v3 offset:21504\n"
"s_waitcnt vmcnt(19) lgkmcnt(8)\n"
"v_mfma_f32_16x16x32_bf16 v[208:211], a[80:83], a[32:35], v[208:211]\n"
"ds_read_b64_tr_b16 a[56:57], v3 offset:32768\n"
"v_mfma_f32_16x16x32_bf16 v[208:211], a[84:87], a[36:39], v[208:211]\n"
"ds_read_b64_tr_b16 a[58:59], v3 offset:33792\n"
"s_waitcnt vmcnt(17)\n"
"v_mfma_f32_16x16x32_bf16 v[240:243], a[80:83], a[40:43], v[240:243]\n"
"buffer_load_dwordx4 v11, s[24:27], s70 offen offset:1024 lds\n"
"v_mfma_f32_16x16x32_bf16 v[240:243], a[84:87], a[44:47], v[240:243]\n"
"s_add_u32 s70, s70, 131072\n"
"ds_read_b64_tr_b16 a[60:61], v3 offset:36864\n"
"v_mfma_f32_16x16x32_bf16 v[144:147], a[80:83], a[128:131], v[144:147]\n"
"ds_read_b64_tr_b16 a[62:63], v3 offset:37888\n"
"v_mfma_f32_16x16x32_bf16 v[144:147], a[84:87], a[132:135], v[144:147]\n"
"ds_read_b64_tr_b16 a[64:65], v4 offset:16384\n"
"v_mfma_f32_16x16x32_bf16 v[176:179], a[80:83], a[136:139], v[176:179]\n"
"buffer_load_dwordx4 a[16:19], v18, s[16:19], s47 offen sc1\n"
"v_mfma_f32_16x16x32_bf16 v[176:179], a[84:87], a[140:143], v[176:179]\n"
"ds_read_b64_tr_b16 a[66:67], v4 offset:17408\n"
"s_waitcnt lgkmcnt(10)\n"
"v_mfma_f32_16x16x32_bf16 v[192:195], a[88:91], a[32:35], v[192:195]\n"
"ds_read_b64_tr_b16 a[68:69], v4 offset:20480\n"
"v_mfma_f32_16x16x32_bf16 v[192:195], a[92:95], a[36:39], v[192:195]\n"
"ds_read_b64_tr_b16 a[70:71], v4 offset:21504\n"
"v_mfma_f32_16x16x32_bf16 v[224:227], a[88:91], a[40:43], v[224:227]\n"
"ds_read_b64_tr_b16 a[72:73], v4 offset:32768\n"
"s_waitcnt vmcnt(15)\n"
"v_mfma_f32_16x16x32_bf16 v[224:227], a[92:95], a[44:47], v[224:227]\n"
"ds_read_b64_tr_b16 a[74:75], v4 offset:33792\n"
"s_waitcnt lgkmcnt(10)\n"
"v_mfma_f32_16x16x32_bf16 v[212:215], a[48:51], a[32:35], v[212:215]\n"
"buffer_load_dwordx4 a[20:23], v19, s[16:19], s47 offen sc1\n"
"v_lshlrev_b32 v22, 16, v30\n"
"v_mfma_f32_16x16x32_bf16 v[212:215], a[52:55], a[36:39], v[212:215]\n"
"ds_read_b64_tr_b16 a[76:77], v4 offset:36864\n"
"v_and_b32 v30, 0xffff0000, v30\n"
"v_mfma_f32_16x16x32_bf16 v[244:247], a[48:51], a[40:43], v[244:247]\n"
"ds_read_b64_tr_b16 a[78:79], v4 offset:37888\n"
"v_lshlrev_b32 v24, 16, v31\n"
"v_mfma_f32_16x16x32_bf16 v[244:247], a[52:55], a[44:47], v[244:247]\n"
"ds_read_b64_tr_b16 a[80:81], v5 offset:16384\n"
"v_and_b32 v31, 0xffff0000, v31\n"
"v_mfma_f32_16x16x32_bf16 v[148:151], a[48:51], a[128:131], v[148:151]\n"
"buffer_load_dwordx4 a[24:27], v20, s[16:19], s47 offen sc1\n"
"v_lshlrev_b32 v25, 16, v32\n"
"v_mfma_f32_16x16x32_bf16 v[148:151], a[52:55], a[132:135], v[148:151]\n"
"ds_read_b64_tr_b16 a[82:83], v5 offset:17408\n"
"v_and_b32 v32, 0xffff0000, v32\n"
"v_mfma_f32_16x16x32_bf16 v[180:183], a[48:51], a[136:139], v[180:183]\n"
"ds_read_b64_tr_b16 a[84:85], v5 offset:20480\n"
"v_lshlrev_b32 v40, 16, v33\n"
"v_mfma_f32_16x16x32_bf16 v[180:183], a[52:55], a[140:143], v[180:183]\n"
"ds_read_b64_tr_b16 a[86:87], v5 offset:21504\n"
"s_waitcnt lgkmcnt(12)\n"
"v_mfma_f32_16x16x32_bf16 v[196:199], a[56:59], a[32:35], v[196:199]\n"
"ds_read_b64_tr_b16 a[88:89], v5 offset:32768\n"
"v_and_b32 v33, 0xffff0000, v33\n"
"v_mfma_f32_16x16x32_bf16 v[196:199], a[60:63], a[36:39], v[196:199]\n"
"buffer_load_dwordx4 a[28:31], v21, s[16:19], s47 offen sc1\n"
"v_lshlrev_b32 v34, 16, v112\n"
"v_mfma_f32_16x16x32_bf16 v[228:231], a[56:59], a[40:43], v[228:231]\n"
"s_add_u32 s47, s47, 32768\n"
"ds_read_b64_tr_b16 a[90:91], v5 offset:33792\n"
"v_mfma_f32_16x16x32_bf16 v[228:231], a[60:63], a[44:47], v[228:231]\n"
"ds_read_b64_tr_b16 a[92:93], v5 offset:36864\n"
"s_waitcnt lgkmcnt(11)\n"
"v_mfma_f32_16x16x32_bf16 v[216:219], a[64:67], a[32:35], v[216:219]\n"
"ds_read_b64_tr_b16 a[94:95], v5 offset:37888\n"
"v_and_b32 v46, 0xffff0000, v112\n"
"v_mfma_f32_16x16x32_bf16 v[216:219], a[68:71], a[36:39], v[216:219]\n"
"s_add_u32 m0, s57, 90112\n"
"v_add_f32 v96, v34, v22\n"
"v_mfma_f32_16x16x32_bf16 v[248:251], a[64:67], a[40:43], v[248:251]\n"
"v_add_f32 v97, v46, v30\n"
"v_lshlrev_b32 v34, 16, v113\n"
"v_mfma_f32_16x16x32_bf16 v[248:251], a[68:71], a[44:47], v[248:251]\n"
"buffer_load_dwordx4 v10, s[24:27], s70 offen offset:0 lds\n"
"v_and_b32 v46, 0xffff0000, v113\n"
"v_mfma_f32_16x16x32_bf16 v[152:155], a[64:67], a[128:131], v[152:155]\n"
"v_add_f32 v98, v34, v24\n"
"v_add_f32 v99, v46, v31\n"
"v_mfma_f32_16x16x32_bf16 v[152:155], a[68:71], a[132:135], v[152:155]\n"
"v_lshlrev_b32 v34, 16, v114\n"
"v_and_b32 v46, 0xffff0000, v114\n"
"v_mfma_f32_16x16x32_bf16 v[184:187], a[64:67], a[136:139], v[184:187]\n"
"v_add_f32 v100, v34, v25\n"
"v_add_f32 v101, v46, v32\n"
"v_mfma_f32_16x16x32_bf16 v[184:187], a[68:71], a[140:143], v[184:187]\n"
"s_waitcnt vmcnt(17) lgkmcnt(0)\n"
"s_barrier\n"
"v_mfma_f32_16x16x32_bf16 v[200:203], a[72:75], a[32:35], v[200:203]\n"
"buffer_load_dwordx4 v11, s[24:27], s70 offen offset:1024 lds\n"
"v_lshlrev_b32 v34, 16, v115\n"
"v_mfma_f32_16x16x32_bf16 v[200:203], a[76:79], a[36:39], v[200:203]\n"
"s_add_u32 s70, s70, 131072\n"
"ds_read_b64_tr_b16 a[48:49], v2 offset:0\n"
"v_mfma_f32_16x16x32_bf16 v[232:235], a[72:75], a[40:43], v[232:235]\n"
"ds_read_b64_tr_b16 a[50:51], v2 offset:1024\n"
"v_and_b32 v46, 0xffff0000, v115\n"
"v_mfma_f32_16x16x32_bf16 v[232:235], a[76:79], a[44:47], v[232:235]\n"
"s_add_u32 m0, s57, 114688\n"
"v_mfma_f32_16x16x32_bf16 v[220:223], a[80:83], a[32:35], v[220:223]\n"
"ds_read_b64_tr_b16 a[52:53], v2 offset:4096\n"
"v_add_f32 v102, v34, v40\n"
"v_mfma_f32_16x16x32_bf16 v[220:223], a[84:87], a[36:39], v[220:223]\n"
"buffer_load_dwordx4 v12, s[24:27], s71 offen offset:0 lds\n"
"v_add_f32 v103, v46, v33\n"
"v_mfma_f32_16x16x32_bf16 v[252:255], a[80:83], a[40:43], v[252:255]\n"
"ds_read_b64_tr_b16 a[54:55], v2 offset:5120\n"
"v_lshlrev_b32 v34, 16, v116\n"
"v_mfma_f32_16x16x32_bf16 v[252:255], a[84:87], a[44:47], v[252:255]\n"
"ds_read_b64_tr_b16 a[56:57], v2 offset:40960\n"
"v_and_b32 v46, 0xffff0000, v116\n"
"v_mfma_f32_16x16x32_bf16 v[156:159], a[80:83], a[128:131], v[156:159]\n"
"ds_read_b64_tr_b16 a[58:59], v2 offset:41984\n"
"v_add_f32 v104, v34, v22\n"
"v_mfma_f32_16x16x32_bf16 v[156:159], a[84:87], a[132:135], v[156:159]\n"
"ds_read_b64_tr_b16 a[60:61], v2 offset:45056\n"
"v_add_f32 v105, v46, v30\n"
"v_mfma_f32_16x16x32_bf16 v[188:191], a[80:83], a[136:139], v[188:191]\n"
"buffer_load_dwordx4 v13, s[24:27], s71 offen offset:1024 lds\n"
"v_lshlrev_b32 v34, 16, v117\n"
"v_mfma_f32_16x16x32_bf16 v[188:191], a[84:87], a[140:143], v[188:191]\n"
"s_add_u32 s71, s71, 131072\n"
"v_mfma_f32_16x16x32_bf16 v[204:207], a[88:91], a[32:35], v[204:207]\n"
"ds_read_b64_tr_b16 a[62:63], v2 offset:46080\n"
"v_and_b32 v46, 0xffff0000, v117\n"
"v_mfma_f32_16x16x32_bf16 v[204:207], a[92:95], a[36:39], v[204:207]\n"
"ds_read_b64_tr_b16 a[64:65], v3 offset:0\n"
"v_add_f32 v106, v34, v24\n"
"v_mfma_f32_16x16x32_bf16 v[236:239], a[88:91], a[40:43], v[236:239]\n"
"buffer_load_dwordx4 a[168:171], v16, s[16:19], s54 offen sc1\n"
"v_add_f32 v107, v46, v31\n"
"v_mfma_f32_16x16x32_bf16 v[236:239], a[92:95], a[44:47], v[236:239]\n"
"ds_read_b64_tr_b16 a[66:67], v3 offset:1024\n"
"s_waitcnt vmcnt(17) lgkmcnt(6)\n"
"v_mfma_f32_16x16x32_bf16 v[208:211], a[48:51], a[0:3], v[208:211]\n"
"ds_read_b64_tr_b16 a[68:69], v3 offset:4096\n"
"v_lshlrev_b32 v34, 16, v118\n"
"v_mfma_f32_16x16x32_bf16 v[208:211], a[52:55], a[4:7], v[208:211]\n"
"ds_read_b64_tr_b16 a[70:71], v3 offset:5120\n"
"v_and_b32 v46, 0xffff0000, v118\n"
"s_waitcnt vmcnt(15)\n"
"v_mfma_f32_16x16x32_bf16 v[240:243], a[48:51], a[8:11], v[240:243]\n"
"ds_read_b64_tr_b16 a[72:73], v3 offset:40960\n"
"v_add_f32_e64 v108, v34, v25\n"
"v_mfma_f32_16x16x32_bf16 v[240:243], a[52:55], a[12:15], v[240:243]\n"
"buffer_load_dwordx4 a[172:175], v17, s[16:19], s54 offen sc1\n"
"v_add_f32 v109, v46, v32\n"
"v_mfma_f32_16x16x32_bf16 v[144:147], a[48:51], a[144:147], v[144:147]\n"
"s_add_u32 s54, s54, 32768\n"
"ds_read_b64_tr_b16 a[74:75], v3 offset:41984\n"
"v_mfma_f32_16x16x32_bf16 v[144:147], a[52:55], a[148:151], v[144:147]\n"
"ds_read_b64_tr_b16 a[76:77], v3 offset:45056\n"
"v_lshlrev_b32 v34, 16, v119\n"
"v_mfma_f32_16x16x32_bf16 v[176:179], a[48:51], a[152:155], v[176:179]\n"
"ds_read_b64_tr_b16 a[78:79], v3 offset:46080\n"
"v_and_b32 v46, 0xffff0000, v119\n"
"v_mfma_f32_16x16x32_bf16 v[176:179], a[52:55], a[156:159], v[176:179]\n"
"ds_read_b64_tr_b16 a[80:81], v4 offset:0\n"
"s_waitcnt lgkmcnt(9)\n"
"v_mfma_f32_16x16x32_bf16 v[192:195], a[56:59], a[0:3], v[192:195]\n"
"buffer_load_dwordx4 a[32:35], v18, s[16:19], s47 offen sc1\n"
"v_add_f32 v110, v34, v40\n"
"v_mfma_f32_16x16x32_bf16 v[192:195], a[60:63], a[4:7], v[192:195]\n"
"ds_read_b64_tr_b16 a[82:83], v4 offset:1024\n"
"v_add_f32 v111, v46, v33\n"
"v_mfma_f32_16x16x32_bf16 v[224:227], a[56:59], a[8:11], v[224:227]\n"
"ds_read_b64_tr_b16 a[84:85], v4 offset:4096\n"
"v_lshlrev_b32 v34, 16, v120\n"
"v_mfma_f32_16x16x32_bf16 v[224:227], a[60:63], a[12:15], v[224:227]\n"
"ds_read_b64_tr_b16 a[86:87], v4 offset:5120\n"
"s_waitcnt lgkmcnt(8)\n"
"v_mfma_f32_16x16x32_bf16 v[212:215], a[64:67], a[0:3], v[212:215]\n"
"ds_read_b64_tr_b16 a[88:89], v4 offset:40960\n"
"v_and_b32 v46, 0xffff0000, v120\n"
"v_mfma_f32_16x16x32_bf16 v[212:215], a[68:71], a[4:7], v[212:215]\n"
"buffer_load_dwordx4 a[36:39], v19, s[16:19], s47 offen sc1\n"
"v_add_f32 v112, v34, v22\n"
"v_mfma_f32_16x16x32_bf16 v[244:247], a[64:67], a[8:11], v[244:247]\n"
"ds_read_b64_tr_b16 a[90:91], v4 offset:41984\n"
"v_add_f32 v113, v46, v30\n"
"v_mfma_f32_16x16x32_bf16 v[244:247], a[68:71], a[12:15], v[244:247]\n"
"ds_read_b64_tr_b16 a[92:93], v4 offset:45056\n"
"v_lshlrev_b32 v34, 16, v121\n"
"v_mfma_f32_16x16x32_bf16 v[148:151], a[64:67], a[144:147], v[148:151]\n"
"ds_read_b64_tr_b16 a[94:95], v4 offset:46080\n"
"v_and_b32 v46, 0xffff0000, v121\n"
"v_mfma_f32_16x16x32_bf16 v[148:151], a[68:71], a[148:151], v[148:151]\n"
"ds_read_b64_tr_b16 a[48:49], v5 offset:0\n"
"v_add_f32 v114, v34, v24\n"
"v_mfma_f32_16x16x32_bf16 v[180:183], a[64:67], a[152:155], v[180:183]\n"
"buffer_load_dwordx4 a[40:43], v20, s[16:19], s47 offen sc1\n"
"v_add_f32 v115, v46, v31\n"
"v_mfma_f32_16x16x32_bf16 v[180:183], a[68:71], a[156:159], v[180:183]\n"
"ds_read_b64_tr_b16 a[50:51], v5 offset:1024\n"
"s_waitcnt lgkmcnt(10)\n"
"v_mfma_f32_16x16x32_bf16 v[196:199], a[72:75], a[0:3], v[196:199]\n"
"ds_read_b64_tr_b16 a[52:53], v5 offset:4096\n"
"v_lshlrev_b32 v34, 16, v122\n"
"v_mfma_f32_16x16x32_bf16 v[196:199], a[76:79], a[4:7], v[196:199]\n"
"ds_read_b64_tr_b16 a[54:55], v5 offset:5120\n"
"v_and_b32 v46, 0xffff0000, v122\n"
"v_mfma_f32_16x16x32_bf16 v[228:231], a[72:75], a[8:11], v[228:231]\n"
"ds_read_b64_tr_b16 a[56:57], v5 offset:40960\n"
"v_add_f32 v116, v34, v25\n"
"v_mfma_f32_16x16x32_bf16 v[228:231], a[76:79], a[12:15], v[228:231]\n"
"ds_read_b64_tr_b16 a[58:59], v5 offset:41984\n"
"s_waitcnt lgkmcnt(10)\n"
"v_mfma_f32_16x16x32_bf16 v[216:219], a[80:83], a[0:3], v[216:219]\n"
"buffer_load_dwordx4 a[44:47], v21, s[16:19], s47 offen sc1\n"
"v_add_f32 v117, v46, v32\n"
"v_mfma_f32_16x16x32_bf16 v[216:219], a[84:87], a[4:7], v[216:219]\n"
"s_add_u32 s47, s47, 32768\n"
"ds_read_b64_tr_b16 a[60:61], v5 offset:45056\n"
"v_mfma_f32_16x16x32_bf16 v[248:251], a[80:83], a[8:11], v[248:251]\n"
"ds_read_b64_tr_b16 a[62:63], v5 offset:46080\n"
"v_lshlrev_b32 v34, 16, v123\n"
"v_mfma_f32_16x16x32_bf16 v[248:251], a[84:87], a[12:15], v[248:251]\n"
"v_and_b32 v46, 0xffff0000, v123\n"
"v_add_f32 v118, v34, v40\n"
"v_mfma_f32_16x16x32_bf16 v[152:155], a[80:83], a[144:147], v[152:155]\n"
"buffer_load_dwordx4 a[184:187], v16, s[16:19], s54 offen sc1\n"
"v_add_f32 v119, v46, v33\n"
"v_mfma_f32_16x16x32_bf16 v[152:155], a[84:87], a[148:151], v[152:155]\n"
"v_lshlrev_b32 v34, 16, v124\n"
"v_and_b32 v46, 0xffff0000, v124\n"
"v_mfma_f32_16x16x32_bf16 v[184:187], a[80:83], a[152:155], v[184:187]\n"
"v_add_f32 v120, v34, v22\n"
"v_add_f32 v121, v46, v30\n"
"v_mfma_f32_16x16x32_bf16 v[184:187], a[84:87], a[156:159], v[184:187]\n"
"s_waitcnt vmcnt(15)\n"
"s_barrier\n"
"s_waitcnt lgkmcnt(8)\n"
"v_mfma_f32_16x16x32_bf16 v[200:203], a[88:91], a[0:3], v[200:203]\n"
"ds_read_b64_tr_b16 a[64:65], v2 offset:8192\n"
"v_lshlrev_b32_e64 v34, 16, v125\n"
"v_mfma_f32_16x16x32_bf16 v[200:203], a[92:95], a[4:7], v[200:203]\n"
"buffer_load_dwordx4 a[188:191], v17, s[16:19], s54 offen sc1\n"
"v_and_b32 v46, 0xffff0000, v125\n"
"v_mfma_f32_16x16x32_bf16 v[232:235], a[88:91], a[8:11], v[232:235]\n"
"s_add_u32 s54, s54, 32768\n"
"ds_read_b64_tr_b16 a[66:67], v2 offset:9216\n"
"v_mfma_f32_16x16x32_bf16 v[232:235], a[92:95], a[12:15], v[232:235]\n"
"ds_read_b64_tr_b16 a[68:69], v2 offset:12288\n"
"s_waitcnt lgkmcnt(7)\n"
"v_mfma_f32_16x16x32_bf16 v[220:223], a[48:51], a[0:3], v[220:223]\n"
"ds_read_b64_tr_b16 a[70:71], v2 offset:13312\n"
"v_add_f32 v122, v34, v24\n"
"v_mfma_f32_16x16x32_bf16 v[220:223], a[52:55], a[4:7], v[220:223]\n"
"ds_read_b64_tr_b16 a[72:73], v3 offset:8192\n"
"v_add_f32 v123, v46, v31\n"
"v_mfma_f32_16x16x32_bf16 v[252:255], a[48:51], a[8:11], v[252:255]\n"
"ds_read_b64_tr_b16 a[74:75], v3 offset:9216\n"
"v_lshlrev_b32 v34, 16, v126\n"
"v_mfma_f32_16x16x32_bf16 v[252:255], a[52:55], a[12:15], v[252:255]\n"
"ds_read_b64_tr_b16 a[76:77], v3 offset:12288\n"
"v_and_b32 v46, 0xffff0000, v126\n"
"v_mfma_f32_16x16x32_bf16 v[156:159], a[48:51], a[144:147], v[156:159]\n"
"ds_read_b64_tr_b16 a[78:79], v3 offset:13312\n"
"v_add_f32 v124, v34, v25\n"
"v_mfma_f32_16x16x32_bf16 v[156:159], a[52:55], a[148:151], v[156:159]\n"
"v_add_f32 v125, v46, v32\n"
"v_lshlrev_b32 v34, 16, v127\n"
"v_mfma_f32_16x16x32_bf16 v[188:191], a[48:51], a[152:155], v[188:191]\n"
"v_and_b32 v46, 0xffff0000, v127\n"
"v_add_f32 v126, v34, v40\n"
"v_mfma_f32_16x16x32_bf16 v[188:191], a[52:55], a[156:159], v[188:191]\n"
"v_add_f32 v127, v46, v33\n"
"s_waitcnt lgkmcnt(8)\n"
"v_mfma_f32_16x16x32_bf16 v[204:207], a[56:59], a[0:3], v[204:207]\n"
"v_and_b32 v18, 1, v0\n"
"v_lshlrev_b32 v18, 4, v18\n"
"v_mfma_f32_16x16x32_bf16 v[204:207], a[60:63], a[4:7], v[204:207]\n"
"v_mov_b32 v19, 0x3f80\n"
"v_lshlrev_b32 v18, v18, v19\n"
"v_mfma_f32_16x16x32_bf16 v[236:239], a[56:59], a[8:11], v[236:239]\n"
"v_bfe_u32 v19, v0, 1, 3\n"
"v_lshrrev_b32 v20, 4, v0\n"
"v_mfma_f32_16x16x32_bf16 v[236:239], a[60:63], a[12:15], v[236:239]\n"
"s_waitcnt vmcnt(8)\n"
"s_barrier\n"
"s_waitcnt lgkmcnt(4)\n"
"v_mfma_f32_16x16x32_bf16 v[208:211], a[64:67], a[16:19], v[208:211]\n"
"ds_read_b64_tr_b16 a[80:81], v2 offset:16384\n"
"v_lshlrev_b32 v20, 1, v20\n"
"v_mfma_f32_16x16x32_bf16 v[208:211], a[68:71], a[20:23], v[208:211]\n"
"ds_read_b64_tr_b16 a[82:83], v2 offset:17408\n"
"v_sub_u32 v19, v19, v20\n"
"v_mfma_f32_16x16x32_bf16 v[240:243], a[64:67], a[24:27], v[240:243]\n"
"ds_read_b64_tr_b16 a[84:85], v2 offset:20480\n"
"v_cmp_eq_u32 vcc, 0, v19\n"
"v_mfma_f32_16x16x32_bf16 v[240:243], a[68:71], a[28:31], v[240:243]\n"
"ds_read_b64_tr_b16 a[86:87], v2 offset:21504\n"
"v_mov_b32 v10, 0\n"
"v_mfma_f32_16x16x32_bf16 v[144:147], a[64:67], a[160:163], v[144:147]\n"
"ds_read_b64_tr_b16 a[88:89], v3 offset:16384\n"
"v_mov_b32 v11, 0\n"
"v_mfma_f32_16x16x32_bf16 v[144:147], a[68:71], a[164:167], v[144:147]\n"
"ds_read_b64_tr_b16 a[90:91], v3 offset:17408\n"
"v_cndmask_b32 v12, 0, v18, vcc\n"
"s_waitcnt vmcnt(6)\n"
"v_mfma_f32_16x16x32_bf16 v[176:179], a[64:67], a[168:171], v[176:179]\n"
"ds_read_b64_tr_b16 a[92:93], v3 offset:20480\n"
"v_cmp_eq_u32 vcc, 1, v19\n"
"v_mfma_f32_16x16x32_bf16 v[176:179], a[68:71], a[172:175], v[176:179]\n"
"ds_read_b64_tr_b16 a[94:95], v3 offset:21504\n"
"s_waitcnt lgkmcnt(8)\n"
"v_mfma_f32_16x16x32_bf16 v[212:215], a[72:75], a[16:19], v[212:215]\n"
"ds_read_b64_tr_b16 a[48:49], v4 offset:8192\n"
"v_mov_b32 v16, 0\n"
"v_mfma_f32_16x16x32_bf16 v[212:215], a[76:79], a[20:23], v[212:215]\n"
"ds_read_b64_tr_b16 a[50:51], v4 offset:9216\n"
"v_mov_b32 v17, 0\n"
"v_mfma_f32_16x16x32_bf16 v[244:247], a[72:75], a[24:27], v[244:247]\n"
"ds_read_b64_tr_b16 a[52:53], v4 offset:12288\n"
"v_cndmask_b32 v13, 0, v18, vcc\n"
"v_mfma_f32_16x16x32_bf16 v[244:247], a[76:79], a[28:31], v[244:247]\n"
"ds_read_b64_tr_b16 a[54:55], v4 offset:13312\n"
"v_mov_b32 v14, v12\n"
"v_mfma_f32_16x16x32_bf16 v[148:151], a[72:75], a[160:163], v[148:151]\n"
"v_mov_b32 v15, v13\n"
"v_mfma_f32_16x16x32_bf16 v[148:151], a[76:79], a[164:167], v[148:151]\n"
"v_mfma_f32_16x16x32_bf16 v[180:183], a[72:75], a[168:171], v[180:183]\n"
"ds_read_b64_tr_b16 a[56:57], v5 offset:8192\n"
"v_mfma_f32_16x16x32_bf16 v[180:183], a[76:79], a[172:175], v[180:183]\n"
"ds_read_b64_tr_b16 a[58:59], v5 offset:9216\n"
"s_waitcnt vmcnt(4) lgkmcnt(10)\n"
"v_mfma_f32_16x16x32_bf16 v[208:211], a[80:83], a[32:35], v[208:211]\n"
"ds_read_b64_tr_b16 a[60:61], v5 offset:12288\n"
"v_mfma_f32_16x16x32_bf16 v[208:211], a[84:87], a[36:39], v[208:211]\n"
"ds_read_b64_tr_b16 a[62:63], v5 offset:13312\n"
"s_waitcnt vmcnt(2)\n"
"v_mfma_f32_16x16x32_bf16 v[240:243], a[80:83], a[40:43], v[240:243]\n"
"v_mfma_f32_16x16x32_bf16 v[240:243], a[84:87], a[44:47], v[240:243]\n"
"v_mfma_f32_16x16x32_bf16 v[144:147], a[80:83], a[176:179], v[144:147]\n"
"v_mfma_f32_16x16x32_bf16 v[144:147], a[84:87], a[180:183], v[144:147]\n"
"s_waitcnt vmcnt(0)\n"
"v_mfma_f32_16x16x32_bf16 v[176:179], a[80:83], a[184:187], v[176:179]\n"
"ds_read_b64_tr_b16 a[64:65], v4 offset:16384\n"
"v_mfma_f32_16x16x32_bf16 v[176:179], a[84:87], a[188:191], v[176:179]\n"
"ds_read_b64_tr_b16 a[66:67], v4 offset:17408\n"
"s_waitcnt lgkmcnt(10)\n"
"v_mfma_f32_16x16x32_bf16 v[212:215], a[88:91], a[32:35], v[212:215]\n"
"ds_read_b64_tr_b16 a[68:69], v4 offset:20480\n"
"v_mfma_f32_16x16x32_bf16 v[212:215], a[92:95], a[36:39], v[212:215]\n"
"ds_read_b64_tr_b16 a[70:71], v4 offset:21504\n"
"v_mfma_f32_16x16x32_bf16 v[244:247], a[88:91], a[40:43], v[244:247]\n"
"v_mfma_f32_16x16x32_bf16 v[244:247], a[92:95], a[44:47], v[244:247]\n"
"v_mfma_f32_16x16x32_bf16 v[148:151], a[88:91], a[176:179], v[148:151]\n"
"v_mfma_f32_16x16x32_bf16 v[148:151], a[92:95], a[180:183], v[148:151]\n"
"v_cvt_pk_bf16_f32 v208, v208, v209\n"
"v_cvt_pk_bf16_f32 v209, v210, v211\n"
"v_mfma_f32_16x16x32_bf16 v[180:183], a[88:91], a[184:187], v[180:183]\n"
"ds_read_b64_tr_b16 a[72:73], v5 offset:16384\n"
"v_cvt_pk_bf16_f32 v210, v212, v213\n"
"v_mfma_f32_16x16x32_bf16 v[180:183], a[92:95], a[188:191], v[180:183]\n"
"ds_read_b64_tr_b16 a[74:75], v5 offset:17408\n"
"s_waitcnt lgkmcnt(10)\n"
"v_mfma_f32_16x16x32_bf16 v[216:219], a[48:51], a[16:19], v[216:219]\n"
"ds_read_b64_tr_b16 a[76:77], v5 offset:20480\n"
"v_cvt_pk_bf16_f32 v211, v214, v215\n"
"v_mfma_f32_16x16x32_bf16 v[216:219], a[52:55], a[20:23], v[216:219]\n"
"ds_read_b64_tr_b16 a[78:79], v5 offset:21504\n"
"v_cvt_pk_bf16_f32 v240, v240, v241\n"
"v_mfma_f32_16x16x32_bf16 v[248:251], a[48:51], a[24:27], v[248:251]\n"
"buffer_store_dwordx4 v[208:211], v1, s[16:19], s63 offen offset:0 sc1\n"
"v_cvt_pk_bf16_f32 v241, v242, v243\n"
"v_mfma_f32_16x16x32_bf16 v[248:251], a[52:55], a[28:31], v[248:251]\n"
"v_cvt_pk_bf16_f32 v242, v244, v245\n"
"v_cvt_pk_bf16_f32 v243, v246, v247\n"
"v_mfma_f32_16x16x32_bf16 v[152:155], a[48:51], a[160:163], v[152:155]\n"
"buffer_store_dwordx4 v[240:243], v1, s[16:19], s63 offen offset:2048 sc1\n"
"v_cvt_pk_bf16_f32 v147, v146, v147\n"
"v_mfma_f32_16x16x32_bf16 v[152:155], a[52:55], a[164:167], v[152:155]\n"
"v_cvt_pk_bf16_f32 v146, v144, v145\n"
"v_cvt_pk_bf16_f32 v144, v148, v149\n"
"v_mfma_f32_16x16x32_bf16 v[184:187], a[48:51], a[168:171], v[184:187]\n"
"ds_read_b64_tr_b16 a[80:81], v2 offset:49152\n"
"v_cvt_pk_bf16_f32 v145, v150, v151\n"
"v_mfma_f32_16x16x32_bf16 v[184:187], a[52:55], a[172:175], v[184:187]\n"
"ds_read_b64_tr_b16 a[82:83], v2 offset:50176\n"
"s_waitcnt lgkmcnt(10)\n"
"v_mfma_f32_16x16x32_bf16 v[220:223], a[56:59], a[16:19], v[220:223]\n"
"ds_read_b64_tr_b16 a[84:85], v2 offset:53248\n"
"v_cvt_pk_bf16_f32 v179, v178, v179\n"
"v_mfma_f32_16x16x32_bf16 v[220:223], a[60:63], a[20:23], v[220:223]\n"
"ds_read_b64_tr_b16 a[86:87], v2 offset:54272\n"
"v_cvt_pk_bf16_f32 v178, v176, v177\n"
"v_mfma_f32_16x16x32_bf16 v[252:255], a[56:59], a[24:27], v[252:255]\n"
"ds_read_b64_tr_b16 a[88:89], v3 offset:49152\n"
"v_cvt_pk_bf16_f32 v176, v180, v181\n"
"v_mfma_f32_16x16x32_bf16 v[252:255], a[60:63], a[28:31], v[252:255]\n"
"ds_read_b64_tr_b16 a[90:91], v3 offset:50176\n"
"v_cvt_pk_bf16_f32 v177, v182, v183\n"
"v_mfma_f32_16x16x32_bf16 v[156:159], a[56:59], a[160:163], v[156:159]\n"
"ds_read_b64_tr_b16 a[92:93], v3 offset:53248\n"
"v_mfma_f32_16x16x32_bf16 v[156:159], a[60:63], a[164:167], v[156:159]\n"
"ds_read_b64_tr_b16 a[94:95], v3 offset:54272\n"
"v_mfma_f32_16x16x32_bf16 v[188:191], a[56:59], a[168:171], v[188:191]\n"
"ds_read_b64_tr_b16 a[48:49], v6 offset:57344\n"
"v_mfma_f32_16x16x32_bf16 v[188:191], a[60:63], a[172:175], v[188:191]\n"
"ds_read_b64_tr_b16 a[50:51], v6 offset:58368\n"
"s_waitcnt lgkmcnt(14)\n"
"v_mfma_f32_16x16x32_bf16 v[216:219], a[64:67], a[32:35], v[216:219]\n"
"ds_read_b64_tr_b16 a[52:53], v6 offset:61440\n"
"v_mfma_f32_16x16x32_bf16 v[216:219], a[68:71], a[36:39], v[216:219]\n"
"ds_read_b64_tr_b16 a[54:55], v6 offset:62464\n"
"v_mfma_f32_16x16x32_bf16 v[248:251], a[64:67], a[40:43], v[248:251]\n"
"ds_read_b64_tr_b16 a[56:57], v7 offset:57344\n"
".Lpad14:\n"
"s_cbranch_execz .Lwr1\n"
"v_mfma_f32_16x16x32_bf16 v[248:251], a[68:71], a[44:47], v[248:251]\n"
"ds_read_b64_tr_b16 a[58:59], v7 offset:58368\n"
"v_mfma_f32_16x16x32_bf16 v[152:155], a[64:67], a[176:179], v[152:155]\n"
"ds_read_b64_tr_b16 a[60:61], v7 offset:61440\n"
"v_mfma_f32_16x16x32_bf16 v[152:155], a[68:71], a[180:183], v[152:155]\n"
"ds_read_b64_tr_b16 a[62:63], v7 offset:62464\n"
"v_mfma_f32_16x16x32_bf16 v[184:187], a[64:67], a[184:187], v[184:187]\n"
"buffer_store_dwordx4 v[144:147], v1, s[16:19], s62 offen offset:0 sc1\n"
"v_mfma_f32_16x16x32_bf16 v[184:187], a[68:71], a[188:191], v[184:187]\n"
"s_waitcnt lgkmcnt(15)\n"
"v_mfma_f32_16x16x32_bf16 v[220:223], a[72:75], a[32:35], v[220:223]\n"
"ds_read_b64_tr_b16 a[64:65], v4 offset:49152\n"
"v_mfma_f32_16x16x32_bf16 v[220:223], a[76:79], a[36:39], v[220:223]\n"
"ds_read_b64_tr_b16 a[66:67], v4 offset:50176\n"
"v_mfma_f32_16x16x32_bf16 v[252:255], a[72:75], a[40:43], v[252:255]\n"
"ds_read_b64_tr_b16 a[68:69], v4 offset:53248\n"
"v_mfma_f32_16x16x32_bf16 v[252:255], a[76:79], a[44:47], v[252:255]\n"
"ds_read_b64_tr_b16 a[70:71], v4 offset:54272\n"
"v_mfma_f32_16x16x32_bf16 v[156:159], a[72:75], a[176:179], v[156:159]\n"
"buffer_store_dwordx4 v[176:179], v1, s[16:19], s62 offen offset:2048 sc1\n"
"v_mfma_f32_16x16x32_bf16 v[156:159], a[76:79], a[180:183], v[156:159]\n"
"v_cvt_pk_bf16_f32 v216, v216, v217\n"
"v_cvt_pk_bf16_f32 v217, v218, v219\n"
"v_mfma_f32_16x16x32_bf16 v[188:191], a[72:75], a[184:187], v[188:191]\n"
"v_cvt_pk_bf16_f32 v218, v220, v221\n"
"v_cvt_pk_bf16_f32 v219, v222, v223\n"
"v_mfma_f32_16x16x32_bf16 v[188:191], a[76:79], a[188:191], v[188:191]\n"
"v_cvt_pk_bf16_f32 v248, v248, v249\n"
"s_waitcnt lgkmcnt(15)\n"
"v_mfma_f32_16x16x32_bf16 v[192:195], a[80:83], a[16:19], v[192:195]\n"
"ds_read_b64_tr_b16 a[72:73], v5 offset:49152\n"
"v_cvt_pk_bf16_f32 v249, v250, v251\n"
"v_mfma_f32_16x16x32_bf16 v[192:195], a[84:87], a[20:23], v[192:195]\n"
"ds_read_b64_tr_b16 a[74:75], v5 offset:50176\n"
"v_cvt_pk_bf16_f32 v250, v252, v253\n"
"v_mfma_f32_16x16x32_bf16 v[224:227], a[80:83], a[24:27], v[224:227]\n"
"ds_read_b64_tr_b16 a[76:77], v5 offset:53248\n"
"buffer_store_dwordx4 v[216:219], v1, s[16:19], s63 offen offset:1024 sc1\n"
"v_mfma_f32_16x16x32_bf16 v[224:227], a[84:87], a[28:31], v[224:227]\n"
"ds_read_b64_tr_b16 a[78:79], v5 offset:54272\n"
"s_waitcnt lgkmcnt(15)\n"
"v_mfma_f32_16x16x32_bf16 v[196:199], a[88:91], a[16:19], v[196:199]\n"
"ds_read_b64_tr_b16 a[80:81], v8 offset:57344\n"
"v_cvt_pk_bf16_f32 v251, v254, v255\n"
"v_mfma_f32_16x16x32_bf16 v[196:199], a[92:95], a[20:23], v[196:199]\n"
"ds_read_b64_tr_b16 a[82:83], v8 offset:58368\n"
"buffer_store_dwordx4 v[248:251], v1, s[16:19], s63 offen offset:3072 sc1\n"
"v_mfma_f32_16x16x32_bf16 v[228:231], a[88:91], a[24:27], v[228:231]\n"
"ds_read_b64_tr_b16 a[84:85], v8 offset:61440\n"
"v_cvt_pk_bf16_f32 v155, v154, v155\n"
"v_mfma_f32_16x16x32_bf16 v[228:231], a[92:95], a[28:31], v[228:231]\n"
"ds_read_b64_tr_b16 a[86:87], v8 offset:62464\n"
"s_waitcnt lgkmcnt(15)\n"
"v_mfma_f32_16x16x32_bf16 v[192:195], a[48:51], a[32:35], v[192:195]\n"
"ds_read_b64_tr_b16 a[88:89], v9 offset:57344\n"
"v_cvt_pk_bf16_f32 v154, v152, v153\n"
"v_mfma_f32_16x16x32_bf16 v[192:195], a[52:55], a[36:39], v[192:195]\n"
"ds_read_b64_tr_b16 a[90:91], v9 offset:58368\n"
"v_cvt_pk_bf16_f32 v152, v156, v157\n"
"v_mfma_f32_16x16x32_bf16 v[224:227], a[48:51], a[40:43], v[224:227]\n"
"ds_read_b64_tr_b16 a[92:93], v9 offset:61440\n"
"v_cvt_pk_bf16_f32 v153, v158, v159\n"
"v_mfma_f32_16x16x32_bf16 v[224:227], a[52:55], a[44:47], v[224:227]\n"
"ds_read_b64_tr_b16 a[94:95], v9 offset:62464\n"
"s_waitcnt lgkmcnt(15)\n"
"v_mfma_f32_16x16x32_bf16 v[196:199], a[56:59], a[32:35], v[196:199]\n"
"buffer_store_dwordx4 v[152:155], v1, s[16:19], s62 offen offset:1024 sc1\n"
"v_cvt_pk_bf16_f32 v187, v186, v187\n"
"v_mfma_f32_16x16x32_bf16 v[196:199], a[60:63], a[36:39], v[196:199]\n"
"v_cvt_pk_bf16_f32 v186, v184, v185\n"
"v_cvt_pk_bf16_f32 v184, v188, v189\n"
"v_mfma_f32_16x16x32_bf16 v[228:231], a[56:59], a[40:43], v[228:231]\n"
"v_cvt_pk_bf16_f32 v185, v190, v191\n"
"v_mfma_f32_16x16x32_bf16 v[228:231], a[60:63], a[44:47], v[228:231]\n"
"s_waitcnt lgkmcnt(12)\n"
"v_mfma_f32_16x16x32_bf16 v[200:203], a[64:67], a[16:19], v[200:203]\n"
"ds_read_b64_tr_b16 a[48:49], v6 offset:49152\n"
"buffer_store_dwordx4 v[184:187], v1, s[16:19], s62 offen offset:3072 sc1\n"
"v_mfma_f32_16x16x32_bf16 v[200:203], a[68:71], a[20:23], v[200:203]\n"
"ds_read_b64_tr_b16 a[50:51], v6 offset:50176\n"
"v_cvt_pk_bf16_f32 v192, v192, v193\n"
"v_mfma_f32_16x16x32_bf16 v[232:235], a[64:67], a[24:27], v[232:235]\n"
"ds_read_b64_tr_b16 a[52:53], v6 offset:53248\n"
"v_cvt_pk_bf16_f32 v193, v194, v195\n"
"v_mfma_f32_16x16x32_bf16 v[232:235], a[68:71], a[28:31], v[232:235]\n"
"ds_read_b64_tr_b16 a[54:55], v6 offset:54272\n"
"s_waitcnt lgkmcnt(12)\n"
"v_mfma_f32_16x16x32_bf16 v[204:207], a[72:75], a[16:19], v[204:207]\n"
"ds_read_b64_tr_b16 a[56:57], v6 offset:40960\n"
"v_cvt_pk_bf16_f32 v194, v196, v197\n"
"v_mfma_f32_16x16x32_bf16 v[204:207], a[76:79], a[20:23], v[204:207]\n"
"ds_read_b64_tr_b16 a[58:59], v6 offset:41984\n"
"v_cvt_pk_bf16_f32 v195, v198, v199\n"
"v_mfma_f32_16x16x32_bf16 v[236:239], a[72:75], a[24:27], v[236:239]\n"
"ds_read_b64_tr_b16 a[60:61], v6 offset:45056\n"
"buffer_store_dwordx4 v[192:195], v1, s[16:19], s61 offen offset:0 sc1\n"
"v_mfma_f32_16x16x32_bf16 v[236:239], a[76:79], a[28:31], v[236:239]\n"
"ds_read_b64_tr_b16 a[62:63], v6 offset:46080\n"
"s_waitcnt lgkmcnt(12)\n"
"v_mfma_f32_16x16x32_bf16 v[200:203], a[80:83], a[32:35], v[200:203]\n"
"ds_read_b64_tr_b16 a[64:65], v6 offset:32768\n"
"v_cvt_pk_bf16_f32 v224, v224, v225\n"
"v_mfma_f32_16x16x32_bf16 v[200:203], a[84:87], a[36:39], v[200:203]\n"
"ds_read_b64_tr_b16 a[66:67], v6 offset:33792\n"
"v_cvt_pk_bf16_f32 v225, v226, v227\n"
"v_mfma_f32_16x16x32_bf16 v[232:235], a[80:83], a[40:43], v[232:235]\n"
"ds_read_b64_tr_b16 a[68:69], v6 offset:36864\n"
"v_cvt_pk_bf16_f32 v226, v228, v229\n"
"v_mfma_f32_16x16x32_bf16 v[232:235], a[84:87], a[44:47], v[232:235]\n"
"ds_read_b64_tr_b16 a[70:71], v6 offset:37888\n"
"s_waitcnt lgkmcnt(12)\n"
"v_mfma_f32_16x16x32_bf16 v[204:207], a[88:91], a[32:35], v[204:207]\n"
"ds_read_b64_tr_b16 a[72:73], v6 offset:24576\n"
"v_cvt_pk_bf16_f32 v227, v230, v231\n"
"v_mfma_f32_16x16x32_bf16 v[204:207], a[92:95], a[36:39], v[204:207]\n"
"ds_read_b64_tr_b16 a[74:75], v6 offset:25600\n"
"buffer_store_dwordx4 v[224:227], v1, s[16:19], s61 offen offset:2048 sc1\n"
"v_mfma_f32_16x16x32_bf16 v[236:239], a[88:91], a[40:43], v[236:239]\n"
"ds_read_b64_tr_b16 a[76:77], v6 offset:28672\n"
"v_mfma_f32_16x16x32_bf16 v[236:239], a[92:95], a[44:47], v[236:239]\n"
"ds_read_b64_tr_b16 a[78:79], v6 offset:29696\n"
"s_waitcnt lgkmcnt(12)\n"
"v_mfma_f32_16x16x32_bf16 v[128:131], a[48:51], a[192:195], v[128:131]\n"
"ds_read_b64_tr_b16 a[80:81], v6 offset:16384\n"
"v_mfma_f32_16x16x32_bf16 v[128:131], a[52:55], a[196:199], v[128:131]\n"
"ds_read_b64_tr_b16 a[82:83], v6 offset:17408\n"
"v_cvt_pk_bf16_f32 v200, v200, v201\n"
"s_waitcnt lgkmcnt(10)\n"
"v_mfma_f32_16x16x32_bf16 v[128:131], a[56:59], a[208:211], v[128:131]\n"
"ds_read_b64_tr_b16 a[84:85], v6 offset:20480\n"
"v_cvt_pk_bf16_f32 v201, v202, v203\n"
"v_mfma_f32_16x16x32_bf16 v[128:131], a[60:63], a[212:215], v[128:131]\n"
"ds_read_b64_tr_b16 a[86:87], v6 offset:21504\n"
"v_mfma_f32_16x16x32_bf16 v[160:163], a[48:51], a[200:203], v[160:163]\n"
"ds_read_b64_tr_b16 a[88:89], v6 offset:8192\n"
"v_cvt_pk_bf16_f32 v202, v204, v205\n"
"v_mfma_f32_16x16x32_bf16 v[160:163], a[52:55], a[204:207], v[160:163]\n"
"ds_read_b64_tr_b16 a[90:91], v6 offset:9216\n"
"v_cvt_pk_bf16_f32 v203, v206, v207\n"
"v_mfma_f32_16x16x32_bf16 v[160:163], a[56:59], a[216:219], v[160:163]\n"
"ds_read_b64_tr_b16 a[92:93], v6 offset:12288\n"
"buffer_store_dwordx4 v[200:203], v1, s[16:19], s61 offen offset:1024 sc1\n"
"v_mfma_f32_16x16x32_bf16 v[160:163], a[60:63], a[220:223], v[160:163]\n"
"ds_read_b64_tr_b16 a[94:95], v6 offset:13312\n"
"s_waitcnt lgkmcnt(12)\n"
"v_mfma_f32_16x16x32_bf16 v[128:131], a[64:67], a[224:227], v[128:131]\n"
"ds_read_b64_tr_b16 a[48:49], v2 offset:57344\n"
"v_cvt_pk_bf16_f32 v232, v232, v233\n"
"v_mfma_f32_16x16x32_bf16 v[128:131], a[68:71], a[228:231], v[128:131]\n"
"ds_read_b64_tr_b16 a[50:51], v2 offset:58368\n"
"v_cvt_pk_bf16_f32 v233, v234, v235\n"
"s_waitcnt lgkmcnt(10)\n"
"v_mfma_f32_16x16x32_bf16 v[128:131], a[72:75], v[48:51], v[128:131]\n"
"ds_read_b64_tr_b16 a[52:53], v2 offset:61440\n"
"v_cvt_pk_bf16_f32 v234, v236, v237\n"
"v_mfma_f32_16x16x32_bf16 v[128:131], a[76:79], v[52:55], v[128:131]\n"
"ds_read_b64_tr_b16 a[54:55], v2 offset:62464\n"
"v_mfma_f32_16x16x32_bf16 v[160:163], a[64:67], a[232:235], v[160:163]\n"
"ds_read_b64_tr_b16 a[56:57], v2 offset:24576\n"
"v_cvt_pk_bf16_f32 v235, v238, v239\n"
"v_mfma_f32_16x16x32_bf16 v[160:163], a[68:71], a[236:239], v[160:163]\n"
"ds_read_b64_tr_b16 a[58:59], v2 offset:25600\n"
"buffer_store_dwordx4 v[232:235], v1, s[16:19], s61 offen offset:3072 sc1\n"
"v_mfma_f32_16x16x32_bf16 v[160:163], a[72:75], v[56:59], v[160:163]\n"
"ds_read_b64_tr_b16 a[60:61], v2 offset:28672\n"
"s_mov_b32 s68, 7\n"
"v_mfma_f32_16x16x32_bf16 v[160:163], a[76:79], v[60:63], v[160:163]\n"
"ds_read_b64_tr_b16 a[62:63], v2 offset:29696\n"
"s_waitcnt lgkmcnt(12)\n"
"v_mfma_f32_16x16x32_bf16 v[128:131], a[80:83], v[64:67], v[128:131]\n"
"ds_read_b64_tr_b16 a[64:65], v2 offset:32768\n"
"v_mfma_f32_16x16x32_bf16 v[128:131], a[84:87], v[68:71], v[128:131]\n"
"ds_read_b64_tr_b16 a[66:67], v2 offset:33792\n"
"s_waitcnt lgkmcnt(10)\n"
"v_mfma_f32_16x16x32_bf16 v[128:131], a[88:91], v[80:83], v[128:131]\n"
"ds_read_b64_tr_b16 a[68:69], v2 offset:36864\n"
"v_mfma_f32_16x16x32_bf16 v[128:131], a[92:95], v[84:87], v[128:131]\n"
"ds_read_b64_tr_b16 a[70:71], v2 offset:37888\n"
"v_mfma_f32_16x16x32_bf16 v[160:163], a[80:83], v[72:75], v[160:163]\n"
"ds_read_b64_tr_b16 a[72:73], v2 offset:40960\n"
"v_mfma_f32_16x16x32_bf16 v[160:163], a[84:87], v[76:79], v[160:163]\n"
"ds_read_b64_tr_b16 a[74:75], v2 offset:41984\n"
"v_mfma_f32_16x16x32_bf16 v[160:163], a[88:91], v[88:91], v[160:163]\n"
"ds_read_b64_tr_b16 a[76:77], v2 offset:45056\n"
"v_mfma_f32_16x16x32_bf16 v[160:163], a[92:95], v[92:95], v[160:163]\n"
"ds_read_b64_tr_b16 a[78:79], v2 offset:46080\n"
"s_waitcnt lgkmcnt(12)\n"
"v_mfma_f32_16x16x32_bf16 v[128:131], a[48:51], a[96:99], v[128:131]\n"
"ds_read_b64_tr_b16 a[80:81], v2 offset:49152\n"
"v_mfma_f32_16x16x32_bf16 v[128:131], a[52:55], a[100:103], v[128:131]\n"
"ds_read_b64_tr_b16 a[82:83], v2 offset:50176\n"
"s_waitcnt lgkmcnt(10)\n"
"v_mfma_f32_16x16x32_bf16 v[128:131], a[56:59], a[112:115], v[128:131]\n"
"ds_read_b64_tr_b16 a[84:85], v2 offset:53248\n"
"v_mfma_f32_16x16x32_bf16 v[128:131], a[60:63], a[116:119], v[128:131]\n"
"ds_read_b64_tr_b16 a[86:87], v2 offset:54272\n"
"v_mfma_f32_16x16x32_bf16 v[160:163], a[48:51], a[104:107], v[160:163]\n"
"ds_read_b64_tr_b16 a[88:89], v6 offset:57344\n"
"v_mfma_f32_16x16x32_bf16 v[160:163], a[52:55], a[108:111], v[160:163]\n"
"ds_read_b64_tr_b16 a[90:91], v6 offset:58368\n"
"v_mfma_f32_16x16x32_bf16 v[160:163], a[56:59], a[120:123], v[160:163]\n"
"ds_read_b64_tr_b16 a[92:93], v6 offset:61440\n"
"v_mfma_f32_16x16x32_bf16 v[160:163], a[60:63], a[124:127], v[160:163]\n"
"ds_read_b64_tr_b16 a[94:95], v6 offset:62464\n"
"s_waitcnt lgkmcnt(12)\n"
"v_mfma_f32_16x16x32_bf16 v[128:131], a[64:67], a[128:131], v[128:131]\n"
"ds_read_b64_tr_b16 a[48:49], v7 offset:49152\n"
"v_mfma_f32_16x16x32_bf16 v[128:131], a[68:71], a[132:135], v[128:131]\n"
"ds_read_b64_tr_b16 a[50:51], v7 offset:50176\n"
"buffer_load_dword v35, v39, s[16:19], 0 offen sc1\n"
"s_waitcnt lgkmcnt(10)\n"
"v_mfma_f32_16x16x32_bf16 v[128:131], a[72:75], a[144:147], v[128:131]\n"
"ds_read_b64_tr_b16 a[52:53], v7 offset:53248\n"
"v_mfma_f32_16x16x32_bf16 v[128:131], a[76:79], a[148:151], v[128:131]\n"
"ds_read_b64_tr_b16 a[54:55], v7 offset:54272\n"
"v_mfma_f32_16x16x32_bf16 v[160:163], a[64:67], a[136:139], v[160:163]\n"
"ds_read_b64_tr_b16 a[56:57], v7 offset:40960\n"
"v_mfma_f32_16x16x32_bf16 v[160:163], a[68:71], a[140:143], v[160:163]\n"
"ds_read_b64_tr_b16 a[58:59], v7 offset:41984\n"
"v_mfma_f32_16x16x32_bf16 v[160:163], a[72:75], a[152:155], v[160:163]\n"
"ds_read_b64_tr_b16 a[60:61], v7 offset:45056\n"
"v_mfma_f32_16x16x32_bf16 v[160:163], a[76:79], a[156:159], v[160:163]\n"
"ds_read_b64_tr_b16 a[62:63], v7 offset:46080\n"
"s_waitcnt lgkmcnt(12)\n"
"v_mfma_f32_16x16x32_bf16 v[128:131], a[80:83], a[160:163], v[128:131]\n"
"ds_read_b64_tr_b16 a[64:65], v7 offset:32768\n"
"v_mfma_f32_16x16x32_bf16 v[128:131], a[84:87], a[164:167], v[128:131]\n"
"ds_read_b64_tr_b16 a[66:67], v7 offset:33792\n"
"s_waitcnt lgkmcnt(10)\n"
"v_mfma_f32_16x16x32_bf16 v[128:131], a[88:91], a[176:179], v[128:131]\n"
"ds_read_b64_tr_b16 a[68:69], v7 offset:36864\n"
"v_mfma_f32_16x16x32_bf16 v[128:131], a[92:95], a[180:183], v[128:131]\n"
"ds_read_b64_tr_b16 a[70:71], v7 offset:37888\n"
"v_mfma_f32_16x16x32_bf16 v[160:163], a[80:83], a[168:171], v[160:163]\n"
"ds_read_b64_tr_b16 a[72:73], v7 offset:24576\n"
"v_mfma_f32_16x16x32_bf16 v[160:163], a[84:87], a[172:175], v[160:163]\n"
"ds_read_b64_tr_b16 a[74:75], v7 offset:25600\n"
"v_mfma_f32_16x16x32_bf16 v[160:163], a[88:91], a[184:187], v[160:163]\n"
"ds_read_b64_tr_b16 a[76:77], v7 offset:28672\n"
"v_mfma_f32_16x16x32_bf16 v[160:163], a[92:95], a[188:191], v[160:163]\n"
"ds_read_b64_tr_b16 a[78:79], v7 offset:29696\n"
"s_waitcnt lgkmcnt(12)\n"
"v_mfma_f32_16x16x32_bf16 v[132:135], a[48:51], a[192:195], v[132:135]\n"
"ds_read_b64_tr_b16 a[80:81], v7 offset:16384\n"
"v_mfma_f32_16x16x32_bf16 v[132:135], a[52:55], a[196:199], v[132:135]\n"
"ds_read_b64_tr_b16 a[82:83], v7 offset:17408\n"
"s_waitcnt lgkmcnt(10)\n"
"v_mfma_f32_16x16x32_bf16 v[132:135], a[56:59], a[208:211], v[132:135]\n"
"ds_read_b64_tr_b16 a[84:85], v7 offset:20480\n"
"v_mfma_f32_16x16x32_bf16 v[132:135], a[60:63], a[212:215], v[132:135]\n"
"ds_read_b64_tr_b16 a[86:87], v7 offset:21504\n"
"v_mfma_f32_16x16x32_bf16 v[164:167], a[48:51], a[200:203], v[164:167]\n"
"ds_read_b64_tr_b16 a[88:89], v7 offset:8192\n"
"v_mfma_f32_16x16x32_bf16 v[164:167], a[52:55], a[204:207], v[164:167]\n"
"ds_read_b64_tr_b16 a[90:91], v7 offset:9216\n"
"v_mfma_f32_16x16x32_bf16 v[164:167], a[56:59], a[216:219], v[164:167]\n"
"ds_read_b64_tr_b16 a[92:93], v7 offset:12288\n"
"v_mfma_f32_16x16x32_bf16 v[164:167], a[60:63], a[220:223], v[164:167]\n"
"ds_read_b64_tr_b16 a[94:95], v7 offset:13312\n"
"s_waitcnt lgkmcnt(12)\n"
"v_mfma_f32_16x16x32_bf16 v[132:135], a[64:67], a[224:227], v[132:135]\n"
"ds_read_b64_tr_b16 a[48:49], v3 offset:57344\n"
"v_mfma_f32_16x16x32_bf16 v[132:135], a[68:71], a[228:231], v[132:135]\n"
"ds_read_b64_tr_b16 a[50:51], v3 offset:58368\n"
"s_waitcnt lgkmcnt(10)\n"
"v_mfma_f32_16x16x32_bf16 v[132:135], a[72:75], v[48:51], v[132:135]\n"
"ds_read_b64_tr_b16 a[52:53], v3 offset:61440\n"
"v_mfma_f32_16x16x32_bf16 v[132:135], a[76:79], v[52:55], v[132:135]\n"
"ds_read_b64_tr_b16 a[54:55], v3 offset:62464\n"
"v_mfma_f32_16x16x32_bf16 v[164:167], a[64:67], a[232:235], v[164:167]\n"
"ds_read_b64_tr_b16 a[56:57], v3 offset:24576\n"
"v_mfma_f32_16x16x32_bf16 v[164:167], a[68:71], a[236:239], v[164:167]\n"
"ds_read_b64_tr_b16 a[58:59], v3 offset:25600\n"
"v_mfma_f32_16x16x32_bf16 v[164:167], a[72:75], v[56:59], v[164:167]\n"
"ds_read_b64_tr_b16 a[60:61], v3 offset:28672\n"
"v_mfma_f32_16x16x32_bf16 v[164:167], a[76:79], v[60:63], v[164:167]\n"
"ds_read_b64_tr_b16 a[62:63], v3 offset:29696\n"
"s_waitcnt vmcnt(0)\n"
"buffer_store_dword v23, v38, s[16:19], 0 offen sc1\n"
"s_waitcnt lgkmcnt(12)\n"
"v_mfma_f32_16x16x32_bf16 v[132:135], a[80:83], v[64:67], v[132:135]\n"
"ds_read_b64_tr_b16 a[64:65], v3 offset:32768\n"
"v_cmp_eq_u32 vcc, 0, v35\n"
"v_mfma_f32_16x16x32_bf16 v[132:135], a[84:87], v[68:71], v[132:135]\n"
"ds_read_b64_tr_b16 a[66:67], v3 offset:33792\n"
"s_waitcnt lgkmcnt(10)\n"
"v_mfma_f32_16x16x32_bf16 v[132:135], a[88:91], v[80:83], v[132:135]\n"
"ds_read_b64_tr_b16 a[68:69], v3 offset:36864\n"
"s_andn2_b32 s68, s68, vcc_lo\n"
"v_mfma_f32_16x16x32_bf16 v[132:135], a[92:95], v[84:87], v[132:135]\n"
"ds_read_b64_tr_b16 a[70:71], v3 offset:37888\n"
"s_bitcmp0_b32 s68, 0\n"
"s_cselect_b32 s69, s60, 2130706432\n"
"v_mfma_f32_16x16x32_bf16 v[164:167], a[80:83], v[72:75], v[164:167]\n"
"ds_read_b64_tr_b16 a[72:73], v3 offset:40960\n"
"buffer_load_dwordx4 v[192:195], v1, s[16:19], s69 offen offset:0 sc1\n"
"v_mfma_f32_16x16x32_bf16 v[164:167], a[84:87], v[76:79], v[164:167]\n"
"ds_read_b64_tr_b16 a[74:75], v3 offset:41984\n"
"v_mfma_f32_16x16x32_bf16 v[164:167], a[88:91], v[88:91], v[164:167]\n"
"ds_read_b64_tr_b16 a[76:77], v3 offset:45056\n"
"v_mfma_f32_16x16x32_bf16 v[164:167], a[92:95], v[92:95], v[164:167]\n"
"ds_read_b64_tr_b16 a[78:79], v3 offset:46080\n"
"s_waitcnt lgkmcnt(12)\n"
"v_mfma_f32_16x16x32_bf16 v[132:135], a[48:51], a[96:99], v[132:135]\n"
"ds_read_b64_tr_b16 a[80:81], v3 offset:49152\n"
"buffer_load_dwordx4 v[204:207], v1, s[16:19], s69 offen offset:1024 sc1\n"
"v_mfma_f32_16x16x32_bf16 v[132:135], a[52:55], a[100:103], v[132:135]\n"
"ds_read_b64_tr_b16 a[82:83], v3 offset:50176\n"
"s_waitcnt lgkmcnt(10)\n"
"v_mfma_f32_16x16x32_bf16 v[132:135], a[56:59], a[112:115], v[132:135]\n"
"ds_read_b64_tr_b16 a[84:85], v3 offset:53248\n"
"v_mfma_f32_16x16x32_bf16 v[132:135], a[60:63], a[116:119], v[132:135]\n"
"ds_read_b64_tr_b16 a[86:87], v3 offset:54272\n"
"v_mfma_f32_16x16x32_bf16 v[164:167], a[48:51], a[104:107], v[164:167]\n"
"ds_read_b64_tr_b16 a[88:89], v7 offset:57344\n"
"buffer_load_dwordx4 v[216:219], v1, s[16:19], s69 offen offset:2048 sc1\n"
"v_mfma_f32_16x16x32_bf16 v[164:167], a[52:55], a[108:111], v[164:167]\n"
"ds_read_b64_tr_b16 a[90:91], v7 offset:58368\n"
"v_mfma_f32_16x16x32_bf16 v[164:167], a[56:59], a[120:123], v[164:167]\n"
"ds_read_b64_tr_b16 a[92:93], v7 offset:61440\n"
"v_mfma_f32_16x16x32_bf16 v[164:167], a[60:63], a[124:127], v[164:167]\n"
"ds_read_b64_tr_b16 a[94:95], v7 offset:62464\n"
"s_waitcnt lgkmcnt(12)\n"
"v_mfma_f32_16x16x32_bf16 v[132:135], a[64:67], a[128:131], v[132:135]\n"
"ds_read_b64_tr_b16 a[48:49], v8 offset:49152\n"
"buffer_load_dwordx4 v[228:231], v1, s[16:19], s69 offen offset:3072 sc1\n"
"v_mfma_f32_16x16x32_bf16 v[132:135], a[68:71], a[132:135], v[132:135]\n"
"ds_read_b64_tr_b16 a[50:51], v8 offset:50176\n"
"s_waitcnt lgkmcnt(10)\n"
"v_mfma_f32_16x16x32_bf16 v[132:135], a[72:75], a[144:147], v[132:135]\n"
"ds_read_b64_tr_b16 a[52:53], v8 offset:53248\n"
"v_mfma_f32_16x16x32_bf16 v[132:135], a[76:79], a[148:151], v[132:135]\n"
"ds_read_b64_tr_b16 a[54:55], v8 offset:54272\n"
"s_bitcmp0_b32 s68, 1\n"
"s_cselect_b32 s69, s65, 2130706432\n"
"v_mfma_f32_16x16x32_bf16 v[164:167], a[64:67], a[136:139], v[164:167]\n"
"ds_read_b64_tr_b16 a[56:57], v8 offset:40960\n"
"buffer_load_dwordx4 v[196:199], v1, s[16:19], s69 offen offset:0 sc1\n"
"v_mfma_f32_16x16x32_bf16 v[164:167], a[68:71], a[140:143], v[164:167]\n"
"ds_read_b64_tr_b16 a[58:59], v8 offset:41984\n"
"v_mfma_f32_16x16x32_bf16 v[164:167], a[72:75], a[152:155], v[164:167]\n"
"ds_read_b64_tr_b16 a[60:61], v8 offset:45056\n"
"v_mfma_f32_16x16x32_bf16 v[164:167], a[76:79], a[156:159], v[164:167]\n"
"ds_read_b64_tr_b16 a[62:63], v8 offset:46080\n"
"s_waitcnt lgkmcnt(12)\n"
"v_mfma_f32_16x16x32_bf16 v[132:135], a[80:83], a[160:163], v[132:135]\n"
"ds_read_b64_tr_b16 a[64:65], v8 offset:32768\n"
"buffer_load_dwordx4 v[208:211], v1, s[16:19], s69 offen offset:1024 sc1\n"
"v_mfma_f32_16x16x32_bf16 v[132:135], a[84:87], a[164:167], v[132:135]\n"
"ds_read_b64_tr_b16 a[66:67], v8 offset:33792\n"
"s_waitcnt lgkmcnt(10)\n"
"v_mfma_f32_16x16x32_bf16 v[132:135], a[88:91], a[176:179], v[132:135]\n"
"ds_read_b64_tr_b16 a[68:69], v8 offset:36864\n"
"v_mfma_f32_16x16x32_bf16 v[132:135], a[92:95], a[180:183], v[132:135]\n"
"ds_read_b64_tr_b16 a[70:71], v8 offset:37888\n"
"v_mfma_f32_16x16x32_bf16 v[164:167], a[80:83], a[168:171], v[164:167]\n"
"ds_read_b64_tr_b16 a[72:73], v8 offset:24576\n"
"buffer_load_dwordx4 v[220:223], v1, s[16:19], s69 offen offset:2048 sc1\n"
"v_mfma_f32_16x16x32_bf16 v[164:167], a[84:87], a[172:175], v[164:167]\n"
"ds_read_b64_tr_b16 a[74:75], v8 offset:25600\n"
"v_mfma_f32_16x16x32_bf16 v[164:167], a[88:91], a[184:187], v[164:167]\n"
"ds_read_b64_tr_b16 a[76:77], v8 offset:28672\n"
"v_mfma_f32_16x16x32_bf16 v[164:167], a[92:95], a[188:191], v[164:167]\n"
"ds_read_b64_tr_b16 a[78:79], v8 offset:29696\n"
"s_waitcnt lgkmcnt(12)\n"
"v_mfma_f32_16x16x32_bf16 v[136:139], a[48:51], a[192:195], v[136:139]\n"
"ds_read_b64_tr_b16 a[80:81], v8 offset:16384\n"
"buffer_load_dwordx4 v[232:235], v1, s[16:19], s69 offen offset:3072 sc1\n"
"v_mfma_f32_16x16x32_bf16 v[136:139], a[52:55], a[196:199], v[136:139]\n"
"ds_read_b64_tr_b16 a[82:83], v8 offset:17408\n"
"s_waitcnt lgkmcnt(10)\n"
"v_mfma_f32_16x16x32_bf16 v[136:139], a[56:59], a[208:211], v[136:139]\n"
"ds_read_b64_tr_b16 a[84:85], v8 offset:20480\n"
"v_mfma_f32_16x16x32_bf16 v[136:139], a[60:63], a[212:215], v[136:139]\n"
"ds_read_b64_tr_b16 a[86:87], v8 offset:21504\n"
"s_bitcmp0_b32 s68, 2\n"
"s_cselect_b32 s69, s66, 2130706432\n"
"v_mfma_f32_16x16x32_bf16 v[168:171], a[48:51], a[200:203], v[168:171]\n"
"ds_read_b64_tr_b16 a[88:89], v8 offset:8192\n"
"buffer_load_dwordx4 v[200:203], v1, s[16:19], s69 offen offset:0 sc1\n"
"v_mfma_f32_16x16x32_bf16 v[168:171], a[52:55], a[204:207], v[168:171]\n"
"ds_read_b64_tr_b16 a[90:91], v8 offset:9216\n"
"v_mfma_f32_16x16x32_bf16 v[168:171], a[56:59], a[216:219], v[168:171]\n"
"ds_read_b64_tr_b16 a[92:93], v8 offset:12288\n"
"v_mfma_f32_16x16x32_bf16 v[168:171], a[60:63], a[220:223], v[168:171]\n"
"ds_read_b64_tr_b16 a[94:95], v8 offset:13312\n"
"s_waitcnt lgkmcnt(12)\n"
"v_mfma_f32_16x16x32_bf16 v[136:139], a[64:67], a[224:227], v[136:139]\n"
"ds_read_b64_tr_b16 a[48:49], v4 offset:57344\n"
"buffer_load_dwordx4 v[212:215], v1, s[16:19], s69 offen offset:1024 sc1\n"
"v_mfma_f32_16x16x32_bf16 v[136:139], a[68:71], a[228:231], v[136:139]\n"
"ds_read_b64_tr_b16 a[50:51], v4 offset:58368\n"
"s_waitcnt lgkmcnt(10)\n"
"v_mfma_f32_16x16x32_bf16 v[136:139], a[72:75], v[48:51], v[136:139]\n"
"ds_read_b64_tr_b16 a[52:53], v4 offset:61440\n"
"v_mfma_f32_16x16x32_bf16 v[136:139], a[76:79], v[52:55], v[136:139]\n"
"ds_read_b64_tr_b16 a[54:55], v4 offset:62464\n"
"v_mfma_f32_16x16x32_bf16 v[168:171], a[64:67], a[232:235], v[168:171]\n"
"ds_read_b64_tr_b16 a[56:57], v4 offset:24576\n"
"buffer_load_dwordx4 v[224:227], v1, s[16:19], s69 offen offset:2048 sc1\n"
"v_mfma_f32_16x16x32_bf16 v[168:171], a[68:71], a[236:239], v[168:171]\n"
"ds_read_b64_tr_b16 a[58:59], v4 offset:25600\n"
"v_mfma_f32_16x16x32_bf16 v[168:171], a[72:75], v[56:59], v[168:171]\n"
"ds_read_b64_tr_b16 a[60:61], v4 offset:28672\n"
"v_mfma_f32_16x16x32_bf16 v[168:171], a[76:79], v[60:63], v[168:171]\n"
"ds_read_b64_tr_b16 a[62:63], v4 offset:29696\n"
"s_waitcnt lgkmcnt(12)\n"
"v_mfma_f32_16x16x32_bf16 v[136:139], a[80:83], v[64:67], v[136:139]\n"
"ds_read_b64_tr_b16 a[64:65], v4 offset:32768\n"
"buffer_load_dwordx4 v[236:239], v1, s[16:19], s69 offen offset:3072 sc1\n"
"v_mfma_f32_16x16x32_bf16 v[136:139], a[84:87], v[68:71], v[136:139]\n"
"ds_read_b64_tr_b16 a[66:67], v4 offset:33792\n"
"s_waitcnt lgkmcnt(10)\n"
"v_mfma_f32_16x16x32_bf16 v[136:139], a[88:91], v[80:83], v[136:139]\n"
"ds_read_b64_tr_b16 a[68:69], v4 offset:36864\n"
"v_mfma_f32_16x16x32_bf16 v[136:139], a[92:95], v[84:87], v[136:139]\n"
"ds_read_b64_tr_b16 a[70:71], v4 offset:37888\n"
"v_mfma_f32_16x16x32_bf16 v[168:171], a[80:83], v[72:75], v[168:171]\n"
"ds_read_b64_tr_b16 a[72:73], v4 offset:40960\n"
"v_mfma_f32_16x16x32_bf16 v[168:171], a[84:87], v[76:79], v[168:171]\n"
"ds_read_b64_tr_b16 a[74:75], v4 offset:41984\n"
"v_mfma_f32_16x16x32_bf16 v[168:171], a[88:91], v[88:91], v[168:171]\n"
"ds_read_b64_tr_b16 a[76:77], v4 offset:45056\n"
"v_mfma_f32_16x16x32_bf16 v[168:171], a[92:95], v[92:95], v[168:171]\n"
"ds_read_b64_tr_b16 a[78:79], v4 offset:46080\n"
"s_waitcnt lgkmcnt(12)\n"
"v_mfma_f32_16x16x32_bf16 v[136:139], a[48:51], a[96:99], v[136:139]\n"
"ds_read_b64_tr_b16 a[80:81], v4 offset:49152\n"
"v_mfma_f32_16x16x32_bf16 v[136:139], a[52:55], a[100:103], v[136:139]\n"
"ds_read_b64_tr_b16 a[82:83], v4 offset:50176\n"
"s_waitcnt lgkmcnt(10)\n"
"v_mfma_f32_16x16x32_bf16 v[136:139], a[56:59], a[112:115], v[136:139]\n"
"ds_read_b64_tr_b16 a[84:85], v4 offset:53248\n"
"v_mfma_f32_16x16x32_bf16 v[136:139], a[60:63], a[116:119], v[136:139]\n"
"ds_read_b64_tr_b16 a[86:87], v4 offset:54272\n"
"s_cmp_eq_u32 s68, 0\n"
"s_cbranch_scc1 .Lrxp\n"
"buffer_load_dword v35, v39, s[16:19], 0 offen sc1\n"
".Lrxp:\n"
"v_mfma_f32_16x16x32_bf16 v[168:171], a[48:51], a[104:107], v[168:171]\n"
"ds_read_b64_tr_b16 a[88:89], v8 offset:57344\n"
"v_mfma_f32_16x16x32_bf16 v[168:171], a[52:55], a[108:111], v[168:171]\n"
"ds_read_b64_tr_b16 a[90:91], v8 offset:58368\n"
"v_mfma_f32_16x16x32_bf16 v[168:171], a[56:59], a[120:123], v[168:171]\n"
"ds_read_b64_tr_b16 a[92:93], v8 offset:61440\n"
"v_mfma_f32_16x16x32_bf16 v[168:171], a[60:63], a[124:127], v[168:171]\n"
"ds_read_b64_tr_b16 a[94:95], v8 offset:62464\n"
"s_waitcnt lgkmcnt(12)\n"
"v_mfma_f32_16x16x32_bf16 v[136:139], a[64:67], a[128:131], v[136:139]\n"
"ds_read_b64_tr_b16 a[48:49], v9 offset:49152\n"
"v_mfma_f32_16x16x32_bf16 v[136:139], a[68:71], a[132:135], v[136:139]\n"
"ds_read_b64_tr_b16 a[50:51], v9 offset:50176\n"
"s_waitcnt lgkmcnt(10)\n"
"v_mfma_f32_16x16x32_bf16 v[136:139], a[72:75], a[144:147], v[136:139]\n"
"ds_read_b64_tr_b16 a[52:53], v9 offset:53248\n"
"v_mfma_f32_16x16x32_bf16 v[136:139], a[76:79], a[148:151], v[136:139]\n"
"ds_read_b64_tr_b16 a[54:55], v9 offset:54272\n"
"v_mfma_f32_16x16x32_bf16 v[168:171], a[64:67], a[136:139], v[168:171]\n"
"ds_read_b64_tr_b16 a[56:57], v9 offset:40960\n"
"v_mfma_f32_16x16x32_bf16 v[168:171], a[68:71], a[140:143], v[168:171]\n"
"ds_read_b64_tr_b16 a[58:59], v9 offset:41984\n"
"v_mfma_f32_16x16x32_bf16 v[168:171], a[72:75], a[152:155], v[168:171]\n"
"ds_read_b64_tr_b16 a[60:61], v9 offset:45056\n"
"v_mfma_f32_16x16x32_bf16 v[168:171], a[76:79], a[156:159], v[168:171]\n"
"ds_read_b64_tr_b16 a[62:63], v9 offset:46080\n"
"s_waitcnt lgkmcnt(12)\n"
"v_mfma_f32_16x16x32_bf16 v[136:139], a[80:83], a[160:163], v[136:139]\n"
"ds_read_b64_tr_b16 a[64:65], v9 offset:32768\n"
"v_mfma_f32_16x16x32_bf16 v[136:139], a[84:87], a[164:167], v[136:139]\n"
"ds_read_b64_tr_b16 a[66:67], v9 offset:33792\n"
"s_waitcnt lgkmcnt(10)\n"
"v_mfma_f32_16x16x32_bf16 v[136:139], a[88:91], a[176:179], v[136:139]\n"
"ds_read_b64_tr_b16 a[68:69], v9 offset:36864\n"
"v_mfma_f32_16x16x32_bf16 v[136:139], a[92:95], a[180:183], v[136:139]\n"
"ds_read_b64_tr_b16 a[70:71], v9 offset:37888\n"
"v_mfma_f32_16x16x32_bf16 v[168:171], a[80:83], a[168:171], v[168:171]\n"
"ds_read_b64_tr_b16 a[72:73], v9 offset:24576\n"
"v_mfma_f32_16x16x32_bf16 v[168:171], a[84:87], a[172:175], v[168:171]\n"
"ds_read_b64_tr_b16 a[74:75], v9 offset:25600\n"
"v_mfma_f32_16x16x32_bf16 v[168:171], a[88:91], a[184:187], v[168:171]\n"
"ds_read_b64_tr_b16 a[76:77], v9 offset:28672\n"
"v_mfma_f32_16x16x32_bf16 v[168:171], a[92:95], a[188:191], v[168:171]\n"
"ds_read_b64_tr_b16 a[78:79], v9 offset:29696\n"
"s_waitcnt lgkmcnt(12)\n"
"v_mfma_f32_16x16x32_bf16 v[140:143], a[48:51], a[192:195], v[140:143]\n"
"ds_read_b64_tr_b16 a[80:81], v9 offset:16384\n"
"v_mfma_f32_16x16x32_bf16 v[140:143], a[52:55], a[196:199], v[140:143]\n"
"ds_read_b64_tr_b16 a[82:83], v9 offset:17408\n"
"s_waitcnt lgkmcnt(10)\n"
"v_mfma_f32_16x16x32_bf16 v[140:143], a[56:59], a[208:211], v[140:143]\n"
"ds_read_b64_tr_b16 a[84:85], v9 offset:20480\n"
"v_mfma_f32_16x16x32_bf16 v[140:143], a[60:63], a[212:215], v[140:143]\n"
"ds_read_b64_tr_b16 a[86:87], v9 offset:21504\n"
"v_mfma_f32_16x16x32_bf16 v[172:175], a[48:51], a[200:203], v[172:175]\n"
"ds_read_b64_tr_b16 a[88:89], v9 offset:8192\n"
"v_mfma_f32_16x16x32_bf16 v[172:175], a[52:55], a[204:207], v[172:175]\n"
"ds_read_b64_tr_b16 a[90:91], v9 offset:9216\n"
"v_mfma_f32_16x16x32_bf16 v[172:175], a[56:59], a[216:219], v[172:175]\n"
"ds_read_b64_tr_b16 a[92:93], v9 offset:12288\n"
"v_mfma_f32_16x16x32_bf16 v[172:175], a[60:63], a[220:223], v[172:175]\n"
"ds_read_b64_tr_b16 a[94:95], v9 offset:13312\n"
"s_waitcnt lgkmcnt(12)\n"
"v_mfma_f32_16x16x32_bf16 v[140:143], a[64:67], a[224:227], v[140:143]\n"
"ds_read_b64_tr_b16 a[48:49], v5 offset:57344\n"
"v_mfma_f32_16x16x32_bf16 v[140:143], a[68:71], a[228:231], v[140:143]\n"
"ds_read_b64_tr_b16 a[50:51], v5 offset:58368\n"
"s_cmp_eq_u32 s68, 0\n"
"s_mov_b32 s72, 0\n"
"s_cbranch_scc1 .Lrxd\n"
".Lrxl:\n"
"s_waitcnt vmcnt(0)\n"
"v_cmp_eq_u32 vcc, 0, v35\n"
"s_nop 1\n"
"s_and_b32 s69, vcc_lo, s68\n"
"s_xor_b32 s68, s68, s69\n"
"s_bitcmp0_b32 s69, 0\n"
"s_cbranch_scc1 .Lrxb0\n"
"buffer_load_dwordx4 v[192:195], v1, s[16:19], s60 offen offset:0 sc1\n"
"buffer_load_dwordx4 v[204:207], v1, s[16:19], s60 offen offset:1024 sc1\n"
"buffer_load_dwordx4 v[216:219], v1, s[16:19], s60 offen offset:2048 sc1\n"
"buffer_load_dwordx4 v[228:231], v1, s[16:19], s60 offen offset:3072 sc1\n"
".Lrxb0:\n"
"s_bitcmp0_b32 s69, 1\n"
"s_cbranch_scc1 .Lrxb1\n"
"buffer_load_dwordx4 v[196:199], v1, s[16:19], s65 offen offset:0 sc1\n"
"buffer_load_dwordx4 v[208:211], v1, s[16:19], s65 offen offset:1024 sc1\n"
"buffer_load_dwordx4 v[220:223], v1, s[16:19], s65 offen offset:2048 sc1\n"
"buffer_load_dwordx4 v[232:235], v1, s[16:19], s65 offen offset:3072 sc1\n"
".Lrxb1:\n"
"s_bitcmp0_b32 s69, 2\n"
"s_cbranch_scc1 .Lrxb2\n"
"buffer_load_dwordx4 v[200:203], v1, s[16:19], s66 offen offset:0 sc1\n"
"buffer_load_dwordx4 v[212:215], v1, s[16:19], s66 offen offset:1024 sc1\n"
"buffer_load_dwordx4 v[224:227], v1, s[16:19], s66 offen offset:2048 sc1\n"
"buffer_load_dwordx4 v[236:239], v1, s[16:19], s66 offen offset:3072 sc1\n"
".Lrxb2:\n"
"s_cmp_eq_u32 s68, 0\n"
"s_cbranch_scc1 .Lrxd\n"
"buffer_load_dword v35, v39, s[16:19], 0 offen sc1\n"
"s_add_u32 s72, s72, 1\n"
"s_cmp_lt_u32 s72, 262144\n"
"s_cbranch_scc1 .Lrxl\n"
".Lrxd:\n"
"s_waitcnt lgkmcnt(10)\n"
"v_mfma_f32_16x16x32_bf16 v[140:143], a[72:75], v[48:51], v[140:143]\n"
"ds_read_b64_tr_b16 a[52:53], v5 offset:61440\n"
"v_mfma_f32_16x16x32_bf16 v[140:143], a[76:79], v[52:55], v[140:143]\n"
"ds_read_b64_tr_b16 a[54:55], v5 offset:62464\n"
"v_mfma_f32_16x16x32_bf16 v[172:175], a[64:67], a[232:235], v[172:175]\n"
"ds_read_b64_tr_b16 a[56:57], v5 offset:24576\n"
"v_mfma_f32_16x16x32_bf16 v[172:175], a[68:71], a[236:239], v[172:175]\n"
"ds_read_b64_tr_b16 a[58:59], v5 offset:25600\n"
"v_mfma_f32_16x16x32_bf16 v[172:175], a[72:75], v[56:59], v[172:175]\n"
"ds_read_b64_tr_b16 a[60:61], v5 offset:28672\n"
"v_mfma_f32_16x16x32_bf16 v[172:175], a[76:79], v[60:63], v[172:175]\n"
".Lpad15:\n"
"s_cbranch_execz .Lwr1\n"
"ds_read_b64_tr_b16 a[62:63], v5 offset:29696\n"
"s_waitcnt lgkmcnt(12)\n"
"v_mfma_f32_16x16x32_bf16 v[140:143], a[80:83], v[64:67], v[140:143]\n"
"ds_read_b64_tr_b16 a[64:65], v5 offset:32768\n"
"v_mfma_f32_16x16x32_bf16 v[140:143], a[84:87], v[68:71], v[140:143]\n"
"ds_read_b64_tr_b16 a[66:67], v5 offset:33792\n"
"s_waitcnt lgkmcnt(10)\n"
"v_mfma_f32_16x16x32_bf16 v[140:143], a[88:91], v[80:83], v[140:143]\n"
"ds_read_b64_tr_b16 a[68:69], v5 offset:36864\n"
"v_mfma_f32_16x16x32_bf16 v[140:143], a[92:95], v[84:87], v[140:143]\n"
"ds_read_b64_tr_b16 a[70:71], v5 offset:37888\n"
"v_mfma_f32_16x16x32_bf16 v[172:175], a[80:83], v[72:75], v[172:175]\n"
"ds_read_b64_tr_b16 a[72:73], v5 offset:40960\n"
"v_mfma_f32_16x16x32_bf16 v[172:175], a[84:87], v[76:79], v[172:175]\n"
"ds_read_b64_tr_b16 a[74:75], v5 offset:41984\n"
"v_mfma_f32_16x16x32_bf16 v[172:175], a[88:91], v[88:91], v[172:175]\n"
"ds_read_b64_tr_b16 a[76:77], v5 offset:45056\n"
"v_mfma_f32_16x16x32_bf16 v[172:175], a[92:95], v[92:95], v[172:175]\n"
"ds_read_b64_tr_b16 a[78:79], v5 offset:46080\n"
"s_waitcnt lgkmcnt(12)\n"
"v_mfma_f32_16x16x32_bf16 v[140:143], a[48:51], a[96:99], v[140:143]\n"
"ds_read_b64_tr_b16 a[80:81], v5 offset:49152\n"
"v_mfma_f32_16x16x32_bf16 v[140:143], a[52:55], a[100:103], v[140:143]\n"
"ds_read_b64_tr_b16 a[82:83], v5 offset:50176\n"
"s_waitcnt lgkmcnt(10)\n"
"v_mfma_f32_16x16x32_bf16 v[140:143], a[56:59], a[112:115], v[140:143]\n"
"ds_read_b64_tr_b16 a[84:85], v5 offset:53248\n"
"v_mfma_f32_16x16x32_bf16 v[140:143], a[60:63], a[116:119], v[140:143]\n"
"ds_read_b64_tr_b16 a[86:87], v5 offset:54272\n"
"v_mfma_f32_16x16x32_bf16 v[172:175], a[48:51], a[104:107], v[172:175]\n"
"ds_read_b64_tr_b16 a[88:89], v9 offset:57344\n"
"v_mfma_f32_16x16x32_bf16 v[172:175], a[52:55], a[108:111], v[172:175]\n"
"ds_read_b64_tr_b16 a[90:91], v9 offset:58368\n"
"v_mfma_f32_16x16x32_bf16 v[172:175], a[56:59], a[120:123], v[172:175]\n"
"ds_read_b64_tr_b16 a[92:93], v9 offset:61440\n"
"v_mfma_f32_16x16x32_bf16 v[172:175], a[60:63], a[124:127], v[172:175]\n"
"ds_read_b64_tr_b16 a[94:95], v9 offset:62464\n"
"s_waitcnt lgkmcnt(12)\n"
"v_mfma_f32_16x16x32_bf16 v[140:143], a[64:67], a[128:131], v[140:143]\n"
"v_mfma_f32_16x16x32_bf16 v[140:143], a[68:71], a[132:135], v[140:143]\n"
"s_waitcnt lgkmcnt(8)\n"
"v_mfma_f32_16x16x32_bf16 v[140:143], a[72:75], a[144:147], v[140:143]\n"
"v_mfma_f32_16x16x32_bf16 v[140:143], a[76:79], a[148:151], v[140:143]\n"
"v_mfma_f32_16x16x32_bf16 v[172:175], a[64:67], a[136:139], v[172:175]\n"
"v_mfma_f32_16x16x32_bf16 v[172:175], a[68:71], a[140:143], v[172:175]\n"
"v_mfma_f32_16x16x32_bf16 v[172:175], a[72:75], a[152:155], v[172:175]\n"
"v_mfma_f32_16x16x32_bf16 v[172:175], a[76:79], a[156:159], v[172:175]\n"
"s_waitcnt lgkmcnt(4)\n"
"v_mfma_f32_16x16x32_bf16 v[140:143], a[80:83], a[160:163], v[140:143]\n"
"v_mfma_f32_16x16x32_bf16 v[140:143], a[84:87], a[164:167], v[140:143]\n"
"s_waitcnt lgkmcnt(0)\n"
"v_mfma_f32_16x16x32_bf16 v[140:143], a[88:91], a[176:179], v[140:143]\n"
"v_mfma_f32_16x16x32_bf16 v[140:143], a[92:95], a[180:183], v[140:143]\n"
"v_mfma_f32_16x16x32_bf16 v[172:175], a[80:83], a[168:171], v[172:175]\n"
"v_mfma_f32_16x16x32_bf16 v[172:175], a[84:87], a[172:175], v[172:175]\n"
"v_mfma_f32_16x16x32_bf16 v[172:175], a[88:91], a[184:187], v[172:175]\n"
"v_mfma_f32_16x16x32_bf16 v[172:175], a[92:95], a[188:191], v[172:175]\n"
"s_waitcnt vmcnt(0)\n"
"v_mfma_f32_16x16x32_bf16 v[128:131], v[14:17], v[192:195], v[128:131]\n"
"v_mfma_f32_16x16x32_bf16 v[128:131], v[12:15], v[198:201], v[128:131]\n"
"v_mfma_f32_16x16x32_bf16 v[132:135], v[12:15], v[194:197], v[132:135]\n"
"v_mfma_f32_16x16x32_bf16 v[132:135], v[10:13], v[200:203], v[132:135]\n"
"v_mfma_f32_16x16x32_bf16 v[136:139], v[14:17], v[204:207], v[136:139]\n"
"v_mfma_f32_16x16x32_bf16 v[136:139], v[12:15], v[210:213], v[136:139]\n"
"v_mfma_f32_16x16x32_bf16 v[140:143], v[12:15], v[206:209], v[140:143]\n"
"v_mfma_f32_16x16x32_bf16 v[140:143], v[10:13], v[212:215], v[140:143]\n"
"s_nop 1\n"
"ds_write_b128 v41, v[128:131] offset:0\n"
"v_mfma_f32_16x16x32_bf16 v[160:163], v[14:17], v[216:219], v[160:163]\n"
"v_mfma_f32_16x16x32_bf16 v[160:163], v[12:15], v[222:225], v[160:163]\n"
"ds_write_b128 v45, v[132:135] offset:0\n"
"v_mfma_f32_16x16x32_bf16 v[164:167], v[12:15], v[218:221], v[164:167]\n"
"v_mfma_f32_16x16x32_bf16 v[164:167], v[10:13], v[224:227], v[164:167]\n"
"ds_write_b128 v41, v[136:139] offset:128\n"
"v_mfma_f32_16x16x32_bf16 v[168:171], v[14:17], v[228:231], v[168:171]\n"
"v_mfma_f32_16x16x32_bf16 v[168:171], v[12:15], v[234:237], v[168:171]\n"
"ds_write_b128 v45, v[140:143] offset:128\n"
"v_mfma_f32_16x16x32_bf16 v[172:175], v[12:15], v[230:233], v[172:175]\n"
"ds_read_b128 v[48:51], v42 offset:0\n"
"ds_read_b128 v[52:55], v43 offset:0\n"
"v_mfma_f32_16x16x32_bf16 v[172:175], v[10:13], v[236:239], v[172:175]\n"
"ds_write_b128 v41, v[160:163] offset:4096\n"
"ds_read_b128 v[56:59], v42 offset:2048\n"
"ds_read_b128 v[60:63], v43 offset:2048\n"
"ds_write_b128 v45, v[164:167] offset:4096\n"
"ds_write_b128 v41, v[168:171] offset:4224\n"
"s_nop 2\n"
"s_nop 0\n"
"ds_write_b128 v45, v[172:175] offset:4224\n"
"ds_read_b128 v[64:67], v42 offset:4096\n"
"ds_read_b128 v[68:71], v43 offset:4096\n"
"ds_read_b128 v[72:75], v42 offset:6144\n"
"ds_read_b128 v[76:79], v43 offset:6144\n"
"s_waitcnt lgkmcnt(11)\n"
"v_pk_add_f32 v[48:49], v[48:49], v[96:97]\n"
"v_pk_add_f32 v[50:51], v[50:51], v[98:99]\n"
"s_waitcnt lgkmcnt(10)\n"
"v_pk_add_f32 v[52:53], v[52:53], v[100:101]\n"
"v_pk_add_f32 v[54:55], v[54:55], v[102:103]\n"
"v_cvt_pk_bf16_f32 v48, v48, v49\n"
"v_cvt_pk_bf16_f32 v49, v50, v51\n"
"v_cvt_pk_bf16_f32 v50, v52, v53\n"
"v_cvt_pk_bf16_f32 v51, v54, v55\n"
"buffer_store_dwordx4 v[48:51], v26, s[36:39], 0 offen sc1\n"
"s_waitcnt lgkmcnt(8)\n"
"v_pk_add_f32 v[56:57], v[56:57], v[104:105]\n"
"v_pk_add_f32 v[58:59], v[58:59], v[106:107]\n"
"s_waitcnt lgkmcnt(7)\n"
"v_pk_add_f32 v[60:61], v[60:61], v[108:109]\n"
"v_pk_add_f32 v[62:63], v[62:63], v[110:111]\n"
"v_cvt_pk_bf16_f32 v56, v56, v57\n"
"v_cvt_pk_bf16_f32 v57, v58, v59\n"
"v_cvt_pk_bf16_f32 v58, v60, v61\n"
"v_cvt_pk_bf16_f32 v59, v62, v63\n"
"buffer_store_dwordx4 v[56:59], v27, s[36:39], 0 offen sc1\n"
"s_waitcnt lgkmcnt(3)\n"
"v_pk_add_f32 v[64:65], v[64:65], v[112:113]\n"
"v_pk_add_f32 v[66:67], v[66:67], v[114:115]\n"
"s_waitcnt lgkmcnt(2)\n"
"v_pk_add_f32 v[68:69], v[68:69], v[116:117]\n"
"v_pk_add_f32 v[70:71], v[70:71], v[118:119]\n"
"v_cvt_pk_bf16_f32 v64, v64, v65\n"
"v_cvt_pk_bf16_f32 v65, v66, v67\n"
"v_cvt_pk_bf16_f32 v66, v68, v69\n"
"v_cvt_pk_bf16_f32 v67, v70, v71\n"
"buffer_store_dwordx4 v[64:67], v28, s[36:39], 0 offen sc1\n"
"s_waitcnt lgkmcnt(1)\n"
"v_pk_add_f32 v[72:73], v[72:73], v[120:121]\n"
"v_pk_add_f32 v[74:75], v[74:75], v[122:123]\n"
"s_waitcnt lgkmcnt(0)\n"
"v_pk_add_f32 v[76:77], v[76:77], v[124:125]\n"
"v_pk_add_f32 v[78:79], v[78:79], v[126:127]\n"
"v_cvt_pk_bf16_f32 v72, v72, v73\n"
"v_cvt_pk_bf16_f32 v73, v74, v75\n"
"v_cvt_pk_bf16_f32 v74, v76, v77\n"
"v_cvt_pk_bf16_f32 v75, v78, v79\n"
"buffer_store_dwordx4 v[72:75], v29, s[36:39], 0 offen sc1\n"
".Lfin:\n"
"s_endpgm\n"
".Ly4_sl:\n"
"s_mov_b32 s72, 0\n"
"s_mov_b64 exec, s[56:57]\n"
"v_mov_b32 v44, 0x400\n"
".Ly4_lp:\n"
"v_cmp_eq_u32 vcc, 32, v28\n"
"s_nop 1\n"
"s_cbranch_vccz .Ly4_h\n"
"s_mov_b64 exec, vcc\n"
"buffer_atomic_add v44, v29, s[16:19], 0 offen sc1\n"
"s_mov_b64 exec, s[56:57]\n"
".Ly4_h:\n"
"v_cmp_gt_i32 vcc, 32, v28\n"
"s_nop 1\n"
"s_cbranch_vccz .Ly4_d\n"
"buffer_load_dword v28, v29, s[16:19], 0 offen sc1\n"
"s_add_u32 s72, s72, 1\n"
"s_waitcnt vmcnt(0)\n"
"v_sub_u32 v28, v28, v27\n"
"s_cmp_lt_u32 s72, 262144\n"
"s_cbranch_scc1 .Ly4_lp\n"
".Ly4_d:\n"
"s_mov_b64 exec, -1\n"
"s_branch .Ly4_ok\n"
".Ltb_sl:\n"
"s_mov_b32 s72, 0\n"
"s_nop 7\n"
".Ltb_lp:\n"
"buffer_load_dwordx2 v[38:39], v41, s[16:19], 0 offen sc1\n"
"s_mov_b32 s53, 0\n"
"buffer_load_dword v30, v42, s[16:19], s53 offen sc1\n"
"buffer_load_dword v31, v42, s[16:19], s53 offen offset:3328 sc1\n"
"s_add_u32 s53, s53, 6656\n"
"buffer_load_dword v32, v42, s[16:19], s53 offen sc1\n"
"buffer_load_dword v33, v42, s[16:19], s53 offen offset:3328 sc1\n"
"s_add_u32 s53, s53, 6656\n"
"buffer_load_dword v34, v42, s[16:19], s53 offen sc1\n"
"buffer_load_dword v35, v42, s[16:19], s53 offen offset:3328 sc1\n"
"s_add_u32 s53, s53, 6656\n"
"buffer_load_dword v36, v42, s[16:19], s53 offen sc1\n"
"buffer_load_dword v37, v42, s[16:19], s53 offen offset:3328 sc1\n"
"s_add_u32 s72, s72, 1\n"
"s_waitcnt vmcnt(0)\n"
"v_max3_u32 v45, v30, v31, v32\n"
"v_max3_u32 v46, v33, v34, v35\n"
"v_max3_u32 v45, v45, v36, v37\n"
"v_max3_u32 v46, v46, v38, v39\n"
"v_max_u32 v45, v45, v46\n"
"v_cmp_eq_u32 vcc, -1, v45\n"
"s_nop 1\n"
"s_cbranch_vccz .Ltb_ok\n"
"s_cmp_lt_u32 s72, 262144\n"
"s_cbranch_scc1 .Ltb_lp\n"
"s_branch .Ltb_ok\n"
".Lax7f:\n"
"s_nop 1\n"
"v_mfma_f32_16x16x32_bf16 v[120:123], v[132:135], v[228:231], v[120:123]\n"
"v_mfma_f32_16x16x32_bf16 v[124:127], v[128:131], v[228:231], v[124:127]\n"
"v_mfma_f32_16x16x32_bf16 v[76:79], a[184:187], v[228:231], v[76:79]\n"
"v_accvgpr_read_b32 v10, a245\n"
"v_accvgpr_read_b32 v11, a246\n"
"v_accvgpr_read_b32 v12, a247\n"
"v_accvgpr_read_b32 v13, a248\n"
"s_add_u32 m0, s57, 122880\n"
"s_add_u32 s53, s70, s30\n"
"buffer_load_dwordx4 v10, s[20:23], s53 offen offset:0 lds\n"
"s_add_u32 s53, s70, s31\n"
"buffer_load_dwordx4 v11, s[20:23], s53 offen offset:1024 lds\n"
"s_add_u32 s70, s70, 131072\n"
"s_add_u32 m0, s57, 131072\n"
"s_nop 0\n"
"buffer_load_dwordx4 v10, s[20:23], s70 offen offset:0 lds\n"
"buffer_load_dwordx4 v11, s[20:23], s70 offen offset:1024 lds\n"
"s_add_u32 s70, s70, 131072\n"
"s_add_u32 m0, s57, 114688\n"
"s_nop 0\n"
"buffer_load_dwordx4 v12, s[20:23], s71 offen offset:0 lds\n"
"buffer_load_dwordx4 v13, s[20:23], s71 offen offset:1024 lds\n"
"s_add_u32 s71, s71, 131072\n"
"s_add_u32 m0, s57, 139264\n"
"s_nop 0\n"
"buffer_load_dwordx4 v10, s[20:23], s70 offen offset:0 lds\n"
"buffer_load_dwordx4 v11, s[20:23], s70 offen offset:1024 lds\n"
"s_add_u32 s70, s70, 131072\n"
"s_add_u32 m0, s57, 147456\n"
"s_nop 0\n"
"buffer_load_dwordx4 v10, s[20:23], s70 offen offset:0 lds\n"
"buffer_load_dwordx4 v11, s[20:23], s70 offen offset:1024 lds\n"
"s_add_u32 s70, s70, 131072\n"
"s_add_u32 m0, s57, 90112\n"
"s_add_u32 s53, s70, s28\n"
"buffer_load_dwordx4 v10, s[20:23], s53 offen offset:0 lds\n"
"s_add_u32 s53, s70, s29\n"
"buffer_load_dwordx4 v11, s[20:23], s53 offen offset:1024 lds\n"
"s_add_u32 s70, s70, 131072\n"
"s_add_u32 m0, s57, 81920\n"
"s_nop 0\n"
"buffer_load_dwordx4 v10, s[20:23], s70 offen offset:0 lds\n"
"buffer_load_dwordx4 v11, s[20:23], s70 offen offset:1024 lds\n"
"s_add_u32 s70, s70, 131072\n"
"s_add_u32 m0, s57, 73728\n"
"s_nop 0\n"
"buffer_load_dwordx4 v10, s[20:23], s70 offen offset:0 lds\n"
"buffer_load_dwordx4 v11, s[20:23], s70 offen offset:1024 lds\n"
"s_add_u32 s70, s70, 131072\n"
"s_add_u32 m0, s57, 65536\n"
"s_add_u32 s53, s70, s30\n"
"buffer_load_dwordx4 v10, s[20:23], s53 offen offset:0 lds\n"
"s_add_u32 s53, s70, s31\n"
"buffer_load_dwordx4 v11, s[20:23], s53 offen offset:1024 lds\n"
"s_add_u32 s70, s70, 131072\n"
"v_cmp_gt_u32 vcc, 8, v0\n"
"v_lshl_add_u32 v34, v0, 2, s88\n"
"v_mov_b32 v36, 2130706432\n"
"v_cndmask_b32 v34, v36, v34, vcc\n"
"buffer_load_dword v35, v34, s[16:19], 0 offen sc1\n"
"v_add_u32 v14, s89, v1\n"
"v_xor_b32 v15, 16384, v14\n"
"v_xor_b32 v16, 1024, v14\n"
"v_xor_b32 v17, 17408, v14\n"
"v_xor_b32 v18, 2048, v14\n"
"v_xor_b32 v19, 18432, v14\n"
"v_xor_b32 v20, 3072, v14\n"
"v_xor_b32 v21, 19456, v14\n"
"s_waitcnt vmcnt(0)\n"
"v_cmp_ne_u32 vcc, 0, v35\n"
"s_mov_b32 s72, 0\n"
"s_nop 0\n"
"s_cbranch_vccz .Lfhx7_ok\n"
".Lfhx7_lp:\n"
"buffer_load_dword v35, v34, s[16:19], 0 offen sc1\n"
"s_add_u32 s72, s72, 1\n"
"s_waitcnt vmcnt(0)\n"
"s_waitcnt vmcnt(0)\n"
"v_cmp_ne_u32 vcc, 0, v35\n"
"s_cmp_lt_u32 s72, 262144\n"
"s_nop 0\n"
"s_cselect_b64 vcc, vcc, 0\n"
"s_cbranch_vccnz .Lfhx7_lp\n"
".Lfhx7_ok:\n"
"buffer_load_dwordx4 a[96:99], v14, s[16:19], s46 offen sc1\n"
"buffer_load_dwordx4 a[100:103], v15, s[16:19], s46 offen sc1\n"
"s_add_u32 s46, s46, 32768\n"
"buffer_load_dwordx4 a[112:115], v14, s[16:19], s46 offen sc1\n"
"buffer_load_dwordx4 a[116:119], v15, s[16:19], s46 offen sc1\n"
"s_add_u32 s46, s46, 32768\n"
"buffer_load_dwordx4 a[0:3], v18, s[16:19], s47 offen sc1\n"
"buffer_load_dwordx4 a[4:7], v19, s[16:19], s47 offen sc1\n"
"buffer_load_dwordx4 a[8:11], v20, s[16:19], s47 offen sc1\n"
"buffer_load_dwordx4 a[12:15], v21, s[16:19], s47 offen sc1\n"
"s_add_u32 s47, s47, 32768\n"
"buffer_load_dwordx4 a[104:107], v16, s[16:19], s54 offen sc1\n"
"buffer_load_dwordx4 a[108:111], v17, s[16:19], s54 offen sc1\n"
"s_add_u32 s54, s54, 32768\n"
"buffer_load_dwordx4 a[128:131], v14, s[16:19], s46 offen sc1\n"
"buffer_load_dwordx4 a[132:135], v15, s[16:19], s46 offen sc1\n"
"s_add_u32 s46, s46, 32768\n"
"buffer_load_dwordx4 a[16:19], v18, s[16:19], s47 offen sc1\n"
"buffer_load_dwordx4 a[20:23], v19, s[16:19], s47 offen sc1\n"
"buffer_load_dwordx4 a[24:27], v20, s[16:19], s47 offen sc1\n"
"buffer_load_dwordx4 a[28:31], v21, s[16:19], s47 offen sc1\n"
"s_add_u32 s47, s47, 32768\n"
"buffer_load_dwordx4 a[120:123], v16, s[16:19], s54 offen sc1\n"
"buffer_load_dwordx4 a[124:127], v17, s[16:19], s54 offen sc1\n"
"s_add_u32 s54, s54, 32768\n"
"v_bfe_u32 v3, v0, 2, 3\n"
"v_lshrrev_b32 v4, 5, v0\n"
"v_lshlrev_b32 v4, 11, v4\n"
"v_lshl_add_u32 v4, v3, 7, v4\n"
"v_lshrrev_b32_e64 v3, 1, v3\n"
"v_lshl_add_u32 v4, v3, 5, v4\n"
"v_and_b32_e64 v3, 3, v0\n"
"v_lshl_add_u32 v4, v3, 3, v4\n"
"v_add_u32 v2, 98304, v4\n"
"v_add_u32 v6, 32768, v4\n"
"v_xor_b32 v3, 32, v2\n"
"v_xor_b32 v4, 64, v2\n"
"v_xor_b32 v5, 96, v2\n"
"s_waitcnt lgkmcnt(0)\n"
"s_nop 7\n"
"s_branch .Latd\n"
".Lax5f:\n"
"s_nop 1\n"
"v_mfma_f32_16x16x32_bf16 v[120:123], a[176:179], v[228:231], v[120:123]\n"
"v_mfma_f32_16x16x32_bf16 v[124:127], a[180:183], v[228:231], v[124:127]\n"
"v_mfma_f32_16x16x32_bf16 v[76:79], a[184:187], v[228:231], v[76:79]\n"
"s_add_u32 m0, s57, 81920\n"
"s_nop 0\n"
"buffer_load_dwordx4 v10, s[20:23], s70 offen offset:0 lds\n"
"buffer_load_dwordx4 v11, s[20:23], s70 offen offset:1024 lds\n"
"s_add_u32 s70, s70, 131072\n"
"s_add_u32 m0, s57, 73728\n"
"s_nop 0\n"
"buffer_load_dwordx4 v10, s[20:23], s70 offen offset:0 lds\n"
"buffer_load_dwordx4 v11, s[20:23], s70 offen offset:1024 lds\n"
"s_add_u32 s70, s70, 131072\n"
"s_add_u32 m0, s57, 65536\n"
"s_add_u32 s53, s70, s30\n"
"buffer_load_dwordx4 v10, s[20:23], s53 offen offset:0 lds\n"
"s_add_u32 s53, s70, s31\n"
"buffer_load_dwordx4 v11, s[20:23], s53 offen offset:1024 lds\n"
"s_add_u32 s70, s70, 131072\n"
"v_cmp_gt_u32 vcc, 8, v0\n"
"v_lshl_add_u32 v34, v0, 2, s88\n"
"v_mov_b32 v36, 2130706432\n"
"v_cndmask_b32 v34, v36, v34, vcc\n"
"buffer_load_dword v35, v34, s[16:19], 0 offen sc1\n"
"v_add_u32_e64 v14, s89, v1\n"
"v_xor_b32 v15, 16384, v14\n"
"v_xor_b32 v16, 1024, v14\n"
"v_xor_b32 v17, 17408, v14\n"
"v_xor_b32 v18, 2048, v14\n"
"v_xor_b32 v19, 18432, v14\n"
"v_xor_b32 v20, 3072, v14\n"
"v_xor_b32 v21, 19456, v14\n"
"s_waitcnt vmcnt(0)\n"
"v_cmp_ne_u32 vcc, 0, v35\n"
"s_mov_b32 s72, 0\n"
"s_nop 0\n"
"s_cbranch_vccz .Lfhx5_ok\n"
".Lfhx5_lp:\n"
"buffer_load_dword v35, v34, s[16:19], 0 offen sc1\n"
"s_add_u32 s72, s72, 1\n"
"s_waitcnt vmcnt(0)\n"
"s_waitcnt vmcnt(0)\n"
"v_cmp_ne_u32 vcc, 0, v35\n"
"s_cmp_lt_u32 s72, 262144\n"
"s_nop 0\n"
"s_cselect_b64 vcc, vcc, 0\n"
"s_cbranch_vccnz .Lfhx5_lp\n"
".Lfhx5_ok:\n"
"buffer_load_dwordx4 a[96:99], v14, s[16:19], s46 offen sc1\n"
"buffer_load_dwordx4 a[100:103], v15, s[16:19], s46 offen sc1\n"
"s_add_u32 s46, s46, 32768\n"
"buffer_load_dwordx4 a[112:115], v14, s[16:19], s46 offen sc1\n"
"buffer_load_dwordx4 a[116:119], v15, s[16:19], s46 offen sc1\n"
"s_add_u32 s46, s46, 32768\n"
"buffer_load_dwordx4 a[0:3], v18, s[16:19], s47 offen sc1\n"
"buffer_load_dwordx4 a[4:7], v19, s[16:19], s47 offen sc1\n"
"buffer_load_dwordx4 a[8:11], v20, s[16:19], s47 offen sc1\n"
"buffer_load_dwordx4 a[12:15], v21, s[16:19], s47 offen sc1\n"
"s_add_u32 s47, s47, 32768\n"
"buffer_load_dwordx4 a[104:107], v16, s[16:19], s54 offen sc1\n"
"buffer_load_dwordx4 a[108:111], v17, s[16:19], s54 offen sc1\n"
"s_add_u32 s54, s54, 32768\n"
"buffer_load_dwordx4 a[128:131], v14, s[16:19], s46 offen sc1\n"
"buffer_load_dwordx4 a[132:135], v15, s[16:19], s46 offen sc1\n"
"s_add_u32 s46, s46, 32768\n"
"buffer_load_dwordx4 a[16:19], v18, s[16:19], s47 offen sc1\n"
"buffer_load_dwordx4 a[20:23], v19, s[16:19], s47 offen sc1\n"
"buffer_load_dwordx4 a[24:27], v20, s[16:19], s47 offen sc1\n"
"buffer_load_dwordx4 a[28:31], v21, s[16:19], s47 offen sc1\n"
"s_add_u32 s47, s47, 32768\n"
"buffer_load_dwordx4 a[120:123], v16, s[16:19], s54 offen sc1\n"
"buffer_load_dwordx4 a[124:127], v17, s[16:19], s54 offen sc1\n"
"s_add_u32 s54, s54, 32768\n"
"v_bfe_u32 v3, v0, 2, 3\n"
"v_lshrrev_b32 v4, 5, v0\n"
"v_lshlrev_b32 v4, 11, v4\n"
"v_lshl_add_u32 v4, v3, 7, v4\n"
"v_lshrrev_b32_e64 v3, 1, v3\n"
"v_lshl_add_u32 v4, v3, 5, v4\n"
"v_and_b32_e64 v3, 3, v0\n"
"v_lshl_add_u32 v4, v3, 3, v4\n"
"v_add_u32 v2, 98304, v4\n"
"v_add_u32 v6, 32768, v4\n"
"v_xor_b32 v3, 32, v2\n"
"v_xor_b32 v4, 64, v2\n"
"v_xor_b32 v5, 96, v2\n"
"s_waitcnt lgkmcnt(0)\n"
"s_nop 7\n"
"s_branch .Latd\n"
".Lax3f:\n"
"s_nop 1\n"
"v_mfma_f32_16x16x32_bf16 v[120:123], a[176:179], v[228:231], v[120:123]\n"
"v_mfma_f32_16x16x32_bf16 v[124:127], a[180:183], v[228:231], v[124:127]\n"
"v_mfma_f32_16x16x32_bf16 v[76:79], a[184:187], v[228:231], v[76:79]\n"
"v_cmp_gt_u32 vcc, 8, v0\n"
"v_lshl_add_u32 v34, v0, 2, s88\n"
"v_mov_b32 v36, 2130706432\n"
"v_cndmask_b32 v34, v36, v34, vcc\n"
"buffer_load_dword v35, v34, s[16:19], 0 offen sc1\n"
"v_add_u32_e64 v14, s89, v1\n"
"v_xor_b32 v15, 16384, v14\n"
"v_xor_b32 v16, 1024, v14\n"
"v_xor_b32 v17, 17408, v14\n"
"v_xor_b32 v18, 2048, v14\n"
"v_xor_b32 v19, 18432, v14\n"
"v_xor_b32 v20, 3072, v14\n"
"v_xor_b32 v21, 19456, v14\n"
"s_waitcnt vmcnt(0)\n"
"v_cmp_ne_u32 vcc, 0, v35\n"
"s_mov_b32 s72, 0\n"
"s_nop 0\n"
"s_cbranch_vccz .Lfhx3_ok\n"
".Lfhx3_lp:\n"
"buffer_load_dword v35, v34, s[16:19], 0 offen sc1\n"
"s_add_u32 s72, s72, 1\n"
"s_waitcnt vmcnt(0)\n"
"s_waitcnt vmcnt(0)\n"
"v_cmp_ne_u32 vcc, 0, v35\n"
"s_cmp_lt_u32 s72, 262144\n"
"s_nop 0\n"
"s_cselect_b64 vcc, vcc, 0\n"
"s_cbranch_vccnz .Lfhx3_lp\n"
".Lfhx3_ok:\n"
"buffer_load_dwordx4 a[96:99], v14, s[16:19], s46 offen sc1\n"
"buffer_load_dwordx4 a[100:103], v15, s[16:19], s46 offen sc1\n"
"s_add_u32 s46, s46, 32768\n"
"buffer_load_dwordx4 a[112:115], v14, s[16:19], s46 offen sc1\n"
"buffer_load_dwordx4 a[116:119], v15, s[16:19], s46 offen sc1\n"
"s_add_u32 s46, s46, 32768\n"
"buffer_load_dwordx4 a[0:3], v18, s[16:19], s47 offen sc1\n"
"buffer_load_dwordx4 a[4:7], v19, s[16:19], s47 offen sc1\n"
"buffer_load_dwordx4 a[8:11], v20, s[16:19], s47 offen sc1\n"
"buffer_load_dwordx4 a[12:15], v21, s[16:19], s47 offen sc1\n"
"s_add_u32 s47, s47, 32768\n"
"buffer_load_dwordx4 a[104:107], v16, s[16:19], s54 offen sc1\n"
"buffer_load_dwordx4 a[108:111], v17, s[16:19], s54 offen sc1\n"
"s_add_u32 s54, s54, 32768\n"
"buffer_load_dwordx4 a[128:131], v14, s[16:19], s46 offen sc1\n"
"buffer_load_dwordx4 a[132:135], v15, s[16:19], s46 offen sc1\n"
"s_add_u32 s46, s46, 32768\n"
"buffer_load_dwordx4 a[16:19], v18, s[16:19], s47 offen sc1\n"
"buffer_load_dwordx4 a[20:23], v19, s[16:19], s47 offen sc1\n"
"buffer_load_dwordx4 a[24:27], v20, s[16:19], s47 offen sc1\n"
"buffer_load_dwordx4 a[28:31], v21, s[16:19], s47 offen sc1\n"
"s_add_u32 s47, s47, 32768\n"
"buffer_load_dwordx4 a[120:123], v16, s[16:19], s54 offen sc1\n"
"buffer_load_dwordx4 a[124:127], v17, s[16:19], s54 offen sc1\n"
"s_add_u32 s54, s54, 32768\n"
"v_bfe_u32 v3, v0, 2, 3\n"
"v_lshrrev_b32 v4, 5, v0\n"
"v_lshlrev_b32 v4, 11, v4\n"
"v_lshl_add_u32 v4, v3, 7, v4\n"
"v_lshrrev_b32_e64 v3, 1, v3\n"
"v_lshl_add_u32 v4, v3, 5, v4\n"
"v_and_b32_e64 v3, 3, v0\n"
"v_lshl_add_u32 v4, v3, 3, v4\n"
"v_add_u32 v2, 98304, v4\n"
"v_add_u32 v6, 32768, v4\n"
"v_xor_b32 v3, 32, v2\n"
"v_xor_b32 v4, 64, v2\n"
"v_xor_b32 v5, 96, v2\n"
"s_waitcnt lgkmcnt(0)\n"
"s_nop 7\n"
"s_branch .Latd\n"
".Lax1f:\n"
"s_nop 1\n"
"v_mfma_f32_16x16x32_bf16 v[120:123], a[176:179], v[228:231], v[120:123]\n"
"v_mfma_f32_16x16x32_bf16 v[124:127], a[180:183], v[228:231], v[124:127]\n"
"v_mfma_f32_16x16x32_bf16 v[76:79], a[184:187], v[228:231], v[76:79]\n"
"buffer_load_dwordx4 a[104:107], v16, s[16:19], s54 offen sc1\n"
"buffer_load_dwordx4 a[108:111], v17, s[16:19], s54 offen sc1\n"
"s_add_u32 s54, s54, 32768\n"
"buffer_load_dwordx4 a[128:131], v14, s[16:19], s46 offen sc1\n"
"buffer_load_dwordx4 a[132:135], v15, s[16:19], s46 offen sc1\n"
"s_add_u32 s46, s46, 32768\n"
"buffer_load_dwordx4 a[16:19], v18, s[16:19], s47 offen sc1\n"
"buffer_load_dwordx4 a[20:23], v19, s[16:19], s47 offen sc1\n"
"buffer_load_dwordx4 a[24:27], v20, s[16:19], s47 offen sc1\n"
"buffer_load_dwordx4 a[28:31], v21, s[16:19], s47 offen sc1\n"
"s_add_u32 s47, s47, 32768\n"
"buffer_load_dwordx4 a[120:123], v16, s[16:19], s54 offen sc1\n"
"buffer_load_dwordx4 a[124:127], v17, s[16:19], s54 offen sc1\n"
"s_add_u32 s54, s54, 32768\n"
"v_bfe_u32 v3, v0, 2, 3\n"
"v_lshrrev_b32 v4, 5, v0\n"
"v_lshlrev_b32 v4, 11, v4\n"
"v_lshl_add_u32 v4, v3, 7, v4\n"
"v_lshrrev_b32_e64 v3, 1, v3\n"
"v_lshl_add_u32 v4, v3, 5, v4\n"
"v_and_b32_e64 v3, 3, v0\n"
"v_lshl_add_u32 v4, v3, 3, v4\n"
"v_add_u32 v2, 98304, v4\n"
"v_add_u32 v6, 32768, v4\n"
"v_xor_b32 v3, 32, v2\n"
"v_xor_b32 v4, 64, v2\n"
"v_xor_b32 v5, 96, v2\n"
"s_waitcnt lgkmcnt(0)\n"
"s_nop 7\n"
"s_branch .Latd\n"
".Lfo_sl:\n"
"s_mov_b32 s72, 0\n"
"s_nop 7\n"
".Lfo_lp:\n"
"buffer_load_dword v35, v34, s[16:19], 0 offen sc1\n"
"s_add_u32 s72, s72, 1\n"
"s_waitcnt vmcnt(0)\n"
"v_cmp_ne_u32 vcc, 0, v35\n"
"s_nop 1\n"
"s_cbranch_vccz .Lfo_ok\n"
"s_cmp_lt_u32 s72, 262144\n"
"s_cbranch_scc1 .Lfo_lp\n"
"s_branch .Lfo_ok\n"
".Lend:\n"
".Lax7s:\n"
"s_nop 1\n"
"v_mfma_f32_16x16x32_bf16 v[120:123], v[132:135], v[228:231], v[120:123]\n"
"v_mfma_f32_16x16x32_bf16 v[124:127], v[128:131], v[228:231], v[124:127]\n"
"v_mfma_f32_16x16x32_bf16 v[76:79], a[184:187], v[228:231], v[76:79]\n"
"s_waitcnt lgkmcnt(0)\n"
"s_nop 7\n"
"s_branch .Lnorm\n"
".Lax5s:\n"
"s_nop 1\n"
"v_mfma_f32_16x16x32_bf16 v[120:123], a[176:179], v[228:231], v[120:123]\n"
"v_mfma_f32_16x16x32_bf16 v[124:127], a[180:183], v[228:231], v[124:127]\n"
"v_mfma_f32_16x16x32_bf16 v[76:79], a[184:187], v[228:231], v[76:79]\n"
"s_waitcnt lgkmcnt(0)\n"
"s_nop 7\n"
"s_branch .Lnorm\n"
".Lax3s:\n"
"s_nop 1\n"
"v_mfma_f32_16x16x32_bf16 v[120:123], a[176:179], v[228:231], v[120:123]\n"
"v_mfma_f32_16x16x32_bf16 v[124:127], a[180:183], v[228:231], v[124:127]\n"
"v_mfma_f32_16x16x32_bf16 v[76:79], a[184:187], v[228:231], v[76:79]\n"
"s_waitcnt lgkmcnt(0)\n"
"s_nop 7\n"
"s_branch .Lnorm\n"
".Lax1s:\n"
"s_nop 1\n"
"v_mfma_f32_16x16x32_bf16 v[120:123], a[176:179], v[228:231], v[120:123]\n"
"v_mfma_f32_16x16x32_bf16 v[124:127], a[180:183], v[228:231], v[124:127]\n"
"v_mfma_f32_16x16x32_bf16 v[76:79], a[184:187], v[228:231], v[76:79]\n"
"s_waitcnt lgkmcnt(0)\n"
"s_nop 7\n"
"s_branch .Lnorm\n"
".Lslow:\n"
"s_waitcnt lgkmcnt(0)\n"
"s_nop 7\n"
"v_mov_b32 v46, 4048155338\n"
"v_mov_b32 v224, v46\n"
"v_mov_b32 v225, v46\n"
"v_mov_b32 v226, v46\n"
"v_mov_b32 v227, v46\n"
"s_lshl_b32 s65, s40, 2\n"
"s_sub_u32 s65, 16, s65\n"
"s_cmp_eq_u32 s43, 0\n"
"s_cselect_b32 s65, s65, 0\n"
"s_cmp_gt_u32 s65, 0\n"
"s_cbranch_scc1 .Lmx0\n"
"ds_read_b128 a[168:171], v22 offset:0\n"
"s_waitcnt lgkmcnt(0)\n"
"v_mfma_f32_16x16x32_bf16 v[144:147], a[168:171], v[48:51], v[26:29]\n"
"s_nop 7\n"
"v_max3_f32 v224, v224, v144, v145\n"
"v_max3_f32 v224, v224, v146, v147\n"
".Lmx0:\n"
"s_cmp_gt_u32 s65, 1\n"
"s_cbranch_scc1 .Lmx1\n"
"ds_read_b128 a[168:171], v22 offset:1024\n"
"s_waitcnt lgkmcnt(0)\n"
"v_mfma_f32_16x16x32_bf16 v[144:147], a[168:171], v[48:51], 0\n"
"v_mfma_f32_16x16x32_bf16 v[148:151], a[168:171], v[52:55], v[26:29]\n"
"s_nop 6\n"
"v_max3_f32 v224, v224, v144, v145\n"
"v_max3_f32 v224, v224, v146, v147\n"
"v_max3_f32 v225, v225, v148, v149\n"
"v_max3_f32 v225, v225, v150, v151\n"
".Lmx1:\n"
"s_cmp_gt_u32 s65, 2\n"
"s_cbranch_scc1 .Lmx2\n"
"ds_read_b128 a[168:171], v22 offset:2048\n"
"s_waitcnt lgkmcnt(0)\n"
"v_mfma_f32_16x16x32_bf16 v[144:147], a[168:171], v[48:51], 0\n"
"v_mfma_f32_16x16x32_bf16 v[148:151], a[168:171], v[52:55], 0\n"
"v_mfma_f32_16x16x32_bf16 v[152:155], a[168:171], v[56:59], v[26:29]\n"
"s_nop 5\n"
"v_max3_f32 v224, v224, v144, v145\n"
"v_max3_f32 v224, v224, v146, v147\n"
"v_max3_f32 v225, v225, v148, v149\n"
"v_max3_f32 v225, v225, v150, v151\n"
"v_max3_f32 v226, v226, v152, v153\n"
"v_max3_f32 v226, v226, v154, v155\n"
".Lmx2:\n"
"s_cmp_gt_u32 s65, 3\n"
"s_cbranch_scc1 .Lmx3\n"
"ds_read_b128 a[168:171], v22 offset:3072\n"
"s_waitcnt lgkmcnt(0)\n"
"v_mfma_f32_16x16x32_bf16 v[144:147], a[168:171], v[48:51], 0\n"
"v_mfma_f32_16x16x32_bf16 v[148:151], a[168:171], v[52:55], 0\n"
"v_mfma_f32_16x16x32_bf16 v[152:155], a[168:171], v[56:59], 0\n"
"v_mfma_f32_16x16x32_bf16 v[156:159], a[168:171], v[60:63], v[26:29]\n"
"s_nop 4\n"
"v_max3_f32 v224, v224, v144, v145\n"
"v_max3_f32 v224, v224, v146, v147\n"
"v_max3_f32 v225, v225, v148, v149\n"
"v_max3_f32 v225, v225, v150, v151\n"
"v_max3_f32 v226, v226, v152, v153\n"
"v_max3_f32 v226, v226, v154, v155\n"
"v_max3_f32 v227, v227, v156, v157\n"
"v_max3_f32 v227, v227, v158, v159\n"
".Lmx3:\n"
"s_cmp_gt_u32 s65, 4\n"
"s_cbranch_scc1 .Lmx4\n"
"ds_read_b128 a[168:171], v22 offset:4096\n"
"s_waitcnt lgkmcnt(0)\n"
"v_mfma_f32_16x16x32_bf16 v[144:147], a[168:171], v[48:51], 0\n"
"v_mfma_f32_16x16x32_bf16 v[148:151], a[168:171], v[52:55], 0\n"
"v_mfma_f32_16x16x32_bf16 v[152:155], a[168:171], v[56:59], 0\n"
"v_mfma_f32_16x16x32_bf16 v[156:159], a[168:171], v[60:63], 0\n"
"s_nop 4\n"
"v_max3_f32 v224, v224, v144, v145\n"
"v_max3_f32 v224, v224, v146, v147\n"
"v_max3_f32 v225, v225, v148, v149\n"
"v_max3_f32 v225, v225, v150, v151\n"
"v_max3_f32 v226, v226, v152, v153\n"
"v_max3_f32 v226, v226, v154, v155\n"
"v_max3_f32 v227, v227, v156, v157\n"
"v_max3_f32 v227, v227, v158, v159\n"
".Lmx4:\n"
"s_cmp_gt_u32 s65, 5\n"
"s_cbranch_scc1 .Lmx5\n"
"ds_read_b128 a[168:171], v22 offset:5120\n"
"s_waitcnt lgkmcnt(0)\n"
"v_mfma_f32_16x16x32_bf16 v[144:147], a[168:171], v[48:51], 0\n"
"v_mfma_f32_16x16x32_bf16 v[148:151], a[168:171], v[52:55], 0\n"
"v_mfma_f32_16x16x32_bf16 v[152:155], a[168:171], v[56:59], 0\n"
"v_mfma_f32_16x16x32_bf16 v[156:159], a[168:171], v[60:63], 0\n"
"s_nop 4\n"
"v_max3_f32 v224, v224, v144, v145\n"
"v_max3_f32 v224, v224, v146, v147\n"
"v_max3_f32 v225, v225, v148, v149\n"
"v_max3_f32 v225, v225, v150, v151\n"
"v_max3_f32 v226, v226, v152, v153\n"
"v_max3_f32 v226, v226, v154, v155\n"
"v_max3_f32 v227, v227, v156, v157\n"
"v_max3_f32 v227, v227, v158, v159\n"
".Lmx5:\n"
"s_cmp_gt_u32 s65, 6\n"
"s_cbranch_scc1 .Lmx6\n"
"ds_read_b128 a[168:171], v22 offset:6144\n"
"s_waitcnt lgkmcnt(0)\n"
"v_mfma_f32_16x16x32_bf16 v[144:147], a[168:171], v[48:51], 0\n"
"v_mfma_f32_16x16x32_bf16 v[148:151], a[168:171], v[52:55], 0\n"
"v_mfma_f32_16x16x32_bf16 v[152:155], a[168:171], v[56:59], 0\n"
"v_mfma_f32_16x16x32_bf16 v[156:159], a[168:171], v[60:63], 0\n"
"s_nop 4\n"
"v_max3_f32 v224, v224, v144, v145\n"
"v_max3_f32 v224, v224, v146, v147\n"
"v_max3_f32 v225, v225, v148, v149\n"
"v_max3_f32 v225, v225, v150, v151\n"
"v_max3_f32 v226, v226, v152, v153\n"
"v_max3_f32 v226, v226, v154, v155\n"
"v_max3_f32 v227, v227, v156, v157\n"
"v_max3_f32 v227, v227, v158, v159\n"
".Lmx6:\n"
"s_cmp_gt_u32 s65, 7\n"
"s_cbranch_scc1 .Lmx7\n"
"ds_read_b128 a[168:171], v22 offset:7168\n"
"s_waitcnt lgkmcnt(0)\n"
"v_mfma_f32_16x16x32_bf16 v[144:147], a[168:171], v[48:51], 0\n"
"v_mfma_f32_16x16x32_bf16 v[148:151], a[168:171], v[52:55], 0\n"
"v_mfma_f32_16x16x32_bf16 v[152:155], a[168:171], v[56:59], 0\n"
"v_mfma_f32_16x16x32_bf16 v[156:159], a[168:171], v[60:63], 0\n"
"s_nop 4\n"
"v_max3_f32 v224, v224, v144, v145\n"
"v_max3_f32 v224, v224, v146, v147\n"
"v_max3_f32 v225, v225, v148, v149\n"
"v_max3_f32 v225, v225, v150, v151\n"
"v_max3_f32 v226, v226, v152, v153\n"
"v_max3_f32 v226, v226, v154, v155\n"
"v_max3_f32 v227, v227, v156, v157\n"
"v_max3_f32 v227, v227, v158, v159\n"
".Lmx7:\n"
"s_cmp_gt_u32 s65, 8\n"
"s_cbranch_scc1 .Lmx8\n"
"ds_read_b128 a[168:171], v22 offset:8192\n"
"s_waitcnt lgkmcnt(0)\n"
"v_mfma_f32_16x16x32_bf16 v[144:147], a[168:171], v[48:51], 0\n"
"v_mfma_f32_16x16x32_bf16 v[148:151], a[168:171], v[52:55], 0\n"
"v_mfma_f32_16x16x32_bf16 v[152:155], a[168:171], v[56:59], 0\n"
"v_mfma_f32_16x16x32_bf16 v[156:159], a[168:171], v[60:63], 0\n"
"s_nop 4\n"
"v_max3_f32 v224, v224, v144, v145\n"
"v_max3_f32 v224, v224, v146, v147\n"
"v_max3_f32 v225, v225, v148, v149\n"
"v_max3_f32 v225, v225, v150, v151\n"
"v_max3_f32 v226, v226, v152, v153\n"
"v_max3_f32 v226, v226, v154, v155\n"
"v_max3_f32 v227, v227, v156, v157\n"
"v_max3_f32 v227, v227, v158, v159\n"
".Lmx8:\n"
"s_cmp_gt_u32 s65, 9\n"
"s_cbranch_scc1 .Lmx9\n"
"ds_read_b128 a[168:171], v22 offset:9216\n"
"s_waitcnt lgkmcnt(0)\n"
"v_mfma_f32_16x16x32_bf16 v[144:147], a[168:171], v[48:51], 0\n"
"v_mfma_f32_16x16x32_bf16 v[148:151], a[168:171], v[52:55], 0\n"
"v_mfma_f32_16x16x32_bf16 v[152:155], a[168:171], v[56:59], 0\n"
"v_mfma_f32_16x16x32_bf16 v[156:159], a[168:171], v[60:63], 0\n"
"s_nop 4\n"
"v_max3_f32 v224, v224, v144, v145\n"
"v_max3_f32 v224, v224, v146, v147\n"
"v_max3_f32 v225, v225, v148, v149\n"
"v_max3_f32 v225, v225, v150, v151\n"
"v_max3_f32 v226, v226, v152, v153\n"
"v_max3_f32 v226, v226, v154, v155\n"
"v_max3_f32 v227, v227, v156, v157\n"
"v_max3_f32 v227, v227, v158, v159\n"
".Lmx9:\n"
"s_cmp_gt_u32 s65, 10\n"
"s_cbranch_scc1 .Lmx10\n"
"ds_read_b128 a[168:171], v22 offset:10240\n"
"s_waitcnt lgkmcnt(0)\n"
"v_mfma_f32_16x16x32_bf16 v[144:147], a[168:171], v[48:51], 0\n"
"v_mfma_f32_16x16x32_bf16 v[148:151], a[168:171], v[52:55], 0\n"
"v_mfma_f32_16x16x32_bf16 v[152:155], a[168:171], v[56:59], 0\n"
"v_mfma_f32_16x16x32_bf16 v[156:159], a[168:171], v[60:63], 0\n"
"s_nop 4\n"
"v_max3_f32 v224, v224, v144, v145\n"
"v_max3_f32 v224, v224, v146, v147\n"
"v_max3_f32 v225, v225, v148, v149\n"
"v_max3_f32 v225, v225, v150, v151\n"
"v_max3_f32 v226, v226, v152, v153\n"
"v_max3_f32 v226, v226, v154, v155\n"
"v_max3_f32 v227, v227, v156, v157\n"
"v_max3_f32 v227, v227, v158, v159\n"
".Lmx10:\n"
"s_cmp_gt_u32 s65, 11\n"
"s_cbranch_scc1 .Lmx11\n"
"ds_read_b128 a[168:171], v22 offset:11264\n"
"s_waitcnt lgkmcnt(0)\n"
"v_mfma_f32_16x16x32_bf16 v[144:147], a[168:171], v[48:51], 0\n"
"v_mfma_f32_16x16x32_bf16 v[148:151], a[168:171], v[52:55], 0\n"
"v_mfma_f32_16x16x32_bf16 v[152:155], a[168:171], v[56:59], 0\n"
"v_mfma_f32_16x16x32_bf16 v[156:159], a[168:171], v[60:63], 0\n"
"s_nop 4\n"
"v_max3_f32 v224, v224, v144, v145\n"
"v_max3_f32 v224, v224, v146, v147\n"
"v_max3_f32 v225, v225, v148, v149\n"
"v_max3_f32 v225, v225, v150, v151\n"
"v_max3_f32 v226, v226, v152, v153\n"
"v_max3_f32 v226, v226, v154, v155\n"
"v_max3_f32 v227, v227, v156, v157\n"
"v_max3_f32 v227, v227, v158, v159\n"
".Lmx11:\n"
"s_cmp_gt_u32 s65, 12\n"
"s_cbranch_scc1 .Lmx12\n"
"ds_read_b128 a[168:171], v22 offset:12288\n"
"s_waitcnt lgkmcnt(0)\n"
"v_mfma_f32_16x16x32_bf16 v[144:147], a[168:171], v[48:51], 0\n"
"v_mfma_f32_16x16x32_bf16 v[148:151], a[168:171], v[52:55], 0\n"
"v_mfma_f32_16x16x32_bf16 v[152:155], a[168:171], v[56:59], 0\n"
"v_mfma_f32_16x16x32_bf16 v[156:159], a[168:171], v[60:63], 0\n"
"s_nop 4\n"
"v_max3_f32 v224, v224, v144, v145\n"
"v_max3_f32 v224, v224, v146, v147\n"
"v_max3_f32 v225, v225, v148, v149\n"
"v_max3_f32 v225, v225, v150, v151\n"
"v_max3_f32 v226, v226, v152, v153\n"
"v_max3_f32 v226, v226, v154, v155\n"
"v_max3_f32 v227, v227, v156, v157\n"
"v_max3_f32 v227, v227, v158, v159\n"
".Lmx12:\n"
"s_cmp_gt_u32 s65, 13\n"
"s_cbranch_scc1 .Lmx13\n"
"ds_read_b128 a[168:171], v22 offset:13312\n"
"s_waitcnt lgkmcnt(0)\n"
"v_mfma_f32_16x16x32_bf16 v[144:147], a[168:171], v[48:51], 0\n"
"v_mfma_f32_16x16x32_bf16 v[148:151], a[168:171], v[52:55], 0\n"
"v_mfma_f32_16x16x32_bf16 v[152:155], a[168:171], v[56:59], 0\n"
"v_mfma_f32_16x16x32_bf16 v[156:159], a[168:171], v[60:63], 0\n"
"s_nop 4\n"
"v_max3_f32 v224, v224, v144, v145\n"
"v_max3_f32 v224, v224, v146, v147\n"
"v_max3_f32 v225, v225, v148, v149\n"
"v_max3_f32 v225, v225, v150, v151\n"
"v_max3_f32 v226, v226, v152, v153\n"
"v_max3_f32 v226, v226, v154, v155\n"
"v_max3_f32 v227, v227, v156, v157\n"
"v_max3_f32 v227, v227, v158, v159\n"
".Lmx13:\n"
"s_cmp_gt_u32 s65, 14\n"
"s_cbranch_scc1 .Lmx14\n"
"ds_read_b128 a[168:171], v22 offset:14336\n"
"s_waitcnt lgkmcnt(0)\n"
"v_mfma_f32_16x16x32_bf16 v[144:147], a[168:171], v[48:51], 0\n"
"v_mfma_f32_16x16x32_bf16 v[148:151], a[168:171], v[52:55], 0\n"
"v_mfma_f32_16x16x32_bf16 v[152:155], a[168:171], v[56:59], 0\n"
"v_mfma_f32_16x16x32_bf16 v[156:159], a[168:171], v[60:63], 0\n"
"s_nop 4\n"
"v_max3_f32 v224, v224, v144, v145\n"
"v_max3_f32 v224, v224, v146, v147\n"
"v_max3_f32 v225, v225, v148, v149\n"
"v_max3_f32 v225, v225, v150, v151\n"
"v_max3_f32 v226, v226, v152, v153\n"
"v_max3_f32 v226, v226, v154, v155\n"
"v_max3_f32 v227, v227, v156, v157\n"
"v_max3_f32 v227, v227, v158, v159\n"
".Lmx14:\n"
"s_cmp_gt_u32 s65, 15\n"
"s_cbranch_scc1 .Lmx15\n"
"ds_read_b128 a[168:171], v22 offset:15360\n"
"s_waitcnt lgkmcnt(0)\n"
"v_mfma_f32_16x16x32_bf16 v[144:147], a[168:171], v[48:51], 0\n"
"v_mfma_f32_16x16x32_bf16 v[148:151], a[168:171], v[52:55], 0\n"
"v_mfma_f32_16x16x32_bf16 v[152:155], a[168:171], v[56:59], 0\n"
"v_mfma_f32_16x16x32_bf16 v[156:159], a[168:171], v[60:63], 0\n"
"s_nop 4\n"
"v_max3_f32 v224, v224, v144, v145\n"
"v_max3_f32 v224, v224, v146, v147\n"
"v_max3_f32 v225, v225, v148, v149\n"
"v_max3_f32 v225, v225, v150, v151\n"
"v_max3_f32 v226, v226, v152, v153\n"
"v_max3_f32 v226, v226, v154, v155\n"
"v_max3_f32 v227, v227, v156, v157\n"
"v_max3_f32 v227, v227, v158, v159\n"
".Lmx15:\n"
"s_cmp_gt_u32 s65, 16\n"
"s_cbranch_scc1 .Lmx16\n"
"ds_read_b128 a[168:171], v22 offset:16384\n"
"s_waitcnt lgkmcnt(0)\n"
"v_mfma_f32_16x16x32_bf16 v[144:147], a[168:171], v[48:51], v[30:33]\n"
"v_mfma_f32_16x16x32_bf16 v[148:151], a[168:171], v[52:55], 0\n"
"v_mfma_f32_16x16x32_bf16 v[152:155], a[168:171], v[56:59], 0\n"
"v_mfma_f32_16x16x32_bf16 v[156:159], a[168:171], v[60:63], 0\n"
"s_nop 4\n"
"v_max3_f32 v224, v224, v144, v145\n"
"v_max3_f32 v224, v224, v146, v147\n"
"v_max3_f32 v225, v225, v148, v149\n"
"v_max3_f32 v225, v225, v150, v151\n"
"v_max3_f32 v226, v226, v152, v153\n"
"v_max3_f32 v226, v226, v154, v155\n"
"v_max3_f32 v227, v227, v156, v157\n"
"v_max3_f32 v227, v227, v158, v159\n"
".Lmx16:\n"
"s_cmp_gt_u32 s65, 17\n"
"s_cbranch_scc1 .Lmx17\n"
"ds_read_b128 a[168:171], v22 offset:17408\n"
"s_waitcnt lgkmcnt(0)\n"
"v_mfma_f32_16x16x32_bf16 v[148:151], a[168:171], v[52:55], v[30:33]\n"
"v_mfma_f32_16x16x32_bf16 v[152:155], a[168:171], v[56:59], 0\n"
"v_mfma_f32_16x16x32_bf16 v[156:159], a[168:171], v[60:63], 0\n"
"s_nop 5\n"
"v_max3_f32 v225, v225, v148, v149\n"
"v_max3_f32 v225, v225, v150, v151\n"
"v_max3_f32 v226, v226, v152, v153\n"
"v_max3_f32 v226, v226, v154, v155\n"
"v_max3_f32 v227, v227, v156, v157\n"
"v_max3_f32 v227, v227, v158, v159\n"
".Lmx17:\n"
"s_cmp_gt_u32 s65, 18\n"
"s_cbranch_scc1 .Lmx18\n"
"ds_read_b128 a[168:171], v22 offset:18432\n"
"s_waitcnt lgkmcnt(0)\n"
"v_mfma_f32_16x16x32_bf16 v[152:155], a[168:171], v[56:59], v[30:33]\n"
"v_mfma_f32_16x16x32_bf16 v[156:159], a[168:171], v[60:63], 0\n"
"s_nop 6\n"
"v_max3_f32 v226, v226, v152, v153\n"
"v_max3_f32 v226, v226, v154, v155\n"
"v_max3_f32 v227, v227, v156, v157\n"
"v_max3_f32 v227, v227, v158, v159\n"
".Lmx18:\n"
"s_cmp_gt_u32 s65, 19\n"
"s_cbranch_scc1 .Lmx19\n"
"ds_read_b128 a[168:171], v22 offset:19456\n"
"s_waitcnt lgkmcnt(0)\n"
"v_mfma_f32_16x16x32_bf16 v[156:159], a[168:171], v[60:63], v[30:33]\n"
"s_nop 7\n"
"v_max3_f32 v227, v227, v156, v157\n"
"v_max3_f32 v227, v227, v158, v159\n"
".Lmx19:\n"
"v_mov_b32 v25, v224\n"
"s_nop 1\n"
"v_permlane32_swap_b32 v25, v224\n"
"v_max_f32 v224, v224, v25\n"
"v_mov_b32 v25, v224\n"
"s_nop 1\n"
"v_permlane16_swap_b32 v25, v224\n"
"v_max_f32 v224, v224, v25\n"
"v_sub_f32 v160, 0, v224\n"
"v_sub_f32 v164, v26, v224\n"
"v_sub_f32 v168, v30, v224\n"
"v_sub_f32 v161, 0, v224\n"
"v_sub_f32 v165, v27, v224\n"
"v_sub_f32 v169, v31, v224\n"
"v_sub_f32 v162, 0, v224\n"
"v_sub_f32 v166, v28, v224\n"
"v_sub_f32 v170, v32, v224\n"
"v_sub_f32 v163, 0, v224\n"
"v_sub_f32 v167, v29, v224\n"
"v_sub_f32 v171, v33, v224\n"
"v_mov_b32 v25, v225\n"
"s_nop 1\n"
"v_permlane32_swap_b32 v25, v225\n"
"v_max_f32 v225, v225, v25\n"
"v_mov_b32 v25, v225\n"
"s_nop 1\n"
"v_permlane16_swap_b32 v25, v225\n"
"v_max_f32 v225, v225, v25\n"
"v_sub_f32 v172, 0, v225\n"
"v_sub_f32 v176, v26, v225\n"
"v_sub_f32 v180, v30, v225\n"
"v_sub_f32 v173, 0, v225\n"
"v_sub_f32 v177, v27, v225\n"
"v_sub_f32 v181, v31, v225\n"
"v_sub_f32 v174, 0, v225\n"
"v_sub_f32 v178, v28, v225\n"
"v_sub_f32 v182, v32, v225\n"
"v_sub_f32 v175, 0, v225\n"
"v_sub_f32 v179, v29, v225\n"
"v_sub_f32 v183, v33, v225\n"
"v_mov_b32 v25, v226\n"
"s_nop 1\n"
"v_permlane32_swap_b32 v25, v226\n"
"v_max_f32 v226, v226, v25\n"
"v_mov_b32 v25, v226\n"
"s_nop 1\n"
"v_permlane16_swap_b32 v25, v226\n"
"v_max_f32 v226, v226, v25\n"
"v_sub_f32 v184, 0, v226\n"
"v_sub_f32 v188, v26, v226\n"
"v_sub_f32 v192, v30, v226\n"
"v_sub_f32 v185, 0, v226\n"
"v_sub_f32 v189, v27, v226\n"
"v_sub_f32 v193, v31, v226\n"
"v_sub_f32 v186, 0, v226\n"
"v_sub_f32 v190, v28, v226\n"
"v_sub_f32 v194, v32, v226\n"
"v_sub_f32 v187, 0, v226\n"
"v_sub_f32 v191, v29, v226\n"
"v_sub_f32 v195, v33, v226\n"
"v_mov_b32 v25, v227\n"
"s_nop 1\n"
"v_permlane32_swap_b32 v25, v227\n"
"v_max_f32 v227, v227, v25\n"
"v_mov_b32 v25, v227\n"
"s_nop 1\n"
"v_permlane16_swap_b32 v25, v227\n"
"v_max_f32 v227, v227, v25\n"
"v_sub_f32 v196, 0, v227\n"
"v_sub_f32 v200, v26, v227\n"
"v_sub_f32 v204, v30, v227\n"
"v_sub_f32 v197, 0, v227\n"
"v_sub_f32 v201, v27, v227\n"
"v_sub_f32 v205, v31, v227\n"
"v_sub_f32 v198, 0, v227\n"
"v_sub_f32 v202, v28, v227\n"
"v_sub_f32 v206, v32, v227\n"
"v_sub_f32 v199, 0, v227\n"
"v_sub_f32 v203, v29, v227\n"
"v_sub_f32 v207, v33, v227\n"
"v_mfma_f32_16x16x32_bf16 v[144:147], v[88:91], v[56:59], v[192:195]\n"
"s_nop 7\n"
"v_exp_f32 v144, v144\n"
"v_mfma_f32_16x16x32_bf16 v[152:155], v[88:91], v[60:63], v[196:199]\n"
"v_exp_f32_e64 v145, v145\n"
"v_mfma_f32_16x16x32_bf16 v[156:159], v[92:95], v[60:63], v[204:207]\n"
"v_exp_f32 v146, v146\n"
"v_exp_f32 v147, v147\n"
"v_cvt_pk_bf16_f32 v224, v144, v145\n"
"v_cvt_pk_bf16_f32 v225, v146, v147\n"
"v_mov_b64 v[226:227], 0\n"
"s_nop 0\n"
"v_exp_f32_e64 v152, v152\n"
"v_mfma_f32_16x16x32_bf16 v[144:147], v[80:83], v[48:51], v[168:171]\n"
"v_exp_f32 v153, v153\n"
"v_exp_f32 v154, v154\n"
"v_mfma_f32_16x16x32_bf16 v[112:115], v[140:143], v[224:227], 0\n"
"v_exp_f32 v155, v155\n"
"v_exp_f32 v156, v156\n"
"v_mfma_f32_16x16x32_bf16 v[116:119], v[136:139], v[224:227], 0\n"
"v_cvt_pk_bf16_f32 v228, v152, v153\n"
"v_exp_f32_e64 v157, v157\n"
"v_mfma_f32_16x16x32_bf16 v[72:75], a[184:187], v[224:227], 0\n"
"v_exp_f32_e64 v158, v158\n"
"v_cvt_pk_bf16_f32 v229, v154, v155\n"
"v_exp_f32_e64 v159, v159\n"
"v_cvt_pk_bf16_f32 v230, v156, v157\n"
"v_cvt_pk_bf16_f32 v231, v158, v159\n"
"v_exp_f32_e64 v144, v144\n"
"v_mfma_f32_16x16x32_bf16 v[152:155], v[80:83], v[52:55], v[172:175]\n"
"v_exp_f32_e64 v145, v145\n"
"v_mfma_f32_16x16x32_bf16 v[156:159], v[84:87], v[52:55], v[180:183]\n"
"v_exp_f32_e64 v146, v146\n"
"v_mfma_f32_16x16x32_bf16 v[120:123], v[140:143], v[228:231], 0\n"
"v_exp_f32_e64 v147, v147\n"
"v_mfma_f32_16x16x32_bf16 v[124:127], v[136:139], v[228:231], 0\n"
"v_cvt_pk_bf16_f32 v224, v144, v145\n"
"v_cvt_pk_bf16_f32 v225, v146, v147\n"
"v_mfma_f32_16x16x32_bf16 v[76:79], a[184:187], v[228:231], 0\n"
"v_mov_b64 v[226:227], 0\n"
"v_exp_f32 v152, v152\n"
"v_mfma_f32_16x16x32_bf16 v[144:147], v[80:83], v[56:59], v[184:187]\n"
"v_exp_f32 v153, v153\n"
"v_exp_f32 v154, v154\n"
"v_mfma_f32_16x16x32_bf16 v[148:151], v[84:87], v[56:59], v[184:187]\n"
"v_exp_f32 v155, v155\n"
"v_exp_f32 v156, v156\n"
"v_mfma_f32_16x16x32_bf16 v[96:99], v[132:135], v[224:227], 0\n"
"v_cvt_pk_bf16_f32 v228, v152, v153\n"
"v_exp_f32_e64 v157, v157\n"
"v_mfma_f32_16x16x32_bf16 v[100:103], v[128:131], v[224:227], 0\n"
"v_exp_f32_e64 v158, v158\n"
"v_cvt_pk_bf16_f32 v229, v154, v155\n"
"v_mfma_f32_16x16x32_bf16 v[64:67], a[184:187], v[224:227], 0\n"
"v_exp_f32_e64 v159, v159\n"
"v_cvt_pk_bf16_f32 v230, v156, v157\n"
"v_cvt_pk_bf16_f32 v231, v158, v159\n"
"v_exp_f32_e64 v144, v144\n"
"v_mfma_f32_16x16x32_bf16 v[152:155], v[80:83], v[60:63], v[196:199]\n"
"ds_read_b128 a[168:171], v22 offset:14336\n"
"v_exp_f32 v145, v145\n"
"v_exp_f32 v146, v146\n"
"v_mfma_f32_16x16x32_bf16 v[156:159], v[84:87], v[60:63], v[196:199]\n"
"ds_read_b128 a[172:175], v22 offset:15360\n"
"v_exp_f32 v147, v147\n"
"v_exp_f32 v148, v148\n"
"v_mfma_f32_16x16x32_bf16 v[104:107], v[132:135], v[228:231], 0\n"
"v_cvt_pk_bf16_f32 v224, v144, v145\n"
"v_exp_f32_e64 v149, v149\n"
"v_mfma_f32_16x16x32_bf16 v[108:111], v[128:131], v[228:231], 0\n"
"v_exp_f32_e64 v150, v150\n"
"v_cvt_pk_bf16_f32 v225, v146, v147\n"
"v_mfma_f32_16x16x32_bf16 v[68:71], a[184:187], v[228:231], 0\n"
"v_exp_f32_e64 v151, v151\n"
"v_cvt_pk_bf16_f32 v226, v148, v149\n"
"v_cvt_pk_bf16_f32 v227, v150, v151\n"
"v_exp_f32 v152, v152\n"
"s_waitcnt lgkmcnt(0)\n"
"v_mfma_f32_16x16x32_bf16 v[144:147], a[168:171], v[48:51], v[160:163]\n"
"v_exp_f32 v153, v153\n"
"v_exp_f32 v154, v154\n"
"v_mfma_f32_16x16x32_bf16 v[148:151], a[172:175], v[48:51], v[160:163]\n"
"v_exp_f32 v155, v155\n"
"v_exp_f32 v156, v156\n"
"v_mfma_f32_16x16x32_bf16 v[112:115], v[132:135], v[224:227], v[112:115]\n"
"v_cvt_pk_bf16_f32 v228, v152, v153\n"
"v_exp_f32_e64 v157, v157\n"
"v_mfma_f32_16x16x32_bf16 v[116:119], v[128:131], v[224:227], v[116:119]\n"
"v_exp_f32_e64 v158, v158\n"
"v_cvt_pk_bf16_f32 v229, v154, v155\n"
"v_mfma_f32_16x16x32_bf16 v[72:75], a[184:187], v[224:227], v[72:75]\n"
"v_exp_f32_e64 v159, v159\n"
"v_cvt_pk_bf16_f32 v230, v156, v157\n"
"v_cvt_pk_bf16_f32 v231, v158, v159\n"
"s_cmp_eq_u32 s91, 7\n"
"s_cbranch_scc1 .Lax7s\n"
"v_exp_f32_e64 v144, v144\n"
"v_mfma_f32_16x16x32_bf16 v[152:155], a[168:171], v[52:55], v[172:175]\n"
"v_exp_f32 v145, v145\n"
"v_exp_f32 v146, v146\n"
"v_mfma_f32_16x16x32_bf16 v[156:159], a[172:175], v[52:55], v[172:175]\n"
"v_exp_f32 v147, v147\n"
"v_exp_f32 v148, v148\n"
"v_mfma_f32_16x16x32_bf16 v[120:123], v[132:135], v[228:231], v[120:123]\n"
"ds_read_b128 a[176:179], v22 offset:47104\n"
"v_cvt_pk_bf16_f32 v224, v144, v145\n"
"v_exp_f32_e64 v149, v149\n"
"v_mfma_f32_16x16x32_bf16 v[124:127], v[128:131], v[228:231], v[124:127]\n"
"ds_read_b128 a[180:183], v22 offset:48128\n"
"v_exp_f32_e64 v150, v150\n"
"v_cvt_pk_bf16_f32 v225, v146, v147\n"
"v_mfma_f32_16x16x32_bf16 v[76:79], a[184:187], v[228:231], v[76:79]\n"
"v_exp_f32_e64 v151, v151\n"
"v_cvt_pk_bf16_f32 v226, v148, v149\n"
"v_cvt_pk_bf16_f32 v227, v150, v151\n"
"v_exp_f32_e64 v152, v152\n"
"v_mfma_f32_16x16x32_bf16 v[144:147], a[168:171], v[56:59], v[184:187]\n"
"v_exp_f32 v153, v153\n"
"v_exp_f32 v154, v154\n"
"v_mfma_f32_16x16x32_bf16 v[148:151], a[172:175], v[56:59], v[184:187]\n"
"v_exp_f32 v155, v155\n"
"v_exp_f32_e64 v156, v156\n"
"s_waitcnt lgkmcnt(0)\n"
"v_mfma_f32_16x16x32_bf16 v[96:99], a[176:179], v[224:227], v[96:99]\n"
"v_cvt_pk_bf16_f32 v228, v152, v153\n"
"v_exp_f32_e64 v157, v157\n"
"v_mfma_f32_16x16x32_bf16 v[100:103], a[180:183], v[224:227], v[100:103]\n"
"v_exp_f32_e64 v158, v158\n"
"v_cvt_pk_bf16_f32 v229, v154, v155\n"
"v_mfma_f32_16x16x32_bf16 v[64:67], a[184:187], v[224:227], v[64:67]\n"
"v_exp_f32_e64 v159, v159\n"
"v_cvt_pk_bf16_f32 v230, v156, v157\n"
"v_cvt_pk_bf16_f32 v231, v158, v159\n"
"v_exp_f32_e64 v144, v144\n"
"v_mfma_f32_16x16x32_bf16 v[152:155], a[168:171], v[60:63], v[196:199]\n"
"ds_read_b128 a[168:171], v22 offset:12288\n"
"v_exp_f32 v145, v145\n"
"v_exp_f32 v146, v146\n"
"v_mfma_f32_16x16x32_bf16 v[156:159], a[172:175], v[60:63], v[196:199]\n"
"ds_read_b128 a[172:175], v22 offset:13312\n"
"v_exp_f32 v147, v147\n"
"v_exp_f32 v148, v148\n"
"v_mfma_f32_16x16x32_bf16 v[104:107], a[176:179], v[228:231], v[104:107]\n"
"v_cvt_pk_bf16_f32 v224, v144, v145\n"
"v_exp_f32_e64 v149, v149\n"
"v_mfma_f32_16x16x32_bf16 v[108:111], a[180:183], v[228:231], v[108:111]\n"
"v_exp_f32_e64 v150, v150\n"
"v_cvt_pk_bf16_f32 v225, v146, v147\n"
"v_mfma_f32_16x16x32_bf16 v[68:71], a[184:187], v[228:231], v[68:71]\n"
"v_exp_f32_e64 v151, v151\n"
"v_cvt_pk_bf16_f32 v226, v148, v149\n"
"v_cvt_pk_bf16_f32 v227, v150, v151\n"
"v_exp_f32 v152, v152\n"
"s_waitcnt lgkmcnt(0)\n"
"v_mfma_f32_16x16x32_bf16 v[144:147], a[168:171], v[48:51], v[160:163]\n"
"v_exp_f32 v153, v153\n"
"v_exp_f32 v154, v154\n"
"v_mfma_f32_16x16x32_bf16 v[148:151], a[172:175], v[48:51], v[160:163]\n"
"v_exp_f32 v155, v155\n"
"v_exp_f32 v156, v156\n"
"v_mfma_f32_16x16x32_bf16 v[112:115], a[176:179], v[224:227], v[112:115]\n"
"v_cvt_pk_bf16_f32 v228, v152, v153\n"
"v_exp_f32_e64 v157, v157\n"
"v_mfma_f32_16x16x32_bf16 v[116:119], a[180:183], v[224:227], v[116:119]\n"
"v_exp_f32_e64 v158, v158\n"
"v_cvt_pk_bf16_f32 v229, v154, v155\n"
"v_mfma_f32_16x16x32_bf16 v[72:75], a[184:187], v[224:227], v[72:75]\n"
"v_exp_f32_e64 v159, v159\n"
"v_cvt_pk_bf16_f32 v230, v156, v157\n"
"v_cvt_pk_bf16_f32 v231, v158, v159\n"
"v_exp_f32_e64 v144, v144\n"
"v_mfma_f32_16x16x32_bf16 v[152:155], a[168:171], v[52:55], v[172:175]\n"
"v_exp_f32 v145, v145\n"
"v_exp_f32 v146, v146\n"
"v_mfma_f32_16x16x32_bf16 v[156:159], a[172:175], v[52:55], v[172:175]\n"
"v_exp_f32 v147, v147\n"
"v_exp_f32 v148, v148\n"
"v_mfma_f32_16x16x32_bf16 v[120:123], a[176:179], v[228:231], v[120:123]\n"
"ds_read_b128 a[176:179], v22 offset:45056\n"
"v_cvt_pk_bf16_f32 v224, v144, v145\n"
"v_exp_f32_e64 v149, v149\n"
"v_mfma_f32_16x16x32_bf16 v[124:127], a[180:183], v[228:231], v[124:127]\n"
"ds_read_b128 a[180:183], v22 offset:46080\n"
"v_exp_f32_e64 v150, v150\n"
"v_cvt_pk_bf16_f32 v225, v146, v147\n"
"v_mfma_f32_16x16x32_bf16 v[76:79], a[184:187], v[228:231], v[76:79]\n"
"v_exp_f32_e64 v151, v151\n"
"v_cvt_pk_bf16_f32 v226, v148, v149\n"
"v_cvt_pk_bf16_f32 v227, v150, v151\n"
"v_exp_f32_e64 v152, v152\n"
"v_mfma_f32_16x16x32_bf16 v[144:147], a[168:171], v[56:59], v[184:187]\n"
"v_exp_f32 v153, v153\n"
"v_exp_f32 v154, v154\n"
"v_mfma_f32_16x16x32_bf16 v[148:151], a[172:175], v[56:59], v[184:187]\n"
"v_exp_f32 v155, v155\n"
"v_exp_f32_e64 v156, v156\n"
"s_waitcnt lgkmcnt(0)\n"
"v_mfma_f32_16x16x32_bf16 v[96:99], a[176:179], v[224:227], v[96:99]\n"
"v_cvt_pk_bf16_f32 v228, v152, v153\n"
"v_exp_f32_e64 v157, v157\n"
"v_mfma_f32_16x16x32_bf16 v[100:103], a[180:183], v[224:227], v[100:103]\n"
"v_exp_f32_e64 v158, v158\n"
"v_cvt_pk_bf16_f32 v229, v154, v155\n"
"v_mfma_f32_16x16x32_bf16 v[64:67], a[184:187], v[224:227], v[64:67]\n"
"v_exp_f32_e64 v159, v159\n"
"v_cvt_pk_bf16_f32 v230, v156, v157\n"
"v_cvt_pk_bf16_f32 v231, v158, v159\n"
"v_exp_f32_e64 v144, v144\n"
"v_mfma_f32_16x16x32_bf16 v[152:155], a[168:171], v[60:63], v[196:199]\n"
"ds_read_b128 a[168:171], v22 offset:10240\n"
"v_exp_f32 v145, v145\n"
"v_exp_f32 v146, v146\n"
"v_mfma_f32_16x16x32_bf16 v[156:159], a[172:175], v[60:63], v[196:199]\n"
"ds_read_b128 a[172:175], v22 offset:11264\n"
"v_exp_f32 v147, v147\n"
"v_exp_f32 v148, v148\n"
"v_mfma_f32_16x16x32_bf16 v[104:107], a[176:179], v[228:231], v[104:107]\n"
"v_cvt_pk_bf16_f32 v224, v144, v145\n"
"v_exp_f32_e64 v149, v149\n"
"v_mfma_f32_16x16x32_bf16 v[108:111], a[180:183], v[228:231], v[108:111]\n"
"v_exp_f32_e64 v150, v150\n"
"v_cvt_pk_bf16_f32 v225, v146, v147\n"
"v_mfma_f32_16x16x32_bf16 v[68:71], a[184:187], v[228:231], v[68:71]\n"
"v_exp_f32_e64 v151, v151\n"
"v_cvt_pk_bf16_f32 v226, v148, v149\n"
"v_cvt_pk_bf16_f32 v227, v150, v151\n"
"v_exp_f32 v152, v152\n"
"s_waitcnt lgkmcnt(0)\n"
"v_mfma_f32_16x16x32_bf16 v[144:147], a[168:171], v[48:51], v[160:163]\n"
"v_exp_f32 v153, v153\n"
"v_exp_f32 v154, v154\n"
"v_mfma_f32_16x16x32_bf16 v[148:151], a[172:175], v[48:51], v[160:163]\n"
"v_exp_f32 v155, v155\n"
"v_exp_f32 v156, v156\n"
"v_mfma_f32_16x16x32_bf16 v[112:115], a[176:179], v[224:227], v[112:115]\n"
"v_cvt_pk_bf16_f32 v228, v152, v153\n"
"v_exp_f32_e64 v157, v157\n"
"v_mfma_f32_16x16x32_bf16 v[116:119], a[180:183], v[224:227], v[116:119]\n"
"v_exp_f32_e64 v158, v158\n"
"v_cvt_pk_bf16_f32 v229, v154, v155\n"
"v_mfma_f32_16x16x32_bf16 v[72:75], a[184:187], v[224:227], v[72:75]\n"
"v_exp_f32_e64 v159, v159\n"
"v_cvt_pk_bf16_f32 v230, v156, v157\n"
"v_cvt_pk_bf16_f32 v231, v158, v159\n"
"s_cmp_eq_u32 s91, 5\n"
"s_cbranch_scc1 .Lax5s\n"
"v_exp_f32_e64 v144, v144\n"
"v_mfma_f32_16x16x32_bf16 v[152:155], a[168:171], v[52:55], v[172:175]\n"
"v_exp_f32 v145, v145\n"
"v_exp_f32 v146, v146\n"
"v_mfma_f32_16x16x32_bf16 v[156:159], a[172:175], v[52:55], v[172:175]\n"
"v_exp_f32 v147, v147\n"
"v_exp_f32 v148, v148\n"
"v_mfma_f32_16x16x32_bf16 v[120:123], a[176:179], v[228:231], v[120:123]\n"
"ds_read_b128 a[176:179], v22 offset:43008\n"
"v_cvt_pk_bf16_f32 v224, v144, v145\n"
"v_exp_f32_e64 v149, v149\n"
"v_mfma_f32_16x16x32_bf16 v[124:127], a[180:183], v[228:231], v[124:127]\n"
"ds_read_b128 a[180:183], v22 offset:44032\n"
"v_exp_f32_e64 v150, v150\n"
"v_cvt_pk_bf16_f32 v225, v146, v147\n"
"v_mfma_f32_16x16x32_bf16 v[76:79], a[184:187], v[228:231], v[76:79]\n"
"v_exp_f32_e64 v151, v151\n"
"v_cvt_pk_bf16_f32 v226, v148, v149\n"
"v_cvt_pk_bf16_f32 v227, v150, v151\n"
"v_exp_f32_e64 v152, v152\n"
"v_mfma_f32_16x16x32_bf16 v[144:147], a[168:171], v[56:59], v[184:187]\n"
"v_exp_f32 v153, v153\n"
"v_exp_f32 v154, v154\n"
"v_mfma_f32_16x16x32_bf16 v[148:151], a[172:175], v[56:59], v[184:187]\n"
"v_exp_f32 v155, v155\n"
"v_exp_f32_e64 v156, v156\n"
"s_waitcnt lgkmcnt(0)\n"
"v_mfma_f32_16x16x32_bf16 v[96:99], a[176:179], v[224:227], v[96:99]\n"
"v_cvt_pk_bf16_f32 v228, v152, v153\n"
"v_exp_f32_e64 v157, v157\n"
"v_mfma_f32_16x16x32_bf16 v[100:103], a[180:183], v[224:227], v[100:103]\n"
"v_exp_f32_e64 v158, v158\n"
"v_cvt_pk_bf16_f32 v229, v154, v155\n"
"v_mfma_f32_16x16x32_bf16 v[64:67], a[184:187], v[224:227], v[64:67]\n"
"v_exp_f32_e64 v159, v159\n"
"v_cvt_pk_bf16_f32 v230, v156, v157\n"
"v_cvt_pk_bf16_f32 v231, v158, v159\n"
"v_exp_f32_e64 v144, v144\n"
"v_mfma_f32_16x16x32_bf16 v[152:155], a[168:171], v[60:63], v[196:199]\n"
"ds_read_b128 a[168:171], v22 offset:8192\n"
"v_exp_f32 v145, v145\n"
"v_exp_f32 v146, v146\n"
"v_mfma_f32_16x16x32_bf16 v[156:159], a[172:175], v[60:63], v[196:199]\n"
"ds_read_b128 a[172:175], v22 offset:9216\n"
"v_exp_f32 v147, v147\n"
"v_exp_f32 v148, v148\n"
"v_mfma_f32_16x16x32_bf16 v[104:107], a[176:179], v[228:231], v[104:107]\n"
"v_cvt_pk_bf16_f32 v224, v144, v145\n"
"v_exp_f32_e64 v149, v149\n"
"v_mfma_f32_16x16x32_bf16 v[108:111], a[180:183], v[228:231], v[108:111]\n"
"v_exp_f32_e64 v150, v150\n"
"v_cvt_pk_bf16_f32 v225, v146, v147\n"
"v_mfma_f32_16x16x32_bf16 v[68:71], a[184:187], v[228:231], v[68:71]\n"
"v_exp_f32_e64 v151, v151\n"
"v_cvt_pk_bf16_f32 v226, v148, v149\n"
"v_cvt_pk_bf16_f32 v227, v150, v151\n"
"v_exp_f32 v152, v152\n"
"s_waitcnt lgkmcnt(0)\n"
"v_mfma_f32_16x16x32_bf16 v[144:147], a[168:171], v[48:51], v[160:163]\n"
"v_exp_f32 v153, v153\n"
"v_exp_f32 v154, v154\n"
"v_mfma_f32_16x16x32_bf16 v[148:151], a[172:175], v[48:51], v[160:163]\n"
"v_exp_f32 v155, v155\n"
"v_exp_f32 v156, v156\n"
"v_mfma_f32_16x16x32_bf16 v[112:115], a[176:179], v[224:227], v[112:115]\n"
"v_cvt_pk_bf16_f32 v228, v152, v153\n"
"v_exp_f32_e64 v157, v157\n"
"v_mfma_f32_16x16x32_bf16 v[116:119], a[180:183], v[224:227], v[116:119]\n"
"v_exp_f32_e64 v158, v158\n"
"v_cvt_pk_bf16_f32 v229, v154, v155\n"
"v_mfma_f32_16x16x32_bf16 v[72:75], a[184:187], v[224:227], v[72:75]\n"
"v_exp_f32_e64 v159, v159\n"
"v_cvt_pk_bf16_f32 v230, v156, v157\n"
"v_cvt_pk_bf16_f32 v231, v158, v159\n"
"v_exp_f32_e64 v144, v144\n"
"v_mfma_f32_16x16x32_bf16 v[152:155], a[168:171], v[52:55], v[172:175]\n"
"v_exp_f32 v145, v145\n"
"v_exp_f32 v146, v146\n"
"v_mfma_f32_16x16x32_bf16 v[156:159], a[172:175], v[52:55], v[172:175]\n"
"v_exp_f32 v147, v147\n"
"v_exp_f32 v148, v148\n"
"v_mfma_f32_16x16x32_bf16 v[120:123], a[176:179], v[228:231], v[120:123]\n"
"ds_read_b128 a[176:179], v22 offset:40960\n"
"v_cvt_pk_bf16_f32 v224, v144, v145\n"
"v_exp_f32_e64 v149, v149\n"
"v_mfma_f32_16x16x32_bf16 v[124:127], a[180:183], v[228:231], v[124:127]\n"
"ds_read_b128 a[180:183], v22 offset:41984\n"
"v_exp_f32_e64 v150, v150\n"
"v_cvt_pk_bf16_f32 v225, v146, v147\n"
"v_mfma_f32_16x16x32_bf16 v[76:79], a[184:187], v[228:231], v[76:79]\n"
"v_exp_f32_e64 v151, v151\n"
"v_cvt_pk_bf16_f32 v226, v148, v149\n"
"v_cvt_pk_bf16_f32 v227, v150, v151\n"
"v_exp_f32_e64 v152, v152\n"
"v_mfma_f32_16x16x32_bf16 v[144:147], a[168:171], v[56:59], v[184:187]\n"
"v_exp_f32 v153, v153\n"
"v_exp_f32 v154, v154\n"
"v_mfma_f32_16x16x32_bf16 v[148:151], a[172:175], v[56:59], v[184:187]\n"
"v_exp_f32 v155, v155\n"
"v_exp_f32_e64 v156, v156\n"
"s_waitcnt lgkmcnt(0)\n"
"v_mfma_f32_16x16x32_bf16 v[96:99], a[176:179], v[224:227], v[96:99]\n"
"v_cvt_pk_bf16_f32 v228, v152, v153\n"
"v_exp_f32_e64 v157, v157\n"
"v_mfma_f32_16x16x32_bf16 v[100:103], a[180:183], v[224:227], v[100:103]\n"
"v_exp_f32_e64 v158, v158\n"
"v_cvt_pk_bf16_f32 v229, v154, v155\n"
"v_mfma_f32_16x16x32_bf16 v[64:67], a[184:187], v[224:227], v[64:67]\n"
"v_exp_f32_e64 v159, v159\n"
"v_cvt_pk_bf16_f32 v230, v156, v157\n"
"v_cvt_pk_bf16_f32 v231, v158, v159\n"
"v_exp_f32_e64 v144, v144\n"
"v_mfma_f32_16x16x32_bf16 v[152:155], a[168:171], v[60:63], v[196:199]\n"
"ds_read_b128 a[168:171], v22 offset:6144\n"
"v_exp_f32 v145, v145\n"
"v_exp_f32 v146, v146\n"
"v_mfma_f32_16x16x32_bf16 v[156:159], a[172:175], v[60:63], v[196:199]\n"
"ds_read_b128 a[172:175], v22 offset:7168\n"
"v_exp_f32 v147, v147\n"
"v_exp_f32 v148, v148\n"
"v_mfma_f32_16x16x32_bf16 v[104:107], a[176:179], v[228:231], v[104:107]\n"
"v_cvt_pk_bf16_f32 v224, v144, v145\n"
"v_exp_f32_e64 v149, v149\n"
"v_mfma_f32_16x16x32_bf16 v[108:111], a[180:183], v[228:231], v[108:111]\n"
"v_exp_f32_e64 v150, v150\n"
"v_cvt_pk_bf16_f32 v225, v146, v147\n"
"v_mfma_f32_16x16x32_bf16 v[68:71], a[184:187], v[228:231], v[68:71]\n"
"v_exp_f32_e64 v151, v151\n"
"v_cvt_pk_bf16_f32 v226, v148, v149\n"
"v_cvt_pk_bf16_f32 v227, v150, v151\n"
"v_exp_f32 v152, v152\n"
"s_waitcnt lgkmcnt(0)\n"
"v_mfma_f32_16x16x32_bf16 v[144:147], a[168:171], v[48:51], v[160:163]\n"
"v_exp_f32 v153, v153\n"
"v_exp_f32 v154, v154\n"
"v_mfma_f32_16x16x32_bf16 v[148:151], a[172:175], v[48:51], v[160:163]\n"
"v_exp_f32 v155, v155\n"
"v_exp_f32 v156, v156\n"
"v_mfma_f32_16x16x32_bf16 v[112:115], a[176:179], v[224:227], v[112:115]\n"
"v_cvt_pk_bf16_f32 v228, v152, v153\n"
"v_exp_f32_e64 v157, v157\n"
"v_mfma_f32_16x16x32_bf16 v[116:119], a[180:183], v[224:227], v[116:119]\n"
"v_exp_f32_e64 v158, v158\n"
"v_cvt_pk_bf16_f32 v229, v154, v155\n"
"v_mfma_f32_16x16x32_bf16 v[72:75], a[184:187], v[224:227], v[72:75]\n"
"v_exp_f32_e64 v159, v159\n"
"v_cvt_pk_bf16_f32 v230, v156, v157\n"
"v_cvt_pk_bf16_f32 v231, v158, v159\n"
"s_cmp_eq_u32 s91, 3\n"
"s_cbranch_scc1 .Lax3s\n"
"v_exp_f32_e64 v144, v144\n"
"v_mfma_f32_16x16x32_bf16 v[152:155], a[168:171], v[52:55], v[172:175]\n"
"v_exp_f32 v145, v145\n"
"v_exp_f32 v146, v146\n"
"v_mfma_f32_16x16x32_bf16 v[156:159], a[172:175], v[52:55], v[172:175]\n"
"v_exp_f32 v147, v147\n"
"v_exp_f32 v148, v148\n"
"v_mfma_f32_16x16x32_bf16 v[120:123], a[176:179], v[228:231], v[120:123]\n"
"ds_read_b128 a[176:179], v22 offset:38912\n"
"v_cvt_pk_bf16_f32 v224, v144, v145\n"
"v_exp_f32_e64 v149, v149\n"
"v_mfma_f32_16x16x32_bf16 v[124:127], a[180:183], v[228:231], v[124:127]\n"
"ds_read_b128 a[180:183], v22 offset:39936\n"
"v_exp_f32_e64 v150, v150\n"
"v_cvt_pk_bf16_f32 v225, v146, v147\n"
"v_mfma_f32_16x16x32_bf16 v[76:79], a[184:187], v[228:231], v[76:79]\n"
"v_exp_f32_e64 v151, v151\n"
"v_cvt_pk_bf16_f32 v226, v148, v149\n"
"v_cvt_pk_bf16_f32 v227, v150, v151\n"
"v_exp_f32_e64 v152, v152\n"
"v_mfma_f32_16x16x32_bf16 v[144:147], a[168:171], v[56:59], v[184:187]\n"
"v_exp_f32 v153, v153\n"
"v_exp_f32 v154, v154\n"
"v_mfma_f32_16x16x32_bf16 v[148:151], a[172:175], v[56:59], v[184:187]\n"
"v_exp_f32 v155, v155\n"
"v_exp_f32_e64 v156, v156\n"
"s_waitcnt lgkmcnt(0)\n"
"v_mfma_f32_16x16x32_bf16 v[96:99], a[176:179], v[224:227], v[96:99]\n"
"v_cvt_pk_bf16_f32 v228, v152, v153\n"
"v_exp_f32_e64 v157, v157\n"
"v_mfma_f32_16x16x32_bf16 v[100:103], a[180:183], v[224:227], v[100:103]\n"
"v_exp_f32_e64 v158, v158\n"
"v_cvt_pk_bf16_f32 v229, v154, v155\n"
"v_mfma_f32_16x16x32_bf16 v[64:67], a[184:187], v[224:227], v[64:67]\n"
"v_exp_f32_e64 v159, v159\n"
"v_cvt_pk_bf16_f32 v230, v156, v157\n"
"v_cvt_pk_bf16_f32 v231, v158, v159\n"
"v_exp_f32_e64 v144, v144\n"
"v_mfma_f32_16x16x32_bf16 v[152:155], a[168:171], v[60:63], v[196:199]\n"
"ds_read_b128 a[168:171], v22 offset:4096\n"
"v_exp_f32 v145, v145\n"
"v_exp_f32 v146, v146\n"
"v_mfma_f32_16x16x32_bf16 v[156:159], a[172:175], v[60:63], v[196:199]\n"
"ds_read_b128 a[172:175], v22 offset:5120\n"
"v_exp_f32 v147, v147\n"
"v_exp_f32 v148, v148\n"
"v_mfma_f32_16x16x32_bf16 v[104:107], a[176:179], v[228:231], v[104:107]\n"
"v_cvt_pk_bf16_f32 v224, v144, v145\n"
"v_exp_f32_e64 v149, v149\n"
"v_mfma_f32_16x16x32_bf16 v[108:111], a[180:183], v[228:231], v[108:111]\n"
"v_exp_f32_e64 v150, v150\n"
"v_cvt_pk_bf16_f32 v225, v146, v147\n"
"v_mfma_f32_16x16x32_bf16 v[68:71], a[184:187], v[228:231], v[68:71]\n"
"v_exp_f32_e64 v151, v151\n"
"v_cvt_pk_bf16_f32 v226, v148, v149\n"
"v_cvt_pk_bf16_f32 v227, v150, v151\n"
"v_exp_f32 v152, v152\n"
"s_waitcnt lgkmcnt(0)\n"
"v_mfma_f32_16x16x32_bf16 v[144:147], a[168:171], v[48:51], v[160:163]\n"
"v_exp_f32 v153, v153\n"
"v_exp_f32 v154, v154\n"
"v_mfma_f32_16x16x32_bf16 v[148:151], a[172:175], v[48:51], v[160:163]\n"
"v_exp_f32 v155, v155\n"
"v_exp_f32 v156, v156\n"
"v_mfma_f32_16x16x32_bf16 v[112:115], a[176:179], v[224:227], v[112:115]\n"
"v_cvt_pk_bf16_f32 v228, v152, v153\n"
"v_exp_f32_e64 v157, v157\n"
"v_mfma_f32_16x16x32_bf16 v[116:119], a[180:183], v[224:227], v[116:119]\n"
"v_exp_f32_e64 v158, v158\n"
"v_cvt_pk_bf16_f32 v229, v154, v155\n"
"v_mfma_f32_16x16x32_bf16 v[72:75], a[184:187], v[224:227], v[72:75]\n"
"v_exp_f32_e64 v159, v159\n"
"v_cvt_pk_bf16_f32 v230, v156, v157\n"
"v_cvt_pk_bf16_f32 v231, v158, v159\n"
"v_exp_f32_e64 v144, v144\n"
"v_mfma_f32_16x16x32_bf16 v[152:155], a[168:171], v[52:55], v[172:175]\n"
"v_exp_f32 v145, v145\n"
"v_exp_f32 v146, v146\n"
"v_mfma_f32_16x16x32_bf16 v[156:159], a[172:175], v[52:55], v[172:175]\n"
"v_exp_f32 v147, v147\n"
"v_exp_f32 v148, v148\n"
"v_mfma_f32_16x16x32_bf16 v[120:123], a[176:179], v[228:231], v[120:123]\n"
"ds_read_b128 a[176:179], v22 offset:36864\n"
"v_cvt_pk_bf16_f32 v224, v144, v145\n"
"v_exp_f32_e64 v149, v149\n"
"v_mfma_f32_16x16x32_bf16 v[124:127], a[180:183], v[228:231], v[124:127]\n"
"ds_read_b128 a[180:183], v22 offset:37888\n"
"v_exp_f32_e64 v150, v150\n"
"v_cvt_pk_bf16_f32 v225, v146, v147\n"
"v_mfma_f32_16x16x32_bf16 v[76:79], a[184:187], v[228:231], v[76:79]\n"
"v_exp_f32_e64 v151, v151\n"
"v_cvt_pk_bf16_f32 v226, v148, v149\n"
"v_cvt_pk_bf16_f32 v227, v150, v151\n"
"v_exp_f32_e64 v152, v152\n"
"v_mfma_f32_16x16x32_bf16 v[144:147], a[168:171], v[56:59], v[184:187]\n"
"v_exp_f32 v153, v153\n"
"v_exp_f32 v154, v154\n"
"v_mfma_f32_16x16x32_bf16 v[148:151], a[172:175], v[56:59], v[184:187]\n"
"v_exp_f32 v155, v155\n"
"v_exp_f32_e64 v156, v156\n"
"s_waitcnt lgkmcnt(0)\n"
"v_mfma_f32_16x16x32_bf16 v[96:99], a[176:179], v[224:227], v[96:99]\n"
"v_cvt_pk_bf16_f32 v228, v152, v153\n"
"v_exp_f32_e64 v157, v157\n"
"v_mfma_f32_16x16x32_bf16 v[100:103], a[180:183], v[224:227], v[100:103]\n"
"v_exp_f32_e64 v158, v158\n"
"v_cvt_pk_bf16_f32 v229, v154, v155\n"
"v_mfma_f32_16x16x32_bf16 v[64:67], a[184:187], v[224:227], v[64:67]\n"
"v_exp_f32_e64 v159, v159\n"
"v_cvt_pk_bf16_f32 v230, v156, v157\n"
"v_cvt_pk_bf16_f32 v231, v158, v159\n"
"v_exp_f32_e64 v144, v144\n"
"v_mfma_f32_16x16x32_bf16 v[152:155], a[168:171], v[60:63], v[196:199]\n"
"ds_read_b128 a[168:171], v22 offset:2048\n"
"v_exp_f32 v145, v145\n"
"v_exp_f32 v146, v146\n"
"v_mfma_f32_16x16x32_bf16 v[156:159], a[172:175], v[60:63], v[196:199]\n"
"ds_read_b128 a[172:175], v22 offset:3072\n"
"v_exp_f32 v147, v147\n"
"v_exp_f32 v148, v148\n"
"v_mfma_f32_16x16x32_bf16 v[104:107], a[176:179], v[228:231], v[104:107]\n"
"v_cvt_pk_bf16_f32 v224, v144, v145\n"
"v_exp_f32_e64 v149, v149\n"
"v_mfma_f32_16x16x32_bf16 v[108:111], a[180:183], v[228:231], v[108:111]\n"
"v_exp_f32_e64 v150, v150\n"
"v_cvt_pk_bf16_f32 v225, v146, v147\n"
"v_mfma_f32_16x16x32_bf16 v[68:71], a[184:187], v[228:231], v[68:71]\n"
"v_exp_f32_e64 v151, v151\n"
"v_cvt_pk_bf16_f32 v226, v148, v149\n"
"v_cvt_pk_bf16_f32 v227, v150, v151\n"
"v_exp_f32 v152, v152\n"
"s_waitcnt lgkmcnt(0)\n"
"v_mfma_f32_16x16x32_bf16 v[144:147], a[168:171], v[48:51], v[160:163]\n"
"v_exp_f32 v153, v153\n"
"v_exp_f32 v154, v154\n"
"v_mfma_f32_16x16x32_bf16 v[148:151], a[172:175], v[48:51], v[160:163]\n"
"v_exp_f32 v155, v155\n"
"v_exp_f32 v156, v156\n"
"v_mfma_f32_16x16x32_bf16 v[112:115], a[176:179], v[224:227], v[112:115]\n"
"v_cvt_pk_bf16_f32 v228, v152, v153\n"
"v_exp_f32_e64 v157, v157\n"
"v_mfma_f32_16x16x32_bf16 v[116:119], a[180:183], v[224:227], v[116:119]\n"
"v_exp_f32_e64 v158, v158\n"
"v_cvt_pk_bf16_f32 v229, v154, v155\n"
"v_mfma_f32_16x16x32_bf16 v[72:75], a[184:187], v[224:227], v[72:75]\n"
"v_exp_f32_e64 v159, v159\n"
"v_cvt_pk_bf16_f32 v230, v156, v157\n"
"v_cvt_pk_bf16_f32 v231, v158, v159\n"
"s_cmp_eq_u32 s91, 1\n"
"s_cbranch_scc1 .Lax1s\n"
"v_exp_f32_e64 v144, v144\n"
"v_mfma_f32_16x16x32_bf16 v[152:155], a[168:171], v[52:55], v[172:175]\n"
"v_exp_f32 v145, v145\n"
"v_exp_f32 v146, v146\n"
"v_mfma_f32_16x16x32_bf16 v[156:159], a[172:175], v[52:55], v[172:175]\n"
"v_exp_f32 v147, v147\n"
"v_exp_f32 v148, v148\n"
"v_mfma_f32_16x16x32_bf16 v[120:123], a[176:179], v[228:231], v[120:123]\n"
"ds_read_b128 a[176:179], v22 offset:34816\n"
"v_cvt_pk_bf16_f32 v224, v144, v145\n"
"v_exp_f32_e64 v149, v149\n"
"v_mfma_f32_16x16x32_bf16 v[124:127], a[180:183], v[228:231], v[124:127]\n"
"ds_read_b128 a[180:183], v22 offset:35840\n"
"v_exp_f32_e64 v150, v150\n"
"v_cvt_pk_bf16_f32 v225, v146, v147\n"
"v_mfma_f32_16x16x32_bf16 v[76:79], a[184:187], v[228:231], v[76:79]\n"
"v_exp_f32_e64 v151, v151\n"
"v_cvt_pk_bf16_f32 v226, v148, v149\n"
"v_cvt_pk_bf16_f32 v227, v150, v151\n"
"v_exp_f32_e64 v152, v152\n"
"v_mfma_f32_16x16x32_bf16 v[144:147], a[168:171], v[56:59], v[188:191]\n"
"v_exp_f32 v153, v153\n"
"v_exp_f32 v154, v154\n"
"v_mfma_f32_16x16x32_bf16 v[148:151], a[172:175], v[56:59], v[184:187]\n"
"v_exp_f32 v155, v155\n"
"v_exp_f32_e64 v156, v156\n"
"s_waitcnt lgkmcnt(0)\n"
"v_mfma_f32_16x16x32_bf16 v[96:99], a[176:179], v[224:227], v[96:99]\n"
"v_cvt_pk_bf16_f32 v228, v152, v153\n"
"v_exp_f32_e64 v157, v157\n"
"v_mfma_f32_16x16x32_bf16 v[100:103], a[180:183], v[224:227], v[100:103]\n"
"v_exp_f32_e64 v158, v158\n"
"v_cvt_pk_bf16_f32 v229, v154, v155\n"
"v_mfma_f32_16x16x32_bf16 v[64:67], a[184:187], v[224:227], v[64:67]\n"
"v_exp_f32_e64 v159, v159\n"
"v_cvt_pk_bf16_f32 v230, v156, v157\n"
"v_cvt_pk_bf16_f32 v231, v158, v159\n"
"v_exp_f32_e64 v144, v144\n"
"ds_read_b128 a[168:171], v22 offset:0\n"
"v_exp_f32 v145, v145\n"
"v_exp_f32 v146, v146\n"
"v_mfma_f32_16x16x32_bf16 v[156:159], a[172:175], v[60:63], v[200:203]\n"
"ds_read_b128 a[172:175], v22 offset:1024\n"
"v_exp_f32 v147, v147\n"
"v_exp_f32 v148, v148\n"
"v_mfma_f32_16x16x32_bf16 v[104:107], a[176:179], v[228:231], v[104:107]\n"
"v_cvt_pk_bf16_f32 v224, v144, v145\n"
"v_exp_f32_e64 v149, v149\n"
"v_mfma_f32_16x16x32_bf16 v[108:111], a[180:183], v[228:231], v[108:111]\n"
"v_exp_f32_e64 v150, v150\n"
"v_cvt_pk_bf16_f32 v225, v146, v147\n"
"v_mfma_f32_16x16x32_bf16 v[68:71], a[184:187], v[228:231], v[68:71]\n"
"v_exp_f32_e64 v151, v151\n"
"v_cvt_pk_bf16_f32 v226, v148, v149\n"
"v_cvt_pk_bf16_f32 v227, v150, v151\n"
"v_exp_f32 v156, v156\n"
"s_waitcnt lgkmcnt(0)\n"
"v_mfma_f32_16x16x32_bf16 v[144:147], a[168:171], v[48:51], v[164:167]\n"
"v_exp_f32_e64 v157, v157\n"
"v_mfma_f32_16x16x32_bf16 v[148:151], a[172:175], v[48:51], v[160:163]\n"
"v_exp_f32_e64 v158, v158\n"
"v_mfma_f32_16x16x32_bf16 v[112:115], a[176:179], v[224:227], v[112:115]\n"
"v_exp_f32_e64 v159, v159\n"
"v_mfma_f32_16x16x32_bf16 v[116:119], a[180:183], v[224:227], v[116:119]\n"
"v_mov_b64_e64 v[228:229], 0\n"
"v_cvt_pk_bf16_f32 v230, v156, v157\n"
"v_mfma_f32_16x16x32_bf16 v[72:75], a[184:187], v[224:227], v[72:75]\n"
"v_cvt_pk_bf16_f32 v231, v158, v159\n"
"v_exp_f32_e64 v144, v144\n"
"v_mfma_f32_16x16x32_bf16 v[156:159], a[172:175], v[52:55], v[176:179]\n"
"v_exp_f32 v145, v145\n"
"v_exp_f32 v146, v146\n"
"v_mfma_f32_16x16x32_bf16 v[120:123], a[176:179], v[228:231], v[120:123]\n"
"ds_read_b128 a[176:179], v22 offset:32768\n"
"v_exp_f32 v147, v147\n"
"v_exp_f32 v148, v148\n"
"v_mfma_f32_16x16x32_bf16 v[124:127], a[180:183], v[228:231], v[124:127]\n"
"ds_read_b128 a[180:183], v22 offset:33792\n"
"v_cvt_pk_bf16_f32 v224, v144, v145\n"
"v_exp_f32_e64 v149, v149\n"
"v_mfma_f32_16x16x32_bf16 v[76:79], a[184:187], v[228:231], v[76:79]\n"
"v_exp_f32_e64 v150, v150\n"
"v_cvt_pk_bf16_f32 v225, v146, v147\n"
"v_exp_f32_e64 v151, v151\n"
"v_cvt_pk_bf16_f32 v226, v148, v149\n"
"v_cvt_pk_bf16_f32 v227, v150, v151\n"
"v_exp_f32 v156, v156\n"
"s_waitcnt lgkmcnt(0)\n"
"v_mfma_f32_16x16x32_bf16 v[96:99], a[176:179], v[224:227], v[96:99]\n"
"v_exp_f32_e64 v157, v157\n"
"v_mfma_f32_16x16x32_bf16 v[100:103], a[180:183], v[224:227], v[100:103]\n"
"v_exp_f32_e64 v158, v158\n"
"v_mfma_f32_16x16x32_bf16 v[64:67], a[184:187], v[224:227], v[64:67]\n"
"v_exp_f32 v159, v159\n"
"v_mov_b64 v[228:229], 0\n"
"v_cvt_pk_bf16_f32 v230, v156, v157\n"
"v_cvt_pk_bf16_f32 v231, v158, v159\n"
"s_nop 1\n"
"v_mfma_f32_16x16x32_bf16 v[104:107], a[176:179], v[228:231], v[104:107]\n"
"v_mfma_f32_16x16x32_bf16 v[108:111], a[180:183], v[228:231], v[108:111]\n"
"v_mfma_f32_16x16x32_bf16 v[68:71], a[184:187], v[228:231], v[68:71]\n"
"s_nop 7\n"
"s_branch .Lnorm\n"
      : : "{s[0:1]}"(__builtin_amdgcn_kernarg_segment_ptr()), "{s2}"(blockIdx.x), "{v0}"(threadIdx.x)
      : "memory","scc","vcc","v1","v2","v3","v4","v5","v6","v7","v8","v9","v10","v11","v12","v13","v14","v15","v16","v17","v18","v19","v20","v21","v22","v23","v24","v25","v26","v27","v28","v29","v30","v31","v32","v33","v34","v35","v36","v37","v38","v39","v40","v41","v42","v43","v44","v45","v46","v47","v48","v49","v50","v51","v52","v53","v54","v55","v56","v57","v58","v59","v60","v61","v62","v63","v64","v65","v66","v67","v68","v69","v70","v71","v72","v73","v74","v75","v76","v77","v78","v79","v80","v81","v82","v83","v84","v85","v86","v87","v88","v89","v90","v91","v92","v93","v94","v95","v96","v97","v98","v99","v100","v101","v102","v103","v104","v105","v106","v107","v108","v109","v110","v111","v112","v113","v114","v115","v116","v117","v118","v119","v120","v121","v122","v123","v124","v125","v126","v127","v128","v129","v130","v131","v132","v133","v134","v135","v136","v137","v138","v139","v140","v141","v142","v143","v144","v145","v146","v147","v148","v149","v150","v151","v152","v153","v154","v155","v156","v157","v158","v159","v160","v161","v162","v163","v164","v165","v166","v167","v168","v169","v170","v171","v172","v173","v174","v175","v176","v177","v178","v179","v180","v181","v182","v183","v184","v185","v186","v187","v188","v189","v190","v191","v192","v193","v194","v195","v196","v197","v198","v199","v200","v201","v202","v203","v204","v205","v206","v207","v208","v209","v210","v211","v212","v213","v214","v215","v216","v217","v218","v219","v220","v221","v222","v223","v224","v225","v226","v227","v228","v229","v230","v231","v232","v233","v234","v235","v236","v237","v238","v239","v240","v241","v242","v243","v244","v245","v246","v247","v248","v249","v250","v251","v252","v253","v254","v255","a0","a1","a2","a3","a4","a5","a6","a7","a8","a9","a10","a11","a12","a13","a14","a15","a16","a17","a18","a19","a20","a21","a22","a23","a24","a25","a26","a27","a28","a29","a30","a31","a32","a33","a34","a35","a36","a37","a38","a39","a40","a41","a42","a43","a44","a45","a46","a47","a48","a49","a50","a51","a52","a53","a54","a55","a56","a57","a58","a59","a60","a61","a62","a63","a64","a65","a66","a67","a68","a69","a70","a71","a72","a73","a74","a75","a76","a77","a78","a79","a80","a81","a82","a83","a84","a85","a86","a87","a88","a89","a90","a91","a92","a93","a94","a95","a96","a97","a98","a99","a100","a101","a102","a103","a104","a105","a106","a107","a108","a109","a110","a111","a112","a113","a114","a115","a116","a117","a118","a119","a120","a121","a122","a123","a124","a125","a126","a127","a128","a129","a130","a131","a132","a133","a134","a135","a136","a137","a138","a139","a140","a141","a142","a143","a144","a145","a146","a147","a148","a149","a150","a151","a152","a153","a154","a155","a156","a157","a158","a159","a160","a161","a162","a163","a164","a165","a166","a167","a168","a169","a170","a171","a172","a173","a174","a175","a176","a177","a178","a179","a180","a181","a182","a183","a184","a185","a186","a187","a188","a189","a190","a191","a192","a193","a194","a195","a196","a197","a198","a199","a200","a201","a202","a203","a204","a205","a206","a207","a208","a209","a210","a211","a212","a213","a214","a215","a216","a217","a218","a219","a220","a221","a222","a223","a224","a225","a226","a227","a228","a229","a230","a231","a232","a233","a234","a235","a236","a237","a238","a239","a240","a241","a242","a243","a244","a245","a246","a247","a248","a249","a250","a251","a252","a253","a254","a255","s3","s4","s5","s6","s7","s8","s9","s10","s11","s12","s13","s14","s15","s16","s17","s18","s19","s20","s21","s22","s23","s24","s25","s26","s27","s28","s29","s30","s31","s33","s34","s35","s36","s37","s38","s39","s40","s41","s42","s43","s44","s45","s46","s47","s48","s49","s50","s51","s52","s53","s54","s55","s56","s57","s58","s59","s60","s61","s62","s63","s64","s65","s66","s67","s68","s69","s70","s71","s72","s73","s74","s75","s76","s77","s78","s79","s80","s81","s82","s83","s84","s85","s86","s87","s88","s89","s90","s91","s92","s93","s94","s95","s96","s97","s98","s99");
}
extern "C" void kernel_launch(void* const* d_in, const int* in_sizes, int n_in, void* d_out, int out_size, void* d_ws, size_t ws_size, hipStream_t stream){
  (void)in_sizes; (void)n_in; (void)out_size; (void)ws_size;
  const unsigned F = 0x20000u;
  Args a;
  a.x    = Buf{d_in[0], 2048u*2048u, F};
  a.wqkv = Buf{d_in[1], 1024u*6144u, F};
  a.w1   = Buf{d_in[4], 1024u*7168u, F};
  a.ws   = Buf{d_ws, 40894464u, F};
  a.w2   = Buf{d_in[6], 3584u*2048u, F};
  a.wo   = Buf{d_in[2], 1024u*2048u, F};
  a.b1   = Buf{d_in[5], 3584u*2u, F};
  a.b2   = Buf{d_in[7], 1024u*2u, F};
  a.out  = Buf{d_out, 2048u*2048u, F};
  (void)hipFuncSetAttribute((const void*)fused_block, hipFuncAttributeMaxDynamicSharedMemorySize, 163840);
  fused_block<<<256,256,163840,stream>>>(a);
}
```
